```python
import torch

def _softmax(x, axis=-1):
    return torch.softmax(x, dim=axis)
def _gelu(x):
    return torch.nn.functional.gelu(x, approximate="tanh")
def _silu(x):
    return torch.nn.functional.silu(x)
def _sigmoid(x):
    return torch.sigmoid(x)
def _layernorm(x, axis=-1, eps=1e-5):
    m = x.mean(dim=axis, keepdim=True)
    v = x.var(dim=axis, keepdim=True, unbiased=False)
    return (x - m) / torch.sqrt(v + eps)
def _scatter_add(tgt, idx, src, axis=0):
    return tgt.clone().index_add_(axis, idx, src)
def _segment_sum(src, seg, n):
    out = torch.zeros((n,) + src.shape[1:], dtype=src.dtype)
    return out.index_add_(0, seg, src)
def _pad(x, pw):
    flat = tuple(v for pair in reversed(pw) for v in pair)
    return torch.nn.functional.pad(x, flat)

kernel_name = 'gated_mix__grouped_kv_mqa__compressed_kv_cla__prompt'

def setup_inputs():

    _w = (lambda a: (lambda t: t.to(torch.bfloat16) if t.is_floating_point() else t)(torch.as_tensor(a)))
    def _A_setup():
        B, S, D, H = 1, 2048, 512, 32
        Dh = D // H
        return {
            "x": _w((lambda *s: torch.randn(*s, dtype=torch.bfloat16) * 0.05)(B, S, D)),
            "wq": _w((lambda *s: torch.randn(*s, dtype=torch.bfloat16) * 1.6)(D, H * Dh)),
            "wk": _w((lambda *s: torch.randn(*s, dtype=torch.bfloat16) * 1.6)(D, Dh)),
            "wv": _w((lambda *s: torch.randn(*s, dtype=torch.bfloat16) * 0.8)(D, Dh)),
            "wo": _w((lambda *s: torch.randn(*s, dtype=torch.bfloat16) * 0.8)(H * Dh, D)),
        }
    _A_in = _A_setup()

    def _B_setup():
        B, H, Sq, Sk, D, M = 1, 32, 2048, 2048, 128, 512
        Dc = 4 * D
        return {
            "x": _w((lambda *s: torch.randn(*s, dtype=torch.bfloat16) * 0.05)(B, Sq, M)),
            "c_kv": _w((lambda *s: torch.randn(*s, dtype=torch.bfloat16) * 1.6)(B, Sk, Dc)),
            "w_q": _w((lambda *s: torch.randn(*s, dtype=torch.bfloat16) * 6.4)(M, H, D) * (M ** -0.5)),
            "w_uk": _w((lambda *s: torch.randn(*s, dtype=torch.bfloat16) * 6.4)(Dc, H, D) * (Dc ** -0.5)),
            "w_uv": _w((lambda *s: torch.randn(*s, dtype=torch.bfloat16) * 1.6)(Dc, H, D) * (Dc ** -0.5)),
            "w_o": _w((lambda *s: torch.randn(*s, dtype=torch.bfloat16) * 1.6)(H, D, M) * ((H * D) ** -0.5)),
        }
    _B_in = _B_setup()

    return {
        "A_x": _A_in["x"],
        "A_wq": _A_in["wq"],
        "A_wk": _A_in["wk"],
        "A_wv": _A_in["wv"],
        "A_wo": _A_in["wo"],
        "B_x": _B_in["x"],
        "B_c_kv": _B_in["c_kv"],
        "B_w_q": _B_in["w_q"],
        "B_w_uk": _B_in["w_uk"],
        "B_w_uv": _B_in["w_uv"],
        "B_w_o": _B_in["w_o"],
        "gate_w": _w((lambda *s: torch.randn(*s, dtype=torch.bfloat16) * 0.1)(512) * 16.0),
    }

def reference(A_x, A_wq, A_wk, A_wv, A_wo, B_x, B_c_kv, B_w_q, B_w_uk, B_w_uv, B_w_o, gate_w):
    def _A_ref(x, wq, wk, wv, wo):
        B, S, D = x.shape
        H = 32
        Dh = D // H
        q = (lambda x, *d: x.permute(*d))((x @ wq).reshape(B, S, H, Dh), 0, 2, 1, 3)
        k = x @ wk
        v = x @ wv
        scores = torch.einsum("bhqd,bkd->bhqk", q, k) * (Dh ** -0.5)
        p = _softmax(scores, axis=-1)
        o = torch.einsum("bhqk,bkd->bhqd", p, v)
        return (lambda x, *d: x.permute(*d))(o, 0, 2, 1, 3).reshape(B, S, H * Dh) @ wo

    def _B_ref(x, c_kv, w_q, w_uk, w_uv, w_o):
        B, Sq, M = x.shape[0], x.shape[1], x.shape[2]
        Sk = c_kv.shape[1]
        H, D = w_q.shape[1], w_q.shape[2]
        q = torch.einsum("bqm,mhd->bhqd", x, w_q)
        k = torch.einsum("bkc,chd->bhkd", c_kv, w_uk)
        v = torch.einsum("bkc,chd->bhkd", c_kv, w_uv)
        scores = torch.einsum("bhqd,bhkd->bhqk", q, k) * (D ** -0.5)
        off = Sk - Sq
        qi = torch.arange(Sq)[:, None]
        kj = torch.arange(Sk)[None, :]
        cmask = kj <= (qi + off)
        scores = torch.where(cmask[None, None], scores, -1e9)
        p = _softmax(scores, axis=-1)
        ctx = torch.einsum("bhqk,bhkd->bhqd", p, v)
        return torch.einsum("bhqd,hdm->bqm", ctx, w_o)

    x = A_x
    g = _sigmoid((x * gate_w).sum(-1, keepdims=True))
    a = _A_ref(x, A_wq, A_wk, A_wv, A_wo)
    b = _B_ref(x, B_c_kv, B_w_q, B_w_uk, B_w_uv, B_w_o)
    return g * a + (1 - g) * b
```

```cpp
#include <hip/hip_runtime.h>
#include <hip/hip_bf16.h>
#include <stdint.h>
#include <type_traits>

typedef short bf16x8 __attribute__((ext_vector_type(8)));
typedef short s16x4 __attribute__((ext_vector_type(4)));
typedef float f32x16 __attribute__((ext_vector_type(16)));
typedef float f32x4 __attribute__((ext_vector_type(4)));
typedef float f32x2 __attribute__((ext_vector_type(2)));
typedef __bf16 bf16x2_t __attribute__((ext_vector_type(2)));
typedef unsigned u32x4 __attribute__((ext_vector_type(4)));
typedef unsigned u32x2 __attribute__((ext_vector_type(2)));
typedef unsigned short u16;
#define DI __device__ __forceinline__
#define MFMA32(a, b, c) __builtin_amdgcn_mfma_f32_32x32x16_bf16((a), (b), (c), 0, 0, 0)
#define LAS __attribute__((address_space(3)))

constexpr int S = 2048, DM = 512, H = 32;
constexpr size_t WS_KV = 0;
constexpr size_t WS_Q = 32u << 20;
constexpr size_t WS_C = 48u << 20;
constexpr size_t WS_QA = 68u << 20;
constexpr size_t WS_KA = 70u << 20;
constexpr size_t WS_VA = WS_KA + (64u << 10);
constexpr size_t WS_G = WS_KA + (256u << 10);
constexpr size_t WS_P = 72u << 20;

constexpr float LOG2E = 1.4426950408889634f;
constexpr float QB_SCALE = LOG2E * 0.08838834764831845f;
constexpr float QA_SCALE = LOG2E * 0.25f;
constexpr float L_LIMIT = 1.0e27f;

DI unsigned cvtpk(float lo, float hi) { f32x2 v = {lo, hi}; bf16x2_t b = __builtin_convertvector(v, bf16x2_t); return __builtin_bit_cast(unsigned, b); }
DI float bf2f(u16 x) { return __uint_as_float((unsigned)x << 16); }
DI float rbf(float x) { return __uint_as_float(cvtpk(x, 0.f) << 16); }
DI u32x4 pack8(const f32x16& a, int o, float sc) {
  u32x4 w = {cvtpk(a[o] * sc, a[o + 1] * sc), cvtpk(a[o + 2] * sc, a[o + 3] * sc), cvtpk(a[o + 4] * sc, a[o + 5] * sc), cvtpk(a[o + 6] * sc, a[o + 7] * sc)};
  return w;
}
DI float swap_max(float m) { auto r = __builtin_amdgcn_permlane32_swap(__float_as_uint(m), __float_as_uint(m), false, false); return fmaxf(__uint_as_float(r[0]), __uint_as_float(r[1])); }
DI float swap_add(float m) { auto r = __builtin_amdgcn_permlane32_swap(__float_as_uint(m), __float_as_uint(m), false, false); return __uint_as_float(r[0]) + __uint_as_float(r[1]); }
DI unsigned rfl(unsigned x) { return __builtin_amdgcn_readfirstlane(x); }
DI void dma16(unsigned voff, const void* sbase, unsigned ldsdst) {
  asm volatile("s_mov_b32 m0, %2\n\ts_nop 0\n\tglobal_load_lds_dwordx4 %0, %1" ::"v"(voff), "s"(sbase), "s"(ldsdst) : "memory");
}
DI bf16x8 lds_b128(const LAS char* p) { return *(const LAS bf16x8*)p; }
typedef short v4i16_t __attribute__((ext_vector_type(4)));
DI s16x4 lds_tr(const LAS char* p) { return __builtin_bit_cast(s16x4, __builtin_amdgcn_ds_read_tr16_b64_v4i16((LAS v4i16_t*)p)); }
DI bf16x8 cat(s16x4 a, s16x4 b) { return __builtin_shufflevector(a, b, 0, 1, 2, 3, 4, 5, 6, 7); }
#define WAIT_BAR(N) asm volatile("s_waitcnt vmcnt(" #N ") lgkmcnt(0)\n\ts_barrier" ::: "memory")

__global__ __launch_bounds__(64) void k_prepA(const u16* __restrict__ x, const u16* __restrict__ wq, const u16* __restrict__ wk,
                                              const u16* __restrict__ wv, const u16* __restrict__ gw, char* __restrict__ ws) {
  const int lane = threadIdx.x, r = lane & 31, hi = lane >> 5;
  const int tb = blockIdx.x / 18, task = blockIdx.x % 18;
  const u16* xr = x + (size_t)(tb * 32 + r) * DM + 8 * hi;
  const u16* W; int ldw, n0; bool valid = true;
  if (task < 16) { W = wq; ldw = 512; n0 = task * 32 + r; }
  else { W = task == 16 ? wk : wv; ldw = 16; n0 = r & 15; valid = r < 16; }
  f32x16 acc = {};
  float gsum = 0.f;
  for (int s = 0; s < 32; ++s) {
    bf16x8 xf = *(const bf16x8*)(xr + 16 * s);
    bf16x8 wf;
#pragma unroll
    for (int j = 0; j < 8; ++j) { u16 v = W[(size_t)(16 * s + 8 * hi + j) * ldw + n0]; wf[j] = valid ? (short)v : (short)0; }
    if (task == 17) acc = MFMA32(xf, wf, acc); else acc = MFMA32(wf, xf, acc);
    if (task == 16) {
      bf16x8 gf = *(const bf16x8*)(gw + 16 * s + 8 * hi);
#pragma unroll
      for (int j = 0; j < 8; ++j) gsum += rbf(bf2f((u16)xf[j]) * bf2f((u16)gf[j]));
    }
  }
  if (task < 16) {
    *(u32x4*)(ws + WS_QA + ((size_t)((2 * task) * 64 + tb) * 64 + lane) * 16) = pack8(acc, 0, QA_SCALE);
    *(u32x4*)(ws + WS_QA + ((size_t)((2 * task + 1) * 64 + tb) * 64 + lane) * 16) = pack8(acc, 8, QA_SCALE);
  } else if (task == 16) {
    *(u32x4*)(ws + WS_KA + ((size_t)tb * 64 + lane) * 16) = pack8(acc, 0, 1.f);
    gsum = rbf(swap_add(gsum));
    float g = rbf(1.f / (1.f + __expf(-gsum)));
    if (hi == 0) ((float*)(ws + WS_G))[tb * 32 + r] = g;
  } else {
    u32x4 w0 = pack8(acc, 0, 1.f), w1 = pack8(acc, 8, 1.f);
    if (r >= 16) { unsigned c = (r == 16 || r == 20) ? 0x3F803F80u : 0u; w0 = (u32x4){c, c, c, c}; w1 = w0; }
    *(u32x4*)(ws + WS_VA + ((size_t)(2 * tb) * 64 + lane) * 16) = w0;
    *(u32x4*)(ws + WS_VA + ((size_t)(2 * tb + 1) * 64 + lane) * 16) = w1;
  }
}

template <int MI, int NJ, bool SWAP>
DI void gemm_ktile(f32x16 (&acc)[MI][NJ], const LAS char* xs, const LAS char* wsm, const unsigned (&xo)[4], unsigned wo) {
#pragma unroll
  for (int s = 0; s < 4; ++s) {
    bf16x8 xa[MI], wb[NJ];
#pragma unroll
    for (int j = 0; j < NJ; ++j) wb[j] = cat(lds_tr(wsm + wo + s * 8192 + j * 256), lds_tr(wsm + wo + s * 8192 + 2048 + j * 256));
#pragma unroll
    for (int i = 0; i < MI; ++i) xa[i] = lds_b128(xs + xo[s] + i * 4096);
#pragma unroll
    for (int i = 0; i < MI; ++i)
#pragma unroll
      for (int j = 0; j < NJ; ++j) acc[i][j] = SWAP ? MFMA32(wb[j], xa[i], acc[i][j]) : MFMA32(xa[i], wb[j], acc[i][j]);
  }
}

__global__ __launch_bounds__(512, 2) void k_gemm1(const u16* __restrict__ x, const u16* __restrict__ ckv, const u16* __restrict__ w_q,
                                                  const u16* __restrict__ w_uk, const u16* __restrict__ w_uv, char* __restrict__ ws) {
  extern __shared__ __attribute__((aligned(16))) char smem[];
  const LAS char* lds = (const LAS char*)smem;
  const unsigned lds0 = (unsigned)(uintptr_t)smem;
  const int tid = threadIdx.x, lane = tid & 63, r = lane & 31, hi = lane >> 5;
  const int wid = rfl(tid >> 6);
  const int xcd = blockIdx.x & 7, idx = blockIdx.x >> 3, h = xcd * 4 + (idx & 3), m = idx >> 2;
  unsigned xsrc[2];
#pragma unroll
  for (int par = 0; par < 2; ++par) xsrc[par] = (lane >> 3) * 1024 + (((lane & 7) ^ ((par << 2) | (lane >> 4))) * 16);
  const unsigned wsrc = ((lane >> 2) & 3) * 8192 + (lane >> 4) * 64 + (lane & 3) * 16;
  unsigned xo[4];
#pragma unroll
  for (int s = 0; s < 4; ++s) xo[s] = r * 128 + (((2 * s + hi) ^ ((r >> 1) & 7)) * 16);
  const unsigned wo = hi * 4096 + ((lane >> 2) & 3) * 64 + ((lane >> 4) & 1) * 32 + (lane & 3) * 8;

  const char* Xc = (const char*)ckv + (size_t)m * 256 * 1024;
  const char* Xx = (const char*)x + (size_t)m * 256 * 1024;
  const char* Wk = (const char*)w_uk + h * 256;
  const char* Wv = (const char*)w_uv + h * 256;
  const char* Wq = (const char*)w_q + h * 256;
  auto stage = [&](int t) {
    const unsigned sl = lds0 + (t & 1) * 65536;
    const int kt = t & 7;
    const char* X = (t < 8 ? Xc : Xx) + kt * 128;
#pragma unroll
    for (int p = 0; p < 4; ++p) { const int pc = wid * 4 + p; dma16(xsrc[p & 1], X + (size_t)pc * 8192, sl + pc * 1024); }
    if (t < 8) {
#pragma unroll
      for (int p = 0; p < 4; ++p) { const int pc = wid * 4 + p, kb4 = pc >> 1, half = pc & 1;
        dma16(wsrc, (half ? Wv : Wk) + (size_t)(kt * 64 + kb4 * 4) * 8192, sl + 32768 + pc * 1024); }
    } else {
#pragma unroll
      for (int p = 0; p < 2; ++p) { const int kb4 = wid * 2 + p;
        dma16(wsrc, Wq + (size_t)(kt * 64 + kb4 * 4) * 8192, sl + 32768 + kb4 * 2048); }
    }
  };
  stage(0);
  WAIT_BAR(0);
  auto kv_phase = [&](auto swp) {
    constexpr bool SWAP = decltype(swp)::value;
    const int wr = wid & 1, wc = wid >> 1;
    f32x16 acc[4][2] = {};
    for (int t = 0; t < 8; ++t) {
      stage(t + 1);
      const LAS char* sl = lds + (t & 1) * 65536;
      gemm_ktile<4, 2, SWAP>(acc, sl + wr * 16384, sl + 32768 + wc * 512, xo, wo);
      WAIT_BAR(0);
    }
#pragma unroll
    for (int i = 0; i < 4; ++i)
#pragma unroll
      for (int j = 0; j < 2; ++j) {
        const int u = m * 8 + wr * 4 + i, db = (wc & 1) * 2 + j;
        char* ub = ws + WS_KV + (size_t)(h * 64 + u) * 16384 + lane * 16;
        if (SWAP) { *(u32x4*)(ub + (2 * db) * 1024) = pack8(acc[i][j], 0, 1.f); *(u32x4*)(ub + (2 * db + 1) * 1024) = pack8(acc[i][j], 8, 1.f); }
        else { *(u32x4*)(ub + 8192 + db * 1024) = pack8(acc[i][j], 0, 1.f); *(u32x4*)(ub + 8192 + 4096 + db * 1024) = pack8(acc[i][j], 8, 1.f); }
      }
  };
  if (wid < 4) kv_phase(std::true_type{}); else kv_phase(std::false_type{});
  {
    const int wr = wid & 3, wc = wid >> 2;
    f32x16 acc[2][2] = {};
    for (int t = 8; t < 16; ++t) {
      if (t + 1 < 16) stage(t + 1);
      const LAS char* sl = lds + (t & 1) * 65536;
      gemm_ktile<2, 2, true>(acc, sl + wr * 8192, sl + 32768 + wc * 512, xo, wo);
      WAIT_BAR(0);
    }
#pragma unroll
    for (int i = 0; i < 2; ++i)
#pragma unroll
      for (int j = 0; j < 2; ++j) {
        const int qb = m * 8 + wr * 2 + i, db = wc * 2 + j;
        char* ub = ws + WS_Q + (size_t)(h * 64 + qb) * 8192 + lane * 16;
        *(u32x4*)(ub + (2 * db) * 1024) = pack8(acc[i][j], 0, QB_SCALE); *(u32x4*)(ub + (2 * db + 1) * 1024) = pack8(acc[i][j], 8, QB_SCALE);
      }
  }
}

DI float rowmax16(const f32x16& s) {
  float a = fmaxf(fmaxf(s[0], s[1]), s[2]);
#pragma unroll
  for (int r = 3; r < 15; r += 2) a = fmaxf(fmaxf(a, s[r]), s[r + 1]);
  a = fmaxf(a, s[15]);
  return swap_max(a);
}
DI void mask_diag(f32x16& s, int r, int hi) {
#pragma unroll
  for (int e = 0; e < 16; ++e) { const int key = (e & 3) + 8 * (e >> 2) + 4 * hi; if (key > r) s[e] = -INFINITY; }
}

__global__ __launch_bounds__(512, 2) void k_attnB(char* __restrict__ ws) {
  extern __shared__ __attribute__((aligned(16))) char smem[];
  const LAS char* lds = (const LAS char*)smem;
  const unsigned lds0 = (unsigned)(uintptr_t)smem;
  const int tid = threadIdx.x, lane = tid & 63, r = lane & 31, hi = lane >> 5;
  const int wid = rfl(tid >> 6);
  const int xcd = blockIdx.x & 7, idx = blockIdx.x >> 3, h = xcd * 4 + (idx & 3), pi = idx >> 2;
  const int rb = wid < 4 ? (15 - pi) * 4 + wid : pi * 4 + (7 - wid);
  const int Ttop = 31 - 2 * pi;
  const char* KV = ws + WS_KV + (size_t)h * 64 * 16384;
  bf16x8 q[8];
#pragma unroll
  for (int s = 0; s < 8; ++s) q[s] = *(const bf16x8*)(ws + WS_Q + (size_t)(h * 64 + rb) * 8192 + s * 1024 + lane * 16);
  const float gate = ((const float*)(ws + WS_G))[rb * 32 + r];
  LAS volatile int* flag = (LAS volatile int*)(lds + 3 * 32768);
  auto stage = [&](int T) {
    const unsigned sl = lds0 + (T % 3) * 32768 + wid * 4096;
    const char* src = KV + (size_t)T * 32768 + wid * 4096;
#pragma unroll
    for (int p = 0; p < 4; ++p) dma16(lane * 16, src + p * 1024, sl + p * 1024);
  };
  float mgiven = 0.f;
  for (int attempt = 0; attempt < 2; ++attempt) {
    f32x16 o[4] = {};
    f32x16 negm = {};
    float l = 0.f;
    if (tid == 0) *flag = 0;
    stage(Ttop); stage(Ttop - 1);
    WAIT_BAR(4);
    for (int T = Ttop; T >= 0; --T) {
      if (T >= 2) stage(T - 2);
      const LAS char* sl = lds + (T % 3) * 32768 + lane * 16;
#pragma unroll
      for (int uu = 1; uu >= 0; --uu) {
        const int u = 2 * T + uu;
        if (u <= rb) {
          const LAS char* kp = sl + uu * 16384;
          f32x16 s = negm;
#pragma unroll
          for (int ks = 0; ks < 8; ++ks) s = MFMA32(lds_b128(kp + ks * 1024), q[ks], s);
          if (u == rb) {
            mask_diag(s, r, hi);
            const float m0 = attempt ? mgiven : rowmax16(s);
#pragma unroll
            for (int e = 0; e < 16; ++e) { s[e] -= m0; negm[e] = -m0; }
          }
          float ps = 0.f;
#pragma unroll
          for (int e = 0; e < 16; ++e) { s[e] = __builtin_amdgcn_exp2f(s[e]); ps += s[e]; }
          l += ps;
          const bf16x8 p0 = __builtin_bit_cast(bf16x8, pack8(s, 0, 1.f)), p1 = __builtin_bit_cast(bf16x8, pack8(s, 8, 1.f));
#pragma unroll
          for (int db = 0; db < 4; ++db) o[db] = MFMA32(lds_b128(kp + 8192 + db * 1024), p0, o[db]);
#pragma unroll
          for (int db = 0; db < 4; ++db) o[db] = MFMA32(lds_b128(kp + 8192 + 4096 + db * 1024), p1, o[db]);
        }
      }
      if (T >= 2) WAIT_BAR(4); else WAIT_BAR(0);
    }
    l = swap_add(l);
    float om = 0.f;
#pragma unroll
    for (int db = 0; db < 4; ++db)
#pragma unroll
      for (int e = 0; e < 16; ++e) om = fmaxf(om, fabsf(o[db][e]));
    const bool bad = !(l < L_LIMIT) || !(om < 3.0e38f);
    if (attempt == 0) {
      if (__any(bad)) *flag = 1;
      __syncthreads();
      const int f = *flag;
      __syncthreads();
      if (f) {
        float mx = -INFINITY;
        for (int u = rb; u >= 0; --u) {
          f32x16 s = {};
#pragma unroll
          for (int ks = 0; ks < 8; ++ks) s = MFMA32(*(const bf16x8*)(KV + (size_t)u * 16384 + ks * 1024 + lane * 16), q[ks], s);
          if (u == rb) mask_diag(s, r, hi);
          mx = fmaxf(mx, rowmax16(s));
        }
        mgiven = mx;
        continue;
      }
    }
    const float sc = rbf(1.f - gate) / l;
    char* cb = ws + WS_C + ((size_t)rb * 288 + h * 8) * 1024 + lane * 16;
#pragma unroll
    for (int db = 0; db < 4; ++db) { *(u32x4*)(cb + (2 * db) * 1024) = pack8(o[db], 0, sc); *(u32x4*)(cb + (2 * db + 1) * 1024) = pack8(o[db], 8, sc); }
    break;
  }
}

__global__ __launch_bounds__(256) void k_attnA(char* __restrict__ ws) {
  const int tid = threadIdx.x, lane = tid & 63, r = lane & 31, hi = lane >> 5;
  const int w = blockIdx.x * 4 + rfl(tid >> 6);
  const int h = w >> 5, qp = w & 31;
  const char* KA = ws + WS_KA + lane * 16;
  const char* VA = ws + WS_VA + lane * 16;
  bf16x8 q[2];
#pragma unroll
  for (int b = 0; b < 2; ++b) q[b] = *(const bf16x8*)(ws + WS_QA + (size_t)(h * 64 + 2 * qp + b) * 1024 + lane * 16);
  float m0[2];
  {
    const bf16x8 kf = *(const bf16x8*)(KA);
    const f32x16 z = {};
#pragma unroll
    for (int b = 0; b < 2; ++b) m0[b] = rowmax16(MFMA32(kf, q[b], z));
  }
  for (int attempt = 0; attempt < 2; ++attempt) {
    f32x16 o[2] = {};
    f32x16 negm[2];
#pragma unroll
    for (int b = 0; b < 2; ++b)
#pragma unroll
      for (int e = 0; e < 16; ++e) negm[b][e] = -m0[b];
    for (int kb = 0; kb < 64; ++kb) {
      const bf16x8 kf = *(const bf16x8*)(KA + kb * 1024);
      const bf16x8 v0 = *(const bf16x8*)(VA + kb * 2048), v1 = *(const bf16x8*)(VA + kb * 2048 + 1024);
#pragma unroll
      for (int b = 0; b < 2; ++b) {
        f32x16 s = MFMA32(kf, q[b], negm[b]);
#pragma unroll
        for (int e = 0; e < 16; ++e) s[e] = __builtin_amdgcn_exp2f(s[e]);
        o[b] = MFMA32(v0, __builtin_bit_cast(bf16x8, pack8(s, 0, 1.f)), o[b]);
        o[b] = MFMA32(v1, __builtin_bit_cast(bf16x8, pack8(s, 8, 1.f)), o[b]);
      }
    }
    bool bad = false;
#pragma unroll
    for (int b = 0; b < 2; ++b) {
      float om = 0.f;
#pragma unroll
      for (int e = 0; e < 8; ++e) om = fmaxf(om, fabsf(o[b][e]));
      bad |= !(o[b][8] < L_LIMIT) || !(om < 3.0e38f);
    }
    if (attempt == 0 && __any(bad)) {
      float mx[2] = {-INFINITY, -INFINITY};
      const f32x16 z = {};
      for (int kb = 0; kb < 64; ++kb) {
        const bf16x8 kf = *(const bf16x8*)(KA + kb * 1024);
#pragma unroll
        for (int b = 0; b < 2; ++b) mx[b] = fmaxf(mx[b], rowmax16(MFMA32(kf, q[b], z)));
      }
      m0[0] = mx[0]; m0[1] = mx[1];
      continue;
    }
#pragma unroll
    for (int b = 0; b < 2; ++b) {
      const int qb = 2 * qp + b;
      const float sc = ((const float*)(ws + WS_G))[qb * 32 + r] / o[b][8];
      *(u32x4*)(ws + WS_C + ((size_t)qb * 288 + 256 + h) * 1024 + lane * 16) = pack8(o[b], 0, sc);
    }
    break;
  }
}

__global__ __launch_bounds__(256, 2) void k_outB(const u16* __restrict__ w_o, char* __restrict__ ws) {
  extern __shared__ __attribute__((aligned(16))) char smem[];
  const LAS char* lds = (const LAS char*)smem;
  const unsigned lds0 = (unsigned)(uintptr_t)smem;
  const int tid = threadIdx.x, lane = tid & 63, hi = lane >> 5;
  const int wid = rfl(tid >> 6);
  const int xcd = blockIdx.x & 7, idx = blockIdx.x >> 3;
  const int mt = xcd * 2 + (idx & 1), nt = (idx >> 1) & 3, sp = idx >> 3;
  const unsigned wsrc = ((lane >> 2) & 3) * 1024 + (lane >> 4) * 64 + (lane & 3) * 16;
  const unsigned wo = hi * 1024 + ((lane >> 2) & 3) * 64 + ((lane >> 4) & 1) * 32 + (lane & 3) * 8;
  const char* X = ws + WS_C + ((size_t)(mt * 4 + wid) * 288 + sp * 64) * 1024;
  const char* W = (const char*)w_o + (size_t)sp * 1024 * 1024 + nt * 256;
  auto stage = [&](int t) {
    const unsigned sl = lds0 + (t % 3) * 32768;
#pragma unroll
    for (int p = 0; p < 4; ++p) dma16(lane * 16, X + (size_t)(t * 4 + p) * 1024, sl + wid * 4096 + p * 1024);
#pragma unroll
    for (int p = 0; p < 4; ++p) { const int kb4 = wid * 4 + p; dma16(wsrc, W + (size_t)(t * 64 + kb4 * 4) * 1024, sl + 16384 + kb4 * 1024); }
  };
  const int wr = wid & 1, wc = wid >> 1;
  f32x16 acc[2][2] = {};
  stage(0); stage(1);
  WAIT_BAR(8);
  for (int t = 0; t < 16; ++t) {
    if (t + 2 < 16) stage(t + 2);
    const LAS char* sl = lds + (t % 3) * 32768;
#pragma unroll
    for (int s = 0; s < 4; ++s) {
      bf16x8 xa[2], wb[2];
#pragma unroll
      for (int j = 0; j < 2; ++j) wb[j] = cat(lds_tr(sl + 16384 + wo + s * 4096 + (wc * 2 + j) * 256), lds_tr(sl + 16384 + wo + s * 4096 + 2048 + (wc * 2 + j) * 256));
#pragma unroll
      for (int i = 0; i < 2; ++i) xa[i] = lds_b128(sl + (wr * 2 + i) * 4096 + s * 1024 + lane * 16);
#pragma unroll
      for (int i = 0; i < 2; ++i)
#pragma unroll
        for (int j = 0; j < 2; ++j) acc[i][j] = MFMA32(wb[j], xa[i], acc[i][j]);
    }
    if (t + 2 < 16) WAIT_BAR(8); else WAIT_BAR(0);
  }
#pragma unroll
  for (int i = 0; i < 2; ++i)
#pragma unroll
    for (int j = 0; j < 2; ++j) {
      const int tb = mt * 4 + wr * 2 + i, nb = nt * 4 + wc * 2 + j;
      char* pb = ws + WS_P + (((size_t)sp * 64 + tb) * 16 + nb) * 4096 + lane * 16;
#pragma unroll
      for (int g = 0; g < 4; ++g) *(f32x4*)(pb + g * 1024) = (f32x4){acc[i][j][4 * g], acc[i][j][4 * g + 1], acc[i][j][4 * g + 2], acc[i][j][4 * g + 3]};
    }
}

__global__ __launch_bounds__(256) void k_final(const u16* __restrict__ wo_a, char* __restrict__ ws, u16* __restrict__ out) {
  extern __shared__ __attribute__((aligned(16))) char smem[];
  const LAS char* lds = (const LAS char*)smem;
  const unsigned lds0 = (unsigned)(uintptr_t)smem;
  const int tid = threadIdx.x, lane = tid & 63, r = lane & 31, hi = lane >> 5;
  const int wid = rfl(tid >> 6);
  const int xcd = blockIdx.x & 7, idx = blockIdx.x >> 3;
  const int mt = xcd * 4 + (idx & 3), nt = idx >> 2;
  const int tb = mt * 2 + (wid & 1), nbl = wid >> 1, nb = nt * 2 + nbl;
  const unsigned wsrc = ((lane >> 5) * 4 + ((lane >> 2) & 3)) * 1024 + ((lane >> 4) & 1) * 64 + (lane & 3) * 16;
  const char* W = (const char*)wo_a + nt * 128;
#pragma unroll
  for (int p = 0; p < 16; ++p) { const int pc = wid * 16 + p; dma16(wsrc, W + (size_t)pc * 8192, lds0 + pc * 1024); }
  f32x16 acc;
  {
    const char* pb = ws + WS_P + ((size_t)tb * 16 + nb) * 4096 + lane * 16;
#pragma unroll
    for (int g = 0; g < 4; ++g) {
      f32x4 v = *(const f32x4*)(pb + g * 1024);
#pragma unroll
      for (int sp = 1; sp < 4; ++sp) v += *(const f32x4*)(pb + (size_t)sp * 64 * 16 * 4096 + g * 1024);
      acc[4 * g] = v[0]; acc[4 * g + 1] = v[1]; acc[4 * g + 2] = v[2]; acc[4 * g + 3] = v[3];
    }
  }
  bf16x8 xa[32];
  const char* X = ws + WS_C + ((size_t)tb * 288 + 256) * 1024 + lane * 16;
#pragma unroll
  for (int s = 0; s < 32; ++s) xa[s] = *(const bf16x8*)(X + s * 1024);
  WAIT_BAR(0);
  const unsigned wo = hi * 512 + nbl * 256 + ((lane >> 2) & 3) * 64 + ((lane >> 4) & 1) * 32 + (lane & 3) * 8;
#pragma unroll
  for (int s = 0; s < 32; ++s) acc = MFMA32(cat(lds_tr(lds + wo + s * 2048), lds_tr(lds + wo + s * 2048 + 1024)), xa[s], acc);
  u16* op = out + (size_t)(tb * 32 + r) * DM + nb * 32 + 4 * hi;
#pragma unroll
  for (int g = 0; g < 4; ++g) *(u32x2*)(op + 8 * g) = (u32x2){cvtpk(acc[4 * g], acc[4 * g + 1]), cvtpk(acc[4 * g + 2], acc[4 * g + 3])};
}

extern "C" void kernel_launch(void* const* d_in, const int* in_sizes, int n_in, void* d_out, int out_size, void* d_ws, size_t ws_size,
                              hipStream_t stream) {
  const u16* x = (const u16*)d_in[0];
  const u16* wq = (const u16*)d_in[1];
  const u16* wk = (const u16*)d_in[2];
  const u16* wv = (const u16*)d_in[3];
  const u16* wo = (const u16*)d_in[4];
  const u16* ckv = (const u16*)d_in[6];
  const u16* w_q = (const u16*)d_in[7];
  const u16* w_uk = (const u16*)d_in[8];
  const u16* w_uv = (const u16*)d_in[9];
  const u16* w_o = (const u16*)d_in[10];
  const u16* gw = (const u16*)d_in[11];
  char* ws = (char*)d_ws;
  (void)hipFuncSetAttribute((const void*)k_gemm1, hipFuncAttributeMaxDynamicSharedMemorySize, 131072);
  (void)hipFuncSetAttribute((const void*)k_attnB, hipFuncAttributeMaxDynamicSharedMemorySize, 3 * 32768 + 64);
  (void)hipFuncSetAttribute((const void*)k_outB, hipFuncAttributeMaxDynamicSharedMemorySize, 3 * 32768);
  (void)hipFuncSetAttribute((const void*)k_final, hipFuncAttributeMaxDynamicSharedMemorySize, 65536);
  k_prepA<<<64 * 18, 64, 0, stream>>>(x, wq, wk, wv, gw, ws);
  k_gemm1<<<256, 512, 131072, stream>>>(x, ckv, w_q, w_uk, w_uv, ws);
  k_attnB<<<256, 512, 3 * 32768 + 64, stream>>>(ws);
  k_attnA<<<256, 256, 0, stream>>>(ws);
  k_outB<<<256, 256, 3 * 32768, stream>>>(w_o, ws);
  k_final<<<256, 256, 65536, stream>>>(wo, ws, (u16*)d_out);
}
```

```cpp
#include <hip/hip_runtime.h>
#include <hip/hip_bf16.h>
#include <stdint.h>
#include <type_traits>

typedef short bf16x8 __attribute__((ext_vector_type(8)));
typedef short s16x4 __attribute__((ext_vector_type(4)));
typedef float f32x16 __attribute__((ext_vector_type(16)));
typedef float f32x4 __attribute__((ext_vector_type(4)));
typedef float f32x2 __attribute__((ext_vector_type(2)));
typedef __bf16 bf16x2_t __attribute__((ext_vector_type(2)));
typedef unsigned u32x4 __attribute__((ext_vector_type(4)));
typedef unsigned u32x2 __attribute__((ext_vector_type(2)));
typedef unsigned u32x8 __attribute__((ext_vector_type(8)));
typedef unsigned short u16;
#define DI __device__ __forceinline__
#define MFMA32(a, b, c) __builtin_amdgcn_mfma_f32_32x32x16_bf16((a), (b), (c), 0, 0, 0)
#define LAS __attribute__((address_space(3)))

constexpr int S = 2048, DM = 512, H = 32;
constexpr size_t WS_KV = 0;
constexpr size_t WS_C = 48u << 20;
constexpr size_t WS_QA = 68u << 20;
constexpr size_t WS_KA = 70u << 20;
constexpr size_t WS_G = WS_KA + (256u << 10);
constexpr size_t WS_XF = 66u << 20;
constexpr size_t WS_FLAG = 71u << 20;
constexpr size_t WS_P = 72u << 20;

constexpr size_t K12_CODE_BYTES = 106496;
constexpr float LOG2E = 1.4426950408889634f;
constexpr float QB_SCALE = LOG2E * 0.08838834764831845f;
constexpr float QA_SCALE = LOG2E * 0.25f;
constexpr float L_LIMIT = 1.0e27f;

DI unsigned cvtpk(float lo, float hi) { f32x2 v = {lo, hi}; bf16x2_t b = __builtin_convertvector(v, bf16x2_t); return __builtin_bit_cast(unsigned, b); }
DI float bf2f(u16 x) { return __uint_as_float((unsigned)x << 16); }
DI float rbf(float x) { return __uint_as_float(cvtpk(x, 0.f) << 16); }
DI u32x4 pack8(const f32x16& a, int o, float sc) {
  u32x4 w = {cvtpk(a[o] * sc, a[o + 1] * sc), cvtpk(a[o + 2] * sc, a[o + 3] * sc), cvtpk(a[o + 4] * sc, a[o + 5] * sc), cvtpk(a[o + 6] * sc, a[o + 7] * sc)};
  return w;
}
DI float swap_max(float m) { auto r = __builtin_amdgcn_permlane32_swap(__float_as_uint(m), __float_as_uint(m), false, false); return fmaxf(__uint_as_float(r[0]), __uint_as_float(r[1])); }
DI float swap_add(float m) { auto r = __builtin_amdgcn_permlane32_swap(__float_as_uint(m), __float_as_uint(m), false, false); return __uint_as_float(r[0]) + __uint_as_float(r[1]); }
DI unsigned rfl(unsigned x) { return __builtin_amdgcn_readfirstlane(x); }
DI int lane_now() { int l; asm volatile("v_mbcnt_lo_u32_b32 %0, -1, 0\n\tv_mbcnt_hi_u32_b32 %0, -1, %0" : "=v"(l)); return l; }
DI void dma16(unsigned voff, const void* sbase, unsigned ldsdst) {
  asm volatile("s_mov_b32 m0, %2\n\ts_nop 0\n\tglobal_load_lds_dwordx4 %0, %1" ::"v"(voff), "s"(sbase), "s"(ldsdst) : "memory");
}
DI bf16x8 lds_b128(const LAS char* p) { return *(const LAS bf16x8*)p; }
typedef short v4i16_t __attribute__((ext_vector_type(4)));
DI s16x4 lds_tr(const LAS char* p) { return __builtin_bit_cast(s16x4, __builtin_amdgcn_ds_read_tr16_b64_v4i16((LAS v4i16_t*)p)); }
DI bf16x8 cat(s16x4 a, s16x4 b) { return __builtin_shufflevector(a, b, 0, 1, 2, 3, 4, 5, 6, 7); }
#define FSLEEP 1
#define K0_WARM 2
#define WAIT_BAR(N) asm volatile("s_waitcnt vmcnt(" #N ") lgkmcnt(0)\n\ts_barrier" ::: "memory")

DI float rowmax16(const f32x16& s) {
  float a = fmaxf(fmaxf(s[0], s[1]), s[2]);
#pragma unroll
  for (int r = 3; r < 15; r += 2) a = fmaxf(fmaxf(a, s[r]), s[r + 1]);
  a = fmaxf(a, s[15]);
  return swap_max(a);
}
DI void mask_diag(f32x16& s, int r, int hi) {
#pragma unroll
  for (int e = 0; e < 16; ++e) { const int key = (e & 3) + 8 * (e >> 2) + 4 * hi; if (key > r) s[e] = -INFINITY; }
}
DI unsigned touch(const char* p) { unsigned d; asm volatile("global_load_dword %0, %1, off" : "=v"(d) : "v"(p) : "memory"); return d; }
__global__ __launch_bounds__(768) void k0_prep(const u16* __restrict__ x, const u16* __restrict__ wq, const u16* __restrict__ wk,
                                               const u16* __restrict__ wv, const u16* __restrict__ gw, const u16* __restrict__ ckv,
                                               const u16* __restrict__ w_q, const u16* __restrict__ w_uk, const u16* __restrict__ w_uv,
                                               char* __restrict__ ws) {
  extern __shared__ __attribute__((aligned(16))) char smem[];
  const LAS char* lds = (const LAS char*)smem;
  const unsigned lds0 = (unsigned)(uintptr_t)smem;
  const int tid = threadIdx.x, lane = tid & 63, r = lane & 31, hi = lane >> 5;
  const int wid = rfl(tid >> 6);
  if (wid >= 8) {
    unsigned long long pc;
    asm volatile("s_getpc_b64 s[40:41]\n\ts_add_u32 s40, s40, _Z8k12_mainPKtS0_S0_S0_S0_S0_PcPt@rel32@lo+4\n\ts_addc_u32 s41, s41, _Z8k12_mainPKtS0_S0_S0_S0_S0_PcPt@rel32@hi+12" : "={s[40:41]}"(pc)::"scc");
#define K0_SLEEP 20
    __builtin_amdgcn_s_sleep(K0_SLEEP);
    const size_t o = ((size_t)blockIdx.x * 256 + (tid - 512)) * 128;
    const size_t o2 = o - 4194304;
    (void)o2;
    const size_t q = o & 1048575; const int w = (int)(o >> 20);
    const unsigned d0 = touch(w == 0 ? (const char*)w_q + q : w == 1 ? (const char*)w_uk + q : w == 2 ? (const char*)w_uv + q : w < 5 ? (const char*)ckv + (o - 3145728)
                                     : (const char*)pc + (o - 5242880 < K12_CODE_BYTES ? o - 5242880 : 0));
    asm volatile("s_barrier\n\ts_waitcnt vmcnt(0)" ::"v"(d0) : "memory");
    return;
  }
  const int xcd = blockIdx.x & 7, idx = blockIdx.x >> 3;
  const int mt = xcd * 4 + (idx & 3), nt = idx >> 2;
  if (blockIdx.x == 0) __hip_atomic_store((unsigned*)(ws + WS_FLAG) + tid * 16, 0u, __ATOMIC_RELAXED, __HIP_MEMORY_SCOPE_AGENT);
  {
    const int rw = (wid + nt + (idx & 3) * 2) & 7;
    const char* X = (const char*)x + (size_t)mt * 64 * 1024 + rw * 128;
    const unsigned wsrc = ((lane >> 5) * 4 + ((lane >> 2) & 3)) * 1024 + ((lane >> 4) & 1) * 64 + (lane & 3) * 16;
    const char* W = (const char*)wq + nt * 128;
#pragma unroll
    for (int p = 0; p < 8; ++p) {
      dma16((lane >> 3) * 1024 + (((lane & 7) ^ (((p & 1) << 2) | (lane >> 4))) * 16), X + p * 8192, lds0 + rw * 8192 + p * 1024);
      const int pc = rw * 8 + ((p + idx) & 7);
      dma16(wsrc, W + (size_t)pc * 8192, lds0 + 65536 + pc * 1024);
    }
    if (nt == 0) {
      const char* E = (const char*)((lane & 2) ? wk : wv) + (lane >> 2) * 32 + (lane & 1) * 16;
#pragma unroll
      for (int p = 0; p < 4; ++p) { const int pc = wid * 4 + p;
        asm volatile("s_mov_b32 m0, %1\n\ts_nop 0\n\tglobal_load_lds_dwordx4 %0, off" ::"v"(E + pc * 512), "s"(lds0 + 131072 + pc * 1024) : "memory"); }
    }
  }
  if (wid >= 6) {
    const int t0 = blockIdx.x * 8 + (wid - 6) * 4;
    const bf16x8 gf = *(const bf16x8*)(gw + lane * 8);
    float gs[4];
#pragma unroll
    for (int i = 0; i < 4; ++i) {
      const bf16x8 xv = *(const bf16x8*)(x + (size_t)(t0 + i) * DM + lane * 8);
      float a = 0.f;
#pragma unroll
      for (int j = 0; j < 8; ++j) a += rbf(bf2f((u16)xv[j]) * bf2f((u16)gf[j]));
      gs[i] = a;
    }
#pragma unroll
    for (int o = 32; o >= 1; o >>= 1)
#pragma unroll
      for (int i = 0; i < 4; ++i) gs[i] += __shfl_xor(gs[i], o);
    const float mine = lane == 0 ? gs[0] : lane == 1 ? gs[1] : lane == 2 ? gs[2] : gs[3];
    if (lane < 4) ((float*)(ws + WS_G))[t0 + lane] = rbf(1.f / (1.f + __expf(-rbf(mine))));
  }
  WAIT_BAR(0);
  const int tbl = wid & 1, tb = mt * 2 + tbl;
  const unsigned tro = ((lane >> 2) & 3) * 64 + ((lane >> 4) & 1) * 32 + (lane & 3) * 8;
  const LAS char* xp = lds + (tbl * 32 + r) * 128;
  const int fx = (r >> 1) & 7;
  if (wid < 4) {
    const int nbl = wid >> 1;
    const LAS char* wp = lds + 65536 + hi * 1024 + nbl * 256 + tro;
    f32x16 acc = {}, acc2 = {};
#pragma unroll
    for (int s = 0; s < 32; s += 2) {
      acc = MFMA32(cat(lds_tr(wp + s * 2048), lds_tr(wp + s * 2048 + 512)), lds_b128(xp + (s >> 2) * 8192 + (((2 * (s & 3) + hi) ^ fx) * 16)), acc);
      acc2 = MFMA32(cat(lds_tr(wp + s * 2048 + 2048), lds_tr(wp + s * 2048 + 2560)), lds_b128(xp + (s >> 2) * 8192 + (((2 * (s & 3) + 2 + hi) ^ fx) * 16)), acc2);
    }
    acc += acc2;
    const int h0 = (nt * 2 + nbl) * 2;
    *(u32x4*)(ws + WS_QA + (size_t)(h0 * 64 + tb) * 1024 + lane * 16) = pack8(acc, 0, QA_SCALE);
    *(u32x4*)(ws + WS_QA + (size_t)((h0 + 1) * 64 + tb) * 1024 + lane * 16) = pack8(acc, 8, QA_SCALE);
  } else if (nt == 0) {
    const LAS char* wp = lds + 131072 + hi * 512 + tro;
    f32x16 acc = {};
    char* kb = ws + WS_KA + (size_t)tb * 3072 + lane * 16;
    if (wid < 6) {
#pragma unroll
      for (int s = 0; s < 32; ++s)
        acc = MFMA32(cat(lds_tr(wp + s * 1024), lds_tr(wp + s * 1024 + 256)), lds_b128(xp + (s >> 2) * 8192 + (((2 * (s & 3) + hi) ^ fx) * 16)), acc);
      *(u32x4*)kb = pack8(acc, 8, 1.f);
    } else {
#pragma unroll
      for (int s = 0; s < 32; ++s)
        acc = MFMA32(lds_b128(xp + (s >> 2) * 8192 + (((2 * (s & 3) + hi) ^ fx) * 16)), cat(lds_tr(wp + s * 1024), lds_tr(wp + s * 1024 + 256)), acc);
      u32x4 w0 = pack8(acc, 0, 1.f), w1 = pack8(acc, 8, 1.f);
      if (r >= 16) { const unsigned c = (r == 16 || r == 20) ? 0x3F803F80u : 0u; w0 = (u32x4){c, c, c, c}; w1 = w0; }
      *(u32x4*)(kb + 1024) = w0;
      *(u32x4*)(kb + 2048) = w1;
    }
  } else if (nt <= 4) {
    const int s0 = (nt - 1) * 8 + ((wid - 4) >> 1) * 4;
#pragma unroll
    for (int i = 0; i < 4; ++i)
      *(bf16x8*)(ws + WS_XF + (size_t)(tb * 32 + s0 + i) * 1024 + lane * 16) = lds_b128(xp + (s0 >> 2) * 8192 + (((2 * i + hi) ^ fx) * 16));
  }
}

typedef unsigned u32x16 __attribute__((ext_vector_type(16)));
#define K12A_LONG \
"s_mov_b32 s59,s66\ns_mov_b32 s52,s64\ns_add_u32 s53,s64,8192\ns_add_u32 s54,s64,16384\ns_add_u32 s55,s64,24576\ns_mov_b32 s56,s65\n" \
"s_add_u32 s57,s65,32768\ns_and_b32 s50,s66,0xffff\ns_add_u32 m0,s50,65536\ns_add_u32 s49,s67,0\nbuffer_load_dwordx4 v249,%[sq],s49 offen lds\n" \
"s_add_u32 m0,m0,1024\ns_add_u32 s49,s49,32768\nbuffer_load_dwordx4 v249,%[sq],s49 offen lds\ns_add_u32 m0,m0,1024\ns_add_u32 s49,s49,32768\n" \
"buffer_load_dwordx4 v249,%[sq],s49 offen lds\ns_add_u32 m0,m0,1024\ns_add_u32 s49,s49,32768\nbuffer_load_dwordx4 v249,%[sq],s49 offen lds\n" \
"s_add_u32 s58,s76,0\nbuffer_load_dwordx4 v[96:99],v248,%[sws],s58 offen offset:0\nbuffer_load_dwordx4 v[100:103],v248,%[sws],s58 offen offset:1024\n" \
"buffer_load_dwordx4 v[104:107],v248,%[sws],s58 offen offset:2048\nbuffer_load_dwordx4 v[108:111],v248,%[sws],s58 offen offset:3072\n" \
"s_add_u32 s58,s76,4096\nbuffer_load_dwordx4 v[112:115],v248,%[sws],s58 offen offset:0\n" \
"buffer_load_dwordx4 v[116:119],v248,%[sws],s58 offen offset:1024\nbuffer_load_dwordx4 v[120:123],v248,%[sws],s58 offen offset:2048\n" \
"buffer_load_dwordx4 v[124:127],v248,%[sws],s58 offen offset:3072\nv_add_u32 v252,65536,v250\nv_add_u32 v255,98304,v250\ns_waitcnt vmcnt(0)\ns_barrier\n" \
"ds_read_b64_tr_b16 v[64:65],v252 offset:0\nds_read_b64_tr_b16 v[66:67],v252 offset:1024\nds_read_b64_tr_b16 v[68:69],v252 offset:256\n" \
"ds_read_b64_tr_b16 v[70:71],v252 offset:1280\nds_read_b64_tr_b16 v[72:73],v252 offset:512\nds_read_b64_tr_b16 v[74:75],v252 offset:1536\n" \
"ds_read_b64_tr_b16 v[76:77],v252 offset:768\nds_read_b64_tr_b16 v[78:79],v252 offset:1792\nds_read_b64_tr_b16 v[80:81],v252 offset:4096\n" \
"ds_read_b64_tr_b16 v[82:83],v252 offset:5120\nds_read_b64_tr_b16 v[84:85],v252 offset:4352\nds_read_b64_tr_b16 v[86:87],v252 offset:5376\n" \
"s_waitcnt lgkmcnt(10)\nv_mfma_f32_32x32x16_bf16 v[0:15],v[64:67],v[96:99],0\nds_read_b64_tr_b16 v[88:89],v252 offset:4608\n" \
"ds_read_b64_tr_b16 v[90:91],v252 offset:5632\ns_add_u32 m0,s50,98304\ns_add_u32 s49,s67,1048576\nbuffer_load_dwordx4 v249,%[sq],s49 offen lds\n" \
"s_waitcnt lgkmcnt(8)\nv_mfma_f32_32x32x16_bf16 v[16:31],v[68:71],v[96:99],0\nds_read_b64_tr_b16 v[92:93],v252 offset:4864\n" \
"ds_read_b64_tr_b16 v[94:95],v252 offset:5888\ns_add_u32 m0,m0,1024\ns_add_u32 s49,s49,32768\nbuffer_load_dwordx4 v249,%[sq],s49 offen lds\n" \
"v_mfma_f32_32x32x16_bf16 v[32:47],v[72:75],v[96:99],0\nds_read_b64_tr_b16 v[64:65],v252 offset:8192\nds_read_b64_tr_b16 v[66:67],v252 offset:9216\n" \
"s_add_u32 m0,m0,1024\ns_add_u32 s49,s49,32768\nbuffer_load_dwordx4 v249,%[sq],s49 offen lds\ns_waitcnt lgkmcnt(8)\n" \
"v_mfma_f32_32x32x16_bf16 v[48:63],v[76:79],v[96:99],0\nds_read_b64_tr_b16 v[68:69],v252 offset:8448\nds_read_b64_tr_b16 v[70:71],v252 offset:9472\n" \
"s_add_u32 m0,m0,1024\ns_add_u32 s49,s49,32768\nbuffer_load_dwordx4 v249,%[sq],s49 offen lds\n" \
"v_mfma_f32_32x32x16_bf16 v[0:15],v[80:83],v[100:103],v[0:15]\nds_read_b64_tr_b16 v[72:73],v252 offset:8704\n" \
"ds_read_b64_tr_b16 v[74:75],v252 offset:9728\ns_add_u32 s58,s76,8192\nbuffer_load_dwordx4 v[128:131],v248,%[sws],s58 offen offset:0\n" \
"s_waitcnt lgkmcnt(8)\nv_mfma_f32_32x32x16_bf16 v[16:31],v[84:87],v[100:103],v[16:31]\nds_read_b64_tr_b16 v[76:77],v252 offset:8960\n" \
"ds_read_b64_tr_b16 v[78:79],v252 offset:9984\nbuffer_load_dwordx4 v[132:135],v248,%[sws],s58 offen offset:1024\n" \
"v_mfma_f32_32x32x16_bf16 v[32:47],v[88:91],v[100:103],v[32:47]\nds_read_b64_tr_b16 v[80:81],v252 offset:12288\n" \
"ds_read_b64_tr_b16 v[82:83],v252 offset:13312\nbuffer_load_dwordx4 v[136:139],v248,%[sws],s58 offen offset:2048\ns_waitcnt lgkmcnt(8)\n" \
"v_mfma_f32_32x32x16_bf16 v[48:63],v[92:95],v[100:103],v[48:63]\nds_read_b64_tr_b16 v[84:85],v252 offset:12544\n" \
"ds_read_b64_tr_b16 v[86:87],v252 offset:13568\nbuffer_load_dwordx4 v[140:143],v248,%[sws],s58 offen offset:3072\n" \
"v_mfma_f32_32x32x16_bf16 v[0:15],v[64:67],v[104:107],v[0:15]\nds_read_b64_tr_b16 v[88:89],v252 offset:12800\n" \
"ds_read_b64_tr_b16 v[90:91],v252 offset:13824\ns_add_u32 s58,s76,12288\nbuffer_load_dwordx4 v[144:147],v248,%[sws],s58 offen offset:0\n" \
"s_waitcnt lgkmcnt(8)\nv_mfma_f32_32x32x16_bf16 v[16:31],v[68:71],v[104:107],v[16:31]\nds_read_b64_tr_b16 v[92:93],v252 offset:13056\n" \
"ds_read_b64_tr_b16 v[94:95],v252 offset:14080\nbuffer_load_dwordx4 v[148:151],v248,%[sws],s58 offen offset:1024\n" \
"v_mfma_f32_32x32x16_bf16 v[32:47],v[72:75],v[104:107],v[32:47]\nds_read_b64_tr_b16 v[64:65],v252 offset:16384\n" \
"ds_read_b64_tr_b16 v[66:67],v252 offset:17408\nbuffer_load_dwordx4 v[152:155],v248,%[sws],s58 offen offset:2048\ns_waitcnt lgkmcnt(8)\n" \
"v_mfma_f32_32x32x16_bf16 v[48:63],v[76:79],v[104:107],v[48:63]\nds_read_b64_tr_b16 v[68:69],v252 offset:16640\n" \
"ds_read_b64_tr_b16 v[70:71],v252 offset:17664\nbuffer_load_dwordx4 v[156:159],v248,%[sws],s58 offen offset:3072\n" \
"v_mfma_f32_32x32x16_bf16 v[0:15],v[80:83],v[108:111],v[0:15]\nds_read_b64_tr_b16 v[72:73],v252 offset:16896\n" \
"ds_read_b64_tr_b16 v[74:75],v252 offset:17920\ns_add_u32 m0,s50,0\ns_add_u32 s49,s67,2097152\nbuffer_load_dwordx4 v249,%[sq],s49 offen lds\n" \
"s_waitcnt lgkmcnt(8)\nv_mfma_f32_32x32x16_bf16 v[16:31],v[84:87],v[108:111],v[16:31]\nds_read_b64_tr_b16 v[76:77],v252 offset:17152\n" \
"ds_read_b64_tr_b16 v[78:79],v252 offset:18176\ns_add_u32 m0,m0,1024\ns_add_u32 s49,s49,32768\nbuffer_load_dwordx4 v249,%[sq],s49 offen lds\n" \
"v_mfma_f32_32x32x16_bf16 v[32:47],v[88:91],v[108:111],v[32:47]\nds_read_b64_tr_b16 v[80:81],v252 offset:20480\n" \
"ds_read_b64_tr_b16 v[82:83],v252 offset:21504\ns_add_u32 m0,m0,1024\ns_add_u32 s49,s49,32768\nbuffer_load_dwordx4 v249,%[sq],s49 offen lds\n" \
"s_waitcnt lgkmcnt(8)\nv_mfma_f32_32x32x16_bf16 v[48:63],v[92:95],v[108:111],v[48:63]\nds_read_b64_tr_b16 v[84:85],v252 offset:20736\n" \
"ds_read_b64_tr_b16 v[86:87],v252 offset:21760\ns_add_u32 m0,m0,1024\ns_add_u32 s49,s49,32768\nbuffer_load_dwordx4 v249,%[sq],s49 offen lds\n" \
"v_mfma_f32_32x32x16_bf16 v[0:15],v[64:67],v[112:115],v[0:15]\nds_read_b64_tr_b16 v[88:89],v252 offset:20992\n" \
"ds_read_b64_tr_b16 v[90:91],v252 offset:22016\ns_add_u32 s58,s76,16384\nbuffer_load_dwordx4 v[160:163],v248,%[sws],s58 offen offset:0\n" \
"s_waitcnt lgkmcnt(8)\nv_mfma_f32_32x32x16_bf16 v[16:31],v[68:71],v[112:115],v[16:31]\nds_read_b64_tr_b16 v[92:93],v252 offset:21248\n" \
"ds_read_b64_tr_b16 v[94:95],v252 offset:22272\nbuffer_load_dwordx4 v[164:167],v248,%[sws],s58 offen offset:1024\n" \
"v_mfma_f32_32x32x16_bf16 v[32:47],v[72:75],v[112:115],v[32:47]\nds_read_b64_tr_b16 v[64:65],v252 offset:24576\n" \
"ds_read_b64_tr_b16 v[66:67],v252 offset:25600\nbuffer_load_dwordx4 v[168:171],v248,%[sws],s58 offen offset:2048\ns_waitcnt lgkmcnt(8)\n" \
"v_mfma_f32_32x32x16_bf16 v[48:63],v[76:79],v[112:115],v[48:63]\nds_read_b64_tr_b16 v[68:69],v252 offset:24832\n" \
"ds_read_b64_tr_b16 v[70:71],v252 offset:25856\nbuffer_load_dwordx4 v[172:175],v248,%[sws],s58 offen offset:3072\n" \
"v_mfma_f32_32x32x16_bf16 v[0:15],v[80:83],v[116:119],v[0:15]\nds_read_b64_tr_b16 v[72:73],v252 offset:25088\n" \
"ds_read_b64_tr_b16 v[74:75],v252 offset:26112\ns_add_u32 s58,s76,20480\nbuffer_load_dwordx4 v[176:179],v248,%[sws],s58 offen offset:0\n" \
"s_waitcnt lgkmcnt(8)\nv_mfma_f32_32x32x16_bf16 v[16:31],v[84:87],v[116:119],v[16:31]\nds_read_b64_tr_b16 v[76:77],v252 offset:25344\n" \
"ds_read_b64_tr_b16 v[78:79],v252 offset:26368\nbuffer_load_dwordx4 v[180:183],v248,%[sws],s58 offen offset:1024\n" \
"v_mfma_f32_32x32x16_bf16 v[32:47],v[88:91],v[116:119],v[32:47]\nds_read_b64_tr_b16 v[80:81],v252 offset:28672\n" \
"ds_read_b64_tr_b16 v[82:83],v252 offset:29696\nbuffer_load_dwordx4 v[184:187],v248,%[sws],s58 offen offset:2048\ns_waitcnt lgkmcnt(8)\n" \
"v_mfma_f32_32x32x16_bf16 v[48:63],v[92:95],v[116:119],v[48:63]\nds_read_b64_tr_b16 v[84:85],v252 offset:28928\n" \
"ds_read_b64_tr_b16 v[86:87],v252 offset:29952\nbuffer_load_dwordx4 v[188:191],v248,%[sws],s58 offen offset:3072\n" \
"v_mfma_f32_32x32x16_bf16 v[0:15],v[64:67],v[120:123],v[0:15]\nds_read_b64_tr_b16 v[88:89],v252 offset:29184\n" \
"ds_read_b64_tr_b16 v[90:91],v252 offset:30208\ns_add_u32 m0,s50,131072\ns_add_u32 s49,s67,3145728\nbuffer_load_dwordx4 v249,%[sq],s49 offen lds\n" \
"s_waitcnt lgkmcnt(8)\nv_mfma_f32_32x32x16_bf16 v[16:31],v[68:71],v[120:123],v[16:31]\ns_waitcnt vmcnt(13)\ns_barrier\n" \
"ds_read_b64_tr_b16 v[92:93],v252 offset:29440\nds_read_b64_tr_b16 v[94:95],v252 offset:30464\ns_add_u32 m0,m0,1024\ns_add_u32 s49,s49,32768\n" \
"buffer_load_dwordx4 v249,%[sq],s49 offen lds\nv_mfma_f32_32x32x16_bf16 v[32:47],v[72:75],v[120:123],v[32:47]\n" \
"ds_read_b64_tr_b16 v[64:65],v255 offset:0\nds_read_b64_tr_b16 v[66:67],v255 offset:1024\ns_add_u32 m0,m0,1024\ns_add_u32 s49,s49,32768\n" \
"buffer_load_dwordx4 v249,%[sq],s49 offen lds\ns_waitcnt lgkmcnt(8)\nv_mfma_f32_32x32x16_bf16 v[48:63],v[76:79],v[120:123],v[48:63]\n" \
"ds_read_b64_tr_b16 v[68:69],v255 offset:256\nds_read_b64_tr_b16 v[70:71],v255 offset:1280\nv_mov_b32 v252,v250\ns_add_u32 m0,m0,1024\n" \
"s_add_u32 s49,s49,32768\nbuffer_load_dwordx4 v249,%[sq],s49 offen lds\nv_mfma_f32_32x32x16_bf16 v[0:15],v[80:83],v[124:127],v[0:15]\n" \
"ds_read_b64_tr_b16 v[72:73],v255 offset:512\nds_read_b64_tr_b16 v[74:75],v255 offset:1536\ns_add_u32 s58,s76,24576\n" \
"buffer_load_dwordx4 v[192:195],v248,%[sws],s58 offen offset:0\ns_waitcnt lgkmcnt(8)\nv_mfma_f32_32x32x16_bf16 v[16:31],v[84:87],v[124:127],v[16:31]\n" \
"ds_read_b64_tr_b16 v[76:77],v255 offset:768\nds_read_b64_tr_b16 v[78:79],v255 offset:1792\n" \
"buffer_load_dwordx4 v[196:199],v248,%[sws],s58 offen offset:1024\nv_mfma_f32_32x32x16_bf16 v[32:47],v[88:91],v[124:127],v[32:47]\n" \
"ds_read_b64_tr_b16 v[80:81],v255 offset:4096\nds_read_b64_tr_b16 v[82:83],v255 offset:5120\n" \
"buffer_load_dwordx4 v[200:203],v248,%[sws],s58 offen offset:2048\ns_waitcnt lgkmcnt(8)\nv_mfma_f32_32x32x16_bf16 v[48:63],v[92:95],v[124:127],v[48:63]\n" \
"ds_read_b64_tr_b16 v[84:85],v255 offset:4352\nds_read_b64_tr_b16 v[86:87],v255 offset:5376\n" \
"buffer_load_dwordx4 v[204:207],v248,%[sws],s58 offen offset:3072\nv_mfma_f32_32x32x16_bf16 v[0:15],v[64:67],v[128:131],v[0:15]\n" \
"ds_read_b64_tr_b16 v[88:89],v255 offset:4608\nds_read_b64_tr_b16 v[90:91],v255 offset:5632\ns_add_u32 s58,s76,28672\n" \
"buffer_load_dwordx4 v[208:211],v248,%[sws],s58 offen offset:0\ns_waitcnt lgkmcnt(8)\nv_mfma_f32_32x32x16_bf16 v[16:31],v[68:71],v[128:131],v[16:31]\n" \
"ds_read_b64_tr_b16 v[92:93],v255 offset:4864\nds_read_b64_tr_b16 v[94:95],v255 offset:5888\n" \
"buffer_load_dwordx4 v[212:215],v248,%[sws],s58 offen offset:1024\nv_mfma_f32_32x32x16_bf16 v[32:47],v[72:75],v[128:131],v[32:47]\n" \
"ds_read_b64_tr_b16 v[64:65],v255 offset:8192\nds_read_b64_tr_b16 v[66:67],v255 offset:9216\n" \
"buffer_load_dwordx4 v[216:219],v248,%[sws],s58 offen offset:2048\ns_waitcnt lgkmcnt(8)\nv_mfma_f32_32x32x16_bf16 v[48:63],v[76:79],v[128:131],v[48:63]\n" \
"ds_read_b64_tr_b16 v[68:69],v255 offset:8448\nds_read_b64_tr_b16 v[70:71],v255 offset:9472\n" \
"buffer_load_dwordx4 v[220:223],v248,%[sws],s58 offen offset:3072\nv_mfma_f32_32x32x16_bf16 v[0:15],v[80:83],v[132:135],v[0:15]\n" \
"ds_read_b64_tr_b16 v[72:73],v255 offset:8704\nds_read_b64_tr_b16 v[74:75],v255 offset:9728\ns_waitcnt lgkmcnt(8)\n" \
"v_mfma_f32_32x32x16_bf16 v[16:31],v[84:87],v[132:135],v[16:31]\nds_read_b64_tr_b16 v[76:77],v255 offset:8960\n" \
"ds_read_b64_tr_b16 v[78:79],v255 offset:9984\nv_mfma_f32_32x32x16_bf16 v[32:47],v[88:91],v[132:135],v[32:47]\n" \
"ds_read_b64_tr_b16 v[80:81],v255 offset:12288\nds_read_b64_tr_b16 v[82:83],v255 offset:13312\ns_waitcnt lgkmcnt(8)\n" \
"v_mfma_f32_32x32x16_bf16 v[48:63],v[92:95],v[132:135],v[48:63]\nds_read_b64_tr_b16 v[84:85],v255 offset:12544\n" \
"ds_read_b64_tr_b16 v[86:87],v255 offset:13568\nv_mfma_f32_32x32x16_bf16 v[0:15],v[64:67],v[136:139],v[0:15]\n" \
"ds_read_b64_tr_b16 v[88:89],v255 offset:12800\nds_read_b64_tr_b16 v[90:91],v255 offset:13824\ns_waitcnt lgkmcnt(8)\n" \
"v_mfma_f32_32x32x16_bf16 v[16:31],v[68:71],v[136:139],v[16:31]\nds_read_b64_tr_b16 v[92:93],v255 offset:13056\n" \
"ds_read_b64_tr_b16 v[94:95],v255 offset:14080\nv_mfma_f32_32x32x16_bf16 v[32:47],v[72:75],v[136:139],v[32:47]\n" \
"ds_read_b64_tr_b16 v[64:65],v255 offset:16384\nds_read_b64_tr_b16 v[66:67],v255 offset:17408\ns_waitcnt lgkmcnt(8)\n" \
"v_mfma_f32_32x32x16_bf16 v[48:63],v[76:79],v[136:139],v[48:63]\nds_read_b64_tr_b16 v[68:69],v255 offset:16640\n" \
"ds_read_b64_tr_b16 v[70:71],v255 offset:17664\nv_mfma_f32_32x32x16_bf16 v[0:15],v[80:83],v[140:143],v[0:15]\n" \
"ds_read_b64_tr_b16 v[72:73],v255 offset:16896\nds_read_b64_tr_b16 v[74:75],v255 offset:17920\ns_waitcnt lgkmcnt(8)\n" \
"v_mfma_f32_32x32x16_bf16 v[16:31],v[84:87],v[140:143],v[16:31]\nds_read_b64_tr_b16 v[76:77],v255 offset:17152\n" \
"ds_read_b64_tr_b16 v[78:79],v255 offset:18176\nv_mfma_f32_32x32x16_bf16 v[32:47],v[88:91],v[140:143],v[32:47]\n" \
"ds_read_b64_tr_b16 v[80:81],v255 offset:20480\nds_read_b64_tr_b16 v[82:83],v255 offset:21504\ns_waitcnt lgkmcnt(8)\n" \
"v_mfma_f32_32x32x16_bf16 v[48:63],v[92:95],v[140:143],v[48:63]\nds_read_b64_tr_b16 v[84:85],v255 offset:20736\n" \
"ds_read_b64_tr_b16 v[86:87],v255 offset:21760\nv_mfma_f32_32x32x16_bf16 v[0:15],v[64:67],v[144:147],v[0:15]\n" \
"ds_read_b64_tr_b16 v[88:89],v255 offset:20992\nds_read_b64_tr_b16 v[90:91],v255 offset:22016\ns_waitcnt lgkmcnt(8)\n" \
"v_mfma_f32_32x32x16_bf16 v[16:31],v[68:71],v[144:147],v[16:31]\nds_read_b64_tr_b16 v[92:93],v255 offset:21248\n" \
"ds_read_b64_tr_b16 v[94:95],v255 offset:22272\nv_mfma_f32_32x32x16_bf16 v[32:47],v[72:75],v[144:147],v[32:47]\n" \
"ds_read_b64_tr_b16 v[64:65],v255 offset:24576\nds_read_b64_tr_b16 v[66:67],v255 offset:25600\ns_waitcnt lgkmcnt(8)\n" \
"v_mfma_f32_32x32x16_bf16 v[48:63],v[76:79],v[144:147],v[48:63]\nds_read_b64_tr_b16 v[68:69],v255 offset:24832\n" \
"ds_read_b64_tr_b16 v[70:71],v255 offset:25856\nv_mfma_f32_32x32x16_bf16 v[0:15],v[80:83],v[148:151],v[0:15]\n" \
"ds_read_b64_tr_b16 v[72:73],v255 offset:25088\nds_read_b64_tr_b16 v[74:75],v255 offset:26112\ns_waitcnt lgkmcnt(8)\n" \
"v_mfma_f32_32x32x16_bf16 v[16:31],v[84:87],v[148:151],v[16:31]\nds_read_b64_tr_b16 v[76:77],v255 offset:25344\n" \
"ds_read_b64_tr_b16 v[78:79],v255 offset:26368\nv_mfma_f32_32x32x16_bf16 v[32:47],v[88:91],v[148:151],v[32:47]\n" \
"ds_read_b64_tr_b16 v[80:81],v255 offset:28672\nds_read_b64_tr_b16 v[82:83],v255 offset:29696\ns_waitcnt lgkmcnt(8)\n" \
"v_mfma_f32_32x32x16_bf16 v[48:63],v[92:95],v[148:151],v[48:63]\nds_read_b64_tr_b16 v[84:85],v255 offset:28928\n" \
"ds_read_b64_tr_b16 v[86:87],v255 offset:29952\nv_mfma_f32_32x32x16_bf16 v[0:15],v[64:67],v[152:155],v[0:15]\n" \
"ds_read_b64_tr_b16 v[88:89],v255 offset:29184\nds_read_b64_tr_b16 v[90:91],v255 offset:30208\ns_waitcnt lgkmcnt(8)\n" \
"v_mfma_f32_32x32x16_bf16 v[16:31],v[68:71],v[152:155],v[16:31]\ns_waitcnt vmcnt(12)\ns_barrier\nds_read_b64_tr_b16 v[92:93],v255 offset:29440\n" \
"ds_read_b64_tr_b16 v[94:95],v255 offset:30464\nv_mfma_f32_32x32x16_bf16 v[32:47],v[72:75],v[152:155],v[32:47]\n" \
"ds_read_b64_tr_b16 v[64:65],v252 offset:0\nds_read_b64_tr_b16 v[66:67],v252 offset:1024\ns_waitcnt lgkmcnt(8)\n" \
"v_mfma_f32_32x32x16_bf16 v[48:63],v[76:79],v[152:155],v[48:63]\nds_read_b64_tr_b16 v[68:69],v252 offset:256\n" \
"ds_read_b64_tr_b16 v[70:71],v252 offset:1280\nv_add_u32 v255,131072,v250\nv_mfma_f32_32x32x16_bf16 v[0:15],v[80:83],v[156:159],v[0:15]\n" \
"ds_read_b64_tr_b16 v[72:73],v252 offset:512\nds_read_b64_tr_b16 v[74:75],v252 offset:1536\ns_waitcnt lgkmcnt(8)\n" \
"v_mfma_f32_32x32x16_bf16 v[16:31],v[84:87],v[156:159],v[16:31]\nds_read_b64_tr_b16 v[76:77],v252 offset:768\n" \
"ds_read_b64_tr_b16 v[78:79],v252 offset:1792\nv_mfma_f32_32x32x16_bf16 v[32:47],v[88:91],v[156:159],v[32:47]\n" \
"ds_read_b64_tr_b16 v[80:81],v252 offset:4096\nds_read_b64_tr_b16 v[82:83],v252 offset:5120\ns_waitcnt lgkmcnt(8)\n" \
"v_mfma_f32_32x32x16_bf16 v[48:63],v[92:95],v[156:159],v[48:63]\nds_read_b64_tr_b16 v[84:85],v252 offset:4352\n" \
"ds_read_b64_tr_b16 v[86:87],v252 offset:5376\nv_mfma_f32_32x32x16_bf16 v[0:15],v[64:67],v[160:163],v[0:15]\n" \
"ds_read_b64_tr_b16 v[88:89],v252 offset:4608\nds_read_b64_tr_b16 v[90:91],v252 offset:5632\ns_waitcnt lgkmcnt(8)\n" \
"v_mfma_f32_32x32x16_bf16 v[16:31],v[68:71],v[160:163],v[16:31]\nds_read_b64_tr_b16 v[92:93],v252 offset:4864\n" \
"ds_read_b64_tr_b16 v[94:95],v252 offset:5888\nv_mfma_f32_32x32x16_bf16 v[32:47],v[72:75],v[160:163],v[32:47]\n" \
"ds_read_b64_tr_b16 v[64:65],v252 offset:8192\nds_read_b64_tr_b16 v[66:67],v252 offset:9216\ns_waitcnt lgkmcnt(8)\n" \
"v_mfma_f32_32x32x16_bf16 v[48:63],v[76:79],v[160:163],v[48:63]\nds_read_b64_tr_b16 v[68:69],v252 offset:8448\n" \
"ds_read_b64_tr_b16 v[70:71],v252 offset:9472\nv_mfma_f32_32x32x16_bf16 v[0:15],v[80:83],v[164:167],v[0:15]\n" \
"ds_read_b64_tr_b16 v[72:73],v252 offset:8704\nds_read_b64_tr_b16 v[74:75],v252 offset:9728\ns_waitcnt lgkmcnt(8)\n" \
"v_mfma_f32_32x32x16_bf16 v[16:31],v[84:87],v[164:167],v[16:31]\nds_read_b64_tr_b16 v[76:77],v252 offset:8960\n" \
"ds_read_b64_tr_b16 v[78:79],v252 offset:9984\nv_mfma_f32_32x32x16_bf16 v[32:47],v[88:91],v[164:167],v[32:47]\n" \
"ds_read_b64_tr_b16 v[80:81],v252 offset:12288\nds_read_b64_tr_b16 v[82:83],v252 offset:13312\ns_waitcnt lgkmcnt(8)\n" \
"v_mfma_f32_32x32x16_bf16 v[48:63],v[92:95],v[164:167],v[48:63]\nds_read_b64_tr_b16 v[84:85],v252 offset:12544\n" \
"ds_read_b64_tr_b16 v[86:87],v252 offset:13568\nv_mfma_f32_32x32x16_bf16 v[0:15],v[64:67],v[168:171],v[0:15]\n" \
"ds_read_b64_tr_b16 v[88:89],v252 offset:12800\nds_read_b64_tr_b16 v[90:91],v252 offset:13824\ns_waitcnt lgkmcnt(8)\n" \
"v_mfma_f32_32x32x16_bf16 v[16:31],v[68:71],v[168:171],v[16:31]\nds_read_b64_tr_b16 v[92:93],v252 offset:13056\n" \
"ds_read_b64_tr_b16 v[94:95],v252 offset:14080\nv_mfma_f32_32x32x16_bf16 v[32:47],v[72:75],v[168:171],v[32:47]\n" \
"ds_read_b64_tr_b16 v[64:65],v252 offset:16384\nds_read_b64_tr_b16 v[66:67],v252 offset:17408\ns_waitcnt lgkmcnt(8)\n" \
"v_mfma_f32_32x32x16_bf16 v[48:63],v[76:79],v[168:171],v[48:63]\nds_read_b64_tr_b16 v[68:69],v252 offset:16640\n" \
"ds_read_b64_tr_b16 v[70:71],v252 offset:17664\nv_mfma_f32_32x32x16_bf16 v[0:15],v[80:83],v[172:175],v[0:15]\n" \
"ds_read_b64_tr_b16 v[72:73],v252 offset:16896\nds_read_b64_tr_b16 v[74:75],v252 offset:17920\ns_waitcnt lgkmcnt(8)\n" \
"v_mfma_f32_32x32x16_bf16 v[16:31],v[84:87],v[172:175],v[16:31]\nds_read_b64_tr_b16 v[76:77],v252 offset:17152\n" \
"ds_read_b64_tr_b16 v[78:79],v252 offset:18176\nv_mfma_f32_32x32x16_bf16 v[32:47],v[88:91],v[172:175],v[32:47]\n" \
"ds_read_b64_tr_b16 v[80:81],v252 offset:20480\nds_read_b64_tr_b16 v[82:83],v252 offset:21504\ns_waitcnt lgkmcnt(8)\n" \
"v_mfma_f32_32x32x16_bf16 v[48:63],v[92:95],v[172:175],v[48:63]\nds_read_b64_tr_b16 v[84:85],v252 offset:20736\n" \
"ds_read_b64_tr_b16 v[86:87],v252 offset:21760\nv_mfma_f32_32x32x16_bf16 v[0:15],v[64:67],v[176:179],v[0:15]\n" \
"ds_read_b64_tr_b16 v[88:89],v252 offset:20992\nds_read_b64_tr_b16 v[90:91],v252 offset:22016\ns_waitcnt lgkmcnt(8)\n" \
"v_mfma_f32_32x32x16_bf16 v[16:31],v[68:71],v[176:179],v[16:31]\nds_read_b64_tr_b16 v[92:93],v252 offset:21248\n" \
"ds_read_b64_tr_b16 v[94:95],v252 offset:22272\nv_mfma_f32_32x32x16_bf16 v[32:47],v[72:75],v[176:179],v[32:47]\n" \
"ds_read_b64_tr_b16 v[64:65],v252 offset:24576\nds_read_b64_tr_b16 v[66:67],v252 offset:25600\ns_waitcnt lgkmcnt(8)\n" \
"v_mfma_f32_32x32x16_bf16 v[48:63],v[76:79],v[176:179],v[48:63]\nds_read_b64_tr_b16 v[68:69],v252 offset:24832\n" \
"ds_read_b64_tr_b16 v[70:71],v252 offset:25856\nv_mfma_f32_32x32x16_bf16 v[0:15],v[80:83],v[180:183],v[0:15]\n" \
"ds_read_b64_tr_b16 v[72:73],v252 offset:25088\nds_read_b64_tr_b16 v[74:75],v252 offset:26112\ns_waitcnt lgkmcnt(8)\n" \
"v_mfma_f32_32x32x16_bf16 v[16:31],v[84:87],v[180:183],v[16:31]\nds_read_b64_tr_b16 v[76:77],v252 offset:25344\n" \
"ds_read_b64_tr_b16 v[78:79],v252 offset:26368\nv_mfma_f32_32x32x16_bf16 v[32:47],v[88:91],v[180:183],v[32:47]\n" \
"ds_read_b64_tr_b16 v[80:81],v252 offset:28672\nds_read_b64_tr_b16 v[82:83],v252 offset:29696\ns_waitcnt lgkmcnt(8)\n" \
"v_mfma_f32_32x32x16_bf16 v[48:63],v[92:95],v[180:183],v[48:63]\nds_read_b64_tr_b16 v[84:85],v252 offset:28928\n" \
"ds_read_b64_tr_b16 v[86:87],v252 offset:29952\nv_mfma_f32_32x32x16_bf16 v[0:15],v[64:67],v[184:187],v[0:15]\n" \
"ds_read_b64_tr_b16 v[88:89],v252 offset:29184\nds_read_b64_tr_b16 v[90:91],v252 offset:30208\ns_waitcnt lgkmcnt(8)\n" \
"v_mfma_f32_32x32x16_bf16 v[16:31],v[68:71],v[184:187],v[16:31]\ns_waitcnt vmcnt(0)\ns_barrier\nds_read_b64_tr_b16 v[92:93],v252 offset:29440\n" \
"ds_read_b64_tr_b16 v[94:95],v252 offset:30464\nv_mfma_f32_32x32x16_bf16 v[32:47],v[72:75],v[184:187],v[32:47]\n" \
"ds_read_b64_tr_b16 v[64:65],v255 offset:0\nds_read_b64_tr_b16 v[66:67],v255 offset:1024\ns_mov_b32 m0,s59\ns_nop 0\n" \
"buffer_load_dwordx4 v228,%[sx],s52 offen lds\ns_waitcnt lgkmcnt(8)\nv_mfma_f32_32x32x16_bf16 v[48:63],v[76:79],v[184:187],v[48:63]\n" \
"ds_read_b64_tr_b16 v[68:69],v255 offset:256\nds_read_b64_tr_b16 v[70:71],v255 offset:1280\ns_add_u32 m0,m0,1024\ns_nop 0\n" \
"buffer_load_dwordx4 v228,%[sx],s53 offen lds\nv_mfma_f32_32x32x16_bf16 v[0:15],v[80:83],v[188:191],v[0:15]\n" \
"ds_read_b64_tr_b16 v[72:73],v255 offset:512\nds_read_b64_tr_b16 v[74:75],v255 offset:1536\ns_add_u32 m0,m0,1024\ns_nop 0\n" \
"buffer_load_dwordx4 v229,%[sx],s54 offen lds\ns_waitcnt lgkmcnt(8)\nv_mfma_f32_32x32x16_bf16 v[16:31],v[84:87],v[188:191],v[16:31]\n" \
"ds_read_b64_tr_b16 v[76:77],v255 offset:768\nds_read_b64_tr_b16 v[78:79],v255 offset:1792\ns_add_u32 m0,m0,1024\ns_nop 0\n" \
"buffer_load_dwordx4 v229,%[sx],s55 offen lds\nv_mfma_f32_32x32x16_bf16 v[32:47],v[88:91],v[188:191],v[32:47]\n" \
"ds_read_b64_tr_b16 v[80:81],v255 offset:4096\nds_read_b64_tr_b16 v[82:83],v255 offset:5120\ns_add_u32 m0,m0,29696\ns_nop 0\n" \
"buffer_load_dwordx4 v230,%[sk],s56 offen lds\ns_waitcnt lgkmcnt(8)\nv_mfma_f32_32x32x16_bf16 v[48:63],v[92:95],v[188:191],v[48:63]\n" \
"ds_read_b64_tr_b16 v[84:85],v255 offset:4352\nds_read_b64_tr_b16 v[86:87],v255 offset:5376\ns_add_u32 m0,m0,1024\ns_nop 0\n" \
"buffer_load_dwordx4 v230,%[sv],s56 offen lds\nv_mfma_f32_32x32x16_bf16 v[0:15],v[64:67],v[192:195],v[0:15]\n" \
"ds_read_b64_tr_b16 v[88:89],v255 offset:4608\nds_read_b64_tr_b16 v[90:91],v255 offset:5632\ns_add_u32 m0,m0,1024\ns_nop 0\n" \
"buffer_load_dwordx4 v230,%[sk],s57 offen lds\ns_waitcnt lgkmcnt(8)\nv_mfma_f32_32x32x16_bf16 v[16:31],v[68:71],v[192:195],v[16:31]\n" \
"ds_read_b64_tr_b16 v[92:93],v255 offset:4864\nds_read_b64_tr_b16 v[94:95],v255 offset:5888\ns_add_u32 m0,m0,1024\ns_nop 0\n" \
"buffer_load_dwordx4 v230,%[sv],s57 offen lds\nv_add_u32 v228,128,v228\nv_add_u32 v229,128,v229\nv_add_u32 v230,0x80000,v230\ns_xor_b32 s59,s59,0x10000\n" \
"v_mfma_f32_32x32x16_bf16 v[32:47],v[72:75],v[192:195],v[32:47]\nds_read_b64_tr_b16 v[64:65],v255 offset:8192\n" \
"ds_read_b64_tr_b16 v[66:67],v255 offset:9216\ns_waitcnt lgkmcnt(8)\nv_mfma_f32_32x32x16_bf16 v[48:63],v[76:79],v[192:195],v[48:63]\n" \
"ds_read_b64_tr_b16 v[68:69],v255 offset:8448\nds_read_b64_tr_b16 v[70:71],v255 offset:9472\n" \
"v_mfma_f32_32x32x16_bf16 v[0:15],v[80:83],v[196:199],v[0:15]\nds_read_b64_tr_b16 v[72:73],v255 offset:8704\n" \
"ds_read_b64_tr_b16 v[74:75],v255 offset:9728\ns_barrier\ns_mov_b32 m0,s59\ns_nop 0\nbuffer_load_dwordx4 v228,%[sx],s52 offen lds\ns_waitcnt lgkmcnt(8)\n" \
"v_mfma_f32_32x32x16_bf16 v[16:31],v[84:87],v[196:199],v[16:31]\nds_read_b64_tr_b16 v[76:77],v255 offset:8960\n" \
"ds_read_b64_tr_b16 v[78:79],v255 offset:9984\ns_add_u32 m0,m0,1024\ns_nop 0\nbuffer_load_dwordx4 v228,%[sx],s53 offen lds\n" \
"v_mfma_f32_32x32x16_bf16 v[32:47],v[88:91],v[196:199],v[32:47]\nds_read_b64_tr_b16 v[80:81],v255 offset:12288\n" \
"ds_read_b64_tr_b16 v[82:83],v255 offset:13312\ns_add_u32 m0,m0,1024\ns_nop 0\nbuffer_load_dwordx4 v229,%[sx],s54 offen lds\ns_waitcnt lgkmcnt(8)\n" \
"v_mfma_f32_32x32x16_bf16 v[48:63],v[92:95],v[196:199],v[48:63]\nds_read_b64_tr_b16 v[84:85],v255 offset:12544\n" \
"ds_read_b64_tr_b16 v[86:87],v255 offset:13568\ns_add_u32 m0,m0,1024\ns_nop 0\nbuffer_load_dwordx4 v229,%[sx],s55 offen lds\n" \
"v_mfma_f32_32x32x16_bf16 v[0:15],v[64:67],v[200:203],v[0:15]\nds_read_b64_tr_b16 v[88:89],v255 offset:12800\n" \
"ds_read_b64_tr_b16 v[90:91],v255 offset:13824\ns_add_u32 m0,m0,29696\ns_nop 0\nbuffer_load_dwordx4 v230,%[sk],s56 offen lds\ns_waitcnt lgkmcnt(8)\n" \
"v_mfma_f32_32x32x16_bf16 v[16:31],v[68:71],v[200:203],v[16:31]\nds_read_b64_tr_b16 v[92:93],v255 offset:13056\n" \
"ds_read_b64_tr_b16 v[94:95],v255 offset:14080\ns_add_u32 m0,m0,1024\ns_nop 0\nbuffer_load_dwordx4 v230,%[sv],s56 offen lds\n" \
"v_mfma_f32_32x32x16_bf16 v[32:47],v[72:75],v[200:203],v[32:47]\nds_read_b64_tr_b16 v[64:65],v255 offset:16384\n" \
"ds_read_b64_tr_b16 v[66:67],v255 offset:17408\ns_add_u32 m0,m0,1024\ns_nop 0\nbuffer_load_dwordx4 v230,%[sk],s57 offen lds\ns_waitcnt lgkmcnt(8)\n" \
"v_mfma_f32_32x32x16_bf16 v[48:63],v[76:79],v[200:203],v[48:63]\nds_read_b64_tr_b16 v[68:69],v255 offset:16640\n" \
"ds_read_b64_tr_b16 v[70:71],v255 offset:17664\ns_add_u32 m0,m0,1024\ns_nop 0\nbuffer_load_dwordx4 v230,%[sv],s57 offen lds\nv_add_u32 v228,128,v228\n" \
"v_add_u32 v229,128,v229\nv_add_u32 v230,0x80000,v230\ns_xor_b32 s59,s59,0x10000\nv_mfma_f32_32x32x16_bf16 v[0:15],v[80:83],v[204:207],v[0:15]\n" \
"ds_read_b64_tr_b16 v[72:73],v255 offset:16896\nds_read_b64_tr_b16 v[74:75],v255 offset:17920\ns_waitcnt lgkmcnt(8)\n" \
"v_mfma_f32_32x32x16_bf16 v[16:31],v[84:87],v[204:207],v[16:31]\nds_read_b64_tr_b16 v[76:77],v255 offset:17152\n" \
"ds_read_b64_tr_b16 v[78:79],v255 offset:18176\nv_mfma_f32_32x32x16_bf16 v[32:47],v[88:91],v[204:207],v[32:47]\n" \
"ds_read_b64_tr_b16 v[80:81],v255 offset:20480\nds_read_b64_tr_b16 v[82:83],v255 offset:21504\ns_waitcnt lgkmcnt(8)\n" \
"v_mfma_f32_32x32x16_bf16 v[48:63],v[92:95],v[204:207],v[48:63]\nds_read_b64_tr_b16 v[84:85],v255 offset:20736\n" \
"ds_read_b64_tr_b16 v[86:87],v255 offset:21760\nv_mfma_f32_32x32x16_bf16 v[0:15],v[64:67],v[208:211],v[0:15]\n" \
"ds_read_b64_tr_b16 v[88:89],v255 offset:20992\nds_read_b64_tr_b16 v[90:91],v255 offset:22016\ns_waitcnt lgkmcnt(8)\n" \
"v_mfma_f32_32x32x16_bf16 v[16:31],v[68:71],v[208:211],v[16:31]\nds_read_b64_tr_b16 v[92:93],v255 offset:21248\n" \
"ds_read_b64_tr_b16 v[94:95],v255 offset:22272\nv_mfma_f32_32x32x16_bf16 v[32:47],v[72:75],v[208:211],v[32:47]\n" \
"ds_read_b64_tr_b16 v[64:65],v255 offset:24576\nds_read_b64_tr_b16 v[66:67],v255 offset:25600\ns_waitcnt lgkmcnt(8)\n" \
"v_mfma_f32_32x32x16_bf16 v[48:63],v[76:79],v[208:211],v[48:63]\nds_read_b64_tr_b16 v[68:69],v255 offset:24832\n" \
"ds_read_b64_tr_b16 v[70:71],v255 offset:25856\nv_mfma_f32_32x32x16_bf16 v[0:15],v[80:83],v[212:215],v[0:15]\n" \
"ds_read_b64_tr_b16 v[72:73],v255 offset:25088\nds_read_b64_tr_b16 v[74:75],v255 offset:26112\ns_waitcnt lgkmcnt(8)\n" \
"v_mfma_f32_32x32x16_bf16 v[16:31],v[84:87],v[212:215],v[16:31]\nds_read_b64_tr_b16 v[76:77],v255 offset:25344\n" \
"ds_read_b64_tr_b16 v[78:79],v255 offset:26368\nv_mfma_f32_32x32x16_bf16 v[32:47],v[88:91],v[212:215],v[32:47]\n" \
"ds_read_b64_tr_b16 v[80:81],v255 offset:28672\nds_read_b64_tr_b16 v[82:83],v255 offset:29696\ns_waitcnt lgkmcnt(8)\n" \
"v_mfma_f32_32x32x16_bf16 v[48:63],v[92:95],v[212:215],v[48:63]\nds_read_b64_tr_b16 v[84:85],v255 offset:28928\n" \
"ds_read_b64_tr_b16 v[86:87],v255 offset:29952\nv_mfma_f32_32x32x16_bf16 v[0:15],v[64:67],v[216:219],v[0:15]\n" \
"ds_read_b64_tr_b16 v[88:89],v255 offset:29184\nds_read_b64_tr_b16 v[90:91],v255 offset:30208\ns_waitcnt lgkmcnt(8)\n" \
"v_mfma_f32_32x32x16_bf16 v[16:31],v[68:71],v[216:219],v[16:31]\nds_read_b64_tr_b16 v[92:93],v255 offset:29440\n" \
"ds_read_b64_tr_b16 v[94:95],v255 offset:30464\nv_mfma_f32_32x32x16_bf16 v[32:47],v[72:75],v[216:219],v[32:47]\ns_waitcnt lgkmcnt(6)\n" \
"v_mfma_f32_32x32x16_bf16 v[48:63],v[76:79],v[216:219],v[48:63]\nv_mfma_f32_32x32x16_bf16 v[0:15],v[80:83],v[220:223],v[0:15]\ns_waitcnt lgkmcnt(2)\n" \
"v_mfma_f32_32x32x16_bf16 v[16:31],v[84:87],v[220:223],v[16:31]\nv_mfma_f32_32x32x16_bf16 v[32:47],v[88:91],v[220:223],v[32:47]\ns_waitcnt lgkmcnt(0)\n" \
"v_mfma_f32_32x32x16_bf16 v[48:63],v[92:95],v[220:223],v[48:63]\ns_nop 7\ns_nop 7\ns_mov_b32 s49,0x3e0293ee\nv_mul_f32 v0,s49,v0\nv_mul_f32 v1,s49,v1\n" \
"v_mul_f32 v2,s49,v2\nv_mul_f32 v3,s49,v3\nv_mul_f32 v4,s49,v4\nv_mul_f32 v5,s49,v5\nv_mul_f32 v6,s49,v6\nv_mul_f32 v7,s49,v7\nv_mul_f32 v8,s49,v8\n" \
"v_mul_f32 v9,s49,v9\nv_mul_f32 v10,s49,v10\nv_mul_f32 v11,s49,v11\nv_mul_f32 v12,s49,v12\nv_mul_f32 v13,s49,v13\nv_mul_f32 v14,s49,v14\n" \
"v_mul_f32 v15,s49,v15\nv_mul_f32 v16,s49,v16\nv_mul_f32 v17,s49,v17\nv_mul_f32 v18,s49,v18\nv_mul_f32 v19,s49,v19\nv_mul_f32 v20,s49,v20\n" \
"v_mul_f32 v21,s49,v21\nv_mul_f32 v22,s49,v22\nv_mul_f32 v23,s49,v23\nv_mul_f32 v24,s49,v24\nv_mul_f32 v25,s49,v25\nv_mul_f32 v26,s49,v26\n" \
"v_mul_f32 v27,s49,v27\nv_mul_f32 v28,s49,v28\nv_mul_f32 v29,s49,v29\nv_mul_f32 v30,s49,v30\nv_mul_f32 v31,s49,v31\nv_mul_f32 v32,s49,v32\n" \
"v_mul_f32 v33,s49,v33\nv_mul_f32 v34,s49,v34\nv_mul_f32 v35,s49,v35\nv_mul_f32 v36,s49,v36\nv_mul_f32 v37,s49,v37\nv_mul_f32 v38,s49,v38\n" \
"v_mul_f32 v39,s49,v39\nv_mul_f32 v40,s49,v40\nv_mul_f32 v41,s49,v41\nv_mul_f32 v42,s49,v42\nv_mul_f32 v43,s49,v43\nv_mul_f32 v44,s49,v44\n" \
"v_mul_f32 v45,s49,v45\nv_mul_f32 v46,s49,v46\nv_mul_f32 v47,s49,v47\nv_mul_f32 v48,s49,v48\nv_mul_f32 v49,s49,v49\nv_mul_f32 v50,s49,v50\n" \
"v_mul_f32 v51,s49,v51\nv_mul_f32 v52,s49,v52\nv_mul_f32 v53,s49,v53\nv_mul_f32 v54,s49,v54\nv_mul_f32 v55,s49,v55\nv_mul_f32 v56,s49,v56\n" \
"v_mul_f32 v57,s49,v57\nv_mul_f32 v58,s49,v58\nv_mul_f32 v59,s49,v59\nv_mul_f32 v60,s49,v60\nv_mul_f32 v61,s49,v61\nv_mul_f32 v62,s49,v62\n" \
"v_mul_f32 v63,s49,v63\nv_cvt_pk_bf16_f32 v192,v0,v1\nv_cvt_pk_bf16_f32 v193,v2,v3\nv_cvt_pk_bf16_f32 v194,v4,v5\nv_cvt_pk_bf16_f32 v195,v6,v7\n" \
"v_cvt_pk_bf16_f32 v196,v8,v9\nv_cvt_pk_bf16_f32 v197,v10,v11\nv_cvt_pk_bf16_f32 v198,v12,v13\nv_cvt_pk_bf16_f32 v199,v14,v15\n" \
"v_cvt_pk_bf16_f32 v200,v16,v17\nv_cvt_pk_bf16_f32 v201,v18,v19\nv_cvt_pk_bf16_f32 v202,v20,v21\nv_cvt_pk_bf16_f32 v203,v22,v23\n" \
"v_cvt_pk_bf16_f32 v204,v24,v25\nv_cvt_pk_bf16_f32 v205,v26,v27\nv_cvt_pk_bf16_f32 v206,v28,v29\nv_cvt_pk_bf16_f32 v207,v30,v31\n" \
"v_cvt_pk_bf16_f32 v208,v32,v33\nv_cvt_pk_bf16_f32 v209,v34,v35\nv_cvt_pk_bf16_f32 v210,v36,v37\nv_cvt_pk_bf16_f32 v211,v38,v39\n" \
"v_cvt_pk_bf16_f32 v212,v40,v41\nv_cvt_pk_bf16_f32 v213,v42,v43\nv_cvt_pk_bf16_f32 v214,v44,v45\nv_cvt_pk_bf16_f32 v215,v46,v47\n" \
"v_cvt_pk_bf16_f32 v216,v48,v49\nv_cvt_pk_bf16_f32 v217,v50,v51\nv_cvt_pk_bf16_f32 v218,v52,v53\nv_cvt_pk_bf16_f32 v219,v54,v55\n" \
"v_cvt_pk_bf16_f32 v220,v56,v57\nv_cvt_pk_bf16_f32 v221,v58,v59\nv_cvt_pk_bf16_f32 v222,v60,v61\nv_cvt_pk_bf16_f32 v223,v62,v63\ns_waitcnt vmcnt(8)\n" \
"s_barrier\nds_read_b64_tr_b16 v[160:161],v226 offset:0\nds_read_b64_tr_b16 v[162:163],v226 offset:2048\nds_read_b64_tr_b16 v[164:165],v227 offset:0\n" \
"ds_read_b64_tr_b16 v[166:167],v227 offset:2048\nds_read_b64_tr_b16 v[168:169],v226 offset:256\nds_read_b64_tr_b16 v[170:171],v226 offset:2304\n" \
"ds_read_b64_tr_b16 v[172:173],v227 offset:256\nds_read_b64_tr_b16 v[174:175],v227 offset:2304\nds_read_b128 v[128:131],v224 offset:0\n" \
"ds_read_b128 v[132:135],v224 offset:1024\nds_read_b128 v[136:139],v224 offset:4096\nds_read_b128 v[140:143],v224 offset:5120\n" \
"ds_read_b128 v[144:147],v224 offset:8192\nds_read_b128 v[148:151],v224 offset:9216\nds_read_b128 v[152:155],v224 offset:12288\n" \
"ds_read_b128 v[156:159],v224 offset:13312\ns_waitcnt lgkmcnt(7)\nv_mfma_f32_16x16x32_bf16 v[0:3],v[160:163],v[128:131],0\n" \
"ds_read_b64_tr_b16 v[176:177],v226 offset:16384\nv_mfma_f32_16x16x32_bf16 v[4:7],v[164:167],v[128:131],0\n" \
"v_mfma_f32_16x16x32_bf16 v[8:11],v[168:171],v[128:131],0\nv_mfma_f32_16x16x32_bf16 v[12:15],v[172:175],v[128:131],0\n" \
"ds_read_b128 v[128:131],v225 offset:0\nds_read_b64_tr_b16 v[178:179],v226 offset:18432\ns_waitcnt lgkmcnt(9)\n" \
"v_mfma_f32_16x16x32_bf16 v[16:19],v[160:163],v[132:135],0\nv_mfma_f32_16x16x32_bf16 v[20:23],v[164:167],v[132:135],0\n" \
"v_mfma_f32_16x16x32_bf16 v[24:27],v[168:171],v[132:135],0\nds_read_b64_tr_b16 v[180:181],v227 offset:16384\n" \
"v_mfma_f32_16x16x32_bf16 v[28:31],v[172:175],v[132:135],0\nds_read_b128 v[132:135],v225 offset:1024\ns_waitcnt lgkmcnt(10)\n" \
"v_mfma_f32_16x16x32_bf16 v[32:35],v[160:163],v[136:139],0\nv_mfma_f32_16x16x32_bf16 v[36:39],v[164:167],v[136:139],0\n" \
"ds_read_b64_tr_b16 v[182:183],v227 offset:18432\nv_mfma_f32_16x16x32_bf16 v[40:43],v[168:171],v[136:139],0\n" \
"v_mfma_f32_16x16x32_bf16 v[44:47],v[172:175],v[136:139],0\nds_read_b128 v[136:139],v225 offset:4096\ns_waitcnt lgkmcnt(11)\n" \
"v_mfma_f32_16x16x32_bf16 v[48:51],v[160:163],v[140:143],0\nds_read_b64_tr_b16 v[184:185],v226 offset:16640\n" \
"v_mfma_f32_16x16x32_bf16 v[52:55],v[164:167],v[140:143],0\nv_mfma_f32_16x16x32_bf16 v[56:59],v[168:171],v[140:143],0\n" \
"v_mfma_f32_16x16x32_bf16 v[60:63],v[172:175],v[140:143],0\nds_read_b128 v[140:143],v225 offset:5120\nds_read_b64_tr_b16 v[186:187],v226 offset:18688\n" \
"s_waitcnt lgkmcnt(13)\nv_mfma_f32_16x16x32_bf16 v[64:67],v[160:163],v[144:147],0\nv_mfma_f32_16x16x32_bf16 v[68:71],v[164:167],v[144:147],0\n" \
"v_mfma_f32_16x16x32_bf16 v[72:75],v[168:171],v[144:147],0\nds_read_b64_tr_b16 v[188:189],v227 offset:16640\n" \
"v_mfma_f32_16x16x32_bf16 v[76:79],v[172:175],v[144:147],0\nds_read_b128 v[144:147],v225 offset:8192\ns_waitcnt lgkmcnt(14)\n" \
"v_mfma_f32_16x16x32_bf16 v[80:83],v[160:163],v[148:151],0\nv_mfma_f32_16x16x32_bf16 v[84:87],v[164:167],v[148:151],0\n" \
"ds_read_b64_tr_b16 v[190:191],v227 offset:18688\nv_mfma_f32_16x16x32_bf16 v[88:91],v[168:171],v[148:151],0\n" \
"v_mfma_f32_16x16x32_bf16 v[92:95],v[172:175],v[148:151],0\nds_read_b128 v[148:151],v225 offset:9216\ns_waitcnt lgkmcnt(15)\n" \
"v_mfma_f32_16x16x32_bf16 v[96:99],v[160:163],v[152:155],0\nv_mfma_f32_16x16x32_bf16 v[100:103],v[164:167],v[152:155],0\n" \
"v_mfma_f32_16x16x32_bf16 v[104:107],v[168:171],v[152:155],0\nv_mfma_f32_16x16x32_bf16 v[108:111],v[172:175],v[152:155],0\n" \
"ds_read_b128 v[152:155],v225 offset:12288\ns_waitcnt lgkmcnt(15)\nv_mfma_f32_16x16x32_bf16 v[112:115],v[160:163],v[156:159],0\n" \
"v_mfma_f32_16x16x32_bf16 v[116:119],v[164:167],v[156:159],0\nv_mfma_f32_16x16x32_bf16 v[120:123],v[168:171],v[156:159],0\n" \
"v_mfma_f32_16x16x32_bf16 v[124:127],v[172:175],v[156:159],0\nds_read_b128 v[156:159],v225 offset:13312\ns_waitcnt lgkmcnt(13)\n" \
"v_mfma_f32_16x16x32_bf16 v[0:3],v[176:179],v[128:131],v[0:3]\ns_waitcnt lgkmcnt(10)\nv_mfma_f32_16x16x32_bf16 v[4:7],v[180:183],v[128:131],v[4:7]\n" \
"s_waitcnt lgkmcnt(6)\nv_mfma_f32_16x16x32_bf16 v[8:11],v[184:187],v[128:131],v[8:11]\ns_waitcnt lgkmcnt(3)\n" \
"v_mfma_f32_16x16x32_bf16 v[12:15],v[188:191],v[128:131],v[12:15]\ns_waitcnt vmcnt(0) lgkmcnt(0)\ns_barrier\nv_xor_b32 v224,0x10000,v224\n" \
"v_xor_b32 v225,0x10000,v225\nv_xor_b32 v226,0x10000,v226\nv_xor_b32 v227,0x10000,v227\n" \
"v_mfma_f32_16x16x32_bf16 v[16:19],v[176:179],v[132:135],v[16:19]\nds_read_b128 v[128:131],v224 offset:0\n" \
"v_mfma_f32_16x16x32_bf16 v[20:23],v[180:183],v[132:135],v[20:23]\nds_read_b64_tr_b16 v[160:161],v226 offset:0\ns_mov_b32 m0,s59\ns_nop 0\n" \
"buffer_load_dwordx4 v228,%[sx],s52 offen lds\nv_mfma_f32_16x16x32_bf16 v[24:27],v[184:187],v[132:135],v[24:27]\n" \
"v_mfma_f32_16x16x32_bf16 v[28:31],v[188:191],v[132:135],v[28:31]\nds_read_b128 v[132:135],v224 offset:1024\n" \
"v_mfma_f32_16x16x32_bf16 v[32:35],v[176:179],v[136:139],v[32:35]\nds_read_b64_tr_b16 v[162:163],v226 offset:2048\ns_add_u32 m0,m0,1024\ns_nop 0\n" \
"buffer_load_dwordx4 v228,%[sx],s53 offen lds\nv_mfma_f32_16x16x32_bf16 v[36:39],v[180:183],v[136:139],v[36:39]\n" \
"v_mfma_f32_16x16x32_bf16 v[40:43],v[184:187],v[136:139],v[40:43]\nv_mfma_f32_16x16x32_bf16 v[44:47],v[188:191],v[136:139],v[44:47]\n" \
"ds_read_b128 v[136:139],v224 offset:4096\nds_read_b64_tr_b16 v[164:165],v227 offset:0\ns_add_u32 m0,m0,1024\ns_nop 0\n" \
"buffer_load_dwordx4 v229,%[sx],s54 offen lds\nv_mfma_f32_16x16x32_bf16 v[48:51],v[176:179],v[140:143],v[48:51]\n" \
"v_mfma_f32_16x16x32_bf16 v[52:55],v[180:183],v[140:143],v[52:55]\nv_mfma_f32_16x16x32_bf16 v[56:59],v[184:187],v[140:143],v[56:59]\n" \
"ds_read_b64_tr_b16 v[166:167],v227 offset:2048\ns_add_u32 m0,m0,1024\ns_nop 0\nbuffer_load_dwordx4 v229,%[sx],s55 offen lds\n" \
"v_mfma_f32_16x16x32_bf16 v[60:63],v[188:191],v[140:143],v[60:63]\nds_read_b128 v[140:143],v224 offset:5120\n" \
"v_mfma_f32_16x16x32_bf16 v[64:67],v[176:179],v[144:147],v[64:67]\nv_mfma_f32_16x16x32_bf16 v[68:71],v[180:183],v[144:147],v[68:71]\n" \
"ds_read_b64_tr_b16 v[168:169],v226 offset:256\ns_add_u32 m0,m0,29696\ns_nop 0\nbuffer_load_dwordx4 v230,%[sk],s56 offen lds\n" \
"v_mfma_f32_16x16x32_bf16 v[72:75],v[184:187],v[144:147],v[72:75]\nv_mfma_f32_16x16x32_bf16 v[76:79],v[188:191],v[144:147],v[76:79]\n" \
"ds_read_b128 v[144:147],v224 offset:8192\nv_mfma_f32_16x16x32_bf16 v[80:83],v[176:179],v[148:151],v[80:83]\n" \
"ds_read_b64_tr_b16 v[170:171],v226 offset:2304\ns_add_u32 m0,m0,1024\ns_nop 0\nbuffer_load_dwordx4 v230,%[sv],s56 offen lds\n" \
"v_mfma_f32_16x16x32_bf16 v[84:87],v[180:183],v[148:151],v[84:87]\nv_mfma_f32_16x16x32_bf16 v[88:91],v[184:187],v[148:151],v[88:91]\n" \
"v_mfma_f32_16x16x32_bf16 v[92:95],v[188:191],v[148:151],v[92:95]\nds_read_b128 v[148:151],v224 offset:9216\n" \
"ds_read_b64_tr_b16 v[172:173],v227 offset:256\ns_add_u32 m0,m0,1024\ns_nop 0\nbuffer_load_dwordx4 v230,%[sk],s57 offen lds\n" \
"v_mfma_f32_16x16x32_bf16 v[96:99],v[176:179],v[152:155],v[96:99]\nv_mfma_f32_16x16x32_bf16 v[100:103],v[180:183],v[152:155],v[100:103]\n" \
"v_mfma_f32_16x16x32_bf16 v[104:107],v[184:187],v[152:155],v[104:107]\nds_read_b64_tr_b16 v[174:175],v227 offset:2304\ns_add_u32 m0,m0,1024\ns_nop 0\n" \
"buffer_load_dwordx4 v230,%[sv],s57 offen lds\nv_add_u32 v228,128,v228\nv_add_u32 v229,128,v229\nv_add_u32 v230,0x80000,v230\ns_xor_b32 s59,s59,0x10000\n" \
"v_mfma_f32_16x16x32_bf16 v[108:111],v[188:191],v[152:155],v[108:111]\nds_read_b128 v[152:155],v224 offset:12288\n" \
"v_mfma_f32_16x16x32_bf16 v[112:115],v[176:179],v[156:159],v[112:115]\nv_mfma_f32_16x16x32_bf16 v[116:119],v[180:183],v[156:159],v[116:119]\n" \
"v_mfma_f32_16x16x32_bf16 v[120:123],v[184:187],v[156:159],v[120:123]\nv_mfma_f32_16x16x32_bf16 v[124:127],v[188:191],v[156:159],v[124:127]\n" \
"ds_read_b128 v[156:159],v224 offset:13312\ns_mov_b32 s60,5\n1:\ns_waitcnt lgkmcnt(12)\nv_mfma_f32_16x16x32_bf16 v[0:3],v[160:163],v[128:131],v[0:3]\n" \
"ds_read_b64_tr_b16 v[176:177],v226 offset:16384\ns_waitcnt lgkmcnt(10)\nv_mfma_f32_16x16x32_bf16 v[4:7],v[164:167],v[128:131],v[4:7]\n" \
"s_waitcnt lgkmcnt(6)\nv_mfma_f32_16x16x32_bf16 v[8:11],v[168:171],v[128:131],v[8:11]\ns_waitcnt lgkmcnt(3)\n" \
"v_mfma_f32_16x16x32_bf16 v[12:15],v[172:175],v[128:131],v[12:15]\nds_read_b128 v[128:131],v225 offset:0\n" \
"ds_read_b64_tr_b16 v[178:179],v226 offset:18432\nv_mfma_f32_16x16x32_bf16 v[16:19],v[160:163],v[132:135],v[16:19]\n" \
"v_mfma_f32_16x16x32_bf16 v[20:23],v[164:167],v[132:135],v[20:23]\nv_mfma_f32_16x16x32_bf16 v[24:27],v[168:171],v[132:135],v[24:27]\n" \
"ds_read_b64_tr_b16 v[180:181],v227 offset:16384\nv_mfma_f32_16x16x32_bf16 v[28:31],v[172:175],v[132:135],v[28:31]\n" \
"ds_read_b128 v[132:135],v225 offset:1024\nv_mfma_f32_16x16x32_bf16 v[32:35],v[160:163],v[136:139],v[32:35]\n" \
"v_mfma_f32_16x16x32_bf16 v[36:39],v[164:167],v[136:139],v[36:39]\nds_read_b64_tr_b16 v[182:183],v227 offset:18432\n" \
"v_mfma_f32_16x16x32_bf16 v[40:43],v[168:171],v[136:139],v[40:43]\nv_mfma_f32_16x16x32_bf16 v[44:47],v[172:175],v[136:139],v[44:47]\n" \
"ds_read_b128 v[136:139],v225 offset:4096\nv_mfma_f32_16x16x32_bf16 v[48:51],v[160:163],v[140:143],v[48:51]\n" \
"ds_read_b64_tr_b16 v[184:185],v226 offset:16640\nv_mfma_f32_16x16x32_bf16 v[52:55],v[164:167],v[140:143],v[52:55]\n" \
"v_mfma_f32_16x16x32_bf16 v[56:59],v[168:171],v[140:143],v[56:59]\nv_mfma_f32_16x16x32_bf16 v[60:63],v[172:175],v[140:143],v[60:63]\n" \
"ds_read_b128 v[140:143],v225 offset:5120\nds_read_b64_tr_b16 v[186:187],v226 offset:18688\n" \
"v_mfma_f32_16x16x32_bf16 v[64:67],v[160:163],v[144:147],v[64:67]\nv_mfma_f32_16x16x32_bf16 v[68:71],v[164:167],v[144:147],v[68:71]\n" \
"v_mfma_f32_16x16x32_bf16 v[72:75],v[168:171],v[144:147],v[72:75]\nds_read_b64_tr_b16 v[188:189],v227 offset:16640\n" \
"v_mfma_f32_16x16x32_bf16 v[76:79],v[172:175],v[144:147],v[76:79]\nds_read_b128 v[144:147],v225 offset:8192\n" \
"v_mfma_f32_16x16x32_bf16 v[80:83],v[160:163],v[148:151],v[80:83]\nv_mfma_f32_16x16x32_bf16 v[84:87],v[164:167],v[148:151],v[84:87]\n" \
"ds_read_b64_tr_b16 v[190:191],v227 offset:18688\nv_mfma_f32_16x16x32_bf16 v[88:91],v[168:171],v[148:151],v[88:91]\n" \
"v_mfma_f32_16x16x32_bf16 v[92:95],v[172:175],v[148:151],v[92:95]\nds_read_b128 v[148:151],v225 offset:9216\ns_waitcnt lgkmcnt(15)\n" \
"v_mfma_f32_16x16x32_bf16 v[96:99],v[160:163],v[152:155],v[96:99]\nv_mfma_f32_16x16x32_bf16 v[100:103],v[164:167],v[152:155],v[100:103]\n" \
"v_mfma_f32_16x16x32_bf16 v[104:107],v[168:171],v[152:155],v[104:107]\nv_mfma_f32_16x16x32_bf16 v[108:111],v[172:175],v[152:155],v[108:111]\n" \
"ds_read_b128 v[152:155],v225 offset:12288\ns_waitcnt lgkmcnt(15)\nv_mfma_f32_16x16x32_bf16 v[112:115],v[160:163],v[156:159],v[112:115]\n" \
"v_mfma_f32_16x16x32_bf16 v[116:119],v[164:167],v[156:159],v[116:119]\nv_mfma_f32_16x16x32_bf16 v[120:123],v[168:171],v[156:159],v[120:123]\n" \
"v_mfma_f32_16x16x32_bf16 v[124:127],v[172:175],v[156:159],v[124:127]\nds_read_b128 v[156:159],v225 offset:13312\ns_waitcnt lgkmcnt(13)\n" \
"v_mfma_f32_16x16x32_bf16 v[0:3],v[176:179],v[128:131],v[0:3]\ns_waitcnt lgkmcnt(10)\nv_mfma_f32_16x16x32_bf16 v[4:7],v[180:183],v[128:131],v[4:7]\n" \
"s_waitcnt lgkmcnt(6)\nv_mfma_f32_16x16x32_bf16 v[8:11],v[184:187],v[128:131],v[8:11]\ns_waitcnt lgkmcnt(3)\n" \
"v_mfma_f32_16x16x32_bf16 v[12:15],v[188:191],v[128:131],v[12:15]\ns_waitcnt vmcnt(0) lgkmcnt(0)\ns_barrier\nv_xor_b32 v224,0x10000,v224\n" \
"v_xor_b32 v225,0x10000,v225\nv_xor_b32 v226,0x10000,v226\nv_xor_b32 v227,0x10000,v227\n" \
"v_mfma_f32_16x16x32_bf16 v[16:19],v[176:179],v[132:135],v[16:19]\nds_read_b128 v[128:131],v224 offset:0\n" \
"v_mfma_f32_16x16x32_bf16 v[20:23],v[180:183],v[132:135],v[20:23]\nds_read_b64_tr_b16 v[160:161],v226 offset:0\ns_mov_b32 m0,s59\ns_nop 0\n" \
"buffer_load_dwordx4 v228,%[sx],s52 offen lds\nv_mfma_f32_16x16x32_bf16 v[24:27],v[184:187],v[132:135],v[24:27]\n" \
"v_mfma_f32_16x16x32_bf16 v[28:31],v[188:191],v[132:135],v[28:31]\nds_read_b128 v[132:135],v224 offset:1024\n" \
"v_mfma_f32_16x16x32_bf16 v[32:35],v[176:179],v[136:139],v[32:35]\nds_read_b64_tr_b16 v[162:163],v226 offset:2048\ns_add_u32 m0,m0,1024\ns_nop 0\n" \
"buffer_load_dwordx4 v228,%[sx],s53 offen lds\nv_mfma_f32_16x16x32_bf16 v[36:39],v[180:183],v[136:139],v[36:39]\n" \
"v_mfma_f32_16x16x32_bf16 v[40:43],v[184:187],v[136:139],v[40:43]\nv_mfma_f32_16x16x32_bf16 v[44:47],v[188:191],v[136:139],v[44:47]\n" \
"ds_read_b128 v[136:139],v224 offset:4096\nds_read_b64_tr_b16 v[164:165],v227 offset:0\ns_add_u32 m0,m0,1024\ns_nop 0\n" \
"buffer_load_dwordx4 v229,%[sx],s54 offen lds\nv_mfma_f32_16x16x32_bf16 v[48:51],v[176:179],v[140:143],v[48:51]\n" \
"v_mfma_f32_16x16x32_bf16 v[52:55],v[180:183],v[140:143],v[52:55]\nv_mfma_f32_16x16x32_bf16 v[56:59],v[184:187],v[140:143],v[56:59]\n" \
"ds_read_b64_tr_b16 v[166:167],v227 offset:2048\ns_add_u32 m0,m0,1024\ns_nop 0\nbuffer_load_dwordx4 v229,%[sx],s55 offen lds\n" \
"v_mfma_f32_16x16x32_bf16 v[60:63],v[188:191],v[140:143],v[60:63]\nds_read_b128 v[140:143],v224 offset:5120\n" \
"v_mfma_f32_16x16x32_bf16 v[64:67],v[176:179],v[144:147],v[64:67]\nv_mfma_f32_16x16x32_bf16 v[68:71],v[180:183],v[144:147],v[68:71]\n" \
"ds_read_b64_tr_b16 v[168:169],v226 offset:256\ns_add_u32 m0,m0,29696\ns_nop 0\nbuffer_load_dwordx4 v230,%[sk],s56 offen lds\n" \
"v_mfma_f32_16x16x32_bf16 v[72:75],v[184:187],v[144:147],v[72:75]\nv_mfma_f32_16x16x32_bf16 v[76:79],v[188:191],v[144:147],v[76:79]\n" \
"ds_read_b128 v[144:147],v224 offset:8192\nv_mfma_f32_16x16x32_bf16 v[80:83],v[176:179],v[148:151],v[80:83]\n" \
"ds_read_b64_tr_b16 v[170:171],v226 offset:2304\ns_add_u32 m0,m0,1024\ns_nop 0\nbuffer_load_dwordx4 v230,%[sv],s56 offen lds\n" \
"v_mfma_f32_16x16x32_bf16 v[84:87],v[180:183],v[148:151],v[84:87]\nv_mfma_f32_16x16x32_bf16 v[88:91],v[184:187],v[148:151],v[88:91]\n" \
"v_mfma_f32_16x16x32_bf16 v[92:95],v[188:191],v[148:151],v[92:95]\nds_read_b128 v[148:151],v224 offset:9216\n" \
"ds_read_b64_tr_b16 v[172:173],v227 offset:256\ns_add_u32 m0,m0,1024\ns_nop 0\nbuffer_load_dwordx4 v230,%[sk],s57 offen lds\n" \
"v_mfma_f32_16x16x32_bf16 v[96:99],v[176:179],v[152:155],v[96:99]\nv_mfma_f32_16x16x32_bf16 v[100:103],v[180:183],v[152:155],v[100:103]\n" \
"v_mfma_f32_16x16x32_bf16 v[104:107],v[184:187],v[152:155],v[104:107]\nds_read_b64_tr_b16 v[174:175],v227 offset:2304\ns_add_u32 m0,m0,1024\ns_nop 0\n" \
"buffer_load_dwordx4 v230,%[sv],s57 offen lds\nv_add_u32 v228,128,v228\nv_add_u32 v229,128,v229\nv_add_u32 v230,0x80000,v230\ns_xor_b32 s59,s59,0x10000\n" \
"v_mfma_f32_16x16x32_bf16 v[108:111],v[188:191],v[152:155],v[108:111]\nds_read_b128 v[152:155],v224 offset:12288\n" \
"v_mfma_f32_16x16x32_bf16 v[112:115],v[176:179],v[156:159],v[112:115]\nv_mfma_f32_16x16x32_bf16 v[116:119],v[180:183],v[156:159],v[116:119]\n" \
"v_mfma_f32_16x16x32_bf16 v[120:123],v[184:187],v[156:159],v[120:123]\nv_mfma_f32_16x16x32_bf16 v[124:127],v[188:191],v[156:159],v[124:127]\n" \
"ds_read_b128 v[156:159],v224 offset:13312\ns_sub_u32 s60,s60,1\ns_cmp_lg_u32 s60,0\ns_cbranch_scc1 1b\ns_waitcnt lgkmcnt(12)\n" \
"v_mfma_f32_16x16x32_bf16 v[0:3],v[160:163],v[128:131],v[0:3]\nds_read_b64_tr_b16 v[176:177],v226 offset:16384\ns_waitcnt lgkmcnt(10)\n" \
"v_mfma_f32_16x16x32_bf16 v[4:7],v[164:167],v[128:131],v[4:7]\ns_waitcnt lgkmcnt(6)\nv_mfma_f32_16x16x32_bf16 v[8:11],v[168:171],v[128:131],v[8:11]\n" \
"s_waitcnt lgkmcnt(3)\nv_mfma_f32_16x16x32_bf16 v[12:15],v[172:175],v[128:131],v[12:15]\nds_read_b128 v[128:131],v225 offset:0\n" \
"ds_read_b64_tr_b16 v[178:179],v226 offset:18432\nv_mfma_f32_16x16x32_bf16 v[16:19],v[160:163],v[132:135],v[16:19]\n" \
"v_mfma_f32_16x16x32_bf16 v[20:23],v[164:167],v[132:135],v[20:23]\nv_mfma_f32_16x16x32_bf16 v[24:27],v[168:171],v[132:135],v[24:27]\n" \
"ds_read_b64_tr_b16 v[180:181],v227 offset:16384\nv_mfma_f32_16x16x32_bf16 v[28:31],v[172:175],v[132:135],v[28:31]\n" \
"ds_read_b128 v[132:135],v225 offset:1024\nv_mfma_f32_16x16x32_bf16 v[32:35],v[160:163],v[136:139],v[32:35]\n" \
"v_mfma_f32_16x16x32_bf16 v[36:39],v[164:167],v[136:139],v[36:39]\nds_read_b64_tr_b16 v[182:183],v227 offset:18432\n" \
"v_mfma_f32_16x16x32_bf16 v[40:43],v[168:171],v[136:139],v[40:43]\nv_mfma_f32_16x16x32_bf16 v[44:47],v[172:175],v[136:139],v[44:47]\n" \
"ds_read_b128 v[136:139],v225 offset:4096\nv_mfma_f32_16x16x32_bf16 v[48:51],v[160:163],v[140:143],v[48:51]\n" \
"ds_read_b64_tr_b16 v[184:185],v226 offset:16640\nv_mfma_f32_16x16x32_bf16 v[52:55],v[164:167],v[140:143],v[52:55]\n" \
"v_mfma_f32_16x16x32_bf16 v[56:59],v[168:171],v[140:143],v[56:59]\nv_mfma_f32_16x16x32_bf16 v[60:63],v[172:175],v[140:143],v[60:63]\n" \
"ds_read_b128 v[140:143],v225 offset:5120\nds_read_b64_tr_b16 v[186:187],v226 offset:18688\n" \
"v_mfma_f32_16x16x32_bf16 v[64:67],v[160:163],v[144:147],v[64:67]\nv_mfma_f32_16x16x32_bf16 v[68:71],v[164:167],v[144:147],v[68:71]\n" \
"v_mfma_f32_16x16x32_bf16 v[72:75],v[168:171],v[144:147],v[72:75]\nds_read_b64_tr_b16 v[188:189],v227 offset:16640\n" \
"v_mfma_f32_16x16x32_bf16 v[76:79],v[172:175],v[144:147],v[76:79]\nds_read_b128 v[144:147],v225 offset:8192\n" \
"v_mfma_f32_16x16x32_bf16 v[80:83],v[160:163],v[148:151],v[80:83]\nv_mfma_f32_16x16x32_bf16 v[84:87],v[164:167],v[148:151],v[84:87]\n" \
"ds_read_b64_tr_b16 v[190:191],v227 offset:18688\nv_mfma_f32_16x16x32_bf16 v[88:91],v[168:171],v[148:151],v[88:91]\n" \
"v_mfma_f32_16x16x32_bf16 v[92:95],v[172:175],v[148:151],v[92:95]\nds_read_b128 v[148:151],v225 offset:9216\ns_waitcnt lgkmcnt(15)\n" \
"v_mfma_f32_16x16x32_bf16 v[96:99],v[160:163],v[152:155],v[96:99]\nv_mfma_f32_16x16x32_bf16 v[100:103],v[164:167],v[152:155],v[100:103]\n" \
"v_mfma_f32_16x16x32_bf16 v[104:107],v[168:171],v[152:155],v[104:107]\nv_mfma_f32_16x16x32_bf16 v[108:111],v[172:175],v[152:155],v[108:111]\n" \
"ds_read_b128 v[152:155],v225 offset:12288\ns_waitcnt lgkmcnt(15)\nv_mfma_f32_16x16x32_bf16 v[112:115],v[160:163],v[156:159],v[112:115]\n" \
"v_mfma_f32_16x16x32_bf16 v[116:119],v[164:167],v[156:159],v[116:119]\nv_mfma_f32_16x16x32_bf16 v[120:123],v[168:171],v[156:159],v[120:123]\n" \
"v_mfma_f32_16x16x32_bf16 v[124:127],v[172:175],v[156:159],v[124:127]\nds_read_b128 v[156:159],v225 offset:13312\ns_waitcnt lgkmcnt(13)\n" \
"v_mfma_f32_16x16x32_bf16 v[0:3],v[176:179],v[128:131],v[0:3]\ns_waitcnt lgkmcnt(10)\nv_mfma_f32_16x16x32_bf16 v[4:7],v[180:183],v[128:131],v[4:7]\n" \
"s_waitcnt lgkmcnt(6)\nv_mfma_f32_16x16x32_bf16 v[8:11],v[184:187],v[128:131],v[8:11]\ns_waitcnt lgkmcnt(3)\n" \
"v_mfma_f32_16x16x32_bf16 v[12:15],v[188:191],v[128:131],v[12:15]\ns_waitcnt vmcnt(0) lgkmcnt(0)\ns_barrier\nv_xor_b32 v224,0x10000,v224\n" \
"v_xor_b32 v225,0x10000,v225\nv_xor_b32 v226,0x10000,v226\nv_xor_b32 v227,0x10000,v227\n" \
"v_mfma_f32_16x16x32_bf16 v[16:19],v[176:179],v[132:135],v[16:19]\nds_read_b128 v[128:131],v224 offset:0\n" \
"v_mfma_f32_16x16x32_bf16 v[20:23],v[180:183],v[132:135],v[20:23]\nds_read_b64_tr_b16 v[160:161],v226 offset:0\ns_add_u32 m0,s80,65536\n" \
"s_add_u32 s49,s68,0\nbuffer_load_dwordx4 v248,%[sws],s49 offen offset:0 lds\nv_mfma_f32_16x16x32_bf16 v[24:27],v[184:187],v[132:135],v[24:27]\n" \
"v_mfma_f32_16x16x32_bf16 v[28:31],v[188:191],v[132:135],v[28:31]\nds_read_b128 v[132:135],v224 offset:1024\n" \
"v_mfma_f32_16x16x32_bf16 v[32:35],v[176:179],v[136:139],v[32:35]\nds_read_b64_tr_b16 v[162:163],v226 offset:2048\n" \
"buffer_load_dwordx4 v248,%[sws],s49 offen offset:1024 lds\nv_mfma_f32_16x16x32_bf16 v[36:39],v[180:183],v[136:139],v[36:39]\n" \
"v_mfma_f32_16x16x32_bf16 v[40:43],v[184:187],v[136:139],v[40:43]\nv_mfma_f32_16x16x32_bf16 v[44:47],v[188:191],v[136:139],v[44:47]\n" \
"ds_read_b128 v[136:139],v224 offset:4096\nds_read_b64_tr_b16 v[164:165],v227 offset:0\nbuffer_load_dwordx4 v248,%[sws],s49 offen offset:2048 lds\n" \
"v_mfma_f32_16x16x32_bf16 v[48:51],v[176:179],v[140:143],v[48:51]\nv_mfma_f32_16x16x32_bf16 v[52:55],v[180:183],v[140:143],v[52:55]\n" \
"v_mfma_f32_16x16x32_bf16 v[56:59],v[184:187],v[140:143],v[56:59]\nds_read_b64_tr_b16 v[166:167],v227 offset:2048\n" \
"buffer_load_dwordx4 v248,%[sws],s49 offen offset:3072 lds\nv_mfma_f32_16x16x32_bf16 v[60:63],v[188:191],v[140:143],v[60:63]\n" \
"ds_read_b128 v[140:143],v224 offset:5120\nv_mfma_f32_16x16x32_bf16 v[64:67],v[176:179],v[144:147],v[64:67]\n" \
"v_mfma_f32_16x16x32_bf16 v[68:71],v[180:183],v[144:147],v[68:71]\nds_read_b64_tr_b16 v[168:169],v226 offset:256\ns_add_u32 m0,s80,69632\n" \
"s_add_u32 s49,s68,4096\nbuffer_load_dwordx4 v248,%[sws],s49 offen offset:0 lds\nv_mfma_f32_16x16x32_bf16 v[72:75],v[184:187],v[144:147],v[72:75]\n" \
"v_mfma_f32_16x16x32_bf16 v[76:79],v[188:191],v[144:147],v[76:79]\nds_read_b128 v[144:147],v224 offset:8192\n" \
"v_mfma_f32_16x16x32_bf16 v[80:83],v[176:179],v[148:151],v[80:83]\nds_read_b64_tr_b16 v[170:171],v226 offset:2304\n" \
"buffer_load_dwordx4 v248,%[sws],s49 offen offset:1024 lds\nv_mfma_f32_16x16x32_bf16 v[84:87],v[180:183],v[148:151],v[84:87]\n" \
"v_mfma_f32_16x16x32_bf16 v[88:91],v[184:187],v[148:151],v[88:91]\nv_mfma_f32_16x16x32_bf16 v[92:95],v[188:191],v[148:151],v[92:95]\n" \
"ds_read_b128 v[148:151],v224 offset:9216\nds_read_b64_tr_b16 v[172:173],v227 offset:256\n" \
"v_mfma_f32_16x16x32_bf16 v[96:99],v[176:179],v[152:155],v[96:99]\nv_mfma_f32_16x16x32_bf16 v[100:103],v[180:183],v[152:155],v[100:103]\n" \
"v_mfma_f32_16x16x32_bf16 v[104:107],v[184:187],v[152:155],v[104:107]\nds_read_b64_tr_b16 v[174:175],v227 offset:2304\n" \
"v_mfma_f32_16x16x32_bf16 v[108:111],v[188:191],v[152:155],v[108:111]\nds_read_b128 v[152:155],v224 offset:12288\n" \
"v_mfma_f32_16x16x32_bf16 v[112:115],v[176:179],v[156:159],v[112:115]\nv_mfma_f32_16x16x32_bf16 v[116:119],v[180:183],v[156:159],v[116:119]\n" \
"v_mfma_f32_16x16x32_bf16 v[120:123],v[184:187],v[156:159],v[120:123]\nv_mfma_f32_16x16x32_bf16 v[124:127],v[188:191],v[156:159],v[124:127]\n" \
"ds_read_b128 v[156:159],v224 offset:13312\ns_waitcnt lgkmcnt(12)\nv_mfma_f32_16x16x32_bf16 v[0:3],v[160:163],v[128:131],v[0:3]\n" \
"ds_read_b64_tr_b16 v[176:177],v226 offset:16384\ns_waitcnt lgkmcnt(10)\nv_mfma_f32_16x16x32_bf16 v[4:7],v[164:167],v[128:131],v[4:7]\n" \
"s_waitcnt lgkmcnt(6)\nv_mfma_f32_16x16x32_bf16 v[8:11],v[168:171],v[128:131],v[8:11]\ns_waitcnt lgkmcnt(3)\n" \
"v_mfma_f32_16x16x32_bf16 v[12:15],v[172:175],v[128:131],v[12:15]\nds_read_b128 v[128:131],v225 offset:0\n" \
"ds_read_b64_tr_b16 v[178:179],v226 offset:18432\nv_mfma_f32_16x16x32_bf16 v[16:19],v[160:163],v[132:135],v[16:19]\n" \
"v_mfma_f32_16x16x32_bf16 v[20:23],v[164:167],v[132:135],v[20:23]\nv_mfma_f32_16x16x32_bf16 v[24:27],v[168:171],v[132:135],v[24:27]\n" \
"ds_read_b64_tr_b16 v[180:181],v227 offset:16384\nv_mfma_f32_16x16x32_bf16 v[28:31],v[172:175],v[132:135],v[28:31]\n" \
"ds_read_b128 v[132:135],v225 offset:1024\nv_mfma_f32_16x16x32_bf16 v[32:35],v[160:163],v[136:139],v[32:35]\n" \
"v_mfma_f32_16x16x32_bf16 v[36:39],v[164:167],v[136:139],v[36:39]\nds_read_b64_tr_b16 v[182:183],v227 offset:18432\n" \
"v_mfma_f32_16x16x32_bf16 v[40:43],v[168:171],v[136:139],v[40:43]\nv_mfma_f32_16x16x32_bf16 v[44:47],v[172:175],v[136:139],v[44:47]\n" \
"ds_read_b128 v[136:139],v225 offset:4096\nv_mfma_f32_16x16x32_bf16 v[48:51],v[160:163],v[140:143],v[48:51]\n" \
"ds_read_b64_tr_b16 v[184:185],v226 offset:16640\nv_mfma_f32_16x16x32_bf16 v[52:55],v[164:167],v[140:143],v[52:55]\n" \
"v_mfma_f32_16x16x32_bf16 v[56:59],v[168:171],v[140:143],v[56:59]\nv_mfma_f32_16x16x32_bf16 v[60:63],v[172:175],v[140:143],v[60:63]\n" \
"ds_read_b128 v[140:143],v225 offset:5120\nds_read_b64_tr_b16 v[186:187],v226 offset:18688\n" \
"v_mfma_f32_16x16x32_bf16 v[64:67],v[160:163],v[144:147],v[64:67]\nv_mfma_f32_16x16x32_bf16 v[68:71],v[164:167],v[144:147],v[68:71]\n" \
"v_mfma_f32_16x16x32_bf16 v[72:75],v[168:171],v[144:147],v[72:75]\nds_read_b64_tr_b16 v[188:189],v227 offset:16640\n" \
"v_mfma_f32_16x16x32_bf16 v[76:79],v[172:175],v[144:147],v[76:79]\nds_read_b128 v[144:147],v225 offset:8192\n" \
"v_mfma_f32_16x16x32_bf16 v[80:83],v[160:163],v[148:151],v[80:83]\nv_mfma_f32_16x16x32_bf16 v[84:87],v[164:167],v[148:151],v[84:87]\n" \
"ds_read_b64_tr_b16 v[190:191],v227 offset:18688\nv_mfma_f32_16x16x32_bf16 v[88:91],v[168:171],v[148:151],v[88:91]\n" \
"v_mfma_f32_16x16x32_bf16 v[92:95],v[172:175],v[148:151],v[92:95]\nds_read_b128 v[148:151],v225 offset:9216\ns_waitcnt lgkmcnt(15)\n" \
"v_mfma_f32_16x16x32_bf16 v[96:99],v[160:163],v[152:155],v[96:99]\nv_mfma_f32_16x16x32_bf16 v[100:103],v[164:167],v[152:155],v[100:103]\n" \
"v_mfma_f32_16x16x32_bf16 v[104:107],v[168:171],v[152:155],v[104:107]\nv_mfma_f32_16x16x32_bf16 v[108:111],v[172:175],v[152:155],v[108:111]\n" \
"ds_read_b128 v[152:155],v225 offset:12288\ns_waitcnt lgkmcnt(15)\nv_mfma_f32_16x16x32_bf16 v[112:115],v[160:163],v[156:159],v[112:115]\n" \
"v_mfma_f32_16x16x32_bf16 v[116:119],v[164:167],v[156:159],v[116:119]\nv_mfma_f32_16x16x32_bf16 v[120:123],v[168:171],v[156:159],v[120:123]\n" \
"v_mfma_f32_16x16x32_bf16 v[124:127],v[172:175],v[156:159],v[124:127]\nds_read_b128 v[156:159],v225 offset:13312\ns_waitcnt lgkmcnt(13)\n" \
"v_mfma_f32_16x16x32_bf16 v[0:3],v[176:179],v[128:131],v[0:3]\ns_waitcnt lgkmcnt(10)\nv_mfma_f32_16x16x32_bf16 v[4:7],v[180:183],v[128:131],v[4:7]\n" \
"s_waitcnt lgkmcnt(6)\nv_mfma_f32_16x16x32_bf16 v[8:11],v[184:187],v[128:131],v[8:11]\ns_waitcnt lgkmcnt(3)\n" \
"v_mfma_f32_16x16x32_bf16 v[12:15],v[188:191],v[128:131],v[12:15]\ns_waitcnt vmcnt(0) lgkmcnt(0)\ns_barrier\n" \
"v_mfma_f32_16x16x32_bf16 v[16:19],v[176:179],v[132:135],v[16:19]\nv_mfma_f32_16x16x32_bf16 v[20:23],v[180:183],v[132:135],v[20:23]\n" \
"v_mfma_f32_16x16x32_bf16 v[24:27],v[184:187],v[132:135],v[24:27]\nv_mfma_f32_16x16x32_bf16 v[28:31],v[188:191],v[132:135],v[28:31]\n" \
"v_mfma_f32_16x16x32_bf16 v[32:35],v[176:179],v[136:139],v[32:35]\nbuffer_load_dwordx4 v[128:131],v248,%[sws],s71 offen offset:0\n" \
"v_mfma_f32_16x16x32_bf16 v[36:39],v[180:183],v[136:139],v[36:39]\nv_mfma_f32_16x16x32_bf16 v[40:43],v[184:187],v[136:139],v[40:43]\n" \
"v_mfma_f32_16x16x32_bf16 v[44:47],v[188:191],v[136:139],v[44:47]\nbuffer_load_dwordx4 v[132:135],v248,%[sws],s71 offen offset:1024\n" \
"v_mfma_f32_16x16x32_bf16 v[48:51],v[176:179],v[140:143],v[48:51]\nv_mfma_f32_16x16x32_bf16 v[52:55],v[180:183],v[140:143],v[52:55]\n" \
"v_mfma_f32_16x16x32_bf16 v[56:59],v[184:187],v[140:143],v[56:59]\ns_add_u32 m0,s80,0\ns_add_u32 s49,s68,24576\n" \
"buffer_load_dwordx4 v248,%[sws],s49 offen offset:0 lds\nv_mfma_f32_16x16x32_bf16 v[60:63],v[188:191],v[140:143],v[60:63]\n" \
"v_mfma_f32_16x16x32_bf16 v[64:67],v[176:179],v[144:147],v[64:67]\nv_mfma_f32_16x16x32_bf16 v[68:71],v[180:183],v[144:147],v[68:71]\n" \
"buffer_load_dwordx4 v248,%[sws],s49 offen offset:1024 lds\nv_mfma_f32_16x16x32_bf16 v[72:75],v[184:187],v[144:147],v[72:75]\n" \
"v_mfma_f32_16x16x32_bf16 v[76:79],v[188:191],v[144:147],v[76:79]\nv_mfma_f32_16x16x32_bf16 v[80:83],v[176:179],v[148:151],v[80:83]\n" \
"buffer_load_dwordx4 v248,%[sws],s49 offen offset:2048 lds\nv_mfma_f32_16x16x32_bf16 v[84:87],v[180:183],v[148:151],v[84:87]\n" \
"v_mfma_f32_16x16x32_bf16 v[88:91],v[184:187],v[148:151],v[88:91]\nv_mfma_f32_16x16x32_bf16 v[92:95],v[188:191],v[148:151],v[92:95]\n" \
"buffer_load_dwordx4 v248,%[sws],s49 offen offset:3072 lds\nv_mfma_f32_16x16x32_bf16 v[96:99],v[176:179],v[152:155],v[96:99]\n" \
"v_mfma_f32_16x16x32_bf16 v[100:103],v[180:183],v[152:155],v[100:103]\nv_mfma_f32_16x16x32_bf16 v[104:107],v[184:187],v[152:155],v[104:107]\n" \
"s_add_u32 m0,s80,4096\ns_add_u32 s49,s68,28672\nbuffer_load_dwordx4 v248,%[sws],s49 offen offset:0 lds\n" \
"v_mfma_f32_16x16x32_bf16 v[108:111],v[188:191],v[152:155],v[108:111]\nv_mfma_f32_16x16x32_bf16 v[112:115],v[176:179],v[156:159],v[112:115]\n" \
"v_mfma_f32_16x16x32_bf16 v[116:119],v[180:183],v[156:159],v[116:119]\nbuffer_load_dwordx4 v248,%[sws],s49 offen offset:1024 lds\n" \
"v_mfma_f32_16x16x32_bf16 v[120:123],v[184:187],v[156:159],v[120:123]\nv_mfma_f32_16x16x32_bf16 v[124:127],v[188:191],v[156:159],v[124:127]\ns_nop 7\n" \
"v_cvt_pk_bf16_f32 v136,v0,v1\nv_cvt_pk_bf16_f32 v137,v2,v3\nv_cvt_pk_bf16_f32 v138,v4,v5\nv_cvt_pk_bf16_f32 v139,v6,v7\nv_cvt_pk_bf16_f32 v140,v16,v17\n" \
"v_cvt_pk_bf16_f32 v141,v18,v19\nv_cvt_pk_bf16_f32 v142,v20,v21\nv_cvt_pk_bf16_f32 v143,v22,v23\nv_cvt_pk_bf16_f32 v144,v8,v9\n" \
"v_cvt_pk_bf16_f32 v145,v10,v11\nv_cvt_pk_bf16_f32 v146,v12,v13\nv_cvt_pk_bf16_f32 v147,v14,v15\nv_cvt_pk_bf16_f32 v148,v24,v25\n" \
"v_cvt_pk_bf16_f32 v149,v26,v27\nv_cvt_pk_bf16_f32 v150,v28,v29\nv_cvt_pk_bf16_f32 v151,v30,v31\nv_cvt_pk_bf16_f32 v152,v32,v33\n" \
"v_cvt_pk_bf16_f32 v153,v34,v35\nv_cvt_pk_bf16_f32 v154,v36,v37\nv_cvt_pk_bf16_f32 v155,v38,v39\nv_cvt_pk_bf16_f32 v156,v48,v49\n" \
"v_cvt_pk_bf16_f32 v157,v50,v51\nv_cvt_pk_bf16_f32 v158,v52,v53\nv_cvt_pk_bf16_f32 v159,v54,v55\nv_cvt_pk_bf16_f32 v160,v40,v41\n" \
"v_cvt_pk_bf16_f32 v161,v42,v43\nv_cvt_pk_bf16_f32 v162,v44,v45\nv_cvt_pk_bf16_f32 v163,v46,v47\nv_cvt_pk_bf16_f32 v164,v56,v57\n" \
"v_cvt_pk_bf16_f32 v165,v58,v59\nv_cvt_pk_bf16_f32 v166,v60,v61\nv_cvt_pk_bf16_f32 v167,v62,v63\nv_cvt_pk_bf16_f32 v168,v64,v65\n" \
"v_cvt_pk_bf16_f32 v169,v66,v67\nv_cvt_pk_bf16_f32 v170,v68,v69\nv_cvt_pk_bf16_f32 v171,v70,v71\nv_cvt_pk_bf16_f32 v172,v80,v81\n" \
"v_cvt_pk_bf16_f32 v173,v82,v83\nv_cvt_pk_bf16_f32 v174,v84,v85\nv_cvt_pk_bf16_f32 v175,v86,v87\nv_cvt_pk_bf16_f32 v176,v72,v73\n" \
"v_cvt_pk_bf16_f32 v177,v74,v75\nv_cvt_pk_bf16_f32 v178,v76,v77\nv_cvt_pk_bf16_f32 v179,v78,v79\nv_cvt_pk_bf16_f32 v180,v88,v89\n" \
"v_cvt_pk_bf16_f32 v181,v90,v91\nv_cvt_pk_bf16_f32 v182,v92,v93\nv_cvt_pk_bf16_f32 v183,v94,v95\nv_cvt_pk_bf16_f32 v184,v96,v97\n" \
"v_cvt_pk_bf16_f32 v185,v98,v99\nv_cvt_pk_bf16_f32 v186,v100,v101\nv_cvt_pk_bf16_f32 v187,v102,v103\nv_cvt_pk_bf16_f32 v188,v112,v113\n" \
"v_cvt_pk_bf16_f32 v189,v114,v115\nv_cvt_pk_bf16_f32 v190,v116,v117\nv_cvt_pk_bf16_f32 v191,v118,v119\nv_cvt_pk_bf16_f32 v224,v104,v105\n" \
"v_cvt_pk_bf16_f32 v225,v106,v107\nv_cvt_pk_bf16_f32 v226,v108,v109\nv_cvt_pk_bf16_f32 v227,v110,v111\nv_cvt_pk_bf16_f32 v228,v120,v121\n" \
"v_cvt_pk_bf16_f32 v229,v122,v123\nv_cvt_pk_bf16_f32 v230,v124,v125\nv_cvt_pk_bf16_f32 v231,v126,v127\ns_nop 1\nv_mov_b32 v0,0\nv_mov_b32 v1,0\n" \
"v_mov_b32 v2,0\nv_mov_b32 v3,0\nv_mov_b32 v4,0\nv_mov_b32 v5,0\nv_mov_b32 v6,0\nv_mov_b32 v7,0\nv_mov_b32 v8,0\nv_mov_b32 v9,0\nv_mov_b32 v10,0\n" \
"v_mov_b32 v11,0\nv_mov_b32 v12,0\nv_mov_b32 v13,0\nv_mov_b32 v14,0\nv_mov_b32 v15,0\nv_mov_b32 v16,0\nv_mov_b32 v17,0\nv_mov_b32 v18,0\n" \
"v_mov_b32 v19,0\nv_mov_b32 v20,0\nv_mov_b32 v21,0\nv_mov_b32 v22,0\nv_mov_b32 v23,0\nv_mov_b32 v24,0\nv_mov_b32 v25,0\nv_mov_b32 v26,0\n" \
"v_mov_b32 v27,0\nv_mov_b32 v28,0\nv_mov_b32 v29,0\nv_mov_b32 v30,0\nv_mov_b32 v31,0\nv_add_u32 v252,65536,v253\nv_add_u32 v255,65536,v253\n" \
"ds_read_b128 v[116:119],v255\ns_waitcnt vmcnt(6)\ns_waitcnt lgkmcnt(0)\nv_mfma_f32_32x32x16_bf16 v[32:47],v[116:119],v[128:131],0\n" \
"v_mfma_f32_32x32x16_bf16 v[48:63],v[116:119],v[132:135],0\ns_nop 7\ns_nop 7\nv_max3_f32 v120,v32,v33,v34\nv_max3_f32 v121,v35,v36,v37\n" \
"v_max3_f32 v120,v120,v38,v39\nv_max3_f32 v121,v121,v40,v41\nv_max3_f32 v120,v120,v42,v43\nv_max3_f32 v121,v121,v44,v45\nv_max3_f32 v120,v120,v46,v47\n" \
"v_max_f32 v120,v120,v121\nv_mov_b32 v121,v120\ns_nop 1\nv_permlane32_swap_b32 v120,v121\ns_nop 1\nv_max_f32 v120,v120,v121\nv_sub_f32 v64,0,v120\n" \
"v_sub_f32 v65,0,v120\nv_sub_f32 v66,0,v120\nv_sub_f32 v67,0,v120\nv_sub_f32 v68,0,v120\nv_sub_f32 v69,0,v120\nv_sub_f32 v70,0,v120\n" \
"v_sub_f32 v71,0,v120\nv_sub_f32 v72,0,v120\nv_sub_f32 v73,0,v120\nv_sub_f32 v74,0,v120\nv_sub_f32 v75,0,v120\nv_sub_f32 v76,0,v120\n" \
"v_sub_f32 v77,0,v120\nv_sub_f32 v78,0,v120\nv_sub_f32 v79,0,v120\nv_max3_f32 v122,v48,v49,v50\nv_max3_f32 v123,v51,v52,v53\n" \
"v_max3_f32 v122,v122,v54,v55\nv_max3_f32 v123,v123,v56,v57\nv_max3_f32 v122,v122,v58,v59\nv_max3_f32 v123,v123,v60,v61\nv_max3_f32 v122,v122,v62,v63\n" \
"v_max_f32 v122,v122,v123\nv_mov_b32 v123,v122\ns_nop 1\nv_permlane32_swap_b32 v122,v123\ns_nop 1\nv_max_f32 v122,v122,v123\nv_sub_f32 v80,0,v122\n" \
"v_sub_f32 v81,0,v122\nv_sub_f32 v82,0,v122\nv_sub_f32 v83,0,v122\nv_sub_f32 v84,0,v122\nv_sub_f32 v85,0,v122\nv_sub_f32 v86,0,v122\n" \
"v_sub_f32 v87,0,v122\nv_sub_f32 v88,0,v122\nv_sub_f32 v89,0,v122\nv_sub_f32 v90,0,v122\nv_sub_f32 v91,0,v122\nv_sub_f32 v92,0,v122\n" \
"v_sub_f32 v93,0,v122\nv_sub_f32 v94,0,v122\nv_sub_f32 v95,0,v122\nv_mov_b32 v100,0\nv_mov_b32 v101,0\nv_mov_b32 v102,0\nv_mov_b32 v103,0\n" \
"v_mov_b32 v60,0\nv_mov_b32 v61,0\nv_mov_b32 v62,0\nv_mov_b32 v63,0\nv_mov_b32 v124,0\nv_mov_b32 v125,0\nv_mov_b32 v126,0\nv_mov_b32 v127,0\n" \
"ds_read_b128 v[104:107],v252 offset:0\nds_read_b128 v[108:111],v252 offset:1024\nds_read_b128 v[112:115],v252 offset:2048\ns_waitcnt lgkmcnt(0)\n" \
"v_mfma_f32_32x32x16_bf16 v[32:47],v[104:107],v[128:131],v[64:79]\ns_nop 7\ns_nop 7\nv_exp_f32 v32,v32\nv_cvt_pk_bf16_f32 v102,v60,v61\n" \
"v_exp_f32 v33,v33\nv_exp_f32 v34,v34\nv_cvt_pk_bf16_f32 v103,v62,v63\nv_mfma_f32_32x32x16_bf16 v[48:63],v[104:107],v[132:135],v[80:95]\n" \
"v_exp_f32 v35,v35\nv_cvt_pk_bf16_f32 v96,v32,v33\nv_exp_f32 v36,v36\nds_read_b128 v[116:119],v252 offset:3072\nv_exp_f32 v37,v37\n" \
"v_cvt_pk_bf16_f32 v97,v34,v35\nv_exp_f32 v38,v38\nv_mfma_f32_32x32x16_bf16 v[16:31],v[124:127],v[100:103],v[16:31]\nv_exp_f32 v39,v39\n" \
"v_cvt_pk_bf16_f32 v98,v36,v37\nv_exp_f32 v40,v40\nds_read_b128 v[120:123],v252 offset:4096\nv_exp_f32 v41,v41\nv_cvt_pk_bf16_f32 v99,v38,v39\n" \
"v_exp_f32 v42,v42\nds_read_b128 v[124:127],v252 offset:5120\nv_exp_f32 v43,v43\nv_cvt_pk_bf16_f32 v100,v40,v41\nv_exp_f32 v44,v44\n" \
"v_mfma_f32_32x32x16_bf16 v[0:15],v[108:111],v[96:99],v[0:15]\nv_exp_f32 v45,v45\nv_cvt_pk_bf16_f32 v101,v42,v43\nv_exp_f32 v46,v46\nv_exp_f32 v47,v47\n" \
"v_exp_f32 v48,v48\nv_cvt_pk_bf16_f32 v102,v44,v45\nv_exp_f32 v49,v49\nv_exp_f32 v50,v50\nv_cvt_pk_bf16_f32 v103,v46,v47\ns_waitcnt lgkmcnt(2)\n" \
"v_mfma_f32_32x32x16_bf16 v[32:47],v[116:119],v[128:131],v[64:79]\nv_exp_f32 v51,v51\nv_cvt_pk_bf16_f32 v96,v48,v49\nv_exp_f32 v52,v52\n" \
"v_exp_f32 v53,v53\nv_cvt_pk_bf16_f32 v97,v50,v51\nv_exp_f32 v54,v54\nv_mfma_f32_32x32x16_bf16 v[0:15],v[112:115],v[100:103],v[0:15]\n" \
"s_waitcnt vmcnt(0)\ns_add_u32 s51,s75,0\nbuffer_store_dwordx4 v[136:139],v251,%[sws],s51 offen offset:0 sc1\nv_exp_f32 v55,v55\n" \
"v_cvt_pk_bf16_f32 v98,v52,v53\nv_exp_f32 v56,v56\nv_exp_f32 v57,v57\nv_cvt_pk_bf16_f32 v99,v54,v55\nv_exp_f32 v58,v58\nv_exp_f32 v59,v59\n" \
"v_cvt_pk_bf16_f32 v100,v56,v57\nv_exp_f32 v60,v60\nv_mfma_f32_32x32x16_bf16 v[16:31],v[108:111],v[96:99],v[16:31]\nv_exp_f32 v61,v61\n" \
"v_cvt_pk_bf16_f32 v101,v58,v59\nv_exp_f32 v62,v62\nv_exp_f32 v63,v63\ns_waitcnt lgkmcnt(0)\nv_exp_f32 v32,v32\nv_cvt_pk_bf16_f32 v102,v60,v61\n" \
"v_exp_f32 v33,v33\nv_exp_f32 v34,v34\nv_cvt_pk_bf16_f32 v103,v62,v63\nv_mfma_f32_32x32x16_bf16 v[48:63],v[116:119],v[132:135],v[80:95]\n" \
"buffer_store_dwordx4 v[140:143],v251,%[sws],s51 offen offset:128 sc1\nv_exp_f32 v35,v35\nv_cvt_pk_bf16_f32 v96,v32,v33\nv_exp_f32 v36,v36\n" \
"ds_read_b128 v[104:107],v252 offset:6144\nv_exp_f32 v37,v37\nv_cvt_pk_bf16_f32 v97,v34,v35\nv_exp_f32 v38,v38\n" \
"v_mfma_f32_32x32x16_bf16 v[16:31],v[112:115],v[100:103],v[16:31]\nv_exp_f32 v39,v39\nv_cvt_pk_bf16_f32 v98,v36,v37\nv_exp_f32 v40,v40\n" \
"ds_read_b128 v[108:111],v252 offset:7168\nv_exp_f32 v41,v41\nv_cvt_pk_bf16_f32 v99,v38,v39\nv_exp_f32 v42,v42\n" \
"ds_read_b128 v[112:115],v252 offset:8192\nv_exp_f32 v43,v43\nv_cvt_pk_bf16_f32 v100,v40,v41\nv_exp_f32 v44,v44\n" \
"v_mfma_f32_32x32x16_bf16 v[0:15],v[120:123],v[96:99],v[0:15]\nv_exp_f32 v45,v45\nv_cvt_pk_bf16_f32 v101,v42,v43\nv_exp_f32 v46,v46\nv_exp_f32 v47,v47\n" \
"v_exp_f32 v48,v48\nv_cvt_pk_bf16_f32 v102,v44,v45\nv_exp_f32 v49,v49\nv_exp_f32 v50,v50\nv_cvt_pk_bf16_f32 v103,v46,v47\ns_waitcnt lgkmcnt(2)\n" \
"v_mfma_f32_32x32x16_bf16 v[32:47],v[104:107],v[128:131],v[64:79]\nbuffer_store_dwordx4 v[144:147],v251,%[sws],s51 offen offset:2048 sc1\n" \
"v_exp_f32 v51,v51\nv_cvt_pk_bf16_f32 v96,v48,v49\nv_exp_f32 v52,v52\nv_exp_f32 v53,v53\nv_cvt_pk_bf16_f32 v97,v50,v51\nv_exp_f32 v54,v54\n" \
"v_mfma_f32_32x32x16_bf16 v[0:15],v[124:127],v[100:103],v[0:15]\nv_exp_f32 v55,v55\nv_cvt_pk_bf16_f32 v98,v52,v53\nv_exp_f32 v56,v56\nv_exp_f32 v57,v57\n" \
"v_cvt_pk_bf16_f32 v99,v54,v55\nv_exp_f32 v58,v58\nv_exp_f32 v59,v59\nv_cvt_pk_bf16_f32 v100,v56,v57\nv_exp_f32 v60,v60\n" \
"v_mfma_f32_32x32x16_bf16 v[16:31],v[120:123],v[96:99],v[16:31]\nbuffer_store_dwordx4 v[148:151],v251,%[sws],s51 offen offset:2176 sc1\n" \
"v_exp_f32 v61,v61\nv_cvt_pk_bf16_f32 v101,v58,v59\nv_exp_f32 v62,v62\nv_exp_f32 v63,v63\ns_waitcnt lgkmcnt(0)\nv_exp_f32 v32,v32\n" \
"v_cvt_pk_bf16_f32 v102,v60,v61\nv_exp_f32 v33,v33\nv_exp_f32 v34,v34\nv_cvt_pk_bf16_f32 v103,v62,v63\n" \
"v_mfma_f32_32x32x16_bf16 v[48:63],v[104:107],v[132:135],v[80:95]\nv_exp_f32 v35,v35\nv_cvt_pk_bf16_f32 v96,v32,v33\nv_exp_f32 v36,v36\n" \
"ds_read_b128 v[116:119],v252 offset:9216\nv_exp_f32 v37,v37\nv_cvt_pk_bf16_f32 v97,v34,v35\nv_exp_f32 v38,v38\n" \
"v_mfma_f32_32x32x16_bf16 v[16:31],v[124:127],v[100:103],v[16:31]\nv_exp_f32 v39,v39\nv_cvt_pk_bf16_f32 v98,v36,v37\nv_exp_f32 v40,v40\n" \
"ds_read_b128 v[120:123],v252 offset:10240\nv_exp_f32 v41,v41\nv_cvt_pk_bf16_f32 v99,v38,v39\nv_exp_f32 v42,v42\n" \
"ds_read_b128 v[124:127],v252 offset:11264\nv_exp_f32 v43,v43\nv_cvt_pk_bf16_f32 v100,v40,v41\nv_exp_f32 v44,v44\n" \
"v_mfma_f32_32x32x16_bf16 v[0:15],v[108:111],v[96:99],v[0:15]\ns_add_u32 s51,s75,16384\n" \
"buffer_store_dwordx4 v[152:155],v251,%[sws],s51 offen offset:0 sc1\nv_exp_f32 v45,v45\nv_cvt_pk_bf16_f32 v101,v42,v43\nv_exp_f32 v46,v46\n" \
"v_exp_f32 v47,v47\nv_exp_f32 v48,v48\nv_cvt_pk_bf16_f32 v102,v44,v45\nv_exp_f32 v49,v49\nv_exp_f32 v50,v50\nv_cvt_pk_bf16_f32 v103,v46,v47\n" \
"s_waitcnt lgkmcnt(2)\nv_mfma_f32_32x32x16_bf16 v[32:47],v[116:119],v[128:131],v[64:79]\nv_exp_f32 v51,v51\nv_cvt_pk_bf16_f32 v96,v48,v49\n" \
"v_exp_f32 v52,v52\nv_exp_f32 v53,v53\nv_cvt_pk_bf16_f32 v97,v50,v51\nv_exp_f32 v54,v54\nv_mfma_f32_32x32x16_bf16 v[0:15],v[112:115],v[100:103],v[0:15]\n" \
"v_exp_f32 v55,v55\nv_cvt_pk_bf16_f32 v98,v52,v53\nv_exp_f32 v56,v56\nv_exp_f32 v57,v57\nv_cvt_pk_bf16_f32 v99,v54,v55\nv_exp_f32 v58,v58\n" \
"buffer_store_dwordx4 v[156:159],v251,%[sws],s51 offen offset:128 sc1\nv_exp_f32 v59,v59\nv_cvt_pk_bf16_f32 v100,v56,v57\nv_exp_f32 v60,v60\n" \
"v_mfma_f32_32x32x16_bf16 v[16:31],v[108:111],v[96:99],v[16:31]\nv_exp_f32 v61,v61\nv_cvt_pk_bf16_f32 v101,v58,v59\nv_exp_f32 v62,v62\n" \
"v_exp_f32 v63,v63\ns_waitcnt lgkmcnt(0)\nv_exp_f32 v32,v32\nv_cvt_pk_bf16_f32 v102,v60,v61\nv_exp_f32 v33,v33\nv_exp_f32 v34,v34\n" \
"v_cvt_pk_bf16_f32 v103,v62,v63\nv_mfma_f32_32x32x16_bf16 v[48:63],v[116:119],v[132:135],v[80:95]\nv_exp_f32 v35,v35\nv_cvt_pk_bf16_f32 v96,v32,v33\n" \
"v_exp_f32 v36,v36\nds_read_b128 v[104:107],v252 offset:12288\nv_exp_f32 v37,v37\nv_cvt_pk_bf16_f32 v97,v34,v35\nv_exp_f32 v38,v38\n" \
"v_mfma_f32_32x32x16_bf16 v[16:31],v[112:115],v[100:103],v[16:31]\nv_exp_f32 v39,v39\nv_cvt_pk_bf16_f32 v98,v36,v37\nv_exp_f32 v40,v40\n" \
"ds_read_b128 v[108:111],v252 offset:13312\nv_exp_f32 v41,v41\nv_cvt_pk_bf16_f32 v99,v38,v39\nv_exp_f32 v42,v42\n" \
"ds_read_b128 v[112:115],v252 offset:14336\nbuffer_store_dwordx4 v[160:163],v251,%[sws],s51 offen offset:2048 sc1\nv_exp_f32 v43,v43\n" \
"v_cvt_pk_bf16_f32 v100,v40,v41\nv_exp_f32 v44,v44\nv_mfma_f32_32x32x16_bf16 v[0:15],v[120:123],v[96:99],v[0:15]\nv_exp_f32 v45,v45\n" \
"v_cvt_pk_bf16_f32 v101,v42,v43\nv_exp_f32 v46,v46\nv_exp_f32 v47,v47\nv_exp_f32 v48,v48\nv_cvt_pk_bf16_f32 v102,v44,v45\nv_exp_f32 v49,v49\n" \
"v_exp_f32 v50,v50\nv_cvt_pk_bf16_f32 v103,v46,v47\ns_waitcnt lgkmcnt(2)\nv_mfma_f32_32x32x16_bf16 v[32:47],v[104:107],v[128:131],v[64:79]\n" \
"v_exp_f32 v51,v51\nv_cvt_pk_bf16_f32 v96,v48,v49\nv_exp_f32 v52,v52\nv_exp_f32 v53,v53\nv_cvt_pk_bf16_f32 v97,v50,v51\nv_exp_f32 v54,v54\n" \
"v_mfma_f32_32x32x16_bf16 v[0:15],v[124:127],v[100:103],v[0:15]\nbuffer_store_dwordx4 v[164:167],v251,%[sws],s51 offen offset:2176 sc1\n" \
"v_exp_f32 v55,v55\nv_cvt_pk_bf16_f32 v98,v52,v53\nv_exp_f32 v56,v56\nv_exp_f32 v57,v57\nv_cvt_pk_bf16_f32 v99,v54,v55\nv_exp_f32 v58,v58\n" \
"v_exp_f32 v59,v59\nv_cvt_pk_bf16_f32 v100,v56,v57\nv_exp_f32 v60,v60\nv_mfma_f32_32x32x16_bf16 v[16:31],v[120:123],v[96:99],v[16:31]\n" \
"v_exp_f32 v61,v61\nv_cvt_pk_bf16_f32 v101,v58,v59\nv_exp_f32 v62,v62\nv_exp_f32 v63,v63\ns_waitcnt lgkmcnt(0)\nv_exp_f32 v32,v32\n" \
"v_cvt_pk_bf16_f32 v102,v60,v61\nv_exp_f32 v33,v33\nv_exp_f32 v34,v34\nv_cvt_pk_bf16_f32 v103,v62,v63\n" \
"v_mfma_f32_32x32x16_bf16 v[48:63],v[104:107],v[132:135],v[80:95]\nv_exp_f32 v35,v35\nv_cvt_pk_bf16_f32 v96,v32,v33\nv_exp_f32 v36,v36\n" \
"ds_read_b128 v[116:119],v252 offset:15360\nv_exp_f32 v37,v37\nv_cvt_pk_bf16_f32 v97,v34,v35\nv_exp_f32 v38,v38\n" \
"v_mfma_f32_32x32x16_bf16 v[16:31],v[124:127],v[100:103],v[16:31]\ns_add_u32 s51,s75,32768\n" \
"buffer_store_dwordx4 v[168:171],v251,%[sws],s51 offen offset:0 sc1\nv_exp_f32 v39,v39\nv_cvt_pk_bf16_f32 v98,v36,v37\nv_exp_f32 v40,v40\n" \
"ds_read_b128 v[120:123],v252 offset:16384\nv_exp_f32 v41,v41\nv_cvt_pk_bf16_f32 v99,v38,v39\nv_exp_f32 v42,v42\n" \
"ds_read_b128 v[124:127],v252 offset:17408\nv_exp_f32 v43,v43\nv_cvt_pk_bf16_f32 v100,v40,v41\nv_exp_f32 v44,v44\n" \
"v_mfma_f32_32x32x16_bf16 v[0:15],v[108:111],v[96:99],v[0:15]\nv_exp_f32 v45,v45\nv_cvt_pk_bf16_f32 v101,v42,v43\nv_exp_f32 v46,v46\nv_exp_f32 v47,v47\n" \
"v_exp_f32 v48,v48\nv_cvt_pk_bf16_f32 v102,v44,v45\nv_exp_f32 v49,v49\nv_exp_f32 v50,v50\nv_cvt_pk_bf16_f32 v103,v46,v47\ns_waitcnt lgkmcnt(2)\n" \
"v_mfma_f32_32x32x16_bf16 v[32:47],v[116:119],v[128:131],v[64:79]\nbuffer_store_dwordx4 v[172:175],v251,%[sws],s51 offen offset:128 sc1\n" \
"v_exp_f32 v51,v51\nv_cvt_pk_bf16_f32 v96,v48,v49\nv_exp_f32 v52,v52\nv_exp_f32 v53,v53\nv_cvt_pk_bf16_f32 v97,v50,v51\nv_exp_f32 v54,v54\n" \
"v_mfma_f32_32x32x16_bf16 v[0:15],v[112:115],v[100:103],v[0:15]\nv_exp_f32 v55,v55\nv_cvt_pk_bf16_f32 v98,v52,v53\nv_exp_f32 v56,v56\nv_exp_f32 v57,v57\n" \
"v_cvt_pk_bf16_f32 v99,v54,v55\nv_exp_f32 v58,v58\nv_exp_f32 v59,v59\nv_cvt_pk_bf16_f32 v100,v56,v57\nv_exp_f32 v60,v60\n" \
"v_mfma_f32_32x32x16_bf16 v[16:31],v[108:111],v[96:99],v[16:31]\nv_exp_f32 v61,v61\nv_cvt_pk_bf16_f32 v101,v58,v59\nv_exp_f32 v62,v62\n" \
"v_exp_f32 v63,v63\ns_waitcnt lgkmcnt(0)\nv_exp_f32 v32,v32\nv_cvt_pk_bf16_f32 v102,v60,v61\nv_exp_f32 v33,v33\nv_exp_f32 v34,v34\n" \
"v_cvt_pk_bf16_f32 v103,v62,v63\nv_mfma_f32_32x32x16_bf16 v[48:63],v[116:119],v[132:135],v[80:95]\n" \
"buffer_store_dwordx4 v[176:179],v251,%[sws],s51 offen offset:2048 sc1\nv_exp_f32 v35,v35\nv_cvt_pk_bf16_f32 v96,v32,v33\nv_exp_f32 v36,v36\n" \
"ds_read_b128 v[104:107],v252 offset:18432\nv_exp_f32 v37,v37\nv_cvt_pk_bf16_f32 v97,v34,v35\nv_exp_f32 v38,v38\n" \
"v_mfma_f32_32x32x16_bf16 v[16:31],v[112:115],v[100:103],v[16:31]\nv_exp_f32 v39,v39\nv_cvt_pk_bf16_f32 v98,v36,v37\nv_exp_f32 v40,v40\n" \
"ds_read_b128 v[108:111],v252 offset:19456\nv_exp_f32 v41,v41\nv_cvt_pk_bf16_f32 v99,v38,v39\nv_exp_f32 v42,v42\n" \
"ds_read_b128 v[112:115],v252 offset:20480\nv_exp_f32 v43,v43\nv_cvt_pk_bf16_f32 v100,v40,v41\nv_exp_f32 v44,v44\n" \
"v_mfma_f32_32x32x16_bf16 v[0:15],v[120:123],v[96:99],v[0:15]\nbuffer_store_dwordx4 v[180:183],v251,%[sws],s51 offen offset:2176 sc1\nv_exp_f32 v45,v45\n" \
"v_cvt_pk_bf16_f32 v101,v42,v43\nv_exp_f32 v46,v46\nv_exp_f32 v47,v47\nv_exp_f32 v48,v48\nv_cvt_pk_bf16_f32 v102,v44,v45\nv_exp_f32 v49,v49\n" \
"v_exp_f32 v50,v50\nv_cvt_pk_bf16_f32 v103,v46,v47\ns_waitcnt lgkmcnt(2)\nv_mfma_f32_32x32x16_bf16 v[32:47],v[104:107],v[128:131],v[64:79]\n" \
"v_exp_f32 v51,v51\nv_cvt_pk_bf16_f32 v96,v48,v49\nv_exp_f32 v52,v52\nv_exp_f32 v53,v53\nv_cvt_pk_bf16_f32 v97,v50,v51\nv_exp_f32 v54,v54\n" \
"v_mfma_f32_32x32x16_bf16 v[0:15],v[124:127],v[100:103],v[0:15]\nv_exp_f32 v55,v55\nv_cvt_pk_bf16_f32 v98,v52,v53\nv_exp_f32 v56,v56\nv_exp_f32 v57,v57\n" \
"v_cvt_pk_bf16_f32 v99,v54,v55\nv_exp_f32 v58,v58\nv_exp_f32 v59,v59\nv_cvt_pk_bf16_f32 v100,v56,v57\nv_exp_f32 v60,v60\n" \
"v_mfma_f32_32x32x16_bf16 v[16:31],v[120:123],v[96:99],v[16:31]\ns_add_u32 s51,s75,49152\n" \
"buffer_store_dwordx4 v[184:187],v251,%[sws],s51 offen offset:0 sc1\nv_exp_f32 v61,v61\nv_cvt_pk_bf16_f32 v101,v58,v59\nv_exp_f32 v62,v62\n" \
"v_exp_f32 v63,v63\ns_waitcnt lgkmcnt(0)\nv_exp_f32 v32,v32\nv_cvt_pk_bf16_f32 v102,v60,v61\nv_exp_f32 v33,v33\nv_exp_f32 v34,v34\n" \
"v_cvt_pk_bf16_f32 v103,v62,v63\nv_mfma_f32_32x32x16_bf16 v[48:63],v[104:107],v[132:135],v[80:95]\nv_exp_f32 v35,v35\nv_cvt_pk_bf16_f32 v96,v32,v33\n" \
"v_exp_f32 v36,v36\nds_read_b128 v[116:119],v252 offset:21504\nv_exp_f32 v37,v37\nv_cvt_pk_bf16_f32 v97,v34,v35\nv_exp_f32 v38,v38\n" \
"v_mfma_f32_32x32x16_bf16 v[16:31],v[124:127],v[100:103],v[16:31]\nv_exp_f32 v39,v39\nv_cvt_pk_bf16_f32 v98,v36,v37\nv_exp_f32 v40,v40\n" \
"ds_read_b128 v[120:123],v252 offset:22528\nv_exp_f32 v41,v41\nv_cvt_pk_bf16_f32 v99,v38,v39\nv_exp_f32 v42,v42\n" \
"ds_read_b128 v[124:127],v252 offset:23552\nbuffer_store_dwordx4 v[188:191],v251,%[sws],s51 offen offset:128 sc1\nv_exp_f32 v43,v43\n" \
"v_cvt_pk_bf16_f32 v100,v40,v41\nv_exp_f32 v44,v44\nv_mfma_f32_32x32x16_bf16 v[0:15],v[108:111],v[96:99],v[0:15]\nv_exp_f32 v45,v45\n" \
"v_cvt_pk_bf16_f32 v101,v42,v43\nv_exp_f32 v46,v46\nv_exp_f32 v47,v47\nv_exp_f32 v48,v48\nv_cvt_pk_bf16_f32 v102,v44,v45\nv_exp_f32 v49,v49\n" \
"v_exp_f32 v50,v50\nv_cvt_pk_bf16_f32 v103,v46,v47\ns_waitcnt lgkmcnt(2)\nv_mfma_f32_32x32x16_bf16 v[32:47],v[116:119],v[128:131],v[64:79]\n" \
"v_exp_f32 v51,v51\nv_cvt_pk_bf16_f32 v96,v48,v49\nv_exp_f32 v52,v52\nv_exp_f32 v53,v53\nv_cvt_pk_bf16_f32 v97,v50,v51\nv_exp_f32 v54,v54\n" \
"v_mfma_f32_32x32x16_bf16 v[0:15],v[112:115],v[100:103],v[0:15]\nv_exp_f32 v55,v55\nv_cvt_pk_bf16_f32 v98,v52,v53\nv_exp_f32 v56,v56\nv_exp_f32 v57,v57\n" \
"v_cvt_pk_bf16_f32 v99,v54,v55\nv_exp_f32 v58,v58\nbuffer_store_dwordx4 v[224:227],v251,%[sws],s51 offen offset:2048 sc1\nv_exp_f32 v59,v59\n" \
"v_cvt_pk_bf16_f32 v100,v56,v57\nv_exp_f32 v60,v60\nv_mfma_f32_32x32x16_bf16 v[16:31],v[108:111],v[96:99],v[16:31]\ns_barrier\nv_add_u32 v252,0,v253\n" \
"v_exp_f32 v61,v61\nv_cvt_pk_bf16_f32 v101,v58,v59\nv_exp_f32 v62,v62\nv_exp_f32 v63,v63\ns_waitcnt lgkmcnt(0)\nv_exp_f32 v32,v32\n" \
"v_cvt_pk_bf16_f32 v102,v60,v61\nv_exp_f32 v33,v33\nv_exp_f32 v34,v34\nv_cvt_pk_bf16_f32 v103,v62,v63\n" \
"v_mfma_f32_32x32x16_bf16 v[48:63],v[116:119],v[132:135],v[80:95]\nv_exp_f32 v35,v35\nv_cvt_pk_bf16_f32 v96,v32,v33\nv_exp_f32 v36,v36\n" \
"ds_read_b128 v[104:107],v252 offset:0\nv_exp_f32 v37,v37\nv_cvt_pk_bf16_f32 v97,v34,v35\nv_exp_f32 v38,v38\n" \
"v_mfma_f32_32x32x16_bf16 v[16:31],v[112:115],v[100:103],v[16:31]\nbuffer_store_dwordx4 v[228:231],v251,%[sws],s51 offen offset:2176 sc1\n" \
"v_exp_f32 v39,v39\nv_cvt_pk_bf16_f32 v98,v36,v37\nv_exp_f32 v40,v40\nds_read_b128 v[108:111],v252 offset:1024\nv_exp_f32 v41,v41\n" \
"v_cvt_pk_bf16_f32 v99,v38,v39\nv_exp_f32 v42,v42\nds_read_b128 v[112:115],v252 offset:2048\nv_exp_f32 v43,v43\nv_cvt_pk_bf16_f32 v100,v40,v41\n" \
"v_exp_f32 v44,v44\nv_mfma_f32_32x32x16_bf16 v[0:15],v[120:123],v[96:99],v[0:15]\nv_exp_f32 v45,v45\nv_cvt_pk_bf16_f32 v101,v42,v43\nv_exp_f32 v46,v46\n" \
"v_exp_f32 v47,v47\nv_exp_f32 v48,v48\nv_cvt_pk_bf16_f32 v102,v44,v45\nv_exp_f32 v49,v49\nv_exp_f32 v50,v50\nv_cvt_pk_bf16_f32 v103,v46,v47\n" \
"s_waitcnt lgkmcnt(2)\nv_mfma_f32_32x32x16_bf16 v[32:47],v[104:107],v[128:131],v[64:79]\nv_exp_f32 v51,v51\nv_cvt_pk_bf16_f32 v96,v48,v49\n" \
"v_exp_f32 v52,v52\nv_exp_f32 v53,v53\nv_cvt_pk_bf16_f32 v97,v50,v51\nv_exp_f32 v54,v54\nv_mfma_f32_32x32x16_bf16 v[0:15],v[124:127],v[100:103],v[0:15]\n" \
"v_exp_f32 v55,v55\nv_cvt_pk_bf16_f32 v98,v52,v53\nv_exp_f32 v56,v56\nv_exp_f32 v57,v57\nv_cvt_pk_bf16_f32 v99,v54,v55\nv_exp_f32 v58,v58\n" \
"v_exp_f32 v59,v59\nv_cvt_pk_bf16_f32 v100,v56,v57\nv_exp_f32 v60,v60\nv_mfma_f32_32x32x16_bf16 v[16:31],v[120:123],v[96:99],v[16:31]\n" \
"v_exp_f32 v61,v61\nv_cvt_pk_bf16_f32 v101,v58,v59\nv_exp_f32 v62,v62\nv_exp_f32 v63,v63\ns_waitcnt lgkmcnt(0)\nv_exp_f32 v32,v32\n" \
"v_cvt_pk_bf16_f32 v102,v60,v61\nv_exp_f32 v33,v33\nv_exp_f32 v34,v34\nv_cvt_pk_bf16_f32 v103,v62,v63\n" \
"v_mfma_f32_32x32x16_bf16 v[48:63],v[104:107],v[132:135],v[80:95]\nv_exp_f32 v35,v35\nv_cvt_pk_bf16_f32 v96,v32,v33\nv_exp_f32 v36,v36\n" \
"ds_read_b128 v[116:119],v252 offset:3072\nv_exp_f32 v37,v37\nv_cvt_pk_bf16_f32 v97,v34,v35\nv_exp_f32 v38,v38\n" \
"v_mfma_f32_32x32x16_bf16 v[16:31],v[124:127],v[100:103],v[16:31]\nv_exp_f32 v39,v39\nv_cvt_pk_bf16_f32 v98,v36,v37\nv_exp_f32 v40,v40\n" \
"ds_read_b128 v[120:123],v252 offset:4096\nv_exp_f32 v41,v41\nv_cvt_pk_bf16_f32 v99,v38,v39\nv_exp_f32 v42,v42\n" \
"ds_read_b128 v[124:127],v252 offset:5120\nv_exp_f32 v43,v43\nv_cvt_pk_bf16_f32 v100,v40,v41\nv_exp_f32 v44,v44\n" \
"v_mfma_f32_32x32x16_bf16 v[0:15],v[108:111],v[96:99],v[0:15]\nv_exp_f32 v45,v45\nv_cvt_pk_bf16_f32 v101,v42,v43\nv_exp_f32 v46,v46\nv_exp_f32 v47,v47\n" \
"v_exp_f32 v48,v48\nv_cvt_pk_bf16_f32 v102,v44,v45\nv_exp_f32 v49,v49\nv_exp_f32 v50,v50\nv_cvt_pk_bf16_f32 v103,v46,v47\ns_waitcnt lgkmcnt(2)\n" \
"v_mfma_f32_32x32x16_bf16 v[32:47],v[116:119],v[128:131],v[64:79]\nv_exp_f32 v51,v51\nv_cvt_pk_bf16_f32 v96,v48,v49\nv_exp_f32 v52,v52\n" \
"v_exp_f32 v53,v53\nv_cvt_pk_bf16_f32 v97,v50,v51\nv_exp_f32 v54,v54\nv_mfma_f32_32x32x16_bf16 v[0:15],v[112:115],v[100:103],v[0:15]\nv_exp_f32 v55,v55\n" \
"v_cvt_pk_bf16_f32 v98,v52,v53\nv_exp_f32 v56,v56\nv_exp_f32 v57,v57\nv_cvt_pk_bf16_f32 v99,v54,v55\nv_exp_f32 v58,v58\nv_exp_f32 v59,v59\n" \
"v_cvt_pk_bf16_f32 v100,v56,v57\nv_exp_f32 v60,v60\nv_mfma_f32_32x32x16_bf16 v[16:31],v[108:111],v[96:99],v[16:31]\nv_exp_f32 v61,v61\n" \
"v_cvt_pk_bf16_f32 v101,v58,v59\nv_exp_f32 v62,v62\nv_exp_f32 v63,v63\ns_waitcnt lgkmcnt(0)\nv_exp_f32 v32,v32\nv_cvt_pk_bf16_f32 v102,v60,v61\n" \
"v_exp_f32 v33,v33\nv_exp_f32 v34,v34\nv_cvt_pk_bf16_f32 v103,v62,v63\nv_mfma_f32_32x32x16_bf16 v[48:63],v[116:119],v[132:135],v[80:95]\n" \
"s_waitcnt vmcnt(0)\ns_barrier\nv_exp_f32 v35,v35\nv_cvt_pk_bf16_f32 v96,v32,v33\nv_exp_f32 v36,v36\nds_read_b128 v[104:107],v252 offset:6144\n" \
"v_exp_f32 v37,v37\nv_cvt_pk_bf16_f32 v97,v34,v35\nv_exp_f32 v38,v38\nv_mfma_f32_32x32x16_bf16 v[16:31],v[112:115],v[100:103],v[16:31]\n" \
"v_exp_f32 v39,v39\nv_cvt_pk_bf16_f32 v98,v36,v37\nv_exp_f32 v40,v40\nds_read_b128 v[108:111],v252 offset:7168\nv_exp_f32 v41,v41\n" \
"v_cvt_pk_bf16_f32 v99,v38,v39\nv_exp_f32 v42,v42\nds_read_b128 v[112:115],v252 offset:8192\nv_exp_f32 v43,v43\nv_cvt_pk_bf16_f32 v100,v40,v41\n" \
"v_exp_f32 v44,v44\nv_mfma_f32_32x32x16_bf16 v[0:15],v[120:123],v[96:99],v[0:15]\nv_exp_f32 v45,v45\nv_cvt_pk_bf16_f32 v101,v42,v43\nv_exp_f32 v46,v46\n" \
"v_exp_f32 v47,v47\nv_exp_f32 v48,v48\nv_cvt_pk_bf16_f32 v102,v44,v45\nv_exp_f32 v49,v49\nv_exp_f32 v50,v50\nv_cvt_pk_bf16_f32 v103,v46,v47\n" \
"s_waitcnt lgkmcnt(2)\nv_mfma_f32_32x32x16_bf16 v[32:47],v[104:107],v[128:131],v[64:79]\nv_exp_f32 v51,v51\nv_cvt_pk_bf16_f32 v96,v48,v49\n" \
"v_exp_f32 v52,v52\nv_exp_f32 v53,v53\nv_cvt_pk_bf16_f32 v97,v50,v51\nv_exp_f32 v54,v54\nv_mfma_f32_32x32x16_bf16 v[0:15],v[124:127],v[100:103],v[0:15]\n" \
"v_exp_f32 v55,v55\nv_cvt_pk_bf16_f32 v98,v52,v53\nv_exp_f32 v56,v56\nv_exp_f32 v57,v57\nv_cvt_pk_bf16_f32 v99,v54,v55\nv_exp_f32 v58,v58\n" \
"v_exp_f32 v59,v59\nv_cvt_pk_bf16_f32 v100,v56,v57\nv_exp_f32 v60,v60\nv_mfma_f32_32x32x16_bf16 v[16:31],v[120:123],v[96:99],v[16:31]\n" \
"v_exp_f32 v61,v61\nv_cvt_pk_bf16_f32 v101,v58,v59\nv_exp_f32 v62,v62\nv_exp_f32 v63,v63\ns_waitcnt lgkmcnt(0)\nv_exp_f32 v32,v32\n" \
"v_cvt_pk_bf16_f32 v102,v60,v61\nv_exp_f32 v33,v33\nv_exp_f32 v34,v34\nv_cvt_pk_bf16_f32 v103,v62,v63\n" \
"v_mfma_f32_32x32x16_bf16 v[48:63],v[104:107],v[132:135],v[80:95]\nv_exp_f32 v35,v35\nv_cvt_pk_bf16_f32 v96,v32,v33\nv_exp_f32 v36,v36\n" \
"ds_read_b128 v[116:119],v252 offset:9216\nv_exp_f32 v37,v37\nv_cvt_pk_bf16_f32 v97,v34,v35\nv_exp_f32 v38,v38\n" \
"v_mfma_f32_32x32x16_bf16 v[16:31],v[124:127],v[100:103],v[16:31]\nv_exp_f32 v39,v39\nv_cvt_pk_bf16_f32 v98,v36,v37\nv_exp_f32 v40,v40\n" \
"ds_read_b128 v[120:123],v252 offset:10240\nv_exp_f32 v41,v41\nv_cvt_pk_bf16_f32 v99,v38,v39\nv_exp_f32 v42,v42\n" \
"ds_read_b128 v[124:127],v252 offset:11264\nv_exp_f32 v43,v43\nv_cvt_pk_bf16_f32 v100,v40,v41\nv_exp_f32 v44,v44\n" \
"v_mfma_f32_32x32x16_bf16 v[0:15],v[108:111],v[96:99],v[0:15]\nv_exp_f32 v45,v45\nv_cvt_pk_bf16_f32 v101,v42,v43\nv_exp_f32 v46,v46\nv_exp_f32 v47,v47\n" \
"v_exp_f32 v48,v48\nv_cvt_pk_bf16_f32 v102,v44,v45\nv_exp_f32 v49,v49\nv_exp_f32 v50,v50\nv_cvt_pk_bf16_f32 v103,v46,v47\ns_waitcnt lgkmcnt(2)\n" \
"v_mfma_f32_32x32x16_bf16 v[32:47],v[116:119],v[128:131],v[64:79]\nv_exp_f32 v51,v51\nv_cvt_pk_bf16_f32 v96,v48,v49\nv_exp_f32 v52,v52\n" \
"v_exp_f32 v53,v53\nv_cvt_pk_bf16_f32 v97,v50,v51\nv_exp_f32 v54,v54\nv_mfma_f32_32x32x16_bf16 v[0:15],v[112:115],v[100:103],v[0:15]\nv_exp_f32 v55,v55\n" \
"v_cvt_pk_bf16_f32 v98,v52,v53\nv_exp_f32 v56,v56\nv_exp_f32 v57,v57\nv_cvt_pk_bf16_f32 v99,v54,v55\nv_exp_f32 v58,v58\nv_exp_f32 v59,v59\n" \
"v_cvt_pk_bf16_f32 v100,v56,v57\nv_exp_f32 v60,v60\nv_mfma_f32_32x32x16_bf16 v[16:31],v[108:111],v[96:99],v[16:31]\nv_exp_f32 v61,v61\n" \
"v_cvt_pk_bf16_f32 v101,v58,v59\nv_exp_f32 v62,v62\nv_exp_f32 v63,v63\ns_waitcnt lgkmcnt(0)\nv_exp_f32 v32,v32\nv_cvt_pk_bf16_f32 v102,v60,v61\n" \
"v_exp_f32 v33,v33\nv_exp_f32 v34,v34\nv_cvt_pk_bf16_f32 v103,v62,v63\nv_mfma_f32_32x32x16_bf16 v[48:63],v[116:119],v[132:135],v[80:95]\n" \
"v_exp_f32 v35,v35\nv_cvt_pk_bf16_f32 v96,v32,v33\nv_exp_f32 v36,v36\nds_read_b128 v[104:107],v252 offset:12288\nv_exp_f32 v37,v37\n" \
"v_cvt_pk_bf16_f32 v97,v34,v35\nv_exp_f32 v38,v38\nv_mfma_f32_32x32x16_bf16 v[16:31],v[112:115],v[100:103],v[16:31]\nv_exp_f32 v39,v39\n" \
"v_cvt_pk_bf16_f32 v98,v36,v37\nv_exp_f32 v40,v40\nds_read_b128 v[108:111],v252 offset:13312\nv_exp_f32 v41,v41\nv_cvt_pk_bf16_f32 v99,v38,v39\n" \
"v_exp_f32 v42,v42\nds_read_b128 v[112:115],v252 offset:14336\nv_exp_f32 v43,v43\nv_cvt_pk_bf16_f32 v100,v40,v41\nv_exp_f32 v44,v44\n" \
"v_mfma_f32_32x32x16_bf16 v[0:15],v[120:123],v[96:99],v[0:15]\nv_exp_f32 v45,v45\nv_cvt_pk_bf16_f32 v101,v42,v43\nv_exp_f32 v46,v46\nv_exp_f32 v47,v47\n" \
"v_exp_f32 v48,v48\nv_cvt_pk_bf16_f32 v102,v44,v45\nv_exp_f32 v49,v49\nv_exp_f32 v50,v50\nv_cvt_pk_bf16_f32 v103,v46,v47\ns_waitcnt lgkmcnt(2)\n" \
"v_mfma_f32_32x32x16_bf16 v[32:47],v[104:107],v[128:131],v[64:79]\nv_exp_f32 v51,v51\nv_cvt_pk_bf16_f32 v96,v48,v49\nv_exp_f32 v52,v52\n" \
"v_exp_f32 v53,v53\nv_cvt_pk_bf16_f32 v97,v50,v51\nv_exp_f32 v54,v54\nv_mfma_f32_32x32x16_bf16 v[0:15],v[124:127],v[100:103],v[0:15]\nv_exp_f32 v55,v55\n" \
"v_cvt_pk_bf16_f32 v98,v52,v53\nv_exp_f32 v56,v56\nv_exp_f32 v57,v57\nv_cvt_pk_bf16_f32 v99,v54,v55\nv_exp_f32 v58,v58\nv_exp_f32 v59,v59\n" \
"v_cvt_pk_bf16_f32 v100,v56,v57\nv_exp_f32 v60,v60\nv_mfma_f32_32x32x16_bf16 v[16:31],v[120:123],v[96:99],v[16:31]\nv_exp_f32 v61,v61\n" \
"v_cvt_pk_bf16_f32 v101,v58,v59\nv_exp_f32 v62,v62\nv_exp_f32 v63,v63\ns_waitcnt lgkmcnt(0)\nv_exp_f32 v32,v32\nv_cvt_pk_bf16_f32 v102,v60,v61\n" \
"v_exp_f32 v33,v33\nv_exp_f32 v34,v34\nv_cvt_pk_bf16_f32 v103,v62,v63\nv_mfma_f32_32x32x16_bf16 v[48:63],v[104:107],v[132:135],v[80:95]\n" \
"v_exp_f32 v35,v35\nv_cvt_pk_bf16_f32 v96,v32,v33\nv_exp_f32 v36,v36\nds_read_b128 v[116:119],v252 offset:15360\nv_exp_f32 v37,v37\n" \
"v_cvt_pk_bf16_f32 v97,v34,v35\nv_exp_f32 v38,v38\nv_mfma_f32_32x32x16_bf16 v[16:31],v[124:127],v[100:103],v[16:31]\nv_exp_f32 v39,v39\n" \
"v_cvt_pk_bf16_f32 v98,v36,v37\nv_exp_f32 v40,v40\nds_read_b128 v[120:123],v252 offset:16384\nv_exp_f32 v41,v41\nv_cvt_pk_bf16_f32 v99,v38,v39\n" \
"v_exp_f32 v42,v42\nds_read_b128 v[124:127],v252 offset:17408\nv_exp_f32 v43,v43\nv_cvt_pk_bf16_f32 v100,v40,v41\nv_exp_f32 v44,v44\n" \
"v_mfma_f32_32x32x16_bf16 v[0:15],v[108:111],v[96:99],v[0:15]\nv_exp_f32 v45,v45\nv_cvt_pk_bf16_f32 v101,v42,v43\nv_exp_f32 v46,v46\nv_exp_f32 v47,v47\n" \
"v_exp_f32 v48,v48\nv_cvt_pk_bf16_f32 v102,v44,v45\nv_exp_f32 v49,v49\nv_exp_f32 v50,v50\nv_cvt_pk_bf16_f32 v103,v46,v47\ns_waitcnt lgkmcnt(2)\n" \
"v_mfma_f32_32x32x16_bf16 v[32:47],v[116:119],v[128:131],v[64:79]\nv_exp_f32 v51,v51\nv_cvt_pk_bf16_f32 v96,v48,v49\nv_exp_f32 v52,v52\n" \
"v_exp_f32 v53,v53\nv_cvt_pk_bf16_f32 v97,v50,v51\nv_exp_f32 v54,v54\nv_mfma_f32_32x32x16_bf16 v[0:15],v[112:115],v[100:103],v[0:15]\nv_exp_f32 v55,v55\n" \
"v_cvt_pk_bf16_f32 v98,v52,v53\nv_exp_f32 v56,v56\nv_exp_f32 v57,v57\nv_cvt_pk_bf16_f32 v99,v54,v55\nv_exp_f32 v58,v58\nv_exp_f32 v59,v59\n" \
"v_cvt_pk_bf16_f32 v100,v56,v57\nv_exp_f32 v60,v60\nv_mfma_f32_32x32x16_bf16 v[16:31],v[108:111],v[96:99],v[16:31]\nv_exp_f32 v61,v61\n" \
"v_cvt_pk_bf16_f32 v101,v58,v59\nv_exp_f32 v62,v62\nv_exp_f32 v63,v63\ns_waitcnt lgkmcnt(0)\nv_exp_f32 v32,v32\nv_cvt_pk_bf16_f32 v102,v60,v61\n" \
"v_exp_f32 v33,v33\nv_exp_f32 v34,v34\nv_cvt_pk_bf16_f32 v103,v62,v63\nv_mfma_f32_32x32x16_bf16 v[48:63],v[116:119],v[132:135],v[80:95]\n" \
"v_exp_f32 v35,v35\nv_cvt_pk_bf16_f32 v96,v32,v33\nv_exp_f32 v36,v36\nds_read_b128 v[104:107],v252 offset:18432\nv_exp_f32 v37,v37\n" \
"v_cvt_pk_bf16_f32 v97,v34,v35\nv_exp_f32 v38,v38\nv_mfma_f32_32x32x16_bf16 v[16:31],v[112:115],v[100:103],v[16:31]\nv_exp_f32 v39,v39\n" \
"v_cvt_pk_bf16_f32 v98,v36,v37\nv_exp_f32 v40,v40\nds_read_b128 v[108:111],v252 offset:19456\nv_exp_f32 v41,v41\nv_cvt_pk_bf16_f32 v99,v38,v39\n" \
"v_exp_f32 v42,v42\nds_read_b128 v[112:115],v252 offset:20480\nv_exp_f32 v43,v43\nv_cvt_pk_bf16_f32 v100,v40,v41\nv_exp_f32 v44,v44\n" \
"v_mfma_f32_32x32x16_bf16 v[0:15],v[120:123],v[96:99],v[0:15]\nv_exp_f32 v45,v45\nv_cvt_pk_bf16_f32 v101,v42,v43\nv_exp_f32 v46,v46\nv_exp_f32 v47,v47\n" \
"v_exp_f32 v48,v48\nv_cvt_pk_bf16_f32 v102,v44,v45\nv_exp_f32 v49,v49\nv_exp_f32 v50,v50\nv_cvt_pk_bf16_f32 v103,v46,v47\ns_waitcnt lgkmcnt(2)\n" \
"v_mfma_f32_32x32x16_bf16 v[32:47],v[104:107],v[128:131],v[64:79]\nv_exp_f32 v51,v51\nv_cvt_pk_bf16_f32 v96,v48,v49\nv_exp_f32 v52,v52\n" \
"v_exp_f32 v53,v53\nv_cvt_pk_bf16_f32 v97,v50,v51\nv_exp_f32 v54,v54\nv_mfma_f32_32x32x16_bf16 v[0:15],v[124:127],v[100:103],v[0:15]\nv_exp_f32 v55,v55\n" \
"v_cvt_pk_bf16_f32 v98,v52,v53\nv_exp_f32 v56,v56\nv_exp_f32 v57,v57\nv_cvt_pk_bf16_f32 v99,v54,v55\nv_exp_f32 v58,v58\nv_exp_f32 v59,v59\n" \
"v_cvt_pk_bf16_f32 v100,v56,v57\nv_exp_f32 v60,v60\nv_mfma_f32_32x32x16_bf16 v[16:31],v[120:123],v[96:99],v[16:31]\nv_exp_f32 v61,v61\n" \
"v_cvt_pk_bf16_f32 v101,v58,v59\nv_exp_f32 v62,v62\nv_exp_f32 v63,v63\ns_waitcnt lgkmcnt(0)\nv_exp_f32 v32,v32\nv_cvt_pk_bf16_f32 v102,v60,v61\n" \
"v_exp_f32 v33,v33\nv_exp_f32 v34,v34\nv_cvt_pk_bf16_f32 v103,v62,v63\nv_mfma_f32_32x32x16_bf16 v[48:63],v[104:107],v[132:135],v[80:95]\n" \
"v_exp_f32 v35,v35\nv_cvt_pk_bf16_f32 v96,v32,v33\nv_exp_f32 v36,v36\nds_read_b128 v[116:119],v252 offset:21504\nv_exp_f32 v37,v37\n" \
"v_cvt_pk_bf16_f32 v97,v34,v35\nv_exp_f32 v38,v38\nv_mfma_f32_32x32x16_bf16 v[16:31],v[124:127],v[100:103],v[16:31]\nv_exp_f32 v39,v39\n" \
"v_cvt_pk_bf16_f32 v98,v36,v37\nv_exp_f32 v40,v40\nds_read_b128 v[120:123],v252 offset:22528\nv_exp_f32 v41,v41\nv_cvt_pk_bf16_f32 v99,v38,v39\n" \
"v_exp_f32 v42,v42\nds_read_b128 v[124:127],v252 offset:23552\nv_exp_f32 v43,v43\nv_cvt_pk_bf16_f32 v100,v40,v41\nv_exp_f32 v44,v44\n" \
"v_mfma_f32_32x32x16_bf16 v[0:15],v[108:111],v[96:99],v[0:15]\nv_exp_f32 v45,v45\nv_cvt_pk_bf16_f32 v101,v42,v43\nv_exp_f32 v46,v46\nv_exp_f32 v47,v47\n" \
"v_exp_f32 v48,v48\nv_cvt_pk_bf16_f32 v102,v44,v45\nv_exp_f32 v49,v49\nv_exp_f32 v50,v50\nv_cvt_pk_bf16_f32 v103,v46,v47\ns_waitcnt lgkmcnt(2)\n" \
"v_mfma_f32_32x32x16_bf16 v[32:47],v[116:119],v[128:131],v[64:79]\nv_exp_f32 v51,v51\nv_cvt_pk_bf16_f32 v96,v48,v49\nv_exp_f32 v52,v52\n" \
"v_exp_f32 v53,v53\nv_cvt_pk_bf16_f32 v97,v50,v51\nv_exp_f32 v54,v54\nv_mfma_f32_32x32x16_bf16 v[0:15],v[112:115],v[100:103],v[0:15]\nv_exp_f32 v55,v55\n" \
"v_cvt_pk_bf16_f32 v98,v52,v53\nv_exp_f32 v56,v56\nv_exp_f32 v57,v57\nv_cvt_pk_bf16_f32 v99,v54,v55\nv_exp_f32 v58,v58\nv_exp_f32 v59,v59\n" \
"v_cvt_pk_bf16_f32 v100,v56,v57\nv_exp_f32 v60,v60\nv_mfma_f32_32x32x16_bf16 v[16:31],v[108:111],v[96:99],v[16:31]\nv_exp_f32 v61,v61\n" \
"v_cvt_pk_bf16_f32 v101,v58,v59\nv_exp_f32 v62,v62\nv_exp_f32 v63,v63\ns_waitcnt lgkmcnt(0)\nv_exp_f32 v32,v32\nv_cvt_pk_bf16_f32 v102,v60,v61\n" \
"v_exp_f32 v33,v33\nv_exp_f32 v34,v34\nv_cvt_pk_bf16_f32 v103,v62,v63\nv_mfma_f32_32x32x16_bf16 v[48:63],v[116:119],v[132:135],v[80:95]\n" \
"v_exp_f32 v35,v35\nv_cvt_pk_bf16_f32 v96,v32,v33\nv_exp_f32 v36,v36\nv_exp_f32 v37,v37\nv_cvt_pk_bf16_f32 v97,v34,v35\nv_exp_f32 v38,v38\n" \
"v_mfma_f32_32x32x16_bf16 v[16:31],v[112:115],v[100:103],v[16:31]\nv_exp_f32 v39,v39\nv_cvt_pk_bf16_f32 v98,v36,v37\nv_exp_f32 v40,v40\n" \
"v_exp_f32 v41,v41\nv_cvt_pk_bf16_f32 v99,v38,v39\nv_exp_f32 v42,v42\nv_exp_f32 v43,v43\nv_cvt_pk_bf16_f32 v100,v40,v41\nv_exp_f32 v44,v44\n" \
"v_mfma_f32_32x32x16_bf16 v[0:15],v[120:123],v[96:99],v[0:15]\nv_exp_f32 v45,v45\nv_cvt_pk_bf16_f32 v101,v42,v43\nv_exp_f32 v46,v46\nv_exp_f32 v47,v47\n" \
"v_exp_f32 v48,v48\nv_cvt_pk_bf16_f32 v102,v44,v45\nv_exp_f32 v49,v49\nv_exp_f32 v50,v50\nv_cvt_pk_bf16_f32 v103,v46,v47\nv_exp_f32 v51,v51\n" \
"v_cvt_pk_bf16_f32 v96,v48,v49\nv_exp_f32 v52,v52\nv_exp_f32 v53,v53\nv_cvt_pk_bf16_f32 v97,v50,v51\nv_exp_f32 v54,v54\n" \
"v_mfma_f32_32x32x16_bf16 v[0:15],v[124:127],v[100:103],v[0:15]\nv_exp_f32 v55,v55\nv_cvt_pk_bf16_f32 v98,v52,v53\nv_exp_f32 v56,v56\nv_exp_f32 v57,v57\n" \
"v_cvt_pk_bf16_f32 v99,v54,v55\nv_exp_f32 v58,v58\nv_exp_f32 v59,v59\nv_cvt_pk_bf16_f32 v100,v56,v57\nv_exp_f32 v60,v60\n" \
"v_mfma_f32_32x32x16_bf16 v[16:31],v[120:123],v[96:99],v[16:31]\nv_exp_f32 v61,v61\nv_cvt_pk_bf16_f32 v101,v58,v59\nv_exp_f32 v62,v62\n" \
"v_exp_f32 v63,v63\ns_nop 1\nv_cvt_pk_bf16_f32 v102,v60,v61\nv_cvt_pk_bf16_f32 v103,v62,v63\ns_nop 1\n" \
"v_mfma_f32_32x32x16_bf16 v[16:31],v[124:127],v[100:103],v[16:31]\ns_waitcnt lgkmcnt(0)\ns_nop 7\ns_nop 7\nv_mov_b32 v228,v64\nv_mov_b32 v229,v80\n" \
"v_mov_b32 v64,v192\nv_mov_b32 v65,v193\nv_mov_b32 v66,v194\nv_mov_b32 v67,v195\nv_mov_b32 v68,v196\nv_mov_b32 v69,v197\nv_mov_b32 v70,v198\n" \
"v_mov_b32 v71,v199\nv_mov_b32 v72,v200\nv_mov_b32 v73,v201\nv_mov_b32 v74,v202\nv_mov_b32 v75,v203\nv_mov_b32 v76,v204\nv_mov_b32 v77,v205\n" \
"v_mov_b32 v78,v206\nv_mov_b32 v79,v207\nv_mov_b32 v80,v208\nv_mov_b32 v81,v209\nv_mov_b32 v82,v210\nv_mov_b32 v83,v211\nv_mov_b32 v84,v212\n" \
"v_mov_b32 v85,v213\nv_mov_b32 v86,v214\nv_mov_b32 v87,v215\nv_mov_b32 v88,v216\nv_mov_b32 v89,v217\nv_mov_b32 v90,v218\nv_mov_b32 v91,v219\n" \
"v_mov_b32 v92,v220\nv_mov_b32 v93,v221\nv_mov_b32 v94,v222\nv_mov_b32 v95,v223\nv_mov_b32 v230,v128\nv_mov_b32 v231,v129\nv_mov_b32 v249,v130\n" \
"v_mov_b32 v250,v131\nv_mov_b32 v251,v132\nv_mov_b32 v252,v133\nv_mov_b32 v255,v134\nv_mov_b32 v222,v135\nv_mov_b32 v128,v232\nv_mov_b32 v129,v233\n" \
"v_mov_b32 v130,v234\nv_mov_b32 v131,v235\nv_mov_b32 v132,v236\nv_mov_b32 v133,v237\nv_mov_b32 v134,v238\nv_mov_b32 v135,v239\nv_mov_b32 v136,v240\n" \
"v_mov_b32 v137,v241\nv_mov_b32 v138,v242\nv_mov_b32 v139,v243\nv_mov_b32 v140,v244\nv_mov_b32 v141,v245\nv_mov_b32 v142,v246\nv_mov_b32 v143,v247\n" \
"v_mov_b32 v232,v0\nv_mov_b32 v233,v1\nv_mov_b32 v234,v2\nv_mov_b32 v235,v3\nv_mov_b32 v236,v4\nv_mov_b32 v237,v5\nv_mov_b32 v238,v6\nv_mov_b32 v239,v7\n" \
"v_mov_b32 v240,v8\nv_mov_b32 v241,v16\nv_mov_b32 v242,v17\nv_mov_b32 v243,v18\nv_mov_b32 v244,v19\nv_mov_b32 v245,v20\nv_mov_b32 v246,v21\n" \
"v_mov_b32 v247,v22\nv_mov_b32 v226,v23\nv_mov_b32 v227,v24\nv_mov_b32 v0,0\nv_mov_b32 v1,0\nv_mov_b32 v2,0\nv_mov_b32 v3,0\nv_mov_b32 v4,0\n" \
"v_mov_b32 v5,0\nv_mov_b32 v6,0\nv_mov_b32 v7,0\nv_mov_b32 v8,0\nv_mov_b32 v9,0\nv_mov_b32 v10,0\nv_mov_b32 v11,0\nv_mov_b32 v12,0\nv_mov_b32 v13,0\n" \
"v_mov_b32 v14,0\nv_mov_b32 v15,0\nv_mov_b32 v16,0\nv_mov_b32 v17,0\nv_mov_b32 v18,0\nv_mov_b32 v19,0\nv_mov_b32 v20,0\nv_mov_b32 v21,0\n" \
"v_mov_b32 v22,0\nv_mov_b32 v23,0\nv_mov_b32 v24,0\nv_mov_b32 v25,0\nv_mov_b32 v26,0\nv_mov_b32 v27,0\nv_mov_b32 v28,0\nv_mov_b32 v29,0\n" \
"v_mov_b32 v30,0\nv_mov_b32 v31,0\nv_mov_b32 v32,0\nv_mov_b32 v33,0\nv_mov_b32 v34,0\nv_mov_b32 v35,0\nv_mov_b32 v36,0\nv_mov_b32 v37,0\n" \
"v_mov_b32 v38,0\nv_mov_b32 v39,0\nv_mov_b32 v40,0\nv_mov_b32 v41,0\nv_mov_b32 v42,0\nv_mov_b32 v43,0\nv_mov_b32 v44,0\nv_mov_b32 v45,0\n" \
"v_mov_b32 v46,0\nv_mov_b32 v47,0\nv_mov_b32 v48,0\nv_mov_b32 v49,0\nv_mov_b32 v50,0\nv_mov_b32 v51,0\nv_mov_b32 v52,0\nv_mov_b32 v53,0\n" \
"v_mov_b32 v54,0\nv_mov_b32 v55,0\nv_mov_b32 v56,0\nv_mov_b32 v57,0\nv_mov_b32 v58,0\nv_mov_b32 v59,0\nv_mov_b32 v60,0\nv_mov_b32 v61,0\n" \
"v_mov_b32 v62,0\nv_mov_b32 v63,0\nv_mov_b32 v216,0\ns_barrier\ns_mov_b32 s40,s72\ns_lshr_b32 s41,s73,1\ns_add_u32 s41,s41,s69\ns_mov_b32 s47,s74\n" \
"v_lshlrev_b32 v220,2,v248\nglobal_load_dword v221,v220,s[84:85]\nglobal_load_dword v221,v220,s[86:87]\n10:\ns_cmp_le_i32 s40,s41\ns_cbranch_scc1 11f\n" \
"s_barrier\ns_sub_u32 s40,s40,1\ns_branch 10b\n11:\ns_and_b32 s43,s40,3\ns_lshl_b32 s43,s43,15\nv_add_u32 v217,s43,v253\nv_mov_b32 v218,v217\n" \
"s_bitcmp1_b32 s73,0\ns_cbranch_scc1 12f\ns_sub_u32 s43,s40,1\ns_and_b32 s43,s43,3\ns_lshl_b32 s43,s43,15\nds_read_b128 v[152:155],v217 offset:0\n" \
"ds_read_b128 v[156:159],v217 offset:1024\nds_read_b128 v[160:163],v217 offset:2048\nv_add_u32 v219,s43,v253\nds_read_b128 v[164:167],v217 offset:3072\n" \
"ds_read_b128 v[168:171],v217 offset:4096\nds_read_b128 v[172:175],v217 offset:5120\nds_read_b128 v[176:179],v217 offset:6144\n" \
"ds_read_b128 v[180:183],v217 offset:7168\ns_waitcnt lgkmcnt(7)\nv_mfma_f32_32x32x16_bf16 v[96:111],v[152:155],v[64:67],v[128:143]\n" \
"s_waitcnt lgkmcnt(5)\nv_mfma_f32_32x32x16_bf16 v[96:111],v[156:159],v[68:71],v[96:111]\n" \
"v_mfma_f32_32x32x16_bf16 v[96:111],v[160:163],v[72:75],v[96:111]\ns_barrier\ns_waitcnt lgkmcnt(3)\n" \
"v_mfma_f32_32x32x16_bf16 v[96:111],v[164:167],v[76:79],v[96:111]\nv_mfma_f32_32x32x16_bf16 v[96:111],v[168:171],v[80:83],v[96:111]\n" \
"s_waitcnt lgkmcnt(1)\nv_mfma_f32_32x32x16_bf16 v[96:111],v[172:175],v[84:87],v[96:111]\n" \
"v_mfma_f32_32x32x16_bf16 v[96:111],v[176:179],v[88:91],v[96:111]\ns_waitcnt lgkmcnt(0)\n" \
"v_mfma_f32_32x32x16_bf16 v[96:111],v[180:183],v[92:95],v[96:111]\nds_read_b128 v[152:155],v219 offset:16384\nds_read_b128 v[156:159],v219 offset:17408\n" \
"s_nop 7\ns_nop 7\nv_max3_f32 v223,v96,v97,v98\nv_max3_f32 v224,v99,v100,v101\nv_max3_f32 v223,v223,v102,v103\nv_max3_f32 v224,v224,v104,v105\n" \
"v_max3_f32 v223,v223,v106,v107\nv_max3_f32 v224,v224,v108,v109\nv_max3_f32 v223,v223,v110,v111\nv_max3_f32 v223,v223,v224,v224\nv_mov_b32 v224,v223\n" \
"s_nop 1\nv_permlane32_swap_b32 v223,v224\ns_nop 1\nv_max_f32 v223,v223,v224\nv_sub_f32 v96,v96,v223\nv_sub_f32 v97,v97,v223\nv_sub_f32 v98,v98,v223\n" \
"v_sub_f32 v99,v99,v223\nv_sub_f32 v100,v100,v223\nv_sub_f32 v101,v101,v223\nv_sub_f32 v102,v102,v223\nv_sub_f32 v103,v103,v223\n" \
"v_sub_f32 v104,v104,v223\nv_sub_f32 v105,v105,v223\nv_sub_f32 v106,v106,v223\nv_sub_f32 v107,v107,v223\nv_sub_f32 v108,v108,v223\n" \
"v_sub_f32 v109,v109,v223\nv_sub_f32 v110,v110,v223\nv_sub_f32 v111,v111,v223\nv_sub_f32 v128,0,v223\nv_sub_f32 v129,0,v223\nv_sub_f32 v130,0,v223\n" \
"v_sub_f32 v131,0,v223\nv_sub_f32 v132,0,v223\nv_sub_f32 v133,0,v223\nv_sub_f32 v134,0,v223\nv_sub_f32 v135,0,v223\nv_sub_f32 v136,0,v223\n" \
"v_sub_f32 v137,0,v223\nv_sub_f32 v138,0,v223\nv_sub_f32 v139,0,v223\nv_sub_f32 v140,0,v223\nv_sub_f32 v141,0,v223\nv_sub_f32 v142,0,v223\n" \
"v_sub_f32 v143,0,v223\ns_nop 1\nv_exp_f32 v96,v96\nds_read_b128 v[160:163],v219 offset:18432\nv_exp_f32 v97,v97\nv_add_f32 v216,v216,v96\n" \
"ds_read_b128 v[164:167],v219 offset:19456\nv_exp_f32 v98,v98\nv_add_f32 v216,v216,v97\nv_cvt_pk_bf16_f32 v144,v96,v97\n" \
"ds_read_b128 v[168:171],v219 offset:20480\nv_exp_f32 v99,v99\nv_add_f32 v216,v216,v98\nv_mov_b32 v218,v217\nds_read_b128 v[172:175],v219 offset:21504\n" \
"v_exp_f32 v100,v100\nv_add_f32 v216,v216,v99\nv_cvt_pk_bf16_f32 v145,v98,v99\ns_sub_u32 s40,s40,1\nds_read_b128 v[176:179],v219 offset:22528\n" \
"v_exp_f32 v101,v101\nv_add_f32 v216,v216,v100\ns_cmp_ge_i32 s40,s69\nds_read_b128 v[180:183],v219 offset:23552\nv_exp_f32 v102,v102\n" \
"v_add_f32 v216,v216,v101\nv_cvt_pk_bf16_f32 v146,v100,v101\nv_mov_b32 v217,v219\ns_branch 13f\n12:\nds_read_b128 v[152:155],v217 offset:16384\n" \
"ds_read_b128 v[156:159],v217 offset:17408\nds_read_b128 v[160:163],v217 offset:18432\nds_read_b128 v[164:167],v217 offset:19456\n" \
"ds_read_b128 v[168:171],v217 offset:20480\nds_read_b128 v[172:175],v217 offset:21504\nds_read_b128 v[176:179],v217 offset:22528\n" \
"ds_read_b128 v[180:183],v217 offset:23552\ns_waitcnt lgkmcnt(7)\nv_mfma_f32_32x32x16_bf16 v[112:127],v[152:155],v[64:67],v[128:143]\n" \
"s_waitcnt lgkmcnt(5)\nv_mfma_f32_32x32x16_bf16 v[112:127],v[156:159],v[68:71],v[112:127]\ns_sub_u32 s43,s40,1\n" \
"v_mfma_f32_32x32x16_bf16 v[112:127],v[160:163],v[72:75],v[112:127]\ns_and_b32 s43,s43,3\ns_waitcnt lgkmcnt(3)\n" \
"v_mfma_f32_32x32x16_bf16 v[112:127],v[164:167],v[76:79],v[112:127]\ns_lshl_b32 s43,s43,15\n" \
"v_mfma_f32_32x32x16_bf16 v[112:127],v[168:171],v[80:83],v[112:127]\ns_waitcnt lgkmcnt(1)\n" \
"v_mfma_f32_32x32x16_bf16 v[112:127],v[172:175],v[84:87],v[112:127]\nv_mfma_f32_32x32x16_bf16 v[112:127],v[176:179],v[88:91],v[112:127]\n" \
"s_waitcnt lgkmcnt(0)\nv_mfma_f32_32x32x16_bf16 v[112:127],v[180:183],v[92:95],v[112:127]\nds_read_b128 v[152:155],v217 offset:0\n" \
"ds_read_b128 v[156:159],v217 offset:1024\ns_nop 7\ns_nop 7\nv_max3_f32 v223,v112,v113,v114\nv_max3_f32 v224,v115,v116,v117\n" \
"v_max3_f32 v223,v223,v118,v119\nv_max3_f32 v224,v224,v120,v121\nv_max3_f32 v223,v223,v122,v123\nv_max3_f32 v224,v224,v124,v125\n" \
"v_max3_f32 v223,v223,v126,v127\nv_max3_f32 v223,v223,v224,v224\nv_mov_b32 v224,v223\ns_nop 1\nv_permlane32_swap_b32 v223,v224\ns_nop 1\n" \
"v_max_f32 v223,v223,v224\nv_sub_f32 v112,v112,v223\nv_sub_f32 v113,v113,v223\nv_sub_f32 v114,v114,v223\nv_sub_f32 v115,v115,v223\n" \
"v_sub_f32 v116,v116,v223\nv_sub_f32 v117,v117,v223\nv_sub_f32 v118,v118,v223\nv_sub_f32 v119,v119,v223\nv_sub_f32 v120,v120,v223\n" \
"v_sub_f32 v121,v121,v223\nv_sub_f32 v122,v122,v223\nv_sub_f32 v123,v123,v223\nv_sub_f32 v124,v124,v223\nv_sub_f32 v125,v125,v223\n" \
"v_sub_f32 v126,v126,v223\nv_sub_f32 v127,v127,v223\nv_sub_f32 v128,0,v223\nv_sub_f32 v129,0,v223\nv_sub_f32 v130,0,v223\nv_sub_f32 v131,0,v223\n" \
"v_sub_f32 v132,0,v223\nv_sub_f32 v133,0,v223\nv_sub_f32 v134,0,v223\nv_sub_f32 v135,0,v223\nv_sub_f32 v136,0,v223\nv_sub_f32 v137,0,v223\n" \
"v_sub_f32 v138,0,v223\nv_sub_f32 v139,0,v223\nv_sub_f32 v140,0,v223\nv_sub_f32 v141,0,v223\nv_sub_f32 v142,0,v223\nv_sub_f32 v143,0,v223\ns_nop 1\n" \
"v_exp_f32 v112,v112\nds_read_b128 v[160:163],v217 offset:2048\nv_exp_f32 v113,v113\nv_add_f32 v216,v216,v112\nv_add_u32 v219,s43,v253\n" \
"ds_read_b128 v[164:167],v217 offset:3072\nv_exp_f32 v114,v114\nv_add_f32 v216,v216,v113\nv_cvt_pk_bf16_f32 v144,v112,v113\n" \
"ds_read_b128 v[168:171],v217 offset:4096\nv_exp_f32 v115,v115\nv_add_f32 v216,v216,v114\nds_read_b128 v[172:175],v217 offset:5120\nv_exp_f32 v116,v116\n" \
"v_add_f32 v216,v216,v115\nv_cvt_pk_bf16_f32 v145,v114,v115\nds_read_b128 v[176:179],v217 offset:6144\nv_exp_f32 v117,v117\nv_add_f32 v216,v216,v116\n" \
"ds_read_b128 v[180:183],v217 offset:7168\nv_exp_f32 v118,v118\nv_add_f32 v216,v216,v117\nv_cvt_pk_bf16_f32 v146,v116,v117\ns_waitcnt lgkmcnt(7)\n" \
"v_mfma_f32_32x32x16_bf16 v[96:111],v[152:155],v[64:67],v[128:143]\nds_read_b128 v[184:187],v217 offset:24576\nv_exp_f32 v119,v119\n" \
"v_add_f32 v216,v216,v118\ns_waitcnt lgkmcnt(6)\nv_mfma_f32_32x32x16_bf16 v[96:111],v[156:159],v[68:71],v[96:111]\n" \
"ds_read_b128 v[188:191],v217 offset:25600\nv_exp_f32 v120,v120\nv_add_f32 v216,v216,v119\nv_cvt_pk_bf16_f32 v147,v118,v119\n" \
"v_mfma_f32_32x32x16_bf16 v[96:111],v[160:163],v[72:75],v[96:111]\nds_read_b128 v[192:195],v217 offset:26624\nv_exp_f32 v121,v121\n" \
"v_add_f32 v216,v216,v120\ns_barrier\ns_waitcnt lgkmcnt(6)\nv_mfma_f32_32x32x16_bf16 v[96:111],v[164:167],v[76:79],v[96:111]\n" \
"ds_read_b128 v[196:199],v217 offset:27648\nv_exp_f32 v122,v122\nv_add_f32 v216,v216,v121\nv_cvt_pk_bf16_f32 v148,v120,v121\n" \
"v_mfma_f32_32x32x16_bf16 v[96:111],v[168:171],v[80:83],v[96:111]\nds_read_b128 v[200:203],v217 offset:28672\nv_exp_f32 v123,v123\n" \
"v_add_f32 v216,v216,v122\ns_waitcnt lgkmcnt(6)\nv_mfma_f32_32x32x16_bf16 v[96:111],v[172:175],v[84:87],v[96:111]\n" \
"ds_read_b128 v[204:207],v217 offset:29696\nv_exp_f32 v124,v124\nv_add_f32 v216,v216,v123\nv_cvt_pk_bf16_f32 v149,v122,v123\n" \
"v_mfma_f32_32x32x16_bf16 v[96:111],v[176:179],v[88:91],v[96:111]\nds_read_b128 v[208:211],v217 offset:30720\nv_exp_f32 v125,v125\n" \
"v_add_f32 v216,v216,v124\ns_waitcnt lgkmcnt(6)\nv_mfma_f32_32x32x16_bf16 v[96:111],v[180:183],v[92:95],v[96:111]\n" \
"ds_read_b128 v[212:215],v217 offset:31744\nv_exp_f32 v126,v126\nv_add_f32 v216,v216,v125\nv_cvt_pk_bf16_f32 v150,v124,v125\n" \
"v_mfma_f32_32x32x16_bf16 v[0:15],v[184:187],v[144:147],v[0:15]\nds_read_b128 v[152:155],v219 offset:16384\nv_exp_f32 v127,v127\n" \
"v_add_f32 v216,v216,v126\ns_waitcnt lgkmcnt(6)\nv_mfma_f32_32x32x16_bf16 v[16:31],v[188:191],v[144:147],v[16:31]\n" \
"ds_read_b128 v[156:159],v219 offset:17408\nv_exp_f32 v96,v96\nv_add_f32 v216,v216,v127\nv_cvt_pk_bf16_f32 v151,v126,v127\n" \
"v_mfma_f32_32x32x16_bf16 v[32:47],v[192:195],v[144:147],v[32:47]\nds_read_b128 v[160:163],v219 offset:18432\nv_exp_f32 v97,v97\n" \
"v_add_f32 v216,v216,v96\ns_waitcnt lgkmcnt(6)\nv_mfma_f32_32x32x16_bf16 v[48:63],v[196:199],v[144:147],v[48:63]\n" \
"ds_read_b128 v[164:167],v219 offset:19456\nv_exp_f32 v98,v98\nv_add_f32 v216,v216,v97\nv_cvt_pk_bf16_f32 v144,v96,v97\n" \
"v_mfma_f32_32x32x16_bf16 v[0:15],v[200:203],v[148:151],v[0:15]\nds_read_b128 v[168:171],v219 offset:20480\nv_exp_f32 v99,v99\nv_add_f32 v216,v216,v98\n" \
"v_mov_b32 v218,v217\ns_waitcnt lgkmcnt(6)\nv_mfma_f32_32x32x16_bf16 v[16:31],v[204:207],v[148:151],v[16:31]\nds_read_b128 v[172:175],v219 offset:21504\n" \
"v_exp_f32 v100,v100\nv_add_f32 v216,v216,v99\nv_cvt_pk_bf16_f32 v145,v98,v99\ns_sub_u32 s40,s40,1\n" \
"v_mfma_f32_32x32x16_bf16 v[32:47],v[208:211],v[148:151],v[32:47]\nds_read_b128 v[176:179],v219 offset:22528\nv_exp_f32 v101,v101\n" \
"v_add_f32 v216,v216,v100\ns_cmp_ge_i32 s40,s69\ns_waitcnt lgkmcnt(7)\nv_mfma_f32_32x32x16_bf16 v[48:63],v[212:215],v[148:151],v[48:63]\n" \
"ds_read_b128 v[180:183],v219 offset:23552\nv_exp_f32 v102,v102\nv_add_f32 v216,v216,v101\nv_cvt_pk_bf16_f32 v146,v100,v101\nv_mov_b32 v217,v219\n13:\n" \
"s_cbranch_scc0 15f\n14:\ns_waitcnt lgkmcnt(7)\nv_mfma_f32_32x32x16_bf16 v[112:127],v[152:155],v[64:67],v[128:143]\n" \
"ds_read_b128 v[184:187],v218 offset:8192\nv_exp_f32 v103,v103\nv_add_f32 v216,v216,v102\ns_waitcnt lgkmcnt(6)\n" \
"v_mfma_f32_32x32x16_bf16 v[112:127],v[156:159],v[68:71],v[112:127]\nds_read_b128 v[188:191],v218 offset:9216\nv_exp_f32 v104,v104\n" \
"v_add_f32 v216,v216,v103\nv_cvt_pk_bf16_f32 v147,v102,v103\ns_sub_u32 s43,s40,1\nv_mfma_f32_32x32x16_bf16 v[112:127],v[160:163],v[72:75],v[112:127]\n" \
"ds_read_b128 v[192:195],v218 offset:10240\nv_exp_f32 v105,v105\nv_add_f32 v216,v216,v104\ns_and_b32 s43,s43,3\ns_waitcnt lgkmcnt(6)\n" \
"v_mfma_f32_32x32x16_bf16 v[112:127],v[164:167],v[76:79],v[112:127]\nds_read_b128 v[196:199],v218 offset:11264\nv_exp_f32 v106,v106\n" \
"v_add_f32 v216,v216,v105\nv_cvt_pk_bf16_f32 v148,v104,v105\ns_lshl_b32 s43,s43,15\nv_mfma_f32_32x32x16_bf16 v[112:127],v[168:171],v[80:83],v[112:127]\n" \
"ds_read_b128 v[200:203],v218 offset:12288\nv_exp_f32 v107,v107\nv_add_f32 v216,v216,v106\ns_waitcnt lgkmcnt(6)\n" \
"v_mfma_f32_32x32x16_bf16 v[112:127],v[172:175],v[84:87],v[112:127]\nds_read_b128 v[204:207],v218 offset:13312\nv_exp_f32 v108,v108\n" \
"v_add_f32 v216,v216,v107\nv_cvt_pk_bf16_f32 v149,v106,v107\nv_mfma_f32_32x32x16_bf16 v[112:127],v[176:179],v[88:91],v[112:127]\n" \
"ds_read_b128 v[208:211],v218 offset:14336\nv_exp_f32 v109,v109\nv_add_f32 v216,v216,v108\ns_waitcnt lgkmcnt(6)\n" \
"v_mfma_f32_32x32x16_bf16 v[112:127],v[180:183],v[92:95],v[112:127]\nds_read_b128 v[212:215],v218 offset:15360\nv_exp_f32 v110,v110\n" \
"v_add_f32 v216,v216,v109\nv_cvt_pk_bf16_f32 v150,v108,v109\nv_mfma_f32_32x32x16_bf16 v[0:15],v[184:187],v[144:147],v[0:15]\n" \
"ds_read_b128 v[152:155],v217 offset:0\nv_exp_f32 v111,v111\nv_add_f32 v216,v216,v110\ns_waitcnt lgkmcnt(6)\n" \
"v_mfma_f32_32x32x16_bf16 v[16:31],v[188:191],v[144:147],v[16:31]\nds_read_b128 v[156:159],v217 offset:1024\nv_exp_f32 v112,v112\n" \
"v_add_f32 v216,v216,v111\nv_cvt_pk_bf16_f32 v151,v110,v111\nv_mfma_f32_32x32x16_bf16 v[32:47],v[192:195],v[144:147],v[32:47]\n" \
"ds_read_b128 v[160:163],v217 offset:2048\nv_exp_f32 v113,v113\nv_add_f32 v216,v216,v112\nv_add_u32 v219,s43,v253\ns_waitcnt lgkmcnt(6)\n" \
"v_mfma_f32_32x32x16_bf16 v[48:63],v[196:199],v[144:147],v[48:63]\nds_read_b128 v[164:167],v217 offset:3072\nv_exp_f32 v114,v114\n" \
"v_add_f32 v216,v216,v113\nv_cvt_pk_bf16_f32 v144,v112,v113\nv_mfma_f32_32x32x16_bf16 v[0:15],v[200:203],v[148:151],v[0:15]\n" \
"ds_read_b128 v[168:171],v217 offset:4096\nv_exp_f32 v115,v115\nv_add_f32 v216,v216,v114\ns_waitcnt lgkmcnt(6)\n" \
"v_mfma_f32_32x32x16_bf16 v[16:31],v[204:207],v[148:151],v[16:31]\nds_read_b128 v[172:175],v217 offset:5120\nv_exp_f32 v116,v116\n" \
"v_add_f32 v216,v216,v115\nv_cvt_pk_bf16_f32 v145,v114,v115\nv_mfma_f32_32x32x16_bf16 v[32:47],v[208:211],v[148:151],v[32:47]\n" \
"ds_read_b128 v[176:179],v217 offset:6144\nv_exp_f32 v117,v117\nv_add_f32 v216,v216,v116\ns_waitcnt lgkmcnt(6)\n" \
"v_mfma_f32_32x32x16_bf16 v[48:63],v[212:215],v[148:151],v[48:63]\nds_read_b128 v[180:183],v217 offset:7168\nv_exp_f32 v118,v118\n" \
"v_add_f32 v216,v216,v117\nv_cvt_pk_bf16_f32 v146,v116,v117\nv_mfma_f32_32x32x16_bf16 v[96:111],v[152:155],v[64:67],v[128:143]\n" \
"ds_read_b128 v[184:187],v217 offset:24576\nv_exp_f32 v119,v119\nv_add_f32 v216,v216,v118\ns_waitcnt lgkmcnt(6)\n" \
"v_mfma_f32_32x32x16_bf16 v[96:111],v[156:159],v[68:71],v[96:111]\nds_read_b128 v[188:191],v217 offset:25600\nv_exp_f32 v120,v120\n" \
"v_add_f32 v216,v216,v119\nv_cvt_pk_bf16_f32 v147,v118,v119\nv_mfma_f32_32x32x16_bf16 v[96:111],v[160:163],v[72:75],v[96:111]\n" \
"ds_read_b128 v[192:195],v217 offset:26624\nv_exp_f32 v121,v121\nv_add_f32 v216,v216,v120\ns_barrier\ns_waitcnt lgkmcnt(6)\n" \
"v_mfma_f32_32x32x16_bf16 v[96:111],v[164:167],v[76:79],v[96:111]\nds_read_b128 v[196:199],v217 offset:27648\nv_exp_f32 v122,v122\n" \
"v_add_f32 v216,v216,v121\nv_cvt_pk_bf16_f32 v148,v120,v121\nv_mfma_f32_32x32x16_bf16 v[96:111],v[168:171],v[80:83],v[96:111]\n" \
"ds_read_b128 v[200:203],v217 offset:28672\nv_exp_f32 v123,v123\nv_add_f32 v216,v216,v122\ns_waitcnt lgkmcnt(6)\n" \
"v_mfma_f32_32x32x16_bf16 v[96:111],v[172:175],v[84:87],v[96:111]\nds_read_b128 v[204:207],v217 offset:29696\nv_exp_f32 v124,v124\n" \
"v_add_f32 v216,v216,v123\nv_cvt_pk_bf16_f32 v149,v122,v123\nv_mfma_f32_32x32x16_bf16 v[96:111],v[176:179],v[88:91],v[96:111]\n" \
"ds_read_b128 v[208:211],v217 offset:30720\nv_exp_f32 v125,v125\nv_add_f32 v216,v216,v124\ns_waitcnt lgkmcnt(6)\n" \
"v_mfma_f32_32x32x16_bf16 v[96:111],v[180:183],v[92:95],v[96:111]\nds_read_b128 v[212:215],v217 offset:31744\nv_exp_f32 v126,v126\n" \
"v_add_f32 v216,v216,v125\nv_cvt_pk_bf16_f32 v150,v124,v125\nv_mfma_f32_32x32x16_bf16 v[0:15],v[184:187],v[144:147],v[0:15]\n" \
"ds_read_b128 v[152:155],v219 offset:16384\nv_exp_f32 v127,v127\nv_add_f32 v216,v216,v126\ns_waitcnt lgkmcnt(6)\n" \
"v_mfma_f32_32x32x16_bf16 v[16:31],v[188:191],v[144:147],v[16:31]\nds_read_b128 v[156:159],v219 offset:17408\nv_exp_f32 v96,v96\n" \
"v_add_f32 v216,v216,v127\nv_cvt_pk_bf16_f32 v151,v126,v127\nv_mfma_f32_32x32x16_bf16 v[32:47],v[192:195],v[144:147],v[32:47]\n" \
"ds_read_b128 v[160:163],v219 offset:18432\nv_exp_f32 v97,v97\nv_add_f32 v216,v216,v96\ns_waitcnt lgkmcnt(6)\n" \
"v_mfma_f32_32x32x16_bf16 v[48:63],v[196:199],v[144:147],v[48:63]\nds_read_b128 v[164:167],v219 offset:19456\nv_exp_f32 v98,v98\n" \
"v_add_f32 v216,v216,v97\nv_cvt_pk_bf16_f32 v144,v96,v97\nv_mfma_f32_32x32x16_bf16 v[0:15],v[200:203],v[148:151],v[0:15]\n" \
"ds_read_b128 v[168:171],v219 offset:20480\nv_exp_f32 v99,v99\nv_add_f32 v216,v216,v98\nv_mov_b32 v218,v217\ns_waitcnt lgkmcnt(6)\n" \
"v_mfma_f32_32x32x16_bf16 v[16:31],v[204:207],v[148:151],v[16:31]\nds_read_b128 v[172:175],v219 offset:21504\nv_exp_f32 v100,v100\n" \
"v_add_f32 v216,v216,v99\nv_cvt_pk_bf16_f32 v145,v98,v99\ns_sub_u32 s40,s40,1\nv_mfma_f32_32x32x16_bf16 v[32:47],v[208:211],v[148:151],v[32:47]\n" \
"ds_read_b128 v[176:179],v219 offset:22528\nv_exp_f32 v101,v101\nv_add_f32 v216,v216,v100\ns_cmp_ge_i32 s40,s69\ns_waitcnt lgkmcnt(7)\n" \
"v_mfma_f32_32x32x16_bf16 v[48:63],v[212:215],v[148:151],v[48:63]\nds_read_b128 v[180:183],v219 offset:23552\nv_exp_f32 v102,v102\n" \
"v_add_f32 v216,v216,v101\nv_cvt_pk_bf16_f32 v146,v100,v101\nv_mov_b32 v217,v219\ns_cbranch_scc1 14b\n15:\nv_lshrrev_b32 v114,2,v248\n" \
"v_and_b32 v114,0x7c,v114\ns_lshl_b32 s49,s73,7\ns_add_u32 s49,s49,73662464\nbuffer_load_dword v112,v114,%[sws],s49 offen\n" \
"buffer_load_dword v113,v114,%[sws],s82 offen\nds_read_b128 v[184:187],v218 offset:8192\nv_exp_f32 v103,v103\nv_add_f32 v216,v216,v102\n" \
"ds_read_b128 v[188:191],v218 offset:9216\nv_exp_f32 v104,v104\nv_add_f32 v216,v216,v103\nv_cvt_pk_bf16_f32 v147,v102,v103\n" \
"ds_read_b128 v[192:195],v218 offset:10240\nv_exp_f32 v105,v105\nv_add_f32 v216,v216,v104\nds_read_b128 v[196:199],v218 offset:11264\n" \
"v_exp_f32 v106,v106\nv_add_f32 v216,v216,v105\nv_cvt_pk_bf16_f32 v148,v104,v105\nds_read_b128 v[200:203],v218 offset:12288\nv_exp_f32 v107,v107\n" \
"v_add_f32 v216,v216,v106\nds_read_b128 v[204:207],v218 offset:13312\nv_exp_f32 v108,v108\nv_add_f32 v216,v216,v107\nv_cvt_pk_bf16_f32 v149,v106,v107\n" \
"ds_read_b128 v[208:211],v218 offset:14336\nv_exp_f32 v109,v109\nv_add_f32 v216,v216,v108\nds_read_b128 v[212:215],v218 offset:15360\n" \
"v_exp_f32 v110,v110\nv_add_f32 v216,v216,v109\nv_cvt_pk_bf16_f32 v150,v108,v109\ns_waitcnt lgkmcnt(7)\n" \
"v_mfma_f32_32x32x16_bf16 v[0:15],v[184:187],v[144:147],v[0:15]\nv_exp_f32 v111,v111\nv_add_f32 v216,v216,v110\ns_waitcnt lgkmcnt(5)\n" \
"v_mfma_f32_32x32x16_bf16 v[16:31],v[188:191],v[144:147],v[16:31]\nv_add_f32 v216,v216,v111\nv_cvt_pk_bf16_f32 v151,v110,v111\n" \
"v_mfma_f32_32x32x16_bf16 v[32:47],v[192:195],v[144:147],v[32:47]\ns_waitcnt lgkmcnt(3)\n" \
"v_mfma_f32_32x32x16_bf16 v[48:63],v[196:199],v[144:147],v[48:63]\nv_mfma_f32_32x32x16_bf16 v[0:15],v[200:203],v[148:151],v[0:15]\ns_waitcnt lgkmcnt(1)\n" \
"v_mfma_f32_32x32x16_bf16 v[16:31],v[204:207],v[148:151],v[16:31]\nv_mfma_f32_32x32x16_bf16 v[32:47],v[208:211],v[148:151],v[32:47]\n" \
"s_waitcnt lgkmcnt(0)\nv_mfma_f32_32x32x16_bf16 v[48:63],v[212:215],v[148:151],v[48:63]\ns_waitcnt vmcnt(0) lgkmcnt(0)\ns_nop 7\ns_nop 7\n"

#define K12A_LOADER \
"s_mov_b32 s59,s66\ns_mov_b32 s52,s64\ns_add_u32 s53,s64,8192\ns_add_u32 s54,s64,16384\ns_add_u32 s55,s64,24576\ns_mov_b32 s56,s65\n" \
"s_add_u32 s57,s65,32768\ns_and_b32 s50,s66,0xffff\ns_add_u32 m0,s50,65536\ns_add_u32 s49,s67,0\nbuffer_load_dwordx4 v249,%[sq],s49 offen lds\n" \
"s_add_u32 m0,m0,1024\ns_add_u32 s49,s49,32768\nbuffer_load_dwordx4 v249,%[sq],s49 offen lds\ns_add_u32 m0,m0,1024\ns_add_u32 s49,s49,32768\n" \
"buffer_load_dwordx4 v249,%[sq],s49 offen lds\ns_add_u32 m0,m0,1024\ns_add_u32 s49,s49,32768\nbuffer_load_dwordx4 v249,%[sq],s49 offen lds\n" \
"s_add_u32 s58,s76,0\nbuffer_load_dwordx4 v[96:99],v248,%[sws],s58 offen offset:0\nbuffer_load_dwordx4 v[100:103],v248,%[sws],s58 offen offset:1024\n" \
"buffer_load_dwordx4 v[104:107],v248,%[sws],s58 offen offset:2048\nbuffer_load_dwordx4 v[108:111],v248,%[sws],s58 offen offset:3072\n" \
"s_add_u32 s58,s76,4096\nbuffer_load_dwordx4 v[112:115],v248,%[sws],s58 offen offset:0\n" \
"buffer_load_dwordx4 v[116:119],v248,%[sws],s58 offen offset:1024\nbuffer_load_dwordx4 v[120:123],v248,%[sws],s58 offen offset:2048\n" \
"buffer_load_dwordx4 v[124:127],v248,%[sws],s58 offen offset:3072\nv_add_u32 v252,65536,v250\nv_add_u32 v255,98304,v250\ns_waitcnt vmcnt(0)\ns_barrier\n" \
"ds_read_b64_tr_b16 v[64:65],v252 offset:0\nds_read_b64_tr_b16 v[66:67],v252 offset:1024\nds_read_b64_tr_b16 v[68:69],v252 offset:256\n" \
"ds_read_b64_tr_b16 v[70:71],v252 offset:1280\nds_read_b64_tr_b16 v[72:73],v252 offset:512\nds_read_b64_tr_b16 v[74:75],v252 offset:1536\n" \
"ds_read_b64_tr_b16 v[76:77],v252 offset:768\nds_read_b64_tr_b16 v[78:79],v252 offset:1792\nds_read_b64_tr_b16 v[80:81],v252 offset:4096\n" \
"ds_read_b64_tr_b16 v[82:83],v252 offset:5120\nds_read_b64_tr_b16 v[84:85],v252 offset:4352\nds_read_b64_tr_b16 v[86:87],v252 offset:5376\n" \
"s_waitcnt lgkmcnt(10)\nv_mfma_f32_32x32x16_bf16 v[0:15],v[64:67],v[96:99],0\nds_read_b64_tr_b16 v[88:89],v252 offset:4608\n" \
"ds_read_b64_tr_b16 v[90:91],v252 offset:5632\ns_add_u32 m0,s50,98304\ns_add_u32 s49,s67,1048576\nbuffer_load_dwordx4 v249,%[sq],s49 offen lds\n" \
"s_waitcnt lgkmcnt(8)\nv_mfma_f32_32x32x16_bf16 v[16:31],v[68:71],v[96:99],0\nds_read_b64_tr_b16 v[92:93],v252 offset:4864\n" \
"ds_read_b64_tr_b16 v[94:95],v252 offset:5888\ns_add_u32 m0,m0,1024\ns_add_u32 s49,s49,32768\nbuffer_load_dwordx4 v249,%[sq],s49 offen lds\n" \
"v_mfma_f32_32x32x16_bf16 v[32:47],v[72:75],v[96:99],0\nds_read_b64_tr_b16 v[64:65],v252 offset:8192\nds_read_b64_tr_b16 v[66:67],v252 offset:9216\n" \
"s_add_u32 m0,m0,1024\ns_add_u32 s49,s49,32768\nbuffer_load_dwordx4 v249,%[sq],s49 offen lds\ns_waitcnt lgkmcnt(8)\n" \
"v_mfma_f32_32x32x16_bf16 v[48:63],v[76:79],v[96:99],0\nds_read_b64_tr_b16 v[68:69],v252 offset:8448\nds_read_b64_tr_b16 v[70:71],v252 offset:9472\n" \
"s_add_u32 m0,m0,1024\ns_add_u32 s49,s49,32768\nbuffer_load_dwordx4 v249,%[sq],s49 offen lds\n" \
"v_mfma_f32_32x32x16_bf16 v[0:15],v[80:83],v[100:103],v[0:15]\nds_read_b64_tr_b16 v[72:73],v252 offset:8704\n" \
"ds_read_b64_tr_b16 v[74:75],v252 offset:9728\ns_add_u32 s58,s76,8192\nbuffer_load_dwordx4 v[128:131],v248,%[sws],s58 offen offset:0\n" \
"s_waitcnt lgkmcnt(8)\nv_mfma_f32_32x32x16_bf16 v[16:31],v[84:87],v[100:103],v[16:31]\nds_read_b64_tr_b16 v[76:77],v252 offset:8960\n" \
"ds_read_b64_tr_b16 v[78:79],v252 offset:9984\nbuffer_load_dwordx4 v[132:135],v248,%[sws],s58 offen offset:1024\n" \
"v_mfma_f32_32x32x16_bf16 v[32:47],v[88:91],v[100:103],v[32:47]\nds_read_b64_tr_b16 v[80:81],v252 offset:12288\n" \
"ds_read_b64_tr_b16 v[82:83],v252 offset:13312\nbuffer_load_dwordx4 v[136:139],v248,%[sws],s58 offen offset:2048\ns_waitcnt lgkmcnt(8)\n" \
"v_mfma_f32_32x32x16_bf16 v[48:63],v[92:95],v[100:103],v[48:63]\nds_read_b64_tr_b16 v[84:85],v252 offset:12544\n" \
"ds_read_b64_tr_b16 v[86:87],v252 offset:13568\nbuffer_load_dwordx4 v[140:143],v248,%[sws],s58 offen offset:3072\n" \
"v_mfma_f32_32x32x16_bf16 v[0:15],v[64:67],v[104:107],v[0:15]\nds_read_b64_tr_b16 v[88:89],v252 offset:12800\n" \
"ds_read_b64_tr_b16 v[90:91],v252 offset:13824\ns_add_u32 s58,s76,12288\nbuffer_load_dwordx4 v[144:147],v248,%[sws],s58 offen offset:0\n" \
"s_waitcnt lgkmcnt(8)\nv_mfma_f32_32x32x16_bf16 v[16:31],v[68:71],v[104:107],v[16:31]\nds_read_b64_tr_b16 v[92:93],v252 offset:13056\n" \
"ds_read_b64_tr_b16 v[94:95],v252 offset:14080\nbuffer_load_dwordx4 v[148:151],v248,%[sws],s58 offen offset:1024\n" \
"v_mfma_f32_32x32x16_bf16 v[32:47],v[72:75],v[104:107],v[32:47]\nds_read_b64_tr_b16 v[64:65],v252 offset:16384\n" \
"ds_read_b64_tr_b16 v[66:67],v252 offset:17408\nbuffer_load_dwordx4 v[152:155],v248,%[sws],s58 offen offset:2048\ns_waitcnt lgkmcnt(8)\n" \
"v_mfma_f32_32x32x16_bf16 v[48:63],v[76:79],v[104:107],v[48:63]\nds_read_b64_tr_b16 v[68:69],v252 offset:16640\n" \
"ds_read_b64_tr_b16 v[70:71],v252 offset:17664\nbuffer_load_dwordx4 v[156:159],v248,%[sws],s58 offen offset:3072\n" \
"v_mfma_f32_32x32x16_bf16 v[0:15],v[80:83],v[108:111],v[0:15]\nds_read_b64_tr_b16 v[72:73],v252 offset:16896\n" \
"ds_read_b64_tr_b16 v[74:75],v252 offset:17920\ns_add_u32 m0,s50,0\ns_add_u32 s49,s67,2097152\nbuffer_load_dwordx4 v249,%[sq],s49 offen lds\n" \
"s_waitcnt lgkmcnt(8)\nv_mfma_f32_32x32x16_bf16 v[16:31],v[84:87],v[108:111],v[16:31]\nds_read_b64_tr_b16 v[76:77],v252 offset:17152\n" \
"ds_read_b64_tr_b16 v[78:79],v252 offset:18176\ns_add_u32 m0,m0,1024\ns_add_u32 s49,s49,32768\nbuffer_load_dwordx4 v249,%[sq],s49 offen lds\n" \
"v_mfma_f32_32x32x16_bf16 v[32:47],v[88:91],v[108:111],v[32:47]\nds_read_b64_tr_b16 v[80:81],v252 offset:20480\n" \
"ds_read_b64_tr_b16 v[82:83],v252 offset:21504\ns_add_u32 m0,m0,1024\ns_add_u32 s49,s49,32768\nbuffer_load_dwordx4 v249,%[sq],s49 offen lds\n" \
"s_waitcnt lgkmcnt(8)\nv_mfma_f32_32x32x16_bf16 v[48:63],v[92:95],v[108:111],v[48:63]\nds_read_b64_tr_b16 v[84:85],v252 offset:20736\n" \
"ds_read_b64_tr_b16 v[86:87],v252 offset:21760\ns_add_u32 m0,m0,1024\ns_add_u32 s49,s49,32768\nbuffer_load_dwordx4 v249,%[sq],s49 offen lds\n" \
"v_mfma_f32_32x32x16_bf16 v[0:15],v[64:67],v[112:115],v[0:15]\nds_read_b64_tr_b16 v[88:89],v252 offset:20992\n" \
"ds_read_b64_tr_b16 v[90:91],v252 offset:22016\ns_add_u32 s58,s76,16384\nbuffer_load_dwordx4 v[160:163],v248,%[sws],s58 offen offset:0\n" \
"s_waitcnt lgkmcnt(8)\nv_mfma_f32_32x32x16_bf16 v[16:31],v[68:71],v[112:115],v[16:31]\nds_read_b64_tr_b16 v[92:93],v252 offset:21248\n" \
"ds_read_b64_tr_b16 v[94:95],v252 offset:22272\nbuffer_load_dwordx4 v[164:167],v248,%[sws],s58 offen offset:1024\n" \
"v_mfma_f32_32x32x16_bf16 v[32:47],v[72:75],v[112:115],v[32:47]\nds_read_b64_tr_b16 v[64:65],v252 offset:24576\n" \
"ds_read_b64_tr_b16 v[66:67],v252 offset:25600\nbuffer_load_dwordx4 v[168:171],v248,%[sws],s58 offen offset:2048\ns_waitcnt lgkmcnt(8)\n" \
"v_mfma_f32_32x32x16_bf16 v[48:63],v[76:79],v[112:115],v[48:63]\nds_read_b64_tr_b16 v[68:69],v252 offset:24832\n" \
"ds_read_b64_tr_b16 v[70:71],v252 offset:25856\nbuffer_load_dwordx4 v[172:175],v248,%[sws],s58 offen offset:3072\n" \
"v_mfma_f32_32x32x16_bf16 v[0:15],v[80:83],v[116:119],v[0:15]\nds_read_b64_tr_b16 v[72:73],v252 offset:25088\n" \
"ds_read_b64_tr_b16 v[74:75],v252 offset:26112\ns_add_u32 s58,s76,20480\nbuffer_load_dwordx4 v[176:179],v248,%[sws],s58 offen offset:0\n" \
"s_waitcnt lgkmcnt(8)\nv_mfma_f32_32x32x16_bf16 v[16:31],v[84:87],v[116:119],v[16:31]\nds_read_b64_tr_b16 v[76:77],v252 offset:25344\n" \
"ds_read_b64_tr_b16 v[78:79],v252 offset:26368\nbuffer_load_dwordx4 v[180:183],v248,%[sws],s58 offen offset:1024\n" \
"v_mfma_f32_32x32x16_bf16 v[32:47],v[88:91],v[116:119],v[32:47]\nds_read_b64_tr_b16 v[80:81],v252 offset:28672\n" \
"ds_read_b64_tr_b16 v[82:83],v252 offset:29696\nbuffer_load_dwordx4 v[184:187],v248,%[sws],s58 offen offset:2048\ns_waitcnt lgkmcnt(8)\n" \
"v_mfma_f32_32x32x16_bf16 v[48:63],v[92:95],v[116:119],v[48:63]\nds_read_b64_tr_b16 v[84:85],v252 offset:28928\n" \
"ds_read_b64_tr_b16 v[86:87],v252 offset:29952\nbuffer_load_dwordx4 v[188:191],v248,%[sws],s58 offen offset:3072\n" \
"v_mfma_f32_32x32x16_bf16 v[0:15],v[64:67],v[120:123],v[0:15]\nds_read_b64_tr_b16 v[88:89],v252 offset:29184\n" \
"ds_read_b64_tr_b16 v[90:91],v252 offset:30208\ns_add_u32 m0,s50,131072\ns_add_u32 s49,s67,3145728\nbuffer_load_dwordx4 v249,%[sq],s49 offen lds\n" \
"s_waitcnt lgkmcnt(8)\nv_mfma_f32_32x32x16_bf16 v[16:31],v[68:71],v[120:123],v[16:31]\ns_waitcnt vmcnt(13)\ns_barrier\n" \
"ds_read_b64_tr_b16 v[92:93],v252 offset:29440\nds_read_b64_tr_b16 v[94:95],v252 offset:30464\ns_add_u32 m0,m0,1024\ns_add_u32 s49,s49,32768\n" \
"buffer_load_dwordx4 v249,%[sq],s49 offen lds\nv_mfma_f32_32x32x16_bf16 v[32:47],v[72:75],v[120:123],v[32:47]\n" \
"ds_read_b64_tr_b16 v[64:65],v255 offset:0\nds_read_b64_tr_b16 v[66:67],v255 offset:1024\ns_add_u32 m0,m0,1024\ns_add_u32 s49,s49,32768\n" \
"buffer_load_dwordx4 v249,%[sq],s49 offen lds\ns_waitcnt lgkmcnt(8)\nv_mfma_f32_32x32x16_bf16 v[48:63],v[76:79],v[120:123],v[48:63]\n" \
"ds_read_b64_tr_b16 v[68:69],v255 offset:256\nds_read_b64_tr_b16 v[70:71],v255 offset:1280\nv_mov_b32 v252,v250\ns_add_u32 m0,m0,1024\n" \
"s_add_u32 s49,s49,32768\nbuffer_load_dwordx4 v249,%[sq],s49 offen lds\nv_mfma_f32_32x32x16_bf16 v[0:15],v[80:83],v[124:127],v[0:15]\n" \
"ds_read_b64_tr_b16 v[72:73],v255 offset:512\nds_read_b64_tr_b16 v[74:75],v255 offset:1536\ns_add_u32 s58,s76,24576\n" \
"buffer_load_dwordx4 v[192:195],v248,%[sws],s58 offen offset:0\ns_waitcnt lgkmcnt(8)\nv_mfma_f32_32x32x16_bf16 v[16:31],v[84:87],v[124:127],v[16:31]\n" \
"ds_read_b64_tr_b16 v[76:77],v255 offset:768\nds_read_b64_tr_b16 v[78:79],v255 offset:1792\n" \
"buffer_load_dwordx4 v[196:199],v248,%[sws],s58 offen offset:1024\nv_mfma_f32_32x32x16_bf16 v[32:47],v[88:91],v[124:127],v[32:47]\n" \
"ds_read_b64_tr_b16 v[80:81],v255 offset:4096\nds_read_b64_tr_b16 v[82:83],v255 offset:5120\n" \
"buffer_load_dwordx4 v[200:203],v248,%[sws],s58 offen offset:2048\ns_waitcnt lgkmcnt(8)\nv_mfma_f32_32x32x16_bf16 v[48:63],v[92:95],v[124:127],v[48:63]\n" \
"ds_read_b64_tr_b16 v[84:85],v255 offset:4352\nds_read_b64_tr_b16 v[86:87],v255 offset:5376\n" \
"buffer_load_dwordx4 v[204:207],v248,%[sws],s58 offen offset:3072\nv_mfma_f32_32x32x16_bf16 v[0:15],v[64:67],v[128:131],v[0:15]\n" \
"ds_read_b64_tr_b16 v[88:89],v255 offset:4608\nds_read_b64_tr_b16 v[90:91],v255 offset:5632\ns_add_u32 s58,s76,28672\n" \
"buffer_load_dwordx4 v[208:211],v248,%[sws],s58 offen offset:0\ns_waitcnt lgkmcnt(8)\nv_mfma_f32_32x32x16_bf16 v[16:31],v[68:71],v[128:131],v[16:31]\n" \
"ds_read_b64_tr_b16 v[92:93],v255 offset:4864\nds_read_b64_tr_b16 v[94:95],v255 offset:5888\n" \
"buffer_load_dwordx4 v[212:215],v248,%[sws],s58 offen offset:1024\nv_mfma_f32_32x32x16_bf16 v[32:47],v[72:75],v[128:131],v[32:47]\n" \
"ds_read_b64_tr_b16 v[64:65],v255 offset:8192\nds_read_b64_tr_b16 v[66:67],v255 offset:9216\n" \
"buffer_load_dwordx4 v[216:219],v248,%[sws],s58 offen offset:2048\ns_waitcnt lgkmcnt(8)\nv_mfma_f32_32x32x16_bf16 v[48:63],v[76:79],v[128:131],v[48:63]\n" \
"ds_read_b64_tr_b16 v[68:69],v255 offset:8448\nds_read_b64_tr_b16 v[70:71],v255 offset:9472\n" \
"buffer_load_dwordx4 v[220:223],v248,%[sws],s58 offen offset:3072\nv_mfma_f32_32x32x16_bf16 v[0:15],v[80:83],v[132:135],v[0:15]\n" \
"ds_read_b64_tr_b16 v[72:73],v255 offset:8704\nds_read_b64_tr_b16 v[74:75],v255 offset:9728\ns_waitcnt lgkmcnt(8)\n" \
"v_mfma_f32_32x32x16_bf16 v[16:31],v[84:87],v[132:135],v[16:31]\nds_read_b64_tr_b16 v[76:77],v255 offset:8960\n" \
"ds_read_b64_tr_b16 v[78:79],v255 offset:9984\nv_mfma_f32_32x32x16_bf16 v[32:47],v[88:91],v[132:135],v[32:47]\n" \
"ds_read_b64_tr_b16 v[80:81],v255 offset:12288\nds_read_b64_tr_b16 v[82:83],v255 offset:13312\ns_waitcnt lgkmcnt(8)\n" \
"v_mfma_f32_32x32x16_bf16 v[48:63],v[92:95],v[132:135],v[48:63]\nds_read_b64_tr_b16 v[84:85],v255 offset:12544\n" \
"ds_read_b64_tr_b16 v[86:87],v255 offset:13568\nv_mfma_f32_32x32x16_bf16 v[0:15],v[64:67],v[136:139],v[0:15]\n" \
"ds_read_b64_tr_b16 v[88:89],v255 offset:12800\nds_read_b64_tr_b16 v[90:91],v255 offset:13824\ns_waitcnt lgkmcnt(8)\n" \
"v_mfma_f32_32x32x16_bf16 v[16:31],v[68:71],v[136:139],v[16:31]\nds_read_b64_tr_b16 v[92:93],v255 offset:13056\n" \
"ds_read_b64_tr_b16 v[94:95],v255 offset:14080\nv_mfma_f32_32x32x16_bf16 v[32:47],v[72:75],v[136:139],v[32:47]\n" \
"ds_read_b64_tr_b16 v[64:65],v255 offset:16384\nds_read_b64_tr_b16 v[66:67],v255 offset:17408\ns_waitcnt lgkmcnt(8)\n" \
"v_mfma_f32_32x32x16_bf16 v[48:63],v[76:79],v[136:139],v[48:63]\nds_read_b64_tr_b16 v[68:69],v255 offset:16640\n" \
"ds_read_b64_tr_b16 v[70:71],v255 offset:17664\nv_mfma_f32_32x32x16_bf16 v[0:15],v[80:83],v[140:143],v[0:15]\n" \
"ds_read_b64_tr_b16 v[72:73],v255 offset:16896\nds_read_b64_tr_b16 v[74:75],v255 offset:17920\ns_waitcnt lgkmcnt(8)\n" \
"v_mfma_f32_32x32x16_bf16 v[16:31],v[84:87],v[140:143],v[16:31]\nds_read_b64_tr_b16 v[76:77],v255 offset:17152\n" \
"ds_read_b64_tr_b16 v[78:79],v255 offset:18176\nv_mfma_f32_32x32x16_bf16 v[32:47],v[88:91],v[140:143],v[32:47]\n" \
"ds_read_b64_tr_b16 v[80:81],v255 offset:20480\nds_read_b64_tr_b16 v[82:83],v255 offset:21504\ns_waitcnt lgkmcnt(8)\n" \
"v_mfma_f32_32x32x16_bf16 v[48:63],v[92:95],v[140:143],v[48:63]\nds_read_b64_tr_b16 v[84:85],v255 offset:20736\n" \
"ds_read_b64_tr_b16 v[86:87],v255 offset:21760\nv_mfma_f32_32x32x16_bf16 v[0:15],v[64:67],v[144:147],v[0:15]\n" \
"ds_read_b64_tr_b16 v[88:89],v255 offset:20992\nds_read_b64_tr_b16 v[90:91],v255 offset:22016\ns_waitcnt lgkmcnt(8)\n" \
"v_mfma_f32_32x32x16_bf16 v[16:31],v[68:71],v[144:147],v[16:31]\nds_read_b64_tr_b16 v[92:93],v255 offset:21248\n" \
"ds_read_b64_tr_b16 v[94:95],v255 offset:22272\nv_mfma_f32_32x32x16_bf16 v[32:47],v[72:75],v[144:147],v[32:47]\n" \
"ds_read_b64_tr_b16 v[64:65],v255 offset:24576\nds_read_b64_tr_b16 v[66:67],v255 offset:25600\ns_waitcnt lgkmcnt(8)\n" \
"v_mfma_f32_32x32x16_bf16 v[48:63],v[76:79],v[144:147],v[48:63]\nds_read_b64_tr_b16 v[68:69],v255 offset:24832\n" \
"ds_read_b64_tr_b16 v[70:71],v255 offset:25856\nv_mfma_f32_32x32x16_bf16 v[0:15],v[80:83],v[148:151],v[0:15]\n" \
"ds_read_b64_tr_b16 v[72:73],v255 offset:25088\nds_read_b64_tr_b16 v[74:75],v255 offset:26112\ns_waitcnt lgkmcnt(8)\n" \
"v_mfma_f32_32x32x16_bf16 v[16:31],v[84:87],v[148:151],v[16:31]\nds_read_b64_tr_b16 v[76:77],v255 offset:25344\n" \
"ds_read_b64_tr_b16 v[78:79],v255 offset:26368\nv_mfma_f32_32x32x16_bf16 v[32:47],v[88:91],v[148:151],v[32:47]\n" \
"ds_read_b64_tr_b16 v[80:81],v255 offset:28672\nds_read_b64_tr_b16 v[82:83],v255 offset:29696\ns_waitcnt lgkmcnt(8)\n" \
"v_mfma_f32_32x32x16_bf16 v[48:63],v[92:95],v[148:151],v[48:63]\nds_read_b64_tr_b16 v[84:85],v255 offset:28928\n" \
"ds_read_b64_tr_b16 v[86:87],v255 offset:29952\nv_mfma_f32_32x32x16_bf16 v[0:15],v[64:67],v[152:155],v[0:15]\n" \
"ds_read_b64_tr_b16 v[88:89],v255 offset:29184\nds_read_b64_tr_b16 v[90:91],v255 offset:30208\ns_waitcnt lgkmcnt(8)\n" \
"v_mfma_f32_32x32x16_bf16 v[16:31],v[68:71],v[152:155],v[16:31]\ns_waitcnt vmcnt(12)\ns_barrier\nds_read_b64_tr_b16 v[92:93],v255 offset:29440\n" \
"ds_read_b64_tr_b16 v[94:95],v255 offset:30464\nv_mfma_f32_32x32x16_bf16 v[32:47],v[72:75],v[152:155],v[32:47]\n" \
"ds_read_b64_tr_b16 v[64:65],v252 offset:0\nds_read_b64_tr_b16 v[66:67],v252 offset:1024\ns_waitcnt lgkmcnt(8)\n" \
"v_mfma_f32_32x32x16_bf16 v[48:63],v[76:79],v[152:155],v[48:63]\nds_read_b64_tr_b16 v[68:69],v252 offset:256\n" \
"ds_read_b64_tr_b16 v[70:71],v252 offset:1280\nv_add_u32 v255,131072,v250\nv_mfma_f32_32x32x16_bf16 v[0:15],v[80:83],v[156:159],v[0:15]\n" \
"ds_read_b64_tr_b16 v[72:73],v252 offset:512\nds_read_b64_tr_b16 v[74:75],v252 offset:1536\ns_waitcnt lgkmcnt(8)\n" \
"v_mfma_f32_32x32x16_bf16 v[16:31],v[84:87],v[156:159],v[16:31]\nds_read_b64_tr_b16 v[76:77],v252 offset:768\n" \
"ds_read_b64_tr_b16 v[78:79],v252 offset:1792\nv_mfma_f32_32x32x16_bf16 v[32:47],v[88:91],v[156:159],v[32:47]\n" \
"ds_read_b64_tr_b16 v[80:81],v252 offset:4096\nds_read_b64_tr_b16 v[82:83],v252 offset:5120\ns_waitcnt lgkmcnt(8)\n" \
"v_mfma_f32_32x32x16_bf16 v[48:63],v[92:95],v[156:159],v[48:63]\nds_read_b64_tr_b16 v[84:85],v252 offset:4352\n" \
"ds_read_b64_tr_b16 v[86:87],v252 offset:5376\nv_mfma_f32_32x32x16_bf16 v[0:15],v[64:67],v[160:163],v[0:15]\n" \
"ds_read_b64_tr_b16 v[88:89],v252 offset:4608\nds_read_b64_tr_b16 v[90:91],v252 offset:5632\ns_waitcnt lgkmcnt(8)\n" \
"v_mfma_f32_32x32x16_bf16 v[16:31],v[68:71],v[160:163],v[16:31]\nds_read_b64_tr_b16 v[92:93],v252 offset:4864\n" \
"ds_read_b64_tr_b16 v[94:95],v252 offset:5888\nv_mfma_f32_32x32x16_bf16 v[32:47],v[72:75],v[160:163],v[32:47]\n" \
"ds_read_b64_tr_b16 v[64:65],v252 offset:8192\nds_read_b64_tr_b16 v[66:67],v252 offset:9216\ns_waitcnt lgkmcnt(8)\n" \
"v_mfma_f32_32x32x16_bf16 v[48:63],v[76:79],v[160:163],v[48:63]\nds_read_b64_tr_b16 v[68:69],v252 offset:8448\n" \
"ds_read_b64_tr_b16 v[70:71],v252 offset:9472\nv_mfma_f32_32x32x16_bf16 v[0:15],v[80:83],v[164:167],v[0:15]\n" \
"ds_read_b64_tr_b16 v[72:73],v252 offset:8704\nds_read_b64_tr_b16 v[74:75],v252 offset:9728\ns_waitcnt lgkmcnt(8)\n" \
"v_mfma_f32_32x32x16_bf16 v[16:31],v[84:87],v[164:167],v[16:31]\nds_read_b64_tr_b16 v[76:77],v252 offset:8960\n" \
"ds_read_b64_tr_b16 v[78:79],v252 offset:9984\nv_mfma_f32_32x32x16_bf16 v[32:47],v[88:91],v[164:167],v[32:47]\n" \
"ds_read_b64_tr_b16 v[80:81],v252 offset:12288\nds_read_b64_tr_b16 v[82:83],v252 offset:13312\ns_waitcnt lgkmcnt(8)\n" \
"v_mfma_f32_32x32x16_bf16 v[48:63],v[92:95],v[164:167],v[48:63]\nds_read_b64_tr_b16 v[84:85],v252 offset:12544\n" \
"ds_read_b64_tr_b16 v[86:87],v252 offset:13568\nv_mfma_f32_32x32x16_bf16 v[0:15],v[64:67],v[168:171],v[0:15]\n" \
"ds_read_b64_tr_b16 v[88:89],v252 offset:12800\nds_read_b64_tr_b16 v[90:91],v252 offset:13824\ns_waitcnt lgkmcnt(8)\n" \
"v_mfma_f32_32x32x16_bf16 v[16:31],v[68:71],v[168:171],v[16:31]\nds_read_b64_tr_b16 v[92:93],v252 offset:13056\n" \
"ds_read_b64_tr_b16 v[94:95],v252 offset:14080\nv_mfma_f32_32x32x16_bf16 v[32:47],v[72:75],v[168:171],v[32:47]\n" \
"ds_read_b64_tr_b16 v[64:65],v252 offset:16384\nds_read_b64_tr_b16 v[66:67],v252 offset:17408\ns_waitcnt lgkmcnt(8)\n" \
"v_mfma_f32_32x32x16_bf16 v[48:63],v[76:79],v[168:171],v[48:63]\nds_read_b64_tr_b16 v[68:69],v252 offset:16640\n" \
"ds_read_b64_tr_b16 v[70:71],v252 offset:17664\nv_mfma_f32_32x32x16_bf16 v[0:15],v[80:83],v[172:175],v[0:15]\n" \
"ds_read_b64_tr_b16 v[72:73],v252 offset:16896\nds_read_b64_tr_b16 v[74:75],v252 offset:17920\ns_waitcnt lgkmcnt(8)\n" \
"v_mfma_f32_32x32x16_bf16 v[16:31],v[84:87],v[172:175],v[16:31]\nds_read_b64_tr_b16 v[76:77],v252 offset:17152\n" \
"ds_read_b64_tr_b16 v[78:79],v252 offset:18176\nv_mfma_f32_32x32x16_bf16 v[32:47],v[88:91],v[172:175],v[32:47]\n" \
"ds_read_b64_tr_b16 v[80:81],v252 offset:20480\nds_read_b64_tr_b16 v[82:83],v252 offset:21504\ns_waitcnt lgkmcnt(8)\n" \
"v_mfma_f32_32x32x16_bf16 v[48:63],v[92:95],v[172:175],v[48:63]\nds_read_b64_tr_b16 v[84:85],v252 offset:20736\n" \
"ds_read_b64_tr_b16 v[86:87],v252 offset:21760\nv_mfma_f32_32x32x16_bf16 v[0:15],v[64:67],v[176:179],v[0:15]\n" \
"ds_read_b64_tr_b16 v[88:89],v252 offset:20992\nds_read_b64_tr_b16 v[90:91],v252 offset:22016\ns_waitcnt lgkmcnt(8)\n" \
"v_mfma_f32_32x32x16_bf16 v[16:31],v[68:71],v[176:179],v[16:31]\nds_read_b64_tr_b16 v[92:93],v252 offset:21248\n" \
"ds_read_b64_tr_b16 v[94:95],v252 offset:22272\nv_mfma_f32_32x32x16_bf16 v[32:47],v[72:75],v[176:179],v[32:47]\n" \
"ds_read_b64_tr_b16 v[64:65],v252 offset:24576\nds_read_b64_tr_b16 v[66:67],v252 offset:25600\ns_waitcnt lgkmcnt(8)\n" \
"v_mfma_f32_32x32x16_bf16 v[48:63],v[76:79],v[176:179],v[48:63]\nds_read_b64_tr_b16 v[68:69],v252 offset:24832\n" \
"ds_read_b64_tr_b16 v[70:71],v252 offset:25856\nv_mfma_f32_32x32x16_bf16 v[0:15],v[80:83],v[180:183],v[0:15]\n" \
"ds_read_b64_tr_b16 v[72:73],v252 offset:25088\nds_read_b64_tr_b16 v[74:75],v252 offset:26112\ns_waitcnt lgkmcnt(8)\n" \
"v_mfma_f32_32x32x16_bf16 v[16:31],v[84:87],v[180:183],v[16:31]\nds_read_b64_tr_b16 v[76:77],v252 offset:25344\n" \
"ds_read_b64_tr_b16 v[78:79],v252 offset:26368\nv_mfma_f32_32x32x16_bf16 v[32:47],v[88:91],v[180:183],v[32:47]\n" \
"ds_read_b64_tr_b16 v[80:81],v252 offset:28672\nds_read_b64_tr_b16 v[82:83],v252 offset:29696\ns_waitcnt lgkmcnt(8)\n" \
"v_mfma_f32_32x32x16_bf16 v[48:63],v[92:95],v[180:183],v[48:63]\nds_read_b64_tr_b16 v[84:85],v252 offset:28928\n" \
"ds_read_b64_tr_b16 v[86:87],v252 offset:29952\nv_mfma_f32_32x32x16_bf16 v[0:15],v[64:67],v[184:187],v[0:15]\n" \
"ds_read_b64_tr_b16 v[88:89],v252 offset:29184\nds_read_b64_tr_b16 v[90:91],v252 offset:30208\ns_waitcnt lgkmcnt(8)\n" \
"v_mfma_f32_32x32x16_bf16 v[16:31],v[68:71],v[184:187],v[16:31]\ns_waitcnt vmcnt(0)\ns_barrier\nds_read_b64_tr_b16 v[92:93],v252 offset:29440\n" \
"ds_read_b64_tr_b16 v[94:95],v252 offset:30464\nv_mfma_f32_32x32x16_bf16 v[32:47],v[72:75],v[184:187],v[32:47]\n" \
"ds_read_b64_tr_b16 v[64:65],v255 offset:0\nds_read_b64_tr_b16 v[66:67],v255 offset:1024\ns_mov_b32 m0,s59\ns_nop 0\n" \
"buffer_load_dwordx4 v228,%[sx],s52 offen lds\ns_waitcnt lgkmcnt(8)\nv_mfma_f32_32x32x16_bf16 v[48:63],v[76:79],v[184:187],v[48:63]\n" \
"ds_read_b64_tr_b16 v[68:69],v255 offset:256\nds_read_b64_tr_b16 v[70:71],v255 offset:1280\ns_add_u32 m0,m0,1024\ns_nop 0\n" \
"buffer_load_dwordx4 v228,%[sx],s53 offen lds\nv_mfma_f32_32x32x16_bf16 v[0:15],v[80:83],v[188:191],v[0:15]\n" \
"ds_read_b64_tr_b16 v[72:73],v255 offset:512\nds_read_b64_tr_b16 v[74:75],v255 offset:1536\ns_add_u32 m0,m0,1024\ns_nop 0\n" \
"buffer_load_dwordx4 v229,%[sx],s54 offen lds\ns_waitcnt lgkmcnt(8)\nv_mfma_f32_32x32x16_bf16 v[16:31],v[84:87],v[188:191],v[16:31]\n" \
"ds_read_b64_tr_b16 v[76:77],v255 offset:768\nds_read_b64_tr_b16 v[78:79],v255 offset:1792\ns_add_u32 m0,m0,1024\ns_nop 0\n" \
"buffer_load_dwordx4 v229,%[sx],s55 offen lds\nv_mfma_f32_32x32x16_bf16 v[32:47],v[88:91],v[188:191],v[32:47]\n" \
"ds_read_b64_tr_b16 v[80:81],v255 offset:4096\nds_read_b64_tr_b16 v[82:83],v255 offset:5120\ns_add_u32 m0,m0,29696\ns_nop 0\n" \
"buffer_load_dwordx4 v230,%[sk],s56 offen lds\ns_waitcnt lgkmcnt(8)\nv_mfma_f32_32x32x16_bf16 v[48:63],v[92:95],v[188:191],v[48:63]\n" \
"ds_read_b64_tr_b16 v[84:85],v255 offset:4352\nds_read_b64_tr_b16 v[86:87],v255 offset:5376\ns_add_u32 m0,m0,1024\ns_nop 0\n" \
"buffer_load_dwordx4 v230,%[sv],s56 offen lds\nv_mfma_f32_32x32x16_bf16 v[0:15],v[64:67],v[192:195],v[0:15]\n" \
"ds_read_b64_tr_b16 v[88:89],v255 offset:4608\nds_read_b64_tr_b16 v[90:91],v255 offset:5632\ns_add_u32 m0,m0,1024\ns_nop 0\n" \
"buffer_load_dwordx4 v230,%[sk],s57 offen lds\ns_waitcnt lgkmcnt(8)\nv_mfma_f32_32x32x16_bf16 v[16:31],v[68:71],v[192:195],v[16:31]\n" \
"ds_read_b64_tr_b16 v[92:93],v255 offset:4864\nds_read_b64_tr_b16 v[94:95],v255 offset:5888\ns_add_u32 m0,m0,1024\ns_nop 0\n" \
"buffer_load_dwordx4 v230,%[sv],s57 offen lds\nv_add_u32 v228,128,v228\nv_add_u32 v229,128,v229\nv_add_u32 v230,0x80000,v230\ns_xor_b32 s59,s59,0x10000\n" \
"v_mfma_f32_32x32x16_bf16 v[32:47],v[72:75],v[192:195],v[32:47]\nds_read_b64_tr_b16 v[64:65],v255 offset:8192\n" \
"ds_read_b64_tr_b16 v[66:67],v255 offset:9216\ns_waitcnt lgkmcnt(8)\nv_mfma_f32_32x32x16_bf16 v[48:63],v[76:79],v[192:195],v[48:63]\n" \
"ds_read_b64_tr_b16 v[68:69],v255 offset:8448\nds_read_b64_tr_b16 v[70:71],v255 offset:9472\n" \
"v_mfma_f32_32x32x16_bf16 v[0:15],v[80:83],v[196:199],v[0:15]\nds_read_b64_tr_b16 v[72:73],v255 offset:8704\n" \
"ds_read_b64_tr_b16 v[74:75],v255 offset:9728\ns_barrier\ns_mov_b32 m0,s59\ns_nop 0\nbuffer_load_dwordx4 v228,%[sx],s52 offen lds\ns_waitcnt lgkmcnt(8)\n" \
"v_mfma_f32_32x32x16_bf16 v[16:31],v[84:87],v[196:199],v[16:31]\nds_read_b64_tr_b16 v[76:77],v255 offset:8960\n" \
"ds_read_b64_tr_b16 v[78:79],v255 offset:9984\ns_add_u32 m0,m0,1024\ns_nop 0\nbuffer_load_dwordx4 v228,%[sx],s53 offen lds\n" \
"v_mfma_f32_32x32x16_bf16 v[32:47],v[88:91],v[196:199],v[32:47]\nds_read_b64_tr_b16 v[80:81],v255 offset:12288\n" \
"ds_read_b64_tr_b16 v[82:83],v255 offset:13312\ns_add_u32 m0,m0,1024\ns_nop 0\nbuffer_load_dwordx4 v229,%[sx],s54 offen lds\ns_waitcnt lgkmcnt(8)\n" \
"v_mfma_f32_32x32x16_bf16 v[48:63],v[92:95],v[196:199],v[48:63]\nds_read_b64_tr_b16 v[84:85],v255 offset:12544\n" \
"ds_read_b64_tr_b16 v[86:87],v255 offset:13568\ns_add_u32 m0,m0,1024\ns_nop 0\nbuffer_load_dwordx4 v229,%[sx],s55 offen lds\n" \
"v_mfma_f32_32x32x16_bf16 v[0:15],v[64:67],v[200:203],v[0:15]\nds_read_b64_tr_b16 v[88:89],v255 offset:12800\n" \
"ds_read_b64_tr_b16 v[90:91],v255 offset:13824\ns_add_u32 m0,m0,29696\ns_nop 0\nbuffer_load_dwordx4 v230,%[sk],s56 offen lds\ns_waitcnt lgkmcnt(8)\n" \
"v_mfma_f32_32x32x16_bf16 v[16:31],v[68:71],v[200:203],v[16:31]\nds_read_b64_tr_b16 v[92:93],v255 offset:13056\n" \
"ds_read_b64_tr_b16 v[94:95],v255 offset:14080\ns_add_u32 m0,m0,1024\ns_nop 0\nbuffer_load_dwordx4 v230,%[sv],s56 offen lds\n" \
"v_mfma_f32_32x32x16_bf16 v[32:47],v[72:75],v[200:203],v[32:47]\nds_read_b64_tr_b16 v[64:65],v255 offset:16384\n" \
"ds_read_b64_tr_b16 v[66:67],v255 offset:17408\ns_add_u32 m0,m0,1024\ns_nop 0\nbuffer_load_dwordx4 v230,%[sk],s57 offen lds\ns_waitcnt lgkmcnt(8)\n" \
"v_mfma_f32_32x32x16_bf16 v[48:63],v[76:79],v[200:203],v[48:63]\nds_read_b64_tr_b16 v[68:69],v255 offset:16640\n" \
"ds_read_b64_tr_b16 v[70:71],v255 offset:17664\ns_add_u32 m0,m0,1024\ns_nop 0\nbuffer_load_dwordx4 v230,%[sv],s57 offen lds\nv_add_u32 v228,128,v228\n" \
"v_add_u32 v229,128,v229\nv_add_u32 v230,0x80000,v230\ns_xor_b32 s59,s59,0x10000\nv_mfma_f32_32x32x16_bf16 v[0:15],v[80:83],v[204:207],v[0:15]\n" \
"ds_read_b64_tr_b16 v[72:73],v255 offset:16896\nds_read_b64_tr_b16 v[74:75],v255 offset:17920\ns_waitcnt lgkmcnt(8)\n" \
"v_mfma_f32_32x32x16_bf16 v[16:31],v[84:87],v[204:207],v[16:31]\nds_read_b64_tr_b16 v[76:77],v255 offset:17152\n" \
"ds_read_b64_tr_b16 v[78:79],v255 offset:18176\nv_mfma_f32_32x32x16_bf16 v[32:47],v[88:91],v[204:207],v[32:47]\n" \
"ds_read_b64_tr_b16 v[80:81],v255 offset:20480\nds_read_b64_tr_b16 v[82:83],v255 offset:21504\ns_waitcnt lgkmcnt(8)\n" \
"v_mfma_f32_32x32x16_bf16 v[48:63],v[92:95],v[204:207],v[48:63]\nds_read_b64_tr_b16 v[84:85],v255 offset:20736\n" \
"ds_read_b64_tr_b16 v[86:87],v255 offset:21760\nv_mfma_f32_32x32x16_bf16 v[0:15],v[64:67],v[208:211],v[0:15]\n" \
"ds_read_b64_tr_b16 v[88:89],v255 offset:20992\nds_read_b64_tr_b16 v[90:91],v255 offset:22016\ns_waitcnt lgkmcnt(8)\n" \
"v_mfma_f32_32x32x16_bf16 v[16:31],v[68:71],v[208:211],v[16:31]\nds_read_b64_tr_b16 v[92:93],v255 offset:21248\n" \
"ds_read_b64_tr_b16 v[94:95],v255 offset:22272\nv_mfma_f32_32x32x16_bf16 v[32:47],v[72:75],v[208:211],v[32:47]\n" \
"ds_read_b64_tr_b16 v[64:65],v255 offset:24576\nds_read_b64_tr_b16 v[66:67],v255 offset:25600\ns_waitcnt lgkmcnt(8)\n" \
"v_mfma_f32_32x32x16_bf16 v[48:63],v[76:79],v[208:211],v[48:63]\nds_read_b64_tr_b16 v[68:69],v255 offset:24832\n" \
"ds_read_b64_tr_b16 v[70:71],v255 offset:25856\nv_mfma_f32_32x32x16_bf16 v[0:15],v[80:83],v[212:215],v[0:15]\n" \
"ds_read_b64_tr_b16 v[72:73],v255 offset:25088\nds_read_b64_tr_b16 v[74:75],v255 offset:26112\ns_waitcnt lgkmcnt(8)\n" \
"v_mfma_f32_32x32x16_bf16 v[16:31],v[84:87],v[212:215],v[16:31]\nds_read_b64_tr_b16 v[76:77],v255 offset:25344\n" \
"ds_read_b64_tr_b16 v[78:79],v255 offset:26368\nv_mfma_f32_32x32x16_bf16 v[32:47],v[88:91],v[212:215],v[32:47]\n" \
"ds_read_b64_tr_b16 v[80:81],v255 offset:28672\nds_read_b64_tr_b16 v[82:83],v255 offset:29696\ns_waitcnt lgkmcnt(8)\n" \
"v_mfma_f32_32x32x16_bf16 v[48:63],v[92:95],v[212:215],v[48:63]\nds_read_b64_tr_b16 v[84:85],v255 offset:28928\n" \
"ds_read_b64_tr_b16 v[86:87],v255 offset:29952\nv_mfma_f32_32x32x16_bf16 v[0:15],v[64:67],v[216:219],v[0:15]\n" \
"ds_read_b64_tr_b16 v[88:89],v255 offset:29184\nds_read_b64_tr_b16 v[90:91],v255 offset:30208\ns_waitcnt lgkmcnt(8)\n" \
"v_mfma_f32_32x32x16_bf16 v[16:31],v[68:71],v[216:219],v[16:31]\nds_read_b64_tr_b16 v[92:93],v255 offset:29440\n" \
"ds_read_b64_tr_b16 v[94:95],v255 offset:30464\nv_mfma_f32_32x32x16_bf16 v[32:47],v[72:75],v[216:219],v[32:47]\ns_waitcnt lgkmcnt(6)\n" \
"v_mfma_f32_32x32x16_bf16 v[48:63],v[76:79],v[216:219],v[48:63]\nv_mfma_f32_32x32x16_bf16 v[0:15],v[80:83],v[220:223],v[0:15]\ns_waitcnt lgkmcnt(2)\n" \
"v_mfma_f32_32x32x16_bf16 v[16:31],v[84:87],v[220:223],v[16:31]\nv_mfma_f32_32x32x16_bf16 v[32:47],v[88:91],v[220:223],v[32:47]\ns_waitcnt lgkmcnt(0)\n" \
"v_mfma_f32_32x32x16_bf16 v[48:63],v[92:95],v[220:223],v[48:63]\ns_nop 7\ns_nop 7\ns_mov_b32 s49,0x3e0293ee\nv_mul_f32 v0,s49,v0\nv_mul_f32 v1,s49,v1\n" \
"v_mul_f32 v2,s49,v2\nv_mul_f32 v3,s49,v3\nv_mul_f32 v4,s49,v4\nv_mul_f32 v5,s49,v5\nv_mul_f32 v6,s49,v6\nv_mul_f32 v7,s49,v7\nv_mul_f32 v8,s49,v8\n" \
"v_mul_f32 v9,s49,v9\nv_mul_f32 v10,s49,v10\nv_mul_f32 v11,s49,v11\nv_mul_f32 v12,s49,v12\nv_mul_f32 v13,s49,v13\nv_mul_f32 v14,s49,v14\n" \
"v_mul_f32 v15,s49,v15\nv_mul_f32 v16,s49,v16\nv_mul_f32 v17,s49,v17\nv_mul_f32 v18,s49,v18\nv_mul_f32 v19,s49,v19\nv_mul_f32 v20,s49,v20\n" \
"v_mul_f32 v21,s49,v21\nv_mul_f32 v22,s49,v22\nv_mul_f32 v23,s49,v23\nv_mul_f32 v24,s49,v24\nv_mul_f32 v25,s49,v25\nv_mul_f32 v26,s49,v26\n" \
"v_mul_f32 v27,s49,v27\nv_mul_f32 v28,s49,v28\nv_mul_f32 v29,s49,v29\nv_mul_f32 v30,s49,v30\nv_mul_f32 v31,s49,v31\nv_mul_f32 v32,s49,v32\n" \
"v_mul_f32 v33,s49,v33\nv_mul_f32 v34,s49,v34\nv_mul_f32 v35,s49,v35\nv_mul_f32 v36,s49,v36\nv_mul_f32 v37,s49,v37\nv_mul_f32 v38,s49,v38\n" \
"v_mul_f32 v39,s49,v39\nv_mul_f32 v40,s49,v40\nv_mul_f32 v41,s49,v41\nv_mul_f32 v42,s49,v42\nv_mul_f32 v43,s49,v43\nv_mul_f32 v44,s49,v44\n" \
"v_mul_f32 v45,s49,v45\nv_mul_f32 v46,s49,v46\nv_mul_f32 v47,s49,v47\nv_mul_f32 v48,s49,v48\nv_mul_f32 v49,s49,v49\nv_mul_f32 v50,s49,v50\n" \
"v_mul_f32 v51,s49,v51\nv_mul_f32 v52,s49,v52\nv_mul_f32 v53,s49,v53\nv_mul_f32 v54,s49,v54\nv_mul_f32 v55,s49,v55\nv_mul_f32 v56,s49,v56\n" \
"v_mul_f32 v57,s49,v57\nv_mul_f32 v58,s49,v58\nv_mul_f32 v59,s49,v59\nv_mul_f32 v60,s49,v60\nv_mul_f32 v61,s49,v61\nv_mul_f32 v62,s49,v62\n" \
"v_mul_f32 v63,s49,v63\nv_cvt_pk_bf16_f32 v192,v0,v1\nv_cvt_pk_bf16_f32 v193,v2,v3\nv_cvt_pk_bf16_f32 v194,v4,v5\nv_cvt_pk_bf16_f32 v195,v6,v7\n" \
"v_cvt_pk_bf16_f32 v196,v8,v9\nv_cvt_pk_bf16_f32 v197,v10,v11\nv_cvt_pk_bf16_f32 v198,v12,v13\nv_cvt_pk_bf16_f32 v199,v14,v15\n" \
"v_cvt_pk_bf16_f32 v200,v16,v17\nv_cvt_pk_bf16_f32 v201,v18,v19\nv_cvt_pk_bf16_f32 v202,v20,v21\nv_cvt_pk_bf16_f32 v203,v22,v23\n" \
"v_cvt_pk_bf16_f32 v204,v24,v25\nv_cvt_pk_bf16_f32 v205,v26,v27\nv_cvt_pk_bf16_f32 v206,v28,v29\nv_cvt_pk_bf16_f32 v207,v30,v31\n" \
"v_cvt_pk_bf16_f32 v208,v32,v33\nv_cvt_pk_bf16_f32 v209,v34,v35\nv_cvt_pk_bf16_f32 v210,v36,v37\nv_cvt_pk_bf16_f32 v211,v38,v39\n" \
"v_cvt_pk_bf16_f32 v212,v40,v41\nv_cvt_pk_bf16_f32 v213,v42,v43\nv_cvt_pk_bf16_f32 v214,v44,v45\nv_cvt_pk_bf16_f32 v215,v46,v47\n" \
"v_cvt_pk_bf16_f32 v216,v48,v49\nv_cvt_pk_bf16_f32 v217,v50,v51\nv_cvt_pk_bf16_f32 v218,v52,v53\nv_cvt_pk_bf16_f32 v219,v54,v55\n" \
"v_cvt_pk_bf16_f32 v220,v56,v57\nv_cvt_pk_bf16_f32 v221,v58,v59\nv_cvt_pk_bf16_f32 v222,v60,v61\nv_cvt_pk_bf16_f32 v223,v62,v63\ns_waitcnt vmcnt(8)\n" \
"s_barrier\nds_read_b64_tr_b16 v[160:161],v226 offset:0\nds_read_b64_tr_b16 v[162:163],v226 offset:2048\nds_read_b64_tr_b16 v[164:165],v227 offset:0\n" \
"ds_read_b64_tr_b16 v[166:167],v227 offset:2048\nds_read_b64_tr_b16 v[168:169],v226 offset:256\nds_read_b64_tr_b16 v[170:171],v226 offset:2304\n" \
"ds_read_b64_tr_b16 v[172:173],v227 offset:256\nds_read_b64_tr_b16 v[174:175],v227 offset:2304\nds_read_b128 v[128:131],v224 offset:0\n" \
"ds_read_b128 v[132:135],v224 offset:1024\nds_read_b128 v[136:139],v224 offset:4096\nds_read_b128 v[140:143],v224 offset:5120\n" \
"ds_read_b128 v[144:147],v224 offset:8192\nds_read_b128 v[148:151],v224 offset:9216\nds_read_b128 v[152:155],v224 offset:12288\n" \
"ds_read_b128 v[156:159],v224 offset:13312\ns_waitcnt lgkmcnt(7)\nv_mfma_f32_16x16x32_bf16 v[0:3],v[128:131],v[160:163],0\n" \
"ds_read_b64_tr_b16 v[176:177],v226 offset:16384\nv_mfma_f32_16x16x32_bf16 v[4:7],v[128:131],v[164:167],0\n" \
"v_mfma_f32_16x16x32_bf16 v[8:11],v[128:131],v[168:171],0\nv_mfma_f32_16x16x32_bf16 v[12:15],v[128:131],v[172:175],0\n" \
"ds_read_b128 v[128:131],v225 offset:0\nds_read_b64_tr_b16 v[178:179],v226 offset:18432\ns_waitcnt lgkmcnt(9)\n" \
"v_mfma_f32_16x16x32_bf16 v[16:19],v[132:135],v[160:163],0\nv_mfma_f32_16x16x32_bf16 v[20:23],v[132:135],v[164:167],0\n" \
"v_mfma_f32_16x16x32_bf16 v[24:27],v[132:135],v[168:171],0\nds_read_b64_tr_b16 v[180:181],v227 offset:16384\n" \
"v_mfma_f32_16x16x32_bf16 v[28:31],v[132:135],v[172:175],0\nds_read_b128 v[132:135],v225 offset:1024\ns_waitcnt lgkmcnt(10)\n" \
"v_mfma_f32_16x16x32_bf16 v[32:35],v[136:139],v[160:163],0\nv_mfma_f32_16x16x32_bf16 v[36:39],v[136:139],v[164:167],0\n" \
"ds_read_b64_tr_b16 v[182:183],v227 offset:18432\nv_mfma_f32_16x16x32_bf16 v[40:43],v[136:139],v[168:171],0\n" \
"v_mfma_f32_16x16x32_bf16 v[44:47],v[136:139],v[172:175],0\nds_read_b128 v[136:139],v225 offset:4096\ns_waitcnt lgkmcnt(11)\n" \
"v_mfma_f32_16x16x32_bf16 v[48:51],v[140:143],v[160:163],0\nds_read_b64_tr_b16 v[184:185],v226 offset:16640\n" \
"v_mfma_f32_16x16x32_bf16 v[52:55],v[140:143],v[164:167],0\nv_mfma_f32_16x16x32_bf16 v[56:59],v[140:143],v[168:171],0\n" \
"v_mfma_f32_16x16x32_bf16 v[60:63],v[140:143],v[172:175],0\nds_read_b128 v[140:143],v225 offset:5120\nds_read_b64_tr_b16 v[186:187],v226 offset:18688\n" \
"s_waitcnt lgkmcnt(13)\nv_mfma_f32_16x16x32_bf16 v[64:67],v[144:147],v[160:163],0\nv_mfma_f32_16x16x32_bf16 v[68:71],v[144:147],v[164:167],0\n" \
"v_mfma_f32_16x16x32_bf16 v[72:75],v[144:147],v[168:171],0\nds_read_b64_tr_b16 v[188:189],v227 offset:16640\n" \
"v_mfma_f32_16x16x32_bf16 v[76:79],v[144:147],v[172:175],0\nds_read_b128 v[144:147],v225 offset:8192\ns_waitcnt lgkmcnt(14)\n" \
"v_mfma_f32_16x16x32_bf16 v[80:83],v[148:151],v[160:163],0\nv_mfma_f32_16x16x32_bf16 v[84:87],v[148:151],v[164:167],0\n" \
"ds_read_b64_tr_b16 v[190:191],v227 offset:18688\nv_mfma_f32_16x16x32_bf16 v[88:91],v[148:151],v[168:171],0\n" \
"v_mfma_f32_16x16x32_bf16 v[92:95],v[148:151],v[172:175],0\nds_read_b128 v[148:151],v225 offset:9216\ns_waitcnt lgkmcnt(15)\n" \
"v_mfma_f32_16x16x32_bf16 v[96:99],v[152:155],v[160:163],0\nv_mfma_f32_16x16x32_bf16 v[100:103],v[152:155],v[164:167],0\n" \
"v_mfma_f32_16x16x32_bf16 v[104:107],v[152:155],v[168:171],0\nv_mfma_f32_16x16x32_bf16 v[108:111],v[152:155],v[172:175],0\n" \
"ds_read_b128 v[152:155],v225 offset:12288\ns_waitcnt lgkmcnt(15)\nv_mfma_f32_16x16x32_bf16 v[112:115],v[156:159],v[160:163],0\n" \
"v_mfma_f32_16x16x32_bf16 v[116:119],v[156:159],v[164:167],0\nv_mfma_f32_16x16x32_bf16 v[120:123],v[156:159],v[168:171],0\n" \
"v_mfma_f32_16x16x32_bf16 v[124:127],v[156:159],v[172:175],0\nds_read_b128 v[156:159],v225 offset:13312\ns_waitcnt lgkmcnt(13)\n" \
"v_mfma_f32_16x16x32_bf16 v[0:3],v[128:131],v[176:179],v[0:3]\ns_waitcnt lgkmcnt(10)\nv_mfma_f32_16x16x32_bf16 v[4:7],v[128:131],v[180:183],v[4:7]\n" \
"s_waitcnt lgkmcnt(6)\nv_mfma_f32_16x16x32_bf16 v[8:11],v[128:131],v[184:187],v[8:11]\ns_waitcnt lgkmcnt(3)\n" \
"v_mfma_f32_16x16x32_bf16 v[12:15],v[128:131],v[188:191],v[12:15]\ns_waitcnt vmcnt(0) lgkmcnt(0)\ns_barrier\nv_xor_b32 v224,0x10000,v224\n" \
"v_xor_b32 v225,0x10000,v225\nv_xor_b32 v226,0x10000,v226\nv_xor_b32 v227,0x10000,v227\n" \
"v_mfma_f32_16x16x32_bf16 v[16:19],v[132:135],v[176:179],v[16:19]\nds_read_b128 v[128:131],v224 offset:0\n" \
"v_mfma_f32_16x16x32_bf16 v[20:23],v[132:135],v[180:183],v[20:23]\nds_read_b64_tr_b16 v[160:161],v226 offset:0\ns_mov_b32 m0,s59\ns_nop 0\n" \
"buffer_load_dwordx4 v228,%[sx],s52 offen lds\nv_mfma_f32_16x16x32_bf16 v[24:27],v[132:135],v[184:187],v[24:27]\n" \
"v_mfma_f32_16x16x32_bf16 v[28:31],v[132:135],v[188:191],v[28:31]\nds_read_b128 v[132:135],v224 offset:1024\n" \
"v_mfma_f32_16x16x32_bf16 v[32:35],v[136:139],v[176:179],v[32:35]\nds_read_b64_tr_b16 v[162:163],v226 offset:2048\ns_add_u32 m0,m0,1024\ns_nop 0\n" \
"buffer_load_dwordx4 v228,%[sx],s53 offen lds\nv_mfma_f32_16x16x32_bf16 v[36:39],v[136:139],v[180:183],v[36:39]\n" \
"v_mfma_f32_16x16x32_bf16 v[40:43],v[136:139],v[184:187],v[40:43]\nv_mfma_f32_16x16x32_bf16 v[44:47],v[136:139],v[188:191],v[44:47]\n" \
"ds_read_b128 v[136:139],v224 offset:4096\nds_read_b64_tr_b16 v[164:165],v227 offset:0\ns_add_u32 m0,m0,1024\ns_nop 0\n" \
"buffer_load_dwordx4 v229,%[sx],s54 offen lds\nv_mfma_f32_16x16x32_bf16 v[48:51],v[140:143],v[176:179],v[48:51]\n" \
"v_mfma_f32_16x16x32_bf16 v[52:55],v[140:143],v[180:183],v[52:55]\nv_mfma_f32_16x16x32_bf16 v[56:59],v[140:143],v[184:187],v[56:59]\n" \
"ds_read_b64_tr_b16 v[166:167],v227 offset:2048\ns_add_u32 m0,m0,1024\ns_nop 0\nbuffer_load_dwordx4 v229,%[sx],s55 offen lds\n" \
"v_mfma_f32_16x16x32_bf16 v[60:63],v[140:143],v[188:191],v[60:63]\nds_read_b128 v[140:143],v224 offset:5120\n" \
"v_mfma_f32_16x16x32_bf16 v[64:67],v[144:147],v[176:179],v[64:67]\nv_mfma_f32_16x16x32_bf16 v[68:71],v[144:147],v[180:183],v[68:71]\n" \
"ds_read_b64_tr_b16 v[168:169],v226 offset:256\ns_add_u32 m0,m0,29696\ns_nop 0\nbuffer_load_dwordx4 v230,%[sk],s56 offen lds\n" \
"v_mfma_f32_16x16x32_bf16 v[72:75],v[144:147],v[184:187],v[72:75]\nv_mfma_f32_16x16x32_bf16 v[76:79],v[144:147],v[188:191],v[76:79]\n" \
"ds_read_b128 v[144:147],v224 offset:8192\nv_mfma_f32_16x16x32_bf16 v[80:83],v[148:151],v[176:179],v[80:83]\n" \
"ds_read_b64_tr_b16 v[170:171],v226 offset:2304\ns_add_u32 m0,m0,1024\ns_nop 0\nbuffer_load_dwordx4 v230,%[sv],s56 offen lds\n" \
"v_mfma_f32_16x16x32_bf16 v[84:87],v[148:151],v[180:183],v[84:87]\nv_mfma_f32_16x16x32_bf16 v[88:91],v[148:151],v[184:187],v[88:91]\n" \
"v_mfma_f32_16x16x32_bf16 v[92:95],v[148:151],v[188:191],v[92:95]\nds_read_b128 v[148:151],v224 offset:9216\n" \
"ds_read_b64_tr_b16 v[172:173],v227 offset:256\ns_add_u32 m0,m0,1024\ns_nop 0\nbuffer_load_dwordx4 v230,%[sk],s57 offen lds\n" \
"v_mfma_f32_16x16x32_bf16 v[96:99],v[152:155],v[176:179],v[96:99]\nv_mfma_f32_16x16x32_bf16 v[100:103],v[152:155],v[180:183],v[100:103]\n" \
"v_mfma_f32_16x16x32_bf16 v[104:107],v[152:155],v[184:187],v[104:107]\nds_read_b64_tr_b16 v[174:175],v227 offset:2304\ns_add_u32 m0,m0,1024\ns_nop 0\n" \
"buffer_load_dwordx4 v230,%[sv],s57 offen lds\nv_add_u32 v228,128,v228\nv_add_u32 v229,128,v229\nv_add_u32 v230,0x80000,v230\ns_xor_b32 s59,s59,0x10000\n" \
"v_mfma_f32_16x16x32_bf16 v[108:111],v[152:155],v[188:191],v[108:111]\nds_read_b128 v[152:155],v224 offset:12288\n" \
"v_mfma_f32_16x16x32_bf16 v[112:115],v[156:159],v[176:179],v[112:115]\nv_mfma_f32_16x16x32_bf16 v[116:119],v[156:159],v[180:183],v[116:119]\n" \
"v_mfma_f32_16x16x32_bf16 v[120:123],v[156:159],v[184:187],v[120:123]\nv_mfma_f32_16x16x32_bf16 v[124:127],v[156:159],v[188:191],v[124:127]\n" \
"ds_read_b128 v[156:159],v224 offset:13312\ns_mov_b32 s60,5\n1:\ns_waitcnt lgkmcnt(12)\nv_mfma_f32_16x16x32_bf16 v[0:3],v[128:131],v[160:163],v[0:3]\n" \
"ds_read_b64_tr_b16 v[176:177],v226 offset:16384\ns_waitcnt lgkmcnt(10)\nv_mfma_f32_16x16x32_bf16 v[4:7],v[128:131],v[164:167],v[4:7]\n" \
"s_waitcnt lgkmcnt(6)\nv_mfma_f32_16x16x32_bf16 v[8:11],v[128:131],v[168:171],v[8:11]\ns_waitcnt lgkmcnt(3)\n" \
"v_mfma_f32_16x16x32_bf16 v[12:15],v[128:131],v[172:175],v[12:15]\nds_read_b128 v[128:131],v225 offset:0\n" \
"ds_read_b64_tr_b16 v[178:179],v226 offset:18432\nv_mfma_f32_16x16x32_bf16 v[16:19],v[132:135],v[160:163],v[16:19]\n" \
"v_mfma_f32_16x16x32_bf16 v[20:23],v[132:135],v[164:167],v[20:23]\nv_mfma_f32_16x16x32_bf16 v[24:27],v[132:135],v[168:171],v[24:27]\n" \
"ds_read_b64_tr_b16 v[180:181],v227 offset:16384\nv_mfma_f32_16x16x32_bf16 v[28:31],v[132:135],v[172:175],v[28:31]\n" \
"ds_read_b128 v[132:135],v225 offset:1024\nv_mfma_f32_16x16x32_bf16 v[32:35],v[136:139],v[160:163],v[32:35]\n" \
"v_mfma_f32_16x16x32_bf16 v[36:39],v[136:139],v[164:167],v[36:39]\nds_read_b64_tr_b16 v[182:183],v227 offset:18432\n" \
"v_mfma_f32_16x16x32_bf16 v[40:43],v[136:139],v[168:171],v[40:43]\nv_mfma_f32_16x16x32_bf16 v[44:47],v[136:139],v[172:175],v[44:47]\n" \
"ds_read_b128 v[136:139],v225 offset:4096\nv_mfma_f32_16x16x32_bf16 v[48:51],v[140:143],v[160:163],v[48:51]\n" \
"ds_read_b64_tr_b16 v[184:185],v226 offset:16640\nv_mfma_f32_16x16x32_bf16 v[52:55],v[140:143],v[164:167],v[52:55]\n" \
"v_mfma_f32_16x16x32_bf16 v[56:59],v[140:143],v[168:171],v[56:59]\nv_mfma_f32_16x16x32_bf16 v[60:63],v[140:143],v[172:175],v[60:63]\n" \
"ds_read_b128 v[140:143],v225 offset:5120\nds_read_b64_tr_b16 v[186:187],v226 offset:18688\n" \
"v_mfma_f32_16x16x32_bf16 v[64:67],v[144:147],v[160:163],v[64:67]\nv_mfma_f32_16x16x32_bf16 v[68:71],v[144:147],v[164:167],v[68:71]\n" \
"v_mfma_f32_16x16x32_bf16 v[72:75],v[144:147],v[168:171],v[72:75]\nds_read_b64_tr_b16 v[188:189],v227 offset:16640\n" \
"v_mfma_f32_16x16x32_bf16 v[76:79],v[144:147],v[172:175],v[76:79]\nds_read_b128 v[144:147],v225 offset:8192\n" \
"v_mfma_f32_16x16x32_bf16 v[80:83],v[148:151],v[160:163],v[80:83]\nv_mfma_f32_16x16x32_bf16 v[84:87],v[148:151],v[164:167],v[84:87]\n" \
"ds_read_b64_tr_b16 v[190:191],v227 offset:18688\nv_mfma_f32_16x16x32_bf16 v[88:91],v[148:151],v[168:171],v[88:91]\n" \
"v_mfma_f32_16x16x32_bf16 v[92:95],v[148:151],v[172:175],v[92:95]\nds_read_b128 v[148:151],v225 offset:9216\ns_waitcnt lgkmcnt(15)\n" \
"v_mfma_f32_16x16x32_bf16 v[96:99],v[152:155],v[160:163],v[96:99]\nv_mfma_f32_16x16x32_bf16 v[100:103],v[152:155],v[164:167],v[100:103]\n" \
"v_mfma_f32_16x16x32_bf16 v[104:107],v[152:155],v[168:171],v[104:107]\nv_mfma_f32_16x16x32_bf16 v[108:111],v[152:155],v[172:175],v[108:111]\n" \
"ds_read_b128 v[152:155],v225 offset:12288\ns_waitcnt lgkmcnt(15)\nv_mfma_f32_16x16x32_bf16 v[112:115],v[156:159],v[160:163],v[112:115]\n" \
"v_mfma_f32_16x16x32_bf16 v[116:119],v[156:159],v[164:167],v[116:119]\nv_mfma_f32_16x16x32_bf16 v[120:123],v[156:159],v[168:171],v[120:123]\n" \
"v_mfma_f32_16x16x32_bf16 v[124:127],v[156:159],v[172:175],v[124:127]\nds_read_b128 v[156:159],v225 offset:13312\ns_waitcnt lgkmcnt(13)\n" \
"v_mfma_f32_16x16x32_bf16 v[0:3],v[128:131],v[176:179],v[0:3]\ns_waitcnt lgkmcnt(10)\nv_mfma_f32_16x16x32_bf16 v[4:7],v[128:131],v[180:183],v[4:7]\n" \
"s_waitcnt lgkmcnt(6)\nv_mfma_f32_16x16x32_bf16 v[8:11],v[128:131],v[184:187],v[8:11]\ns_waitcnt lgkmcnt(3)\n" \
"v_mfma_f32_16x16x32_bf16 v[12:15],v[128:131],v[188:191],v[12:15]\ns_waitcnt vmcnt(0) lgkmcnt(0)\ns_barrier\nv_xor_b32 v224,0x10000,v224\n" \
"v_xor_b32 v225,0x10000,v225\nv_xor_b32 v226,0x10000,v226\nv_xor_b32 v227,0x10000,v227\n" \
"v_mfma_f32_16x16x32_bf16 v[16:19],v[132:135],v[176:179],v[16:19]\nds_read_b128 v[128:131],v224 offset:0\n" \
"v_mfma_f32_16x16x32_bf16 v[20:23],v[132:135],v[180:183],v[20:23]\nds_read_b64_tr_b16 v[160:161],v226 offset:0\ns_mov_b32 m0,s59\ns_nop 0\n" \
"buffer_load_dwordx4 v228,%[sx],s52 offen lds\nv_mfma_f32_16x16x32_bf16 v[24:27],v[132:135],v[184:187],v[24:27]\n" \
"v_mfma_f32_16x16x32_bf16 v[28:31],v[132:135],v[188:191],v[28:31]\nds_read_b128 v[132:135],v224 offset:1024\n" \
"v_mfma_f32_16x16x32_bf16 v[32:35],v[136:139],v[176:179],v[32:35]\nds_read_b64_tr_b16 v[162:163],v226 offset:2048\ns_add_u32 m0,m0,1024\ns_nop 0\n" \
"buffer_load_dwordx4 v228,%[sx],s53 offen lds\nv_mfma_f32_16x16x32_bf16 v[36:39],v[136:139],v[180:183],v[36:39]\n" \
"v_mfma_f32_16x16x32_bf16 v[40:43],v[136:139],v[184:187],v[40:43]\nv_mfma_f32_16x16x32_bf16 v[44:47],v[136:139],v[188:191],v[44:47]\n" \
"ds_read_b128 v[136:139],v224 offset:4096\nds_read_b64_tr_b16 v[164:165],v227 offset:0\ns_add_u32 m0,m0,1024\ns_nop 0\n" \
"buffer_load_dwordx4 v229,%[sx],s54 offen lds\nv_mfma_f32_16x16x32_bf16 v[48:51],v[140:143],v[176:179],v[48:51]\n" \
"v_mfma_f32_16x16x32_bf16 v[52:55],v[140:143],v[180:183],v[52:55]\nv_mfma_f32_16x16x32_bf16 v[56:59],v[140:143],v[184:187],v[56:59]\n" \
"ds_read_b64_tr_b16 v[166:167],v227 offset:2048\ns_add_u32 m0,m0,1024\ns_nop 0\nbuffer_load_dwordx4 v229,%[sx],s55 offen lds\n" \
"v_mfma_f32_16x16x32_bf16 v[60:63],v[140:143],v[188:191],v[60:63]\nds_read_b128 v[140:143],v224 offset:5120\n" \
"v_mfma_f32_16x16x32_bf16 v[64:67],v[144:147],v[176:179],v[64:67]\nv_mfma_f32_16x16x32_bf16 v[68:71],v[144:147],v[180:183],v[68:71]\n" \
"ds_read_b64_tr_b16 v[168:169],v226 offset:256\ns_add_u32 m0,m0,29696\ns_nop 0\nbuffer_load_dwordx4 v230,%[sk],s56 offen lds\n" \
"v_mfma_f32_16x16x32_bf16 v[72:75],v[144:147],v[184:187],v[72:75]\nv_mfma_f32_16x16x32_bf16 v[76:79],v[144:147],v[188:191],v[76:79]\n" \
"ds_read_b128 v[144:147],v224 offset:8192\nv_mfma_f32_16x16x32_bf16 v[80:83],v[148:151],v[176:179],v[80:83]\n" \
"ds_read_b64_tr_b16 v[170:171],v226 offset:2304\ns_add_u32 m0,m0,1024\ns_nop 0\nbuffer_load_dwordx4 v230,%[sv],s56 offen lds\n" \
"v_mfma_f32_16x16x32_bf16 v[84:87],v[148:151],v[180:183],v[84:87]\nv_mfma_f32_16x16x32_bf16 v[88:91],v[148:151],v[184:187],v[88:91]\n" \
"v_mfma_f32_16x16x32_bf16 v[92:95],v[148:151],v[188:191],v[92:95]\nds_read_b128 v[148:151],v224 offset:9216\n" \
"ds_read_b64_tr_b16 v[172:173],v227 offset:256\ns_add_u32 m0,m0,1024\ns_nop 0\nbuffer_load_dwordx4 v230,%[sk],s57 offen lds\n" \
"v_mfma_f32_16x16x32_bf16 v[96:99],v[152:155],v[176:179],v[96:99]\nv_mfma_f32_16x16x32_bf16 v[100:103],v[152:155],v[180:183],v[100:103]\n" \
"v_mfma_f32_16x16x32_bf16 v[104:107],v[152:155],v[184:187],v[104:107]\nds_read_b64_tr_b16 v[174:175],v227 offset:2304\ns_add_u32 m0,m0,1024\ns_nop 0\n" \
"buffer_load_dwordx4 v230,%[sv],s57 offen lds\nv_add_u32 v228,128,v228\nv_add_u32 v229,128,v229\nv_add_u32 v230,0x80000,v230\ns_xor_b32 s59,s59,0x10000\n" \
"v_mfma_f32_16x16x32_bf16 v[108:111],v[152:155],v[188:191],v[108:111]\nds_read_b128 v[152:155],v224 offset:12288\n" \
"v_mfma_f32_16x16x32_bf16 v[112:115],v[156:159],v[176:179],v[112:115]\nv_mfma_f32_16x16x32_bf16 v[116:119],v[156:159],v[180:183],v[116:119]\n" \
"v_mfma_f32_16x16x32_bf16 v[120:123],v[156:159],v[184:187],v[120:123]\nv_mfma_f32_16x16x32_bf16 v[124:127],v[156:159],v[188:191],v[124:127]\n" \
"ds_read_b128 v[156:159],v224 offset:13312\ns_sub_u32 s60,s60,1\ns_cmp_lg_u32 s60,0\ns_cbranch_scc1 1b\ns_waitcnt lgkmcnt(12)\n" \
"v_mfma_f32_16x16x32_bf16 v[0:3],v[128:131],v[160:163],v[0:3]\nds_read_b64_tr_b16 v[176:177],v226 offset:16384\ns_waitcnt lgkmcnt(10)\n" \
"v_mfma_f32_16x16x32_bf16 v[4:7],v[128:131],v[164:167],v[4:7]\ns_waitcnt lgkmcnt(6)\nv_mfma_f32_16x16x32_bf16 v[8:11],v[128:131],v[168:171],v[8:11]\n" \
"s_waitcnt lgkmcnt(3)\nv_mfma_f32_16x16x32_bf16 v[12:15],v[128:131],v[172:175],v[12:15]\nds_read_b128 v[128:131],v225 offset:0\n" \
"ds_read_b64_tr_b16 v[178:179],v226 offset:18432\nv_mfma_f32_16x16x32_bf16 v[16:19],v[132:135],v[160:163],v[16:19]\n" \
"v_mfma_f32_16x16x32_bf16 v[20:23],v[132:135],v[164:167],v[20:23]\nv_mfma_f32_16x16x32_bf16 v[24:27],v[132:135],v[168:171],v[24:27]\n" \
"ds_read_b64_tr_b16 v[180:181],v227 offset:16384\nv_mfma_f32_16x16x32_bf16 v[28:31],v[132:135],v[172:175],v[28:31]\n" \
"ds_read_b128 v[132:135],v225 offset:1024\nv_mfma_f32_16x16x32_bf16 v[32:35],v[136:139],v[160:163],v[32:35]\n" \
"v_mfma_f32_16x16x32_bf16 v[36:39],v[136:139],v[164:167],v[36:39]\nds_read_b64_tr_b16 v[182:183],v227 offset:18432\n" \
"v_mfma_f32_16x16x32_bf16 v[40:43],v[136:139],v[168:171],v[40:43]\nv_mfma_f32_16x16x32_bf16 v[44:47],v[136:139],v[172:175],v[44:47]\n" \
"ds_read_b128 v[136:139],v225 offset:4096\nv_mfma_f32_16x16x32_bf16 v[48:51],v[140:143],v[160:163],v[48:51]\n" \
"ds_read_b64_tr_b16 v[184:185],v226 offset:16640\nv_mfma_f32_16x16x32_bf16 v[52:55],v[140:143],v[164:167],v[52:55]\n" \
"v_mfma_f32_16x16x32_bf16 v[56:59],v[140:143],v[168:171],v[56:59]\nv_mfma_f32_16x16x32_bf16 v[60:63],v[140:143],v[172:175],v[60:63]\n" \
"ds_read_b128 v[140:143],v225 offset:5120\nds_read_b64_tr_b16 v[186:187],v226 offset:18688\n" \
"v_mfma_f32_16x16x32_bf16 v[64:67],v[144:147],v[160:163],v[64:67]\nv_mfma_f32_16x16x32_bf16 v[68:71],v[144:147],v[164:167],v[68:71]\n" \
"v_mfma_f32_16x16x32_bf16 v[72:75],v[144:147],v[168:171],v[72:75]\nds_read_b64_tr_b16 v[188:189],v227 offset:16640\n" \
"v_mfma_f32_16x16x32_bf16 v[76:79],v[144:147],v[172:175],v[76:79]\nds_read_b128 v[144:147],v225 offset:8192\n" \
"v_mfma_f32_16x16x32_bf16 v[80:83],v[148:151],v[160:163],v[80:83]\nv_mfma_f32_16x16x32_bf16 v[84:87],v[148:151],v[164:167],v[84:87]\n" \
"ds_read_b64_tr_b16 v[190:191],v227 offset:18688\nv_mfma_f32_16x16x32_bf16 v[88:91],v[148:151],v[168:171],v[88:91]\n" \
"v_mfma_f32_16x16x32_bf16 v[92:95],v[148:151],v[172:175],v[92:95]\nds_read_b128 v[148:151],v225 offset:9216\ns_waitcnt lgkmcnt(15)\n" \
"v_mfma_f32_16x16x32_bf16 v[96:99],v[152:155],v[160:163],v[96:99]\nv_mfma_f32_16x16x32_bf16 v[100:103],v[152:155],v[164:167],v[100:103]\n" \
"v_mfma_f32_16x16x32_bf16 v[104:107],v[152:155],v[168:171],v[104:107]\nv_mfma_f32_16x16x32_bf16 v[108:111],v[152:155],v[172:175],v[108:111]\n" \
"ds_read_b128 v[152:155],v225 offset:12288\ns_waitcnt lgkmcnt(15)\nv_mfma_f32_16x16x32_bf16 v[112:115],v[156:159],v[160:163],v[112:115]\n" \
"v_mfma_f32_16x16x32_bf16 v[116:119],v[156:159],v[164:167],v[116:119]\nv_mfma_f32_16x16x32_bf16 v[120:123],v[156:159],v[168:171],v[120:123]\n" \
"v_mfma_f32_16x16x32_bf16 v[124:127],v[156:159],v[172:175],v[124:127]\nds_read_b128 v[156:159],v225 offset:13312\ns_waitcnt lgkmcnt(13)\n" \
"v_mfma_f32_16x16x32_bf16 v[0:3],v[128:131],v[176:179],v[0:3]\ns_waitcnt lgkmcnt(10)\nv_mfma_f32_16x16x32_bf16 v[4:7],v[128:131],v[180:183],v[4:7]\n" \
"s_waitcnt lgkmcnt(6)\nv_mfma_f32_16x16x32_bf16 v[8:11],v[128:131],v[184:187],v[8:11]\ns_waitcnt lgkmcnt(3)\n" \
"v_mfma_f32_16x16x32_bf16 v[12:15],v[128:131],v[188:191],v[12:15]\ns_waitcnt vmcnt(0) lgkmcnt(0)\ns_barrier\nv_xor_b32 v224,0x10000,v224\n" \
"v_xor_b32 v225,0x10000,v225\nv_xor_b32 v226,0x10000,v226\nv_xor_b32 v227,0x10000,v227\n" \
"v_mfma_f32_16x16x32_bf16 v[16:19],v[132:135],v[176:179],v[16:19]\nds_read_b128 v[128:131],v224 offset:0\n" \
"v_mfma_f32_16x16x32_bf16 v[20:23],v[132:135],v[180:183],v[20:23]\nds_read_b64_tr_b16 v[160:161],v226 offset:0\ns_add_u32 m0,s80,65536\n" \
"s_add_u32 s49,s68,0\nbuffer_load_dwordx4 v248,%[sws],s49 offen offset:0 lds\nv_mfma_f32_16x16x32_bf16 v[24:27],v[132:135],v[184:187],v[24:27]\n" \
"v_mfma_f32_16x16x32_bf16 v[28:31],v[132:135],v[188:191],v[28:31]\nds_read_b128 v[132:135],v224 offset:1024\n" \
"v_mfma_f32_16x16x32_bf16 v[32:35],v[136:139],v[176:179],v[32:35]\nds_read_b64_tr_b16 v[162:163],v226 offset:2048\n" \
"buffer_load_dwordx4 v248,%[sws],s49 offen offset:1024 lds\nv_mfma_f32_16x16x32_bf16 v[36:39],v[136:139],v[180:183],v[36:39]\n" \
"v_mfma_f32_16x16x32_bf16 v[40:43],v[136:139],v[184:187],v[40:43]\nv_mfma_f32_16x16x32_bf16 v[44:47],v[136:139],v[188:191],v[44:47]\n" \
"ds_read_b128 v[136:139],v224 offset:4096\nds_read_b64_tr_b16 v[164:165],v227 offset:0\nbuffer_load_dwordx4 v248,%[sws],s49 offen offset:2048 lds\n" \
"v_mfma_f32_16x16x32_bf16 v[48:51],v[140:143],v[176:179],v[48:51]\nv_mfma_f32_16x16x32_bf16 v[52:55],v[140:143],v[180:183],v[52:55]\n" \
"v_mfma_f32_16x16x32_bf16 v[56:59],v[140:143],v[184:187],v[56:59]\nds_read_b64_tr_b16 v[166:167],v227 offset:2048\n" \
"buffer_load_dwordx4 v248,%[sws],s49 offen offset:3072 lds\nv_mfma_f32_16x16x32_bf16 v[60:63],v[140:143],v[188:191],v[60:63]\n" \
"ds_read_b128 v[140:143],v224 offset:5120\nv_mfma_f32_16x16x32_bf16 v[64:67],v[144:147],v[176:179],v[64:67]\n" \
"v_mfma_f32_16x16x32_bf16 v[68:71],v[144:147],v[180:183],v[68:71]\nds_read_b64_tr_b16 v[168:169],v226 offset:256\ns_add_u32 m0,s80,69632\n" \
"s_add_u32 s49,s68,4096\nbuffer_load_dwordx4 v248,%[sws],s49 offen offset:0 lds\nv_mfma_f32_16x16x32_bf16 v[72:75],v[144:147],v[184:187],v[72:75]\n" \
"v_mfma_f32_16x16x32_bf16 v[76:79],v[144:147],v[188:191],v[76:79]\nds_read_b128 v[144:147],v224 offset:8192\n" \
"v_mfma_f32_16x16x32_bf16 v[80:83],v[148:151],v[176:179],v[80:83]\nds_read_b64_tr_b16 v[170:171],v226 offset:2304\n" \
"buffer_load_dwordx4 v248,%[sws],s49 offen offset:1024 lds\nv_mfma_f32_16x16x32_bf16 v[84:87],v[148:151],v[180:183],v[84:87]\n" \
"v_mfma_f32_16x16x32_bf16 v[88:91],v[148:151],v[184:187],v[88:91]\nv_mfma_f32_16x16x32_bf16 v[92:95],v[148:151],v[188:191],v[92:95]\n" \
"ds_read_b128 v[148:151],v224 offset:9216\nds_read_b64_tr_b16 v[172:173],v227 offset:256\n" \
"v_mfma_f32_16x16x32_bf16 v[96:99],v[152:155],v[176:179],v[96:99]\nv_mfma_f32_16x16x32_bf16 v[100:103],v[152:155],v[180:183],v[100:103]\n" \
"v_mfma_f32_16x16x32_bf16 v[104:107],v[152:155],v[184:187],v[104:107]\nds_read_b64_tr_b16 v[174:175],v227 offset:2304\n" \
"v_mfma_f32_16x16x32_bf16 v[108:111],v[152:155],v[188:191],v[108:111]\nds_read_b128 v[152:155],v224 offset:12288\n" \
"v_mfma_f32_16x16x32_bf16 v[112:115],v[156:159],v[176:179],v[112:115]\nv_mfma_f32_16x16x32_bf16 v[116:119],v[156:159],v[180:183],v[116:119]\n" \
"v_mfma_f32_16x16x32_bf16 v[120:123],v[156:159],v[184:187],v[120:123]\nv_mfma_f32_16x16x32_bf16 v[124:127],v[156:159],v[188:191],v[124:127]\n" \
"ds_read_b128 v[156:159],v224 offset:13312\ns_waitcnt lgkmcnt(12)\nv_mfma_f32_16x16x32_bf16 v[0:3],v[128:131],v[160:163],v[0:3]\n" \
"ds_read_b64_tr_b16 v[176:177],v226 offset:16384\ns_waitcnt lgkmcnt(10)\nv_mfma_f32_16x16x32_bf16 v[4:7],v[128:131],v[164:167],v[4:7]\n" \
"s_waitcnt lgkmcnt(6)\nv_mfma_f32_16x16x32_bf16 v[8:11],v[128:131],v[168:171],v[8:11]\ns_waitcnt lgkmcnt(3)\n" \
"v_mfma_f32_16x16x32_bf16 v[12:15],v[128:131],v[172:175],v[12:15]\nds_read_b128 v[128:131],v225 offset:0\n" \
"ds_read_b64_tr_b16 v[178:179],v226 offset:18432\nv_mfma_f32_16x16x32_bf16 v[16:19],v[132:135],v[160:163],v[16:19]\n" \
"v_mfma_f32_16x16x32_bf16 v[20:23],v[132:135],v[164:167],v[20:23]\nv_mfma_f32_16x16x32_bf16 v[24:27],v[132:135],v[168:171],v[24:27]\n" \
"ds_read_b64_tr_b16 v[180:181],v227 offset:16384\nv_mfma_f32_16x16x32_bf16 v[28:31],v[132:135],v[172:175],v[28:31]\n" \
"ds_read_b128 v[132:135],v225 offset:1024\nv_mfma_f32_16x16x32_bf16 v[32:35],v[136:139],v[160:163],v[32:35]\n" \
"v_mfma_f32_16x16x32_bf16 v[36:39],v[136:139],v[164:167],v[36:39]\nds_read_b64_tr_b16 v[182:183],v227 offset:18432\n" \
"v_mfma_f32_16x16x32_bf16 v[40:43],v[136:139],v[168:171],v[40:43]\nv_mfma_f32_16x16x32_bf16 v[44:47],v[136:139],v[172:175],v[44:47]\n" \
"ds_read_b128 v[136:139],v225 offset:4096\nv_mfma_f32_16x16x32_bf16 v[48:51],v[140:143],v[160:163],v[48:51]\n" \
"ds_read_b64_tr_b16 v[184:185],v226 offset:16640\nv_mfma_f32_16x16x32_bf16 v[52:55],v[140:143],v[164:167],v[52:55]\n" \
"v_mfma_f32_16x16x32_bf16 v[56:59],v[140:143],v[168:171],v[56:59]\nv_mfma_f32_16x16x32_bf16 v[60:63],v[140:143],v[172:175],v[60:63]\n" \
"ds_read_b128 v[140:143],v225 offset:5120\nds_read_b64_tr_b16 v[186:187],v226 offset:18688\n" \
"v_mfma_f32_16x16x32_bf16 v[64:67],v[144:147],v[160:163],v[64:67]\nv_mfma_f32_16x16x32_bf16 v[68:71],v[144:147],v[164:167],v[68:71]\n" \
"v_mfma_f32_16x16x32_bf16 v[72:75],v[144:147],v[168:171],v[72:75]\nds_read_b64_tr_b16 v[188:189],v227 offset:16640\n" \
"v_mfma_f32_16x16x32_bf16 v[76:79],v[144:147],v[172:175],v[76:79]\nds_read_b128 v[144:147],v225 offset:8192\n" \
"v_mfma_f32_16x16x32_bf16 v[80:83],v[148:151],v[160:163],v[80:83]\nv_mfma_f32_16x16x32_bf16 v[84:87],v[148:151],v[164:167],v[84:87]\n" \
"ds_read_b64_tr_b16 v[190:191],v227 offset:18688\nv_mfma_f32_16x16x32_bf16 v[88:91],v[148:151],v[168:171],v[88:91]\n" \
"v_mfma_f32_16x16x32_bf16 v[92:95],v[148:151],v[172:175],v[92:95]\nds_read_b128 v[148:151],v225 offset:9216\ns_waitcnt lgkmcnt(15)\n" \
"v_mfma_f32_16x16x32_bf16 v[96:99],v[152:155],v[160:163],v[96:99]\nv_mfma_f32_16x16x32_bf16 v[100:103],v[152:155],v[164:167],v[100:103]\n" \
"v_mfma_f32_16x16x32_bf16 v[104:107],v[152:155],v[168:171],v[104:107]\nv_mfma_f32_16x16x32_bf16 v[108:111],v[152:155],v[172:175],v[108:111]\n" \
"ds_read_b128 v[152:155],v225 offset:12288\ns_waitcnt lgkmcnt(15)\nv_mfma_f32_16x16x32_bf16 v[112:115],v[156:159],v[160:163],v[112:115]\n" \
"v_mfma_f32_16x16x32_bf16 v[116:119],v[156:159],v[164:167],v[116:119]\nv_mfma_f32_16x16x32_bf16 v[120:123],v[156:159],v[168:171],v[120:123]\n" \
"v_mfma_f32_16x16x32_bf16 v[124:127],v[156:159],v[172:175],v[124:127]\nds_read_b128 v[156:159],v225 offset:13312\ns_waitcnt lgkmcnt(13)\n" \
"v_mfma_f32_16x16x32_bf16 v[0:3],v[128:131],v[176:179],v[0:3]\ns_waitcnt lgkmcnt(10)\nv_mfma_f32_16x16x32_bf16 v[4:7],v[128:131],v[180:183],v[4:7]\n" \
"s_waitcnt lgkmcnt(6)\nv_mfma_f32_16x16x32_bf16 v[8:11],v[128:131],v[184:187],v[8:11]\ns_waitcnt lgkmcnt(3)\n" \
"v_mfma_f32_16x16x32_bf16 v[12:15],v[128:131],v[188:191],v[12:15]\ns_waitcnt vmcnt(0) lgkmcnt(0)\ns_barrier\n" \
"v_mfma_f32_16x16x32_bf16 v[16:19],v[132:135],v[176:179],v[16:19]\nv_mfma_f32_16x16x32_bf16 v[20:23],v[132:135],v[180:183],v[20:23]\n" \
"v_mfma_f32_16x16x32_bf16 v[24:27],v[132:135],v[184:187],v[24:27]\nv_mfma_f32_16x16x32_bf16 v[28:31],v[132:135],v[188:191],v[28:31]\n" \
"v_mfma_f32_16x16x32_bf16 v[32:35],v[136:139],v[176:179],v[32:35]\nbuffer_load_dwordx4 v[128:131],v248,%[sws],s71 offen offset:0\n" \
"v_mfma_f32_16x16x32_bf16 v[36:39],v[136:139],v[180:183],v[36:39]\nv_mfma_f32_16x16x32_bf16 v[40:43],v[136:139],v[184:187],v[40:43]\n" \
"v_mfma_f32_16x16x32_bf16 v[44:47],v[136:139],v[188:191],v[44:47]\nbuffer_load_dwordx4 v[132:135],v248,%[sws],s71 offen offset:1024\n" \
"v_mfma_f32_16x16x32_bf16 v[48:51],v[140:143],v[176:179],v[48:51]\nv_mfma_f32_16x16x32_bf16 v[52:55],v[140:143],v[180:183],v[52:55]\n" \
"v_mfma_f32_16x16x32_bf16 v[56:59],v[140:143],v[184:187],v[56:59]\ns_add_u32 m0,s80,0\ns_add_u32 s49,s68,24576\n" \
"buffer_load_dwordx4 v248,%[sws],s49 offen offset:0 lds\nv_mfma_f32_16x16x32_bf16 v[60:63],v[140:143],v[188:191],v[60:63]\n" \
"v_mfma_f32_16x16x32_bf16 v[64:67],v[144:147],v[176:179],v[64:67]\nv_mfma_f32_16x16x32_bf16 v[68:71],v[144:147],v[180:183],v[68:71]\n" \
"buffer_load_dwordx4 v248,%[sws],s49 offen offset:1024 lds\nv_mfma_f32_16x16x32_bf16 v[72:75],v[144:147],v[184:187],v[72:75]\n" \
"v_mfma_f32_16x16x32_bf16 v[76:79],v[144:147],v[188:191],v[76:79]\nv_mfma_f32_16x16x32_bf16 v[80:83],v[148:151],v[176:179],v[80:83]\n" \
"buffer_load_dwordx4 v248,%[sws],s49 offen offset:2048 lds\nv_mfma_f32_16x16x32_bf16 v[84:87],v[148:151],v[180:183],v[84:87]\n" \
"v_mfma_f32_16x16x32_bf16 v[88:91],v[148:151],v[184:187],v[88:91]\nv_mfma_f32_16x16x32_bf16 v[92:95],v[148:151],v[188:191],v[92:95]\n" \
"buffer_load_dwordx4 v248,%[sws],s49 offen offset:3072 lds\nv_mfma_f32_16x16x32_bf16 v[96:99],v[152:155],v[176:179],v[96:99]\n" \
"v_mfma_f32_16x16x32_bf16 v[100:103],v[152:155],v[180:183],v[100:103]\nv_mfma_f32_16x16x32_bf16 v[104:107],v[152:155],v[184:187],v[104:107]\n" \
"s_add_u32 m0,s80,4096\ns_add_u32 s49,s68,28672\nbuffer_load_dwordx4 v248,%[sws],s49 offen offset:0 lds\n" \
"v_mfma_f32_16x16x32_bf16 v[108:111],v[152:155],v[188:191],v[108:111]\nv_mfma_f32_16x16x32_bf16 v[112:115],v[156:159],v[176:179],v[112:115]\n" \
"v_mfma_f32_16x16x32_bf16 v[116:119],v[156:159],v[180:183],v[116:119]\nbuffer_load_dwordx4 v248,%[sws],s49 offen offset:1024 lds\n" \
"v_mfma_f32_16x16x32_bf16 v[120:123],v[156:159],v[184:187],v[120:123]\nv_mfma_f32_16x16x32_bf16 v[124:127],v[156:159],v[188:191],v[124:127]\ns_nop 7\n" \
"v_cvt_pk_bf16_f32 v136,v0,v1\nv_cvt_pk_bf16_f32 v137,v2,v3\nv_cvt_pk_bf16_f32 v138,v16,v17\nv_cvt_pk_bf16_f32 v139,v18,v19\n" \
"v_cvt_pk_bf16_f32 v140,v4,v5\nv_cvt_pk_bf16_f32 v141,v6,v7\nv_cvt_pk_bf16_f32 v142,v20,v21\nv_cvt_pk_bf16_f32 v143,v22,v23\n" \
"v_cvt_pk_bf16_f32 v144,v8,v9\nv_cvt_pk_bf16_f32 v145,v10,v11\nv_cvt_pk_bf16_f32 v146,v24,v25\nv_cvt_pk_bf16_f32 v147,v26,v27\n" \
"v_cvt_pk_bf16_f32 v148,v12,v13\nv_cvt_pk_bf16_f32 v149,v14,v15\nv_cvt_pk_bf16_f32 v150,v28,v29\nv_cvt_pk_bf16_f32 v151,v30,v31\n" \
"v_cvt_pk_bf16_f32 v152,v32,v33\nv_cvt_pk_bf16_f32 v153,v34,v35\nv_cvt_pk_bf16_f32 v154,v48,v49\nv_cvt_pk_bf16_f32 v155,v50,v51\n" \
"v_cvt_pk_bf16_f32 v156,v36,v37\nv_cvt_pk_bf16_f32 v157,v38,v39\nv_cvt_pk_bf16_f32 v158,v52,v53\nv_cvt_pk_bf16_f32 v159,v54,v55\n" \
"v_cvt_pk_bf16_f32 v160,v40,v41\nv_cvt_pk_bf16_f32 v161,v42,v43\nv_cvt_pk_bf16_f32 v162,v56,v57\nv_cvt_pk_bf16_f32 v163,v58,v59\n" \
"v_cvt_pk_bf16_f32 v164,v44,v45\nv_cvt_pk_bf16_f32 v165,v46,v47\nv_cvt_pk_bf16_f32 v166,v60,v61\nv_cvt_pk_bf16_f32 v167,v62,v63\n" \
"v_cvt_pk_bf16_f32 v168,v64,v65\nv_cvt_pk_bf16_f32 v169,v66,v67\nv_cvt_pk_bf16_f32 v170,v80,v81\nv_cvt_pk_bf16_f32 v171,v82,v83\n" \
"v_cvt_pk_bf16_f32 v172,v68,v69\nv_cvt_pk_bf16_f32 v173,v70,v71\nv_cvt_pk_bf16_f32 v174,v84,v85\nv_cvt_pk_bf16_f32 v175,v86,v87\n" \
"v_cvt_pk_bf16_f32 v176,v72,v73\nv_cvt_pk_bf16_f32 v177,v74,v75\nv_cvt_pk_bf16_f32 v178,v88,v89\nv_cvt_pk_bf16_f32 v179,v90,v91\n" \
"v_cvt_pk_bf16_f32 v180,v76,v77\nv_cvt_pk_bf16_f32 v181,v78,v79\nv_cvt_pk_bf16_f32 v182,v92,v93\nv_cvt_pk_bf16_f32 v183,v94,v95\n" \
"v_cvt_pk_bf16_f32 v184,v96,v97\nv_cvt_pk_bf16_f32 v185,v98,v99\nv_cvt_pk_bf16_f32 v186,v112,v113\nv_cvt_pk_bf16_f32 v187,v114,v115\n" \
"v_cvt_pk_bf16_f32 v188,v100,v101\nv_cvt_pk_bf16_f32 v189,v102,v103\nv_cvt_pk_bf16_f32 v190,v116,v117\nv_cvt_pk_bf16_f32 v191,v118,v119\n" \
"v_cvt_pk_bf16_f32 v224,v104,v105\nv_cvt_pk_bf16_f32 v225,v106,v107\nv_cvt_pk_bf16_f32 v226,v120,v121\nv_cvt_pk_bf16_f32 v227,v122,v123\n" \
"v_cvt_pk_bf16_f32 v228,v108,v109\nv_cvt_pk_bf16_f32 v229,v110,v111\nv_cvt_pk_bf16_f32 v230,v124,v125\nv_cvt_pk_bf16_f32 v231,v126,v127\ns_nop 1\n" \
"v_mov_b32 v0,0\nv_mov_b32 v1,0\nv_mov_b32 v2,0\nv_mov_b32 v3,0\nv_mov_b32 v4,0\nv_mov_b32 v5,0\nv_mov_b32 v6,0\nv_mov_b32 v7,0\nv_mov_b32 v8,0\n" \
"v_mov_b32 v9,0\nv_mov_b32 v10,0\nv_mov_b32 v11,0\nv_mov_b32 v12,0\nv_mov_b32 v13,0\nv_mov_b32 v14,0\nv_mov_b32 v15,0\nv_mov_b32 v16,0\nv_mov_b32 v17,0\n" \
"v_mov_b32 v18,0\nv_mov_b32 v19,0\nv_mov_b32 v20,0\nv_mov_b32 v21,0\nv_mov_b32 v22,0\nv_mov_b32 v23,0\nv_mov_b32 v24,0\nv_mov_b32 v25,0\n" \
"v_mov_b32 v26,0\nv_mov_b32 v27,0\nv_mov_b32 v28,0\nv_mov_b32 v29,0\nv_mov_b32 v30,0\nv_mov_b32 v31,0\nv_add_u32 v252,90112,v253\n" \
"v_add_u32 v255,65536,v253\nds_read_b128 v[116:119],v255\ns_waitcnt vmcnt(6)\ns_waitcnt lgkmcnt(0)\n" \
"v_mfma_f32_32x32x16_bf16 v[32:47],v[116:119],v[128:131],0\nv_mfma_f32_32x32x16_bf16 v[48:63],v[116:119],v[132:135],0\ns_nop 7\ns_nop 7\n" \
"v_max3_f32 v120,v32,v33,v34\nv_max3_f32 v121,v35,v36,v37\nv_max3_f32 v120,v120,v38,v39\nv_max3_f32 v121,v121,v40,v41\nv_max3_f32 v120,v120,v42,v43\n" \
"v_max3_f32 v121,v121,v44,v45\nv_max3_f32 v120,v120,v46,v47\nv_max_f32 v120,v120,v121\nv_mov_b32 v121,v120\ns_nop 1\nv_permlane32_swap_b32 v120,v121\n" \
"s_nop 1\nv_max_f32 v120,v120,v121\nv_sub_f32 v64,0,v120\nv_sub_f32 v65,0,v120\nv_sub_f32 v66,0,v120\nv_sub_f32 v67,0,v120\nv_sub_f32 v68,0,v120\n" \
"v_sub_f32 v69,0,v120\nv_sub_f32 v70,0,v120\nv_sub_f32 v71,0,v120\nv_sub_f32 v72,0,v120\nv_sub_f32 v73,0,v120\nv_sub_f32 v74,0,v120\n" \
"v_sub_f32 v75,0,v120\nv_sub_f32 v76,0,v120\nv_sub_f32 v77,0,v120\nv_sub_f32 v78,0,v120\nv_sub_f32 v79,0,v120\nv_max3_f32 v122,v48,v49,v50\n" \
"v_max3_f32 v123,v51,v52,v53\nv_max3_f32 v122,v122,v54,v55\nv_max3_f32 v123,v123,v56,v57\nv_max3_f32 v122,v122,v58,v59\nv_max3_f32 v123,v123,v60,v61\n" \
"v_max3_f32 v122,v122,v62,v63\nv_max_f32 v122,v122,v123\nv_mov_b32 v123,v122\ns_nop 1\nv_permlane32_swap_b32 v122,v123\ns_nop 1\n" \
"v_max_f32 v122,v122,v123\nv_sub_f32 v80,0,v122\nv_sub_f32 v81,0,v122\nv_sub_f32 v82,0,v122\nv_sub_f32 v83,0,v122\nv_sub_f32 v84,0,v122\n" \
"v_sub_f32 v85,0,v122\nv_sub_f32 v86,0,v122\nv_sub_f32 v87,0,v122\nv_sub_f32 v88,0,v122\nv_sub_f32 v89,0,v122\nv_sub_f32 v90,0,v122\n" \
"v_sub_f32 v91,0,v122\nv_sub_f32 v92,0,v122\nv_sub_f32 v93,0,v122\nv_sub_f32 v94,0,v122\nv_sub_f32 v95,0,v122\nv_mov_b32 v100,0\nv_mov_b32 v101,0\n" \
"v_mov_b32 v102,0\nv_mov_b32 v103,0\nv_mov_b32 v60,0\nv_mov_b32 v61,0\nv_mov_b32 v62,0\nv_mov_b32 v63,0\nv_mov_b32 v124,0\nv_mov_b32 v125,0\n" \
"v_mov_b32 v126,0\nv_mov_b32 v127,0\nds_read_b128 v[104:107],v252 offset:0\nds_read_b128 v[108:111],v252 offset:1024\n" \
"ds_read_b128 v[112:115],v252 offset:2048\ns_waitcnt lgkmcnt(0)\nv_mfma_f32_32x32x16_bf16 v[32:47],v[104:107],v[128:131],v[64:79]\ns_nop 7\ns_nop 7\n" \
"v_exp_f32 v32,v32\nv_cvt_pk_bf16_f32 v102,v60,v61\nv_exp_f32 v33,v33\nv_exp_f32 v34,v34\nv_cvt_pk_bf16_f32 v103,v62,v63\n" \
"v_mfma_f32_32x32x16_bf16 v[48:63],v[104:107],v[132:135],v[80:95]\ns_setprio 1\nv_exp_f32 v35,v35\nv_cvt_pk_bf16_f32 v96,v32,v33\nv_exp_f32 v36,v36\n" \
"ds_read_b128 v[116:119],v252 offset:3072\nv_exp_f32 v37,v37\nv_cvt_pk_bf16_f32 v97,v34,v35\nv_exp_f32 v38,v38\n" \
"v_mfma_f32_32x32x16_bf16 v[16:31],v[124:127],v[100:103],v[16:31]\nv_exp_f32 v39,v39\nv_cvt_pk_bf16_f32 v98,v36,v37\nv_exp_f32 v40,v40\n" \
"ds_read_b128 v[120:123],v252 offset:4096\nv_exp_f32 v41,v41\nv_cvt_pk_bf16_f32 v99,v38,v39\nv_exp_f32 v42,v42\n" \
"ds_read_b128 v[124:127],v252 offset:5120\nv_exp_f32 v43,v43\nv_cvt_pk_bf16_f32 v100,v40,v41\nv_exp_f32 v44,v44\n" \
"v_mfma_f32_32x32x16_bf16 v[0:15],v[108:111],v[96:99],v[0:15]\nv_exp_f32 v45,v45\nv_cvt_pk_bf16_f32 v101,v42,v43\nv_exp_f32 v46,v46\nv_exp_f32 v47,v47\n" \
"v_exp_f32 v48,v48\nv_cvt_pk_bf16_f32 v102,v44,v45\nv_exp_f32 v49,v49\nv_exp_f32 v50,v50\nv_cvt_pk_bf16_f32 v103,v46,v47\ns_waitcnt lgkmcnt(2)\n" \
"v_mfma_f32_32x32x16_bf16 v[32:47],v[116:119],v[128:131],v[64:79]\nv_exp_f32 v51,v51\nv_cvt_pk_bf16_f32 v96,v48,v49\nv_exp_f32 v52,v52\n" \
"v_exp_f32 v53,v53\nv_cvt_pk_bf16_f32 v97,v50,v51\nv_exp_f32 v54,v54\nv_mfma_f32_32x32x16_bf16 v[0:15],v[112:115],v[100:103],v[0:15]\n" \
"s_waitcnt vmcnt(0)\ns_add_u32 s51,s75,0\nbuffer_store_dwordx4 v[136:139],v251,%[sws],s51 offen offset:0 sc1\nv_exp_f32 v55,v55\n" \
"v_cvt_pk_bf16_f32 v98,v52,v53\nv_exp_f32 v56,v56\nv_exp_f32 v57,v57\nv_cvt_pk_bf16_f32 v99,v54,v55\nv_exp_f32 v58,v58\nv_exp_f32 v59,v59\n" \
"v_cvt_pk_bf16_f32 v100,v56,v57\nv_exp_f32 v60,v60\nv_mfma_f32_32x32x16_bf16 v[16:31],v[108:111],v[96:99],v[16:31]\nv_exp_f32 v61,v61\n" \
"v_cvt_pk_bf16_f32 v101,v58,v59\nv_exp_f32 v62,v62\nv_exp_f32 v63,v63\ns_waitcnt lgkmcnt(0)\nv_exp_f32 v32,v32\nv_cvt_pk_bf16_f32 v102,v60,v61\n" \
"v_exp_f32 v33,v33\nv_exp_f32 v34,v34\nv_cvt_pk_bf16_f32 v103,v62,v63\nv_mfma_f32_32x32x16_bf16 v[48:63],v[116:119],v[132:135],v[80:95]\n" \
"buffer_store_dwordx4 v[140:143],v251,%[sws],s51 offen offset:128 sc1\nv_exp_f32 v35,v35\nv_cvt_pk_bf16_f32 v96,v32,v33\nv_exp_f32 v36,v36\n" \
"ds_read_b128 v[104:107],v252 offset:6144\nv_exp_f32 v37,v37\nv_cvt_pk_bf16_f32 v97,v34,v35\nv_exp_f32 v38,v38\n" \
"v_mfma_f32_32x32x16_bf16 v[16:31],v[112:115],v[100:103],v[16:31]\nv_exp_f32 v39,v39\nv_cvt_pk_bf16_f32 v98,v36,v37\nv_exp_f32 v40,v40\n" \
"ds_read_b128 v[108:111],v252 offset:7168\nv_exp_f32 v41,v41\nv_cvt_pk_bf16_f32 v99,v38,v39\nv_exp_f32 v42,v42\n" \
"ds_read_b128 v[112:115],v252 offset:8192\nv_exp_f32 v43,v43\nv_cvt_pk_bf16_f32 v100,v40,v41\nv_exp_f32 v44,v44\n" \
"v_mfma_f32_32x32x16_bf16 v[0:15],v[120:123],v[96:99],v[0:15]\nv_exp_f32 v45,v45\nv_cvt_pk_bf16_f32 v101,v42,v43\nv_exp_f32 v46,v46\nv_exp_f32 v47,v47\n" \
"v_exp_f32 v48,v48\nv_cvt_pk_bf16_f32 v102,v44,v45\nv_exp_f32 v49,v49\nv_exp_f32 v50,v50\nv_cvt_pk_bf16_f32 v103,v46,v47\ns_waitcnt lgkmcnt(2)\n" \
"v_mfma_f32_32x32x16_bf16 v[32:47],v[104:107],v[128:131],v[64:79]\nbuffer_store_dwordx4 v[144:147],v251,%[sws],s51 offen offset:1024 sc1\n" \
"v_exp_f32 v51,v51\nv_cvt_pk_bf16_f32 v96,v48,v49\nv_exp_f32 v52,v52\nv_exp_f32 v53,v53\nv_cvt_pk_bf16_f32 v97,v50,v51\nv_exp_f32 v54,v54\n" \
"v_mfma_f32_32x32x16_bf16 v[0:15],v[124:127],v[100:103],v[0:15]\nv_exp_f32 v55,v55\nv_cvt_pk_bf16_f32 v98,v52,v53\nv_exp_f32 v56,v56\nv_exp_f32 v57,v57\n" \
"v_cvt_pk_bf16_f32 v99,v54,v55\nv_exp_f32 v58,v58\nv_exp_f32 v59,v59\nv_cvt_pk_bf16_f32 v100,v56,v57\nv_exp_f32 v60,v60\n" \
"v_mfma_f32_32x32x16_bf16 v[16:31],v[120:123],v[96:99],v[16:31]\nbuffer_store_dwordx4 v[148:151],v251,%[sws],s51 offen offset:1152 sc1\n" \
"v_exp_f32 v61,v61\nv_cvt_pk_bf16_f32 v101,v58,v59\nv_exp_f32 v62,v62\nv_exp_f32 v63,v63\ns_waitcnt lgkmcnt(0)\nv_exp_f32 v32,v32\n" \
"v_cvt_pk_bf16_f32 v102,v60,v61\nv_exp_f32 v33,v33\nv_exp_f32 v34,v34\nv_cvt_pk_bf16_f32 v103,v62,v63\n" \
"v_mfma_f32_32x32x16_bf16 v[48:63],v[104:107],v[132:135],v[80:95]\nv_exp_f32 v35,v35\nv_cvt_pk_bf16_f32 v96,v32,v33\nv_exp_f32 v36,v36\n" \
"ds_read_b128 v[116:119],v252 offset:9216\nv_exp_f32 v37,v37\nv_cvt_pk_bf16_f32 v97,v34,v35\nv_exp_f32 v38,v38\n" \
"v_mfma_f32_32x32x16_bf16 v[16:31],v[124:127],v[100:103],v[16:31]\nv_exp_f32 v39,v39\nv_cvt_pk_bf16_f32 v98,v36,v37\nv_exp_f32 v40,v40\n" \
"ds_read_b128 v[120:123],v252 offset:10240\nv_exp_f32 v41,v41\nv_cvt_pk_bf16_f32 v99,v38,v39\nv_exp_f32 v42,v42\n" \
"ds_read_b128 v[124:127],v252 offset:11264\nv_exp_f32 v43,v43\nv_cvt_pk_bf16_f32 v100,v40,v41\nv_exp_f32 v44,v44\n" \
"v_mfma_f32_32x32x16_bf16 v[0:15],v[108:111],v[96:99],v[0:15]\ns_add_u32 s51,s75,16384\n" \
"buffer_store_dwordx4 v[152:155],v251,%[sws],s51 offen offset:0 sc1\nv_exp_f32 v45,v45\nv_cvt_pk_bf16_f32 v101,v42,v43\nv_exp_f32 v46,v46\n" \
"v_exp_f32 v47,v47\nv_exp_f32 v48,v48\nv_cvt_pk_bf16_f32 v102,v44,v45\nv_exp_f32 v49,v49\nv_exp_f32 v50,v50\nv_cvt_pk_bf16_f32 v103,v46,v47\n" \
"s_waitcnt lgkmcnt(2)\nv_mfma_f32_32x32x16_bf16 v[32:47],v[116:119],v[128:131],v[64:79]\nv_exp_f32 v51,v51\nv_cvt_pk_bf16_f32 v96,v48,v49\n" \
"v_exp_f32 v52,v52\nv_exp_f32 v53,v53\nv_cvt_pk_bf16_f32 v97,v50,v51\nv_exp_f32 v54,v54\nv_mfma_f32_32x32x16_bf16 v[0:15],v[112:115],v[100:103],v[0:15]\n" \
"v_exp_f32 v55,v55\nv_cvt_pk_bf16_f32 v98,v52,v53\nv_exp_f32 v56,v56\nv_exp_f32 v57,v57\nv_cvt_pk_bf16_f32 v99,v54,v55\nv_exp_f32 v58,v58\n" \
"buffer_store_dwordx4 v[156:159],v251,%[sws],s51 offen offset:128 sc1\nv_exp_f32 v59,v59\nv_cvt_pk_bf16_f32 v100,v56,v57\nv_exp_f32 v60,v60\n" \
"v_mfma_f32_32x32x16_bf16 v[16:31],v[108:111],v[96:99],v[16:31]\nv_exp_f32 v61,v61\nv_cvt_pk_bf16_f32 v101,v58,v59\nv_exp_f32 v62,v62\n" \
"v_exp_f32 v63,v63\ns_waitcnt lgkmcnt(0)\nv_exp_f32 v32,v32\nv_cvt_pk_bf16_f32 v102,v60,v61\nv_exp_f32 v33,v33\nv_exp_f32 v34,v34\n" \
"v_cvt_pk_bf16_f32 v103,v62,v63\nv_mfma_f32_32x32x16_bf16 v[48:63],v[116:119],v[132:135],v[80:95]\nv_exp_f32 v35,v35\nv_cvt_pk_bf16_f32 v96,v32,v33\n" \
"v_exp_f32 v36,v36\nds_read_b128 v[104:107],v252 offset:12288\nv_exp_f32 v37,v37\nv_cvt_pk_bf16_f32 v97,v34,v35\nv_exp_f32 v38,v38\n" \
"v_mfma_f32_32x32x16_bf16 v[16:31],v[112:115],v[100:103],v[16:31]\nv_exp_f32 v39,v39\nv_cvt_pk_bf16_f32 v98,v36,v37\nv_exp_f32 v40,v40\n" \
"ds_read_b128 v[108:111],v252 offset:13312\nv_exp_f32 v41,v41\nv_cvt_pk_bf16_f32 v99,v38,v39\nv_exp_f32 v42,v42\n" \
"ds_read_b128 v[112:115],v252 offset:14336\nbuffer_store_dwordx4 v[160:163],v251,%[sws],s51 offen offset:1024 sc1\nv_exp_f32 v43,v43\n" \
"v_cvt_pk_bf16_f32 v100,v40,v41\nv_exp_f32 v44,v44\nv_mfma_f32_32x32x16_bf16 v[0:15],v[120:123],v[96:99],v[0:15]\nv_exp_f32 v45,v45\n" \
"v_cvt_pk_bf16_f32 v101,v42,v43\nv_exp_f32 v46,v46\nv_exp_f32 v47,v47\nv_exp_f32 v48,v48\nv_cvt_pk_bf16_f32 v102,v44,v45\nv_exp_f32 v49,v49\n" \
"v_exp_f32 v50,v50\nv_cvt_pk_bf16_f32 v103,v46,v47\ns_waitcnt lgkmcnt(2)\nv_mfma_f32_32x32x16_bf16 v[32:47],v[104:107],v[128:131],v[64:79]\n" \
"v_exp_f32 v51,v51\nv_cvt_pk_bf16_f32 v96,v48,v49\nv_exp_f32 v52,v52\nv_exp_f32 v53,v53\nv_cvt_pk_bf16_f32 v97,v50,v51\nv_exp_f32 v54,v54\n" \
"v_mfma_f32_32x32x16_bf16 v[0:15],v[124:127],v[100:103],v[0:15]\nbuffer_store_dwordx4 v[164:167],v251,%[sws],s51 offen offset:1152 sc1\n" \
"v_exp_f32 v55,v55\nv_cvt_pk_bf16_f32 v98,v52,v53\nv_exp_f32 v56,v56\nv_exp_f32 v57,v57\nv_cvt_pk_bf16_f32 v99,v54,v55\nv_exp_f32 v58,v58\n" \
"v_exp_f32 v59,v59\nv_cvt_pk_bf16_f32 v100,v56,v57\nv_exp_f32 v60,v60\nv_mfma_f32_32x32x16_bf16 v[16:31],v[120:123],v[96:99],v[16:31]\n" \
"v_exp_f32 v61,v61\nv_cvt_pk_bf16_f32 v101,v58,v59\nv_exp_f32 v62,v62\nv_exp_f32 v63,v63\ns_waitcnt lgkmcnt(0)\nv_exp_f32 v32,v32\n" \
"v_cvt_pk_bf16_f32 v102,v60,v61\nv_exp_f32 v33,v33\nv_exp_f32 v34,v34\nv_cvt_pk_bf16_f32 v103,v62,v63\n" \
"v_mfma_f32_32x32x16_bf16 v[48:63],v[104:107],v[132:135],v[80:95]\nv_exp_f32 v35,v35\nv_cvt_pk_bf16_f32 v96,v32,v33\nv_exp_f32 v36,v36\n" \
"ds_read_b128 v[116:119],v252 offset:15360\nv_exp_f32 v37,v37\nv_cvt_pk_bf16_f32 v97,v34,v35\nv_exp_f32 v38,v38\n" \
"v_mfma_f32_32x32x16_bf16 v[16:31],v[124:127],v[100:103],v[16:31]\ns_add_u32 s51,s75,32768\n" \
"buffer_store_dwordx4 v[168:171],v251,%[sws],s51 offen offset:0 sc1\nv_exp_f32 v39,v39\nv_cvt_pk_bf16_f32 v98,v36,v37\nv_exp_f32 v40,v40\n" \
"ds_read_b128 v[120:123],v252 offset:16384\nv_exp_f32 v41,v41\nv_cvt_pk_bf16_f32 v99,v38,v39\nv_exp_f32 v42,v42\n" \
"ds_read_b128 v[124:127],v252 offset:17408\nv_exp_f32 v43,v43\nv_cvt_pk_bf16_f32 v100,v40,v41\nv_exp_f32 v44,v44\n" \
"v_mfma_f32_32x32x16_bf16 v[0:15],v[108:111],v[96:99],v[0:15]\nv_exp_f32 v45,v45\nv_cvt_pk_bf16_f32 v101,v42,v43\nv_exp_f32 v46,v46\nv_exp_f32 v47,v47\n" \
"v_exp_f32 v48,v48\nv_cvt_pk_bf16_f32 v102,v44,v45\nv_exp_f32 v49,v49\nv_exp_f32 v50,v50\nv_cvt_pk_bf16_f32 v103,v46,v47\ns_waitcnt lgkmcnt(2)\n" \
"v_mfma_f32_32x32x16_bf16 v[32:47],v[116:119],v[128:131],v[64:79]\nbuffer_store_dwordx4 v[172:175],v251,%[sws],s51 offen offset:128 sc1\n" \
"v_exp_f32 v51,v51\nv_cvt_pk_bf16_f32 v96,v48,v49\nv_exp_f32 v52,v52\nv_exp_f32 v53,v53\nv_cvt_pk_bf16_f32 v97,v50,v51\nv_exp_f32 v54,v54\n" \
"v_mfma_f32_32x32x16_bf16 v[0:15],v[112:115],v[100:103],v[0:15]\nv_exp_f32 v55,v55\nv_cvt_pk_bf16_f32 v98,v52,v53\nv_exp_f32 v56,v56\nv_exp_f32 v57,v57\n" \
"v_cvt_pk_bf16_f32 v99,v54,v55\nv_exp_f32 v58,v58\nv_exp_f32 v59,v59\nv_cvt_pk_bf16_f32 v100,v56,v57\nv_exp_f32 v60,v60\n" \
"v_mfma_f32_32x32x16_bf16 v[16:31],v[108:111],v[96:99],v[16:31]\nv_exp_f32 v61,v61\nv_cvt_pk_bf16_f32 v101,v58,v59\nv_exp_f32 v62,v62\n" \
"v_exp_f32 v63,v63\ns_waitcnt lgkmcnt(0)\nv_exp_f32 v32,v32\nv_cvt_pk_bf16_f32 v102,v60,v61\nv_exp_f32 v33,v33\nv_exp_f32 v34,v34\n" \
"v_cvt_pk_bf16_f32 v103,v62,v63\nv_mfma_f32_32x32x16_bf16 v[48:63],v[116:119],v[132:135],v[80:95]\n" \
"buffer_store_dwordx4 v[176:179],v251,%[sws],s51 offen offset:1024 sc1\nv_exp_f32 v35,v35\nv_cvt_pk_bf16_f32 v96,v32,v33\nv_exp_f32 v36,v36\n" \
"ds_read_b128 v[104:107],v252 offset:18432\nv_exp_f32 v37,v37\nv_cvt_pk_bf16_f32 v97,v34,v35\nv_exp_f32 v38,v38\n" \
"v_mfma_f32_32x32x16_bf16 v[16:31],v[112:115],v[100:103],v[16:31]\nv_exp_f32 v39,v39\nv_cvt_pk_bf16_f32 v98,v36,v37\nv_exp_f32 v40,v40\n" \
"ds_read_b128 v[108:111],v252 offset:19456\nv_exp_f32 v41,v41\nv_cvt_pk_bf16_f32 v99,v38,v39\nv_exp_f32 v42,v42\n" \
"ds_read_b128 v[112:115],v252 offset:20480\nv_exp_f32 v43,v43\nv_cvt_pk_bf16_f32 v100,v40,v41\nv_exp_f32 v44,v44\n" \
"v_mfma_f32_32x32x16_bf16 v[0:15],v[120:123],v[96:99],v[0:15]\nbuffer_store_dwordx4 v[180:183],v251,%[sws],s51 offen offset:1152 sc1\nv_exp_f32 v45,v45\n" \
"v_cvt_pk_bf16_f32 v101,v42,v43\nv_exp_f32 v46,v46\nv_exp_f32 v47,v47\nv_exp_f32 v48,v48\nv_cvt_pk_bf16_f32 v102,v44,v45\nv_exp_f32 v49,v49\n" \
"v_exp_f32 v50,v50\nv_cvt_pk_bf16_f32 v103,v46,v47\ns_waitcnt lgkmcnt(2)\nv_mfma_f32_32x32x16_bf16 v[32:47],v[104:107],v[128:131],v[64:79]\ns_setprio 0\n" \
"v_exp_f32 v51,v51\nv_cvt_pk_bf16_f32 v96,v48,v49\nv_exp_f32 v52,v52\nv_exp_f32 v53,v53\nv_cvt_pk_bf16_f32 v97,v50,v51\nv_exp_f32 v54,v54\n" \
"v_mfma_f32_32x32x16_bf16 v[0:15],v[124:127],v[100:103],v[0:15]\nv_exp_f32 v55,v55\nv_cvt_pk_bf16_f32 v98,v52,v53\nv_exp_f32 v56,v56\nv_exp_f32 v57,v57\n" \
"v_cvt_pk_bf16_f32 v99,v54,v55\nv_exp_f32 v58,v58\nv_exp_f32 v59,v59\nv_cvt_pk_bf16_f32 v100,v56,v57\nv_exp_f32 v60,v60\n" \
"v_mfma_f32_32x32x16_bf16 v[16:31],v[120:123],v[96:99],v[16:31]\ns_add_u32 s51,s75,49152\n" \
"buffer_store_dwordx4 v[184:187],v251,%[sws],s51 offen offset:0 sc1\nv_exp_f32 v61,v61\nv_cvt_pk_bf16_f32 v101,v58,v59\nv_exp_f32 v62,v62\n" \
"v_exp_f32 v63,v63\ns_waitcnt lgkmcnt(0)\nv_exp_f32 v32,v32\nv_cvt_pk_bf16_f32 v102,v60,v61\nv_exp_f32 v33,v33\nv_exp_f32 v34,v34\n" \
"v_cvt_pk_bf16_f32 v103,v62,v63\nv_mfma_f32_32x32x16_bf16 v[48:63],v[104:107],v[132:135],v[80:95]\nv_exp_f32 v35,v35\nv_cvt_pk_bf16_f32 v96,v32,v33\n" \
"v_exp_f32 v36,v36\nds_read_b128 v[116:119],v252 offset:21504\nv_exp_f32 v37,v37\nv_cvt_pk_bf16_f32 v97,v34,v35\nv_exp_f32 v38,v38\n" \
"v_mfma_f32_32x32x16_bf16 v[16:31],v[124:127],v[100:103],v[16:31]\nv_exp_f32 v39,v39\nv_cvt_pk_bf16_f32 v98,v36,v37\nv_exp_f32 v40,v40\n" \
"ds_read_b128 v[120:123],v252 offset:22528\nv_exp_f32 v41,v41\nv_cvt_pk_bf16_f32 v99,v38,v39\nv_exp_f32 v42,v42\n" \
"ds_read_b128 v[124:127],v252 offset:23552\nbuffer_store_dwordx4 v[188:191],v251,%[sws],s51 offen offset:128 sc1\nv_exp_f32 v43,v43\n" \
"v_cvt_pk_bf16_f32 v100,v40,v41\nv_exp_f32 v44,v44\nv_mfma_f32_32x32x16_bf16 v[0:15],v[108:111],v[96:99],v[0:15]\nv_exp_f32 v45,v45\n" \
"v_cvt_pk_bf16_f32 v101,v42,v43\nv_exp_f32 v46,v46\nv_exp_f32 v47,v47\nv_exp_f32 v48,v48\nv_cvt_pk_bf16_f32 v102,v44,v45\nv_exp_f32 v49,v49\n" \
"v_exp_f32 v50,v50\nv_cvt_pk_bf16_f32 v103,v46,v47\ns_waitcnt lgkmcnt(2)\nv_mfma_f32_32x32x16_bf16 v[32:47],v[116:119],v[128:131],v[64:79]\n" \
"v_exp_f32 v51,v51\nv_cvt_pk_bf16_f32 v96,v48,v49\nv_exp_f32 v52,v52\nv_exp_f32 v53,v53\nv_cvt_pk_bf16_f32 v97,v50,v51\nv_exp_f32 v54,v54\n" \
"v_mfma_f32_32x32x16_bf16 v[0:15],v[112:115],v[100:103],v[0:15]\nv_exp_f32 v55,v55\nv_cvt_pk_bf16_f32 v98,v52,v53\nv_exp_f32 v56,v56\nv_exp_f32 v57,v57\n" \
"v_cvt_pk_bf16_f32 v99,v54,v55\nv_exp_f32 v58,v58\nbuffer_store_dwordx4 v[224:227],v251,%[sws],s51 offen offset:1024 sc1\nv_exp_f32 v59,v59\n" \
"v_cvt_pk_bf16_f32 v100,v56,v57\nv_exp_f32 v60,v60\nv_mfma_f32_32x32x16_bf16 v[16:31],v[108:111],v[96:99],v[16:31]\ns_barrier\n" \
"v_add_u32 v252,24576,v253\ns_setprio 1\nv_exp_f32 v61,v61\nv_cvt_pk_bf16_f32 v101,v58,v59\nv_exp_f32 v62,v62\nv_exp_f32 v63,v63\ns_waitcnt lgkmcnt(0)\n" \
"v_exp_f32 v32,v32\nv_cvt_pk_bf16_f32 v102,v60,v61\nv_exp_f32 v33,v33\nv_exp_f32 v34,v34\nv_cvt_pk_bf16_f32 v103,v62,v63\n" \
"v_mfma_f32_32x32x16_bf16 v[48:63],v[116:119],v[132:135],v[80:95]\nv_exp_f32 v35,v35\nv_cvt_pk_bf16_f32 v96,v32,v33\nv_exp_f32 v36,v36\n" \
"ds_read_b128 v[104:107],v252 offset:0\nv_exp_f32 v37,v37\nv_cvt_pk_bf16_f32 v97,v34,v35\nv_exp_f32 v38,v38\n" \
"v_mfma_f32_32x32x16_bf16 v[16:31],v[112:115],v[100:103],v[16:31]\nbuffer_store_dwordx4 v[228:231],v251,%[sws],s51 offen offset:1152 sc1\n" \
"v_exp_f32 v39,v39\nv_cvt_pk_bf16_f32 v98,v36,v37\nv_exp_f32 v40,v40\nds_read_b128 v[108:111],v252 offset:1024\nv_exp_f32 v41,v41\n" \
"v_cvt_pk_bf16_f32 v99,v38,v39\nv_exp_f32 v42,v42\nds_read_b128 v[112:115],v252 offset:2048\nv_exp_f32 v43,v43\nv_cvt_pk_bf16_f32 v100,v40,v41\n" \
"v_exp_f32 v44,v44\nv_mfma_f32_32x32x16_bf16 v[0:15],v[120:123],v[96:99],v[0:15]\nv_exp_f32 v45,v45\nv_cvt_pk_bf16_f32 v101,v42,v43\nv_exp_f32 v46,v46\n" \
"v_exp_f32 v47,v47\nv_exp_f32 v48,v48\nv_cvt_pk_bf16_f32 v102,v44,v45\nv_exp_f32 v49,v49\nv_exp_f32 v50,v50\nv_cvt_pk_bf16_f32 v103,v46,v47\n" \
"s_waitcnt lgkmcnt(2)\nv_mfma_f32_32x32x16_bf16 v[32:47],v[104:107],v[128:131],v[64:79]\nv_exp_f32 v51,v51\nv_cvt_pk_bf16_f32 v96,v48,v49\n" \
"v_exp_f32 v52,v52\nv_exp_f32 v53,v53\nv_cvt_pk_bf16_f32 v97,v50,v51\nv_exp_f32 v54,v54\nv_mfma_f32_32x32x16_bf16 v[0:15],v[124:127],v[100:103],v[0:15]\n" \
"v_exp_f32 v55,v55\nv_cvt_pk_bf16_f32 v98,v52,v53\nv_exp_f32 v56,v56\nv_exp_f32 v57,v57\nv_cvt_pk_bf16_f32 v99,v54,v55\nv_exp_f32 v58,v58\n" \
"v_exp_f32 v59,v59\nv_cvt_pk_bf16_f32 v100,v56,v57\nv_exp_f32 v60,v60\nv_mfma_f32_32x32x16_bf16 v[16:31],v[120:123],v[96:99],v[16:31]\n" \
"v_exp_f32 v61,v61\nv_cvt_pk_bf16_f32 v101,v58,v59\nv_exp_f32 v62,v62\nv_exp_f32 v63,v63\ns_waitcnt lgkmcnt(0)\nv_exp_f32 v32,v32\n" \
"v_cvt_pk_bf16_f32 v102,v60,v61\nv_exp_f32 v33,v33\nv_exp_f32 v34,v34\nv_cvt_pk_bf16_f32 v103,v62,v63\n" \
"v_mfma_f32_32x32x16_bf16 v[48:63],v[104:107],v[132:135],v[80:95]\nv_exp_f32 v35,v35\nv_cvt_pk_bf16_f32 v96,v32,v33\nv_exp_f32 v36,v36\n" \
"ds_read_b128 v[116:119],v252 offset:3072\nv_exp_f32 v37,v37\nv_cvt_pk_bf16_f32 v97,v34,v35\nv_exp_f32 v38,v38\n" \
"v_mfma_f32_32x32x16_bf16 v[16:31],v[124:127],v[100:103],v[16:31]\nv_exp_f32 v39,v39\nv_cvt_pk_bf16_f32 v98,v36,v37\nv_exp_f32 v40,v40\n" \
"ds_read_b128 v[120:123],v252 offset:4096\nv_exp_f32 v41,v41\nv_cvt_pk_bf16_f32 v99,v38,v39\nv_exp_f32 v42,v42\n" \
"ds_read_b128 v[124:127],v252 offset:5120\nv_exp_f32 v43,v43\nv_cvt_pk_bf16_f32 v100,v40,v41\nv_exp_f32 v44,v44\n" \
"v_mfma_f32_32x32x16_bf16 v[0:15],v[108:111],v[96:99],v[0:15]\nv_exp_f32 v45,v45\nv_cvt_pk_bf16_f32 v101,v42,v43\nv_exp_f32 v46,v46\nv_exp_f32 v47,v47\n" \
"v_exp_f32 v48,v48\nv_cvt_pk_bf16_f32 v102,v44,v45\nv_exp_f32 v49,v49\nv_exp_f32 v50,v50\nv_cvt_pk_bf16_f32 v103,v46,v47\ns_waitcnt lgkmcnt(2)\n" \
"v_mfma_f32_32x32x16_bf16 v[32:47],v[116:119],v[128:131],v[64:79]\ns_setprio 0\nv_exp_f32 v51,v51\nv_cvt_pk_bf16_f32 v96,v48,v49\nv_exp_f32 v52,v52\n" \
"v_exp_f32 v53,v53\nv_cvt_pk_bf16_f32 v97,v50,v51\nv_exp_f32 v54,v54\nv_mfma_f32_32x32x16_bf16 v[0:15],v[112:115],v[100:103],v[0:15]\nv_exp_f32 v55,v55\n" \
"v_cvt_pk_bf16_f32 v98,v52,v53\nv_exp_f32 v56,v56\nv_exp_f32 v57,v57\nv_cvt_pk_bf16_f32 v99,v54,v55\nv_exp_f32 v58,v58\nv_exp_f32 v59,v59\n" \
"v_cvt_pk_bf16_f32 v100,v56,v57\nv_exp_f32 v60,v60\nv_mfma_f32_32x32x16_bf16 v[16:31],v[108:111],v[96:99],v[16:31]\nv_exp_f32 v61,v61\n" \
"v_cvt_pk_bf16_f32 v101,v58,v59\nv_exp_f32 v62,v62\nv_exp_f32 v63,v63\ns_waitcnt lgkmcnt(0)\nv_exp_f32 v32,v32\nv_cvt_pk_bf16_f32 v102,v60,v61\n" \
"v_exp_f32 v33,v33\nv_exp_f32 v34,v34\nv_cvt_pk_bf16_f32 v103,v62,v63\nv_mfma_f32_32x32x16_bf16 v[48:63],v[116:119],v[132:135],v[80:95]\n" \
"s_waitcnt vmcnt(0)\ns_barrier\ns_cmp_eq_u32 s77,0\ns_cbranch_scc1 20f\nv_mov_b32 v136,0\nv_mov_b32 v137,1\ns_mov_b64 exec,1\n" \
"global_atomic_add v136,v137,s[78:79] offset:64\ns_mov_b64 exec,-1\n20:\ns_setprio 1\nv_exp_f32 v35,v35\nv_cvt_pk_bf16_f32 v96,v32,v33\n" \
"v_exp_f32 v36,v36\nds_read_b128 v[104:107],v252 offset:6144\nv_exp_f32 v37,v37\nv_cvt_pk_bf16_f32 v97,v34,v35\nv_exp_f32 v38,v38\n" \
"v_mfma_f32_32x32x16_bf16 v[16:31],v[112:115],v[100:103],v[16:31]\nv_exp_f32 v39,v39\nv_cvt_pk_bf16_f32 v98,v36,v37\nv_exp_f32 v40,v40\n" \
"ds_read_b128 v[108:111],v252 offset:7168\nv_exp_f32 v41,v41\nv_cvt_pk_bf16_f32 v99,v38,v39\nv_exp_f32 v42,v42\n" \
"ds_read_b128 v[112:115],v252 offset:8192\nv_exp_f32 v43,v43\nv_cvt_pk_bf16_f32 v100,v40,v41\nv_exp_f32 v44,v44\n" \
"v_mfma_f32_32x32x16_bf16 v[0:15],v[120:123],v[96:99],v[0:15]\nv_mov_b32 v138,0\nglobal_load_dword v139,v138,s[78:79] sc1\nv_exp_f32 v45,v45\n" \
"v_cvt_pk_bf16_f32 v101,v42,v43\nv_exp_f32 v46,v46\nv_exp_f32 v47,v47\nv_exp_f32 v48,v48\nv_cvt_pk_bf16_f32 v102,v44,v45\nv_exp_f32 v49,v49\n" \
"v_exp_f32 v50,v50\nv_cvt_pk_bf16_f32 v103,v46,v47\ns_waitcnt lgkmcnt(2)\nv_mfma_f32_32x32x16_bf16 v[32:47],v[104:107],v[128:131],v[64:79]\n" \
"v_exp_f32 v51,v51\nv_cvt_pk_bf16_f32 v96,v48,v49\nv_exp_f32 v52,v52\nv_exp_f32 v53,v53\nv_cvt_pk_bf16_f32 v97,v50,v51\nv_exp_f32 v54,v54\n" \
"v_mfma_f32_32x32x16_bf16 v[0:15],v[124:127],v[100:103],v[0:15]\nv_exp_f32 v55,v55\nv_cvt_pk_bf16_f32 v98,v52,v53\nv_exp_f32 v56,v56\nv_exp_f32 v57,v57\n" \
"v_cvt_pk_bf16_f32 v99,v54,v55\nv_exp_f32 v58,v58\nv_exp_f32 v59,v59\nv_cvt_pk_bf16_f32 v100,v56,v57\nv_exp_f32 v60,v60\n" \
"v_mfma_f32_32x32x16_bf16 v[16:31],v[120:123],v[96:99],v[16:31]\nv_exp_f32 v61,v61\nv_cvt_pk_bf16_f32 v101,v58,v59\nv_exp_f32 v62,v62\n" \
"v_exp_f32 v63,v63\ns_waitcnt lgkmcnt(0)\nv_exp_f32 v32,v32\nv_cvt_pk_bf16_f32 v102,v60,v61\nv_exp_f32 v33,v33\nv_exp_f32 v34,v34\n" \
"v_cvt_pk_bf16_f32 v103,v62,v63\nv_mfma_f32_32x32x16_bf16 v[48:63],v[104:107],v[132:135],v[80:95]\nv_exp_f32 v35,v35\nv_cvt_pk_bf16_f32 v96,v32,v33\n" \
"v_exp_f32 v36,v36\nds_read_b128 v[116:119],v252 offset:9216\nv_exp_f32 v37,v37\nv_cvt_pk_bf16_f32 v97,v34,v35\nv_exp_f32 v38,v38\n" \
"v_mfma_f32_32x32x16_bf16 v[16:31],v[124:127],v[100:103],v[16:31]\nv_exp_f32 v39,v39\nv_cvt_pk_bf16_f32 v98,v36,v37\nv_exp_f32 v40,v40\n" \
"ds_read_b128 v[120:123],v252 offset:10240\nv_exp_f32 v41,v41\nv_cvt_pk_bf16_f32 v99,v38,v39\nv_exp_f32 v42,v42\n" \
"ds_read_b128 v[124:127],v252 offset:11264\ns_waitcnt vmcnt(0)\nv_readfirstlane_b32 s50,v139\ns_nop 0\ns_cmp_ge_u32 s50,4\ns_cbranch_scc1 22f\n" \
"v_mov_b32 v136,0\ns_mov_b32 s49,0x400000\n21:\nglobal_load_dword v137,v136,s[78:79] sc1\ns_waitcnt vmcnt(0)\nv_readfirstlane_b32 s50,v137\n" \
"s_sub_u32 s49,s49,1\ns_cbranch_scc1 22f\ns_cmp_ge_u32 s50,4\ns_cbranch_scc1 22f\ns_sleep 8\ns_branch 21b\n22:\ns_sub_u32 s48,s72,0\n" \
"s_and_b32 s46,s48,3\ns_sub_u32 s48,s48,s69\ns_lshl_b32 s44,s48,15\ns_add_u32 s45,s44,4096\ns_lshl_b32 s46,s46,15\ns_add_u32 s46,s46,s74\n" \
"s_mov_b32 m0,s46\ns_nop 0\nbuffer_load_dwordx4 v254,%[srd],s44 offen offset:0 sc1 lds\nbuffer_load_dwordx4 v254,%[srd],s44 offen offset:1024 sc1 lds\n" \
"v_exp_f32 v43,v43\nv_cvt_pk_bf16_f32 v100,v40,v41\nv_exp_f32 v44,v44\nv_mfma_f32_32x32x16_bf16 v[0:15],v[108:111],v[96:99],v[0:15]\n" \
"buffer_load_dwordx4 v254,%[srd],s44 offen offset:2048 sc1 lds\nbuffer_load_dwordx4 v254,%[srd],s44 offen offset:3072 sc1 lds\nv_exp_f32 v45,v45\n" \
"v_cvt_pk_bf16_f32 v101,v42,v43\nv_exp_f32 v46,v46\nv_exp_f32 v47,v47\nv_exp_f32 v48,v48\nv_cvt_pk_bf16_f32 v102,v44,v45\nv_exp_f32 v49,v49\n" \
"v_exp_f32 v50,v50\nv_cvt_pk_bf16_f32 v103,v46,v47\ns_waitcnt lgkmcnt(2)\nv_mfma_f32_32x32x16_bf16 v[32:47],v[116:119],v[128:131],v[64:79]\n" \
"s_add_u32 m0,s46,4096\ns_nop 0\nbuffer_load_dwordx4 v254,%[srd],s45 offen offset:0 sc1 lds\n" \
"buffer_load_dwordx4 v254,%[srd],s45 offen offset:1024 sc1 lds\nv_exp_f32 v51,v51\nv_cvt_pk_bf16_f32 v96,v48,v49\nv_exp_f32 v52,v52\nv_exp_f32 v53,v53\n" \
"v_cvt_pk_bf16_f32 v97,v50,v51\nv_exp_f32 v54,v54\nv_mfma_f32_32x32x16_bf16 v[0:15],v[112:115],v[100:103],v[0:15]\n" \
"buffer_load_dwordx4 v254,%[srd],s45 offen offset:2048 sc1 lds\nbuffer_load_dwordx4 v254,%[srd],s45 offen offset:3072 sc1 lds\nv_exp_f32 v55,v55\n" \
"v_cvt_pk_bf16_f32 v98,v52,v53\nv_exp_f32 v56,v56\nv_exp_f32 v57,v57\nv_cvt_pk_bf16_f32 v99,v54,v55\nv_exp_f32 v58,v58\ns_sub_u32 s48,s72,1\n" \
"s_and_b32 s46,s48,3\ns_sub_u32 s48,s48,s69\ns_lshl_b32 s44,s48,15\ns_add_u32 s45,s44,4096\ns_lshl_b32 s46,s46,15\ns_add_u32 s46,s46,s74\n" \
"s_mov_b32 m0,s46\ns_nop 0\nbuffer_load_dwordx4 v254,%[srd],s44 offen offset:0 sc1 lds\nbuffer_load_dwordx4 v254,%[srd],s44 offen offset:1024 sc1 lds\n" \
"v_exp_f32 v59,v59\nv_cvt_pk_bf16_f32 v100,v56,v57\nv_exp_f32 v60,v60\nv_mfma_f32_32x32x16_bf16 v[16:31],v[108:111],v[96:99],v[16:31]\n" \
"buffer_load_dwordx4 v254,%[srd],s44 offen offset:2048 sc1 lds\nbuffer_load_dwordx4 v254,%[srd],s44 offen offset:3072 sc1 lds\nv_exp_f32 v61,v61\n" \
"v_cvt_pk_bf16_f32 v101,v58,v59\nv_exp_f32 v62,v62\nv_exp_f32 v63,v63\ns_waitcnt lgkmcnt(0)\nv_exp_f32 v32,v32\nv_cvt_pk_bf16_f32 v102,v60,v61\n" \
"v_exp_f32 v33,v33\nv_exp_f32 v34,v34\nv_cvt_pk_bf16_f32 v103,v62,v63\nv_mfma_f32_32x32x16_bf16 v[48:63],v[116:119],v[132:135],v[80:95]\n" \
"s_add_u32 m0,s46,4096\ns_nop 0\nbuffer_load_dwordx4 v254,%[srd],s45 offen offset:0 sc1 lds\n" \
"buffer_load_dwordx4 v254,%[srd],s45 offen offset:1024 sc1 lds\nv_exp_f32 v35,v35\nv_cvt_pk_bf16_f32 v96,v32,v33\nv_exp_f32 v36,v36\n" \
"ds_read_b128 v[104:107],v252 offset:12288\nv_exp_f32 v37,v37\nv_cvt_pk_bf16_f32 v97,v34,v35\nv_exp_f32 v38,v38\n" \
"v_mfma_f32_32x32x16_bf16 v[16:31],v[112:115],v[100:103],v[16:31]\nbuffer_load_dwordx4 v254,%[srd],s45 offen offset:2048 sc1 lds\n" \
"buffer_load_dwordx4 v254,%[srd],s45 offen offset:3072 sc1 lds\nv_exp_f32 v39,v39\nv_cvt_pk_bf16_f32 v98,v36,v37\nv_exp_f32 v40,v40\n" \
"ds_read_b128 v[108:111],v252 offset:13312\nv_exp_f32 v41,v41\nv_cvt_pk_bf16_f32 v99,v38,v39\nv_exp_f32 v42,v42\n" \
"ds_read_b128 v[112:115],v252 offset:14336\nv_exp_f32 v43,v43\nv_cvt_pk_bf16_f32 v100,v40,v41\nv_exp_f32 v44,v44\n" \
"v_mfma_f32_32x32x16_bf16 v[0:15],v[120:123],v[96:99],v[0:15]\nv_exp_f32 v45,v45\nv_cvt_pk_bf16_f32 v101,v42,v43\nv_exp_f32 v46,v46\nv_exp_f32 v47,v47\n" \
"v_exp_f32 v48,v48\nv_cvt_pk_bf16_f32 v102,v44,v45\nv_exp_f32 v49,v49\nv_exp_f32 v50,v50\nv_cvt_pk_bf16_f32 v103,v46,v47\ns_waitcnt lgkmcnt(2)\n" \
"v_mfma_f32_32x32x16_bf16 v[32:47],v[104:107],v[128:131],v[64:79]\nv_exp_f32 v51,v51\nv_cvt_pk_bf16_f32 v96,v48,v49\nv_exp_f32 v52,v52\n" \
"v_exp_f32 v53,v53\nv_cvt_pk_bf16_f32 v97,v50,v51\nv_exp_f32 v54,v54\nv_mfma_f32_32x32x16_bf16 v[0:15],v[124:127],v[100:103],v[0:15]\nv_exp_f32 v55,v55\n" \
"v_cvt_pk_bf16_f32 v98,v52,v53\nv_exp_f32 v56,v56\nv_exp_f32 v57,v57\nv_cvt_pk_bf16_f32 v99,v54,v55\nv_exp_f32 v58,v58\nv_exp_f32 v59,v59\n" \
"v_cvt_pk_bf16_f32 v100,v56,v57\nv_exp_f32 v60,v60\nv_mfma_f32_32x32x16_bf16 v[16:31],v[120:123],v[96:99],v[16:31]\nv_exp_f32 v61,v61\n" \
"v_cvt_pk_bf16_f32 v101,v58,v59\nv_exp_f32 v62,v62\nv_exp_f32 v63,v63\ns_waitcnt lgkmcnt(0)\nv_exp_f32 v32,v32\nv_cvt_pk_bf16_f32 v102,v60,v61\n" \
"v_exp_f32 v33,v33\nv_exp_f32 v34,v34\nv_cvt_pk_bf16_f32 v103,v62,v63\nv_mfma_f32_32x32x16_bf16 v[48:63],v[104:107],v[132:135],v[80:95]\n" \
"v_exp_f32 v35,v35\nv_cvt_pk_bf16_f32 v96,v32,v33\nv_exp_f32 v36,v36\nds_read_b128 v[116:119],v252 offset:15360\nv_exp_f32 v37,v37\n" \
"v_cvt_pk_bf16_f32 v97,v34,v35\nv_exp_f32 v38,v38\nv_mfma_f32_32x32x16_bf16 v[16:31],v[124:127],v[100:103],v[16:31]\nv_exp_f32 v39,v39\n" \
"v_cvt_pk_bf16_f32 v98,v36,v37\nv_exp_f32 v40,v40\nds_read_b128 v[120:123],v252 offset:16384\nv_exp_f32 v41,v41\nv_cvt_pk_bf16_f32 v99,v38,v39\n" \
"v_exp_f32 v42,v42\nds_read_b128 v[124:127],v252 offset:17408\nv_exp_f32 v43,v43\nv_cvt_pk_bf16_f32 v100,v40,v41\nv_exp_f32 v44,v44\n" \
"v_mfma_f32_32x32x16_bf16 v[0:15],v[108:111],v[96:99],v[0:15]\nv_exp_f32 v45,v45\nv_cvt_pk_bf16_f32 v101,v42,v43\nv_exp_f32 v46,v46\nv_exp_f32 v47,v47\n" \
"v_exp_f32 v48,v48\nv_cvt_pk_bf16_f32 v102,v44,v45\nv_exp_f32 v49,v49\nv_exp_f32 v50,v50\nv_cvt_pk_bf16_f32 v103,v46,v47\ns_waitcnt lgkmcnt(2)\n" \
"v_mfma_f32_32x32x16_bf16 v[32:47],v[116:119],v[128:131],v[64:79]\nv_exp_f32 v51,v51\nv_cvt_pk_bf16_f32 v96,v48,v49\nv_exp_f32 v52,v52\n" \
"v_exp_f32 v53,v53\nv_cvt_pk_bf16_f32 v97,v50,v51\nv_exp_f32 v54,v54\nv_mfma_f32_32x32x16_bf16 v[0:15],v[112:115],v[100:103],v[0:15]\nv_exp_f32 v55,v55\n" \
"v_cvt_pk_bf16_f32 v98,v52,v53\nv_exp_f32 v56,v56\nv_exp_f32 v57,v57\nv_cvt_pk_bf16_f32 v99,v54,v55\nv_exp_f32 v58,v58\nv_exp_f32 v59,v59\n" \
"v_cvt_pk_bf16_f32 v100,v56,v57\nv_exp_f32 v60,v60\nv_mfma_f32_32x32x16_bf16 v[16:31],v[108:111],v[96:99],v[16:31]\nv_exp_f32 v61,v61\n" \
"v_cvt_pk_bf16_f32 v101,v58,v59\nv_exp_f32 v62,v62\nv_exp_f32 v63,v63\ns_waitcnt lgkmcnt(0)\nv_exp_f32 v32,v32\nv_cvt_pk_bf16_f32 v102,v60,v61\n" \
"v_exp_f32 v33,v33\nv_exp_f32 v34,v34\nv_cvt_pk_bf16_f32 v103,v62,v63\nv_mfma_f32_32x32x16_bf16 v[48:63],v[116:119],v[132:135],v[80:95]\n" \
"v_exp_f32 v35,v35\nv_cvt_pk_bf16_f32 v96,v32,v33\nv_exp_f32 v36,v36\nds_read_b128 v[104:107],v252 offset:18432\nv_exp_f32 v37,v37\n" \
"v_cvt_pk_bf16_f32 v97,v34,v35\nv_exp_f32 v38,v38\nv_mfma_f32_32x32x16_bf16 v[16:31],v[112:115],v[100:103],v[16:31]\nv_exp_f32 v39,v39\n" \
"v_cvt_pk_bf16_f32 v98,v36,v37\nv_exp_f32 v40,v40\nds_read_b128 v[108:111],v252 offset:19456\nv_exp_f32 v41,v41\nv_cvt_pk_bf16_f32 v99,v38,v39\n" \
"v_exp_f32 v42,v42\nds_read_b128 v[112:115],v252 offset:20480\nv_exp_f32 v43,v43\nv_cvt_pk_bf16_f32 v100,v40,v41\nv_exp_f32 v44,v44\n" \
"v_mfma_f32_32x32x16_bf16 v[0:15],v[120:123],v[96:99],v[0:15]\nv_exp_f32 v45,v45\nv_cvt_pk_bf16_f32 v101,v42,v43\nv_exp_f32 v46,v46\nv_exp_f32 v47,v47\n" \
"v_exp_f32 v48,v48\nv_cvt_pk_bf16_f32 v102,v44,v45\nv_exp_f32 v49,v49\nv_exp_f32 v50,v50\nv_cvt_pk_bf16_f32 v103,v46,v47\ns_waitcnt lgkmcnt(2)\n" \
"v_mfma_f32_32x32x16_bf16 v[32:47],v[104:107],v[128:131],v[64:79]\nv_exp_f32 v51,v51\nv_cvt_pk_bf16_f32 v96,v48,v49\nv_exp_f32 v52,v52\n" \
"v_exp_f32 v53,v53\nv_cvt_pk_bf16_f32 v97,v50,v51\nv_exp_f32 v54,v54\nv_mfma_f32_32x32x16_bf16 v[0:15],v[124:127],v[100:103],v[0:15]\nv_exp_f32 v55,v55\n" \
"v_cvt_pk_bf16_f32 v98,v52,v53\nv_exp_f32 v56,v56\nv_exp_f32 v57,v57\nv_cvt_pk_bf16_f32 v99,v54,v55\nv_exp_f32 v58,v58\nv_exp_f32 v59,v59\n" \
"v_cvt_pk_bf16_f32 v100,v56,v57\nv_exp_f32 v60,v60\nv_mfma_f32_32x32x16_bf16 v[16:31],v[120:123],v[96:99],v[16:31]\nv_exp_f32 v61,v61\n" \
"v_cvt_pk_bf16_f32 v101,v58,v59\nv_exp_f32 v62,v62\nv_exp_f32 v63,v63\ns_waitcnt lgkmcnt(0)\nv_exp_f32 v32,v32\nv_cvt_pk_bf16_f32 v102,v60,v61\n" \
"v_exp_f32 v33,v33\nv_exp_f32 v34,v34\nv_cvt_pk_bf16_f32 v103,v62,v63\nv_mfma_f32_32x32x16_bf16 v[48:63],v[104:107],v[132:135],v[80:95]\n" \
"v_exp_f32 v35,v35\nv_cvt_pk_bf16_f32 v96,v32,v33\nv_exp_f32 v36,v36\nds_read_b128 v[116:119],v252 offset:21504\nv_exp_f32 v37,v37\n" \
"v_cvt_pk_bf16_f32 v97,v34,v35\nv_exp_f32 v38,v38\nv_mfma_f32_32x32x16_bf16 v[16:31],v[124:127],v[100:103],v[16:31]\nv_exp_f32 v39,v39\n" \
"v_cvt_pk_bf16_f32 v98,v36,v37\nv_exp_f32 v40,v40\nds_read_b128 v[120:123],v252 offset:22528\nv_exp_f32 v41,v41\nv_cvt_pk_bf16_f32 v99,v38,v39\n" \
"v_exp_f32 v42,v42\nds_read_b128 v[124:127],v252 offset:23552\nv_exp_f32 v43,v43\nv_cvt_pk_bf16_f32 v100,v40,v41\nv_exp_f32 v44,v44\n" \
"v_mfma_f32_32x32x16_bf16 v[0:15],v[108:111],v[96:99],v[0:15]\nv_exp_f32 v45,v45\nv_cvt_pk_bf16_f32 v101,v42,v43\nv_exp_f32 v46,v46\nv_exp_f32 v47,v47\n" \
"v_exp_f32 v48,v48\nv_cvt_pk_bf16_f32 v102,v44,v45\nv_exp_f32 v49,v49\nv_exp_f32 v50,v50\nv_cvt_pk_bf16_f32 v103,v46,v47\ns_waitcnt lgkmcnt(2)\n" \
"v_mfma_f32_32x32x16_bf16 v[32:47],v[116:119],v[128:131],v[64:79]\ns_setprio 0\nv_exp_f32 v51,v51\nv_cvt_pk_bf16_f32 v96,v48,v49\nv_exp_f32 v52,v52\n" \
"v_exp_f32 v53,v53\nv_cvt_pk_bf16_f32 v97,v50,v51\nv_exp_f32 v54,v54\nv_mfma_f32_32x32x16_bf16 v[0:15],v[112:115],v[100:103],v[0:15]\nv_exp_f32 v55,v55\n" \
"v_cvt_pk_bf16_f32 v98,v52,v53\nv_exp_f32 v56,v56\nv_exp_f32 v57,v57\nv_cvt_pk_bf16_f32 v99,v54,v55\nv_exp_f32 v58,v58\nv_exp_f32 v59,v59\n" \
"v_cvt_pk_bf16_f32 v100,v56,v57\nv_exp_f32 v60,v60\nv_mfma_f32_32x32x16_bf16 v[16:31],v[108:111],v[96:99],v[16:31]\nv_exp_f32 v61,v61\n" \
"v_cvt_pk_bf16_f32 v101,v58,v59\nv_exp_f32 v62,v62\nv_exp_f32 v63,v63\ns_waitcnt lgkmcnt(0)\nv_exp_f32 v32,v32\nv_cvt_pk_bf16_f32 v102,v60,v61\n" \
"v_exp_f32 v33,v33\nv_exp_f32 v34,v34\nv_cvt_pk_bf16_f32 v103,v62,v63\nv_mfma_f32_32x32x16_bf16 v[48:63],v[116:119],v[132:135],v[80:95]\n" \
"v_exp_f32 v35,v35\nv_cvt_pk_bf16_f32 v96,v32,v33\nv_exp_f32 v36,v36\nv_exp_f32 v37,v37\nv_cvt_pk_bf16_f32 v97,v34,v35\nv_exp_f32 v38,v38\n" \
"v_mfma_f32_32x32x16_bf16 v[16:31],v[112:115],v[100:103],v[16:31]\nv_exp_f32 v39,v39\nv_cvt_pk_bf16_f32 v98,v36,v37\nv_exp_f32 v40,v40\n" \
"v_exp_f32 v41,v41\nv_cvt_pk_bf16_f32 v99,v38,v39\nv_exp_f32 v42,v42\nv_exp_f32 v43,v43\nv_cvt_pk_bf16_f32 v100,v40,v41\nv_exp_f32 v44,v44\n" \
"v_mfma_f32_32x32x16_bf16 v[0:15],v[120:123],v[96:99],v[0:15]\nv_exp_f32 v45,v45\nv_cvt_pk_bf16_f32 v101,v42,v43\nv_exp_f32 v46,v46\nv_exp_f32 v47,v47\n" \
"v_exp_f32 v48,v48\nv_cvt_pk_bf16_f32 v102,v44,v45\nv_exp_f32 v49,v49\nv_exp_f32 v50,v50\nv_cvt_pk_bf16_f32 v103,v46,v47\nv_exp_f32 v51,v51\n" \
"v_cvt_pk_bf16_f32 v96,v48,v49\nv_exp_f32 v52,v52\nv_exp_f32 v53,v53\nv_cvt_pk_bf16_f32 v97,v50,v51\nv_exp_f32 v54,v54\n" \
"v_mfma_f32_32x32x16_bf16 v[0:15],v[124:127],v[100:103],v[0:15]\nv_exp_f32 v55,v55\nv_cvt_pk_bf16_f32 v98,v52,v53\nv_exp_f32 v56,v56\nv_exp_f32 v57,v57\n" \
"v_cvt_pk_bf16_f32 v99,v54,v55\nv_exp_f32 v58,v58\nv_exp_f32 v59,v59\nv_cvt_pk_bf16_f32 v100,v56,v57\nv_exp_f32 v60,v60\n" \
"v_mfma_f32_32x32x16_bf16 v[16:31],v[120:123],v[96:99],v[16:31]\nv_exp_f32 v61,v61\nv_cvt_pk_bf16_f32 v101,v58,v59\nv_exp_f32 v62,v62\n" \
"v_exp_f32 v63,v63\ns_nop 1\nv_cvt_pk_bf16_f32 v102,v60,v61\nv_cvt_pk_bf16_f32 v103,v62,v63\ns_nop 1\n" \
"v_mfma_f32_32x32x16_bf16 v[16:31],v[124:127],v[100:103],v[16:31]\ns_waitcnt lgkmcnt(0)\ns_nop 7\ns_nop 7\nv_mov_b32 v228,v64\nv_mov_b32 v229,v80\n" \
"v_mov_b32 v64,v192\nv_mov_b32 v65,v193\nv_mov_b32 v66,v194\nv_mov_b32 v67,v195\nv_mov_b32 v68,v196\nv_mov_b32 v69,v197\nv_mov_b32 v70,v198\n" \
"v_mov_b32 v71,v199\nv_mov_b32 v72,v200\nv_mov_b32 v73,v201\nv_mov_b32 v74,v202\nv_mov_b32 v75,v203\nv_mov_b32 v76,v204\nv_mov_b32 v77,v205\n" \
"v_mov_b32 v78,v206\nv_mov_b32 v79,v207\nv_mov_b32 v80,v208\nv_mov_b32 v81,v209\nv_mov_b32 v82,v210\nv_mov_b32 v83,v211\nv_mov_b32 v84,v212\n" \
"v_mov_b32 v85,v213\nv_mov_b32 v86,v214\nv_mov_b32 v87,v215\nv_mov_b32 v88,v216\nv_mov_b32 v89,v217\nv_mov_b32 v90,v218\nv_mov_b32 v91,v219\n" \
"v_mov_b32 v92,v220\nv_mov_b32 v93,v221\nv_mov_b32 v94,v222\nv_mov_b32 v95,v223\nv_mov_b32 v230,v128\nv_mov_b32 v231,v129\nv_mov_b32 v249,v130\n" \
"v_mov_b32 v250,v131\nv_mov_b32 v251,v132\nv_mov_b32 v252,v133\nv_mov_b32 v255,v134\nv_mov_b32 v222,v135\nv_mov_b32 v128,v232\nv_mov_b32 v129,v233\n" \
"v_mov_b32 v130,v234\nv_mov_b32 v131,v235\nv_mov_b32 v132,v236\nv_mov_b32 v133,v237\nv_mov_b32 v134,v238\nv_mov_b32 v135,v239\nv_mov_b32 v136,v240\n" \
"v_mov_b32 v137,v241\nv_mov_b32 v138,v242\nv_mov_b32 v139,v243\nv_mov_b32 v140,v244\nv_mov_b32 v141,v245\nv_mov_b32 v142,v246\nv_mov_b32 v143,v247\n" \
"v_mov_b32 v232,v0\nv_mov_b32 v233,v1\nv_mov_b32 v234,v2\nv_mov_b32 v235,v3\nv_mov_b32 v236,v4\nv_mov_b32 v237,v5\nv_mov_b32 v238,v6\nv_mov_b32 v239,v7\n" \
"v_mov_b32 v240,v8\nv_mov_b32 v241,v16\nv_mov_b32 v242,v17\nv_mov_b32 v243,v18\nv_mov_b32 v244,v19\nv_mov_b32 v245,v20\nv_mov_b32 v246,v21\n" \
"v_mov_b32 v247,v22\nv_mov_b32 v226,v23\nv_mov_b32 v227,v24\nv_mov_b32 v0,0\nv_mov_b32 v1,0\nv_mov_b32 v2,0\nv_mov_b32 v3,0\nv_mov_b32 v4,0\n" \
"v_mov_b32 v5,0\nv_mov_b32 v6,0\nv_mov_b32 v7,0\nv_mov_b32 v8,0\nv_mov_b32 v9,0\nv_mov_b32 v10,0\nv_mov_b32 v11,0\nv_mov_b32 v12,0\nv_mov_b32 v13,0\n" \
"v_mov_b32 v14,0\nv_mov_b32 v15,0\nv_mov_b32 v16,0\nv_mov_b32 v17,0\nv_mov_b32 v18,0\nv_mov_b32 v19,0\nv_mov_b32 v20,0\nv_mov_b32 v21,0\n" \
"v_mov_b32 v22,0\nv_mov_b32 v23,0\nv_mov_b32 v24,0\nv_mov_b32 v25,0\nv_mov_b32 v26,0\nv_mov_b32 v27,0\nv_mov_b32 v28,0\nv_mov_b32 v29,0\n" \
"v_mov_b32 v30,0\nv_mov_b32 v31,0\nv_mov_b32 v32,0\nv_mov_b32 v33,0\nv_mov_b32 v34,0\nv_mov_b32 v35,0\nv_mov_b32 v36,0\nv_mov_b32 v37,0\n" \
"v_mov_b32 v38,0\nv_mov_b32 v39,0\nv_mov_b32 v40,0\nv_mov_b32 v41,0\nv_mov_b32 v42,0\nv_mov_b32 v43,0\nv_mov_b32 v44,0\nv_mov_b32 v45,0\n" \
"v_mov_b32 v46,0\nv_mov_b32 v47,0\nv_mov_b32 v48,0\nv_mov_b32 v49,0\nv_mov_b32 v50,0\nv_mov_b32 v51,0\nv_mov_b32 v52,0\nv_mov_b32 v53,0\n" \
"v_mov_b32 v54,0\nv_mov_b32 v55,0\nv_mov_b32 v56,0\nv_mov_b32 v57,0\nv_mov_b32 v58,0\nv_mov_b32 v59,0\nv_mov_b32 v60,0\nv_mov_b32 v61,0\n" \
"v_mov_b32 v62,0\nv_mov_b32 v63,0\nv_mov_b32 v216,0\ns_waitcnt vmcnt(8)\ns_barrier\ns_sub_u32 s48,s72,2\ns_and_b32 s46,s48,3\ns_sub_u32 s48,s48,s69\n" \
"s_lshl_b32 s44,s48,15\ns_add_u32 s45,s44,4096\ns_lshl_b32 s46,s46,15\ns_add_u32 s46,s46,s74\ns_mov_b32 m0,s46\ns_nop 0\n" \
"buffer_load_dwordx4 v254,%[srd],s44 offen offset:0 sc1 lds\nbuffer_load_dwordx4 v254,%[srd],s44 offen offset:1024 sc1 lds\n" \
"buffer_load_dwordx4 v254,%[srd],s44 offen offset:2048 sc1 lds\nbuffer_load_dwordx4 v254,%[srd],s44 offen offset:3072 sc1 lds\ns_add_u32 m0,s46,4096\n" \
"s_nop 0\nbuffer_load_dwordx4 v254,%[srd],s45 offen offset:0 sc1 lds\nbuffer_load_dwordx4 v254,%[srd],s45 offen offset:1024 sc1 lds\n" \
"buffer_load_dwordx4 v254,%[srd],s45 offen offset:2048 sc1 lds\nbuffer_load_dwordx4 v254,%[srd],s45 offen offset:3072 sc1 lds\ns_mov_b32 s40,s72\n" \
"s_lshr_b32 s41,s73,1\ns_add_u32 s41,s41,s69\ns_mov_b32 s47,s74\n10:\ns_cmp_le_i32 s40,s41\ns_cbranch_scc1 11f\ns_sub_u32 s43,s40,1\n" \
"s_and_b32 s43,s43,3\ns_lshl_b32 s43,s43,15\ns_sub_u32 s48,s40,s70\ns_lshl_b32 s44,s48,15\ns_cmp_lg_u32 s48,15\ns_cbranch_scc1 702f\nv_mov_b32 v220,0\n" \
"s_mov_b32 s49,0x400000\n701:\nglobal_load_dword v221,v220,s[78:79] offset:64 sc1\ns_waitcnt vmcnt(0)\nv_readfirstlane_b32 s50,v221\n" \
"s_sub_u32 s49,s49,1\ns_cbranch_scc1 702f\ns_cmp_ge_u32 s50,4\ns_cbranch_scc1 702f\ns_sleep 8\ns_branch 701b\n702:\ns_sub_u32 s49,s81,s44\n" \
"s_cmp_lt_i32 s48,0\ns_cselect_b32 s44,s49,s44\ns_add_u32 s45,s44,4096\ns_xor_b32 s46,s43,0x10000\ns_add_u32 s46,s46,s47\ns_waitcnt vmcnt(8)\ns_barrier\n" \
"s_mov_b32 m0,s46\ns_nop 0\nbuffer_load_dwordx4 v254,%[srd],s44 offen offset:0 sc1 lds\nbuffer_load_dwordx4 v254,%[srd],s44 offen offset:1024 sc1 lds\n" \
"buffer_load_dwordx4 v254,%[srd],s44 offen offset:2048 sc1 lds\nbuffer_load_dwordx4 v254,%[srd],s44 offen offset:3072 sc1 lds\ns_add_u32 m0,s46,4096\n" \
"s_nop 0\nbuffer_load_dwordx4 v254,%[srd],s45 offen offset:0 sc1 lds\nbuffer_load_dwordx4 v254,%[srd],s45 offen offset:1024 sc1 lds\n" \
"buffer_load_dwordx4 v254,%[srd],s45 offen offset:2048 sc1 lds\nbuffer_load_dwordx4 v254,%[srd],s45 offen offset:3072 sc1 lds\ns_sub_u32 s40,s40,1\n" \
"s_branch 10b\n11:\ns_and_b32 s43,s40,3\ns_lshl_b32 s43,s43,15\nv_add_u32 v217,s43,v253\nv_mov_b32 v218,v217\ns_bitcmp1_b32 s73,0\ns_cbranch_scc1 12f\n" \
"s_sub_u32 s43,s40,1\ns_and_b32 s43,s43,3\ns_lshl_b32 s43,s43,15\ns_sub_u32 s48,s40,s70\ns_lshl_b32 s44,s48,15\ns_cmp_lg_u32 s48,15\n" \
"s_cbranch_scc1 712f\nv_mov_b32 v220,0\ns_mov_b32 s49,0x400000\n711:\nglobal_load_dword v221,v220,s[78:79] offset:64 sc1\ns_waitcnt vmcnt(0)\n" \
"v_readfirstlane_b32 s50,v221\ns_sub_u32 s49,s49,1\ns_cbranch_scc1 712f\ns_cmp_ge_u32 s50,4\ns_cbranch_scc1 712f\ns_sleep 8\ns_branch 711b\n712:\n" \
"s_sub_u32 s49,s81,s44\ns_cmp_lt_i32 s48,0\ns_cselect_b32 s44,s49,s44\ns_add_u32 s45,s44,4096\nds_read_b128 v[152:155],v217 offset:0\n" \
"s_xor_b32 s46,s43,0x10000\nds_read_b128 v[156:159],v217 offset:1024\ns_add_u32 s46,s46,s47\nds_read_b128 v[160:163],v217 offset:2048\n" \
"v_add_u32 v219,s43,v253\nds_read_b128 v[164:167],v217 offset:3072\nds_read_b128 v[168:171],v217 offset:4096\nds_read_b128 v[172:175],v217 offset:5120\n" \
"ds_read_b128 v[176:179],v217 offset:6144\nds_read_b128 v[180:183],v217 offset:7168\ns_waitcnt lgkmcnt(7)\n" \
"v_mfma_f32_32x32x16_bf16 v[96:111],v[152:155],v[64:67],v[128:143]\ns_waitcnt lgkmcnt(5)\n" \
"v_mfma_f32_32x32x16_bf16 v[96:111],v[156:159],v[68:71],v[96:111]\nv_mfma_f32_32x32x16_bf16 v[96:111],v[160:163],v[72:75],v[96:111]\ns_waitcnt vmcnt(8)\n" \
"s_barrier\ns_mov_b32 m0,s46\ns_waitcnt lgkmcnt(3)\nv_mfma_f32_32x32x16_bf16 v[96:111],v[164:167],v[76:79],v[96:111]\n" \
"buffer_load_dwordx4 v254,%[srd],s44 offen offset:0 sc1 lds\nv_mfma_f32_32x32x16_bf16 v[96:111],v[168:171],v[80:83],v[96:111]\n" \
"buffer_load_dwordx4 v254,%[srd],s44 offen offset:1024 sc1 lds\ns_waitcnt lgkmcnt(1)\nv_mfma_f32_32x32x16_bf16 v[96:111],v[172:175],v[84:87],v[96:111]\n" \
"buffer_load_dwordx4 v254,%[srd],s44 offen offset:2048 sc1 lds\nv_mfma_f32_32x32x16_bf16 v[96:111],v[176:179],v[88:91],v[96:111]\n" \
"buffer_load_dwordx4 v254,%[srd],s44 offen offset:3072 sc1 lds\ns_add_u32 m0,s46,4096\ns_waitcnt lgkmcnt(0)\n" \
"v_mfma_f32_32x32x16_bf16 v[96:111],v[180:183],v[92:95],v[96:111]\nbuffer_load_dwordx4 v254,%[srd],s45 offen offset:0 sc1 lds\n" \
"ds_read_b128 v[152:155],v219 offset:16384\nbuffer_load_dwordx4 v254,%[srd],s45 offen offset:1024 sc1 lds\nds_read_b128 v[156:159],v219 offset:17408\n" \
"s_nop 7\ns_nop 7\nv_max3_f32 v223,v96,v97,v98\nv_max3_f32 v224,v99,v100,v101\nv_max3_f32 v223,v223,v102,v103\nv_max3_f32 v224,v224,v104,v105\n" \
"v_max3_f32 v223,v223,v106,v107\nv_max3_f32 v224,v224,v108,v109\nv_max3_f32 v223,v223,v110,v111\nv_max3_f32 v223,v223,v224,v224\nv_mov_b32 v224,v223\n" \
"s_nop 1\nv_permlane32_swap_b32 v223,v224\ns_nop 1\nv_max_f32 v223,v223,v224\nv_sub_f32 v96,v96,v223\nv_sub_f32 v97,v97,v223\nv_sub_f32 v98,v98,v223\n" \
"v_sub_f32 v99,v99,v223\nv_sub_f32 v100,v100,v223\nv_sub_f32 v101,v101,v223\nv_sub_f32 v102,v102,v223\nv_sub_f32 v103,v103,v223\n" \
"v_sub_f32 v104,v104,v223\nv_sub_f32 v105,v105,v223\nv_sub_f32 v106,v106,v223\nv_sub_f32 v107,v107,v223\nv_sub_f32 v108,v108,v223\n" \
"v_sub_f32 v109,v109,v223\nv_sub_f32 v110,v110,v223\nv_sub_f32 v111,v111,v223\nv_sub_f32 v128,0,v223\nv_sub_f32 v129,0,v223\nv_sub_f32 v130,0,v223\n" \
"v_sub_f32 v131,0,v223\nv_sub_f32 v132,0,v223\nv_sub_f32 v133,0,v223\nv_sub_f32 v134,0,v223\nv_sub_f32 v135,0,v223\nv_sub_f32 v136,0,v223\n" \
"v_sub_f32 v137,0,v223\nv_sub_f32 v138,0,v223\nv_sub_f32 v139,0,v223\nv_sub_f32 v140,0,v223\nv_sub_f32 v141,0,v223\nv_sub_f32 v142,0,v223\n" \
"v_sub_f32 v143,0,v223\ns_nop 1\nv_exp_f32 v96,v96\nbuffer_load_dwordx4 v254,%[srd],s45 offen offset:2048 sc1 lds\n" \
"ds_read_b128 v[160:163],v219 offset:18432\nv_exp_f32 v97,v97\nv_add_f32 v216,v216,v96\nbuffer_load_dwordx4 v254,%[srd],s45 offen offset:3072 sc1 lds\n" \
"ds_read_b128 v[164:167],v219 offset:19456\nv_exp_f32 v98,v98\nv_add_f32 v216,v216,v97\nv_cvt_pk_bf16_f32 v144,v96,v97\n" \
"ds_read_b128 v[168:171],v219 offset:20480\nv_exp_f32 v99,v99\nv_add_f32 v216,v216,v98\nv_mov_b32 v218,v217\nds_read_b128 v[172:175],v219 offset:21504\n" \
"v_exp_f32 v100,v100\nv_add_f32 v216,v216,v99\nv_cvt_pk_bf16_f32 v145,v98,v99\ns_sub_u32 s40,s40,1\nds_read_b128 v[176:179],v219 offset:22528\n" \
"v_exp_f32 v101,v101\nv_add_f32 v216,v216,v100\ns_cmp_ge_i32 s40,s69\nds_read_b128 v[180:183],v219 offset:23552\nv_exp_f32 v102,v102\n" \
"v_add_f32 v216,v216,v101\nv_cvt_pk_bf16_f32 v146,v100,v101\nv_mov_b32 v217,v219\ns_branch 13f\n12:\nds_read_b128 v[152:155],v217 offset:16384\n" \
"ds_read_b128 v[156:159],v217 offset:17408\nds_read_b128 v[160:163],v217 offset:18432\nds_read_b128 v[164:167],v217 offset:19456\n" \
"ds_read_b128 v[168:171],v217 offset:20480\nds_read_b128 v[172:175],v217 offset:21504\nds_read_b128 v[176:179],v217 offset:22528\n" \
"ds_read_b128 v[180:183],v217 offset:23552\ns_waitcnt lgkmcnt(7)\nv_mfma_f32_32x32x16_bf16 v[112:127],v[152:155],v[64:67],v[128:143]\n" \
"s_waitcnt lgkmcnt(5)\nv_mfma_f32_32x32x16_bf16 v[112:127],v[156:159],v[68:71],v[112:127]\ns_sub_u32 s43,s40,1\n" \
"v_mfma_f32_32x32x16_bf16 v[112:127],v[160:163],v[72:75],v[112:127]\ns_and_b32 s43,s43,3\ns_waitcnt lgkmcnt(3)\n" \
"v_mfma_f32_32x32x16_bf16 v[112:127],v[164:167],v[76:79],v[112:127]\ns_lshl_b32 s43,s43,15\n" \
"v_mfma_f32_32x32x16_bf16 v[112:127],v[168:171],v[80:83],v[112:127]\ns_sub_u32 s48,s40,s70\ns_waitcnt lgkmcnt(1)\n" \
"v_mfma_f32_32x32x16_bf16 v[112:127],v[172:175],v[84:87],v[112:127]\ns_lshl_b32 s44,s48,15\n" \
"v_mfma_f32_32x32x16_bf16 v[112:127],v[176:179],v[88:91],v[112:127]\ns_cmp_lg_u32 s48,15\ns_cbranch_scc1 722f\nv_mov_b32 v220,0\ns_mov_b32 s49,0x400000\n" \
"721:\nglobal_load_dword v221,v220,s[78:79] offset:64 sc1\ns_waitcnt vmcnt(0)\nv_readfirstlane_b32 s50,v221\ns_sub_u32 s49,s49,1\ns_cbranch_scc1 722f\n" \
"s_cmp_ge_u32 s50,4\ns_cbranch_scc1 722f\ns_sleep 8\ns_branch 721b\n722:\ns_sub_u32 s49,s81,s44\ns_cmp_lt_i32 s48,0\ns_cselect_b32 s44,s49,s44\n" \
"s_waitcnt lgkmcnt(0)\nv_mfma_f32_32x32x16_bf16 v[112:127],v[180:183],v[92:95],v[112:127]\ns_add_u32 s45,s44,4096\n" \
"ds_read_b128 v[152:155],v217 offset:0\ns_xor_b32 s46,s43,0x10000\nds_read_b128 v[156:159],v217 offset:1024\ns_nop 7\ns_nop 7\n" \
"v_max3_f32 v223,v112,v113,v114\nv_max3_f32 v224,v115,v116,v117\nv_max3_f32 v223,v223,v118,v119\nv_max3_f32 v224,v224,v120,v121\n" \
"v_max3_f32 v223,v223,v122,v123\nv_max3_f32 v224,v224,v124,v125\nv_max3_f32 v223,v223,v126,v127\nv_max3_f32 v223,v223,v224,v224\nv_mov_b32 v224,v223\n" \
"s_nop 1\nv_permlane32_swap_b32 v223,v224\ns_nop 1\nv_max_f32 v223,v223,v224\nv_sub_f32 v112,v112,v223\nv_sub_f32 v113,v113,v223\n" \
"v_sub_f32 v114,v114,v223\nv_sub_f32 v115,v115,v223\nv_sub_f32 v116,v116,v223\nv_sub_f32 v117,v117,v223\nv_sub_f32 v118,v118,v223\n" \
"v_sub_f32 v119,v119,v223\nv_sub_f32 v120,v120,v223\nv_sub_f32 v121,v121,v223\nv_sub_f32 v122,v122,v223\nv_sub_f32 v123,v123,v223\n" \
"v_sub_f32 v124,v124,v223\nv_sub_f32 v125,v125,v223\nv_sub_f32 v126,v126,v223\nv_sub_f32 v127,v127,v223\nv_sub_f32 v128,0,v223\nv_sub_f32 v129,0,v223\n" \
"v_sub_f32 v130,0,v223\nv_sub_f32 v131,0,v223\nv_sub_f32 v132,0,v223\nv_sub_f32 v133,0,v223\nv_sub_f32 v134,0,v223\nv_sub_f32 v135,0,v223\n" \
"v_sub_f32 v136,0,v223\nv_sub_f32 v137,0,v223\nv_sub_f32 v138,0,v223\nv_sub_f32 v139,0,v223\nv_sub_f32 v140,0,v223\nv_sub_f32 v141,0,v223\n" \
"v_sub_f32 v142,0,v223\nv_sub_f32 v143,0,v223\ns_nop 1\nv_exp_f32 v112,v112\ns_add_u32 s46,s46,s47\nds_read_b128 v[160:163],v217 offset:2048\n" \
"v_exp_f32 v113,v113\nv_add_f32 v216,v216,v112\nv_add_u32 v219,s43,v253\nds_read_b128 v[164:167],v217 offset:3072\nv_exp_f32 v114,v114\n" \
"v_add_f32 v216,v216,v113\nv_cvt_pk_bf16_f32 v144,v112,v113\nds_read_b128 v[168:171],v217 offset:4096\nv_exp_f32 v115,v115\nv_add_f32 v216,v216,v114\n" \
"ds_read_b128 v[172:175],v217 offset:5120\nv_exp_f32 v116,v116\nv_add_f32 v216,v216,v115\nv_cvt_pk_bf16_f32 v145,v114,v115\n" \
"ds_read_b128 v[176:179],v217 offset:6144\nv_exp_f32 v117,v117\nv_add_f32 v216,v216,v116\nds_read_b128 v[180:183],v217 offset:7168\nv_exp_f32 v118,v118\n" \
"v_add_f32 v216,v216,v117\nv_cvt_pk_bf16_f32 v146,v116,v117\ns_waitcnt lgkmcnt(7)\nv_mfma_f32_32x32x16_bf16 v[96:111],v[152:155],v[64:67],v[128:143]\n" \
"ds_read_b128 v[184:187],v217 offset:24576\nv_exp_f32 v119,v119\nv_add_f32 v216,v216,v118\ns_waitcnt lgkmcnt(6)\n" \
"v_mfma_f32_32x32x16_bf16 v[96:111],v[156:159],v[68:71],v[96:111]\nds_read_b128 v[188:191],v217 offset:25600\nv_exp_f32 v120,v120\n" \
"v_add_f32 v216,v216,v119\nv_cvt_pk_bf16_f32 v147,v118,v119\nv_mfma_f32_32x32x16_bf16 v[96:111],v[160:163],v[72:75],v[96:111]\n" \
"ds_read_b128 v[192:195],v217 offset:26624\nv_exp_f32 v121,v121\nv_add_f32 v216,v216,v120\ns_waitcnt vmcnt(8)\ns_barrier\ns_mov_b32 m0,s46\n" \
"s_waitcnt lgkmcnt(6)\nv_mfma_f32_32x32x16_bf16 v[96:111],v[164:167],v[76:79],v[96:111]\nds_read_b128 v[196:199],v217 offset:27648\nv_exp_f32 v122,v122\n" \
"v_add_f32 v216,v216,v121\nv_cvt_pk_bf16_f32 v148,v120,v121\nbuffer_load_dwordx4 v254,%[srd],s44 offen offset:0 sc1 lds\n" \
"v_mfma_f32_32x32x16_bf16 v[96:111],v[168:171],v[80:83],v[96:111]\nds_read_b128 v[200:203],v217 offset:28672\nv_exp_f32 v123,v123\n" \
"v_add_f32 v216,v216,v122\nbuffer_load_dwordx4 v254,%[srd],s44 offen offset:1024 sc1 lds\ns_waitcnt lgkmcnt(6)\n" \
"v_mfma_f32_32x32x16_bf16 v[96:111],v[172:175],v[84:87],v[96:111]\nds_read_b128 v[204:207],v217 offset:29696\nv_exp_f32 v124,v124\n" \
"v_add_f32 v216,v216,v123\nv_cvt_pk_bf16_f32 v149,v122,v123\nbuffer_load_dwordx4 v254,%[srd],s44 offen offset:2048 sc1 lds\n" \
"v_mfma_f32_32x32x16_bf16 v[96:111],v[176:179],v[88:91],v[96:111]\nds_read_b128 v[208:211],v217 offset:30720\nv_exp_f32 v125,v125\n" \
"v_add_f32 v216,v216,v124\nbuffer_load_dwordx4 v254,%[srd],s44 offen offset:3072 sc1 lds\ns_add_u32 m0,s46,4096\ns_waitcnt lgkmcnt(6)\n" \
"v_mfma_f32_32x32x16_bf16 v[96:111],v[180:183],v[92:95],v[96:111]\nds_read_b128 v[212:215],v217 offset:31744\nv_exp_f32 v126,v126\n" \
"v_add_f32 v216,v216,v125\nv_cvt_pk_bf16_f32 v150,v124,v125\nbuffer_load_dwordx4 v254,%[srd],s45 offen offset:0 sc1 lds\n" \
"v_mfma_f32_32x32x16_bf16 v[0:15],v[184:187],v[144:147],v[0:15]\nds_read_b128 v[152:155],v219 offset:16384\nv_exp_f32 v127,v127\n" \
"v_add_f32 v216,v216,v126\nbuffer_load_dwordx4 v254,%[srd],s45 offen offset:1024 sc1 lds\ns_waitcnt lgkmcnt(6)\n" \
"v_mfma_f32_32x32x16_bf16 v[16:31],v[188:191],v[144:147],v[16:31]\nds_read_b128 v[156:159],v219 offset:17408\nv_exp_f32 v96,v96\n" \
"v_add_f32 v216,v216,v127\nv_cvt_pk_bf16_f32 v151,v126,v127\nbuffer_load_dwordx4 v254,%[srd],s45 offen offset:2048 sc1 lds\n" \
"v_mfma_f32_32x32x16_bf16 v[32:47],v[192:195],v[144:147],v[32:47]\nds_read_b128 v[160:163],v219 offset:18432\nv_exp_f32 v97,v97\n" \
"v_add_f32 v216,v216,v96\nbuffer_load_dwordx4 v254,%[srd],s45 offen offset:3072 sc1 lds\ns_waitcnt lgkmcnt(6)\n" \
"v_mfma_f32_32x32x16_bf16 v[48:63],v[196:199],v[144:147],v[48:63]\nds_read_b128 v[164:167],v219 offset:19456\nv_exp_f32 v98,v98\n" \
"v_add_f32 v216,v216,v97\nv_cvt_pk_bf16_f32 v144,v96,v97\nv_mfma_f32_32x32x16_bf16 v[0:15],v[200:203],v[148:151],v[0:15]\n" \
"ds_read_b128 v[168:171],v219 offset:20480\nv_exp_f32 v99,v99\nv_add_f32 v216,v216,v98\nv_mov_b32 v218,v217\ns_waitcnt lgkmcnt(6)\n" \
"v_mfma_f32_32x32x16_bf16 v[16:31],v[204:207],v[148:151],v[16:31]\nds_read_b128 v[172:175],v219 offset:21504\nv_exp_f32 v100,v100\n" \
"v_add_f32 v216,v216,v99\nv_cvt_pk_bf16_f32 v145,v98,v99\ns_sub_u32 s40,s40,1\nv_mfma_f32_32x32x16_bf16 v[32:47],v[208:211],v[148:151],v[32:47]\n" \
"ds_read_b128 v[176:179],v219 offset:22528\nv_exp_f32 v101,v101\nv_add_f32 v216,v216,v100\ns_cmp_ge_i32 s40,s69\ns_waitcnt lgkmcnt(7)\n" \
"v_mfma_f32_32x32x16_bf16 v[48:63],v[212:215],v[148:151],v[48:63]\nds_read_b128 v[180:183],v219 offset:23552\nv_exp_f32 v102,v102\n" \
"v_add_f32 v216,v216,v101\nv_cvt_pk_bf16_f32 v146,v100,v101\nv_mov_b32 v217,v219\n13:\ns_cbranch_scc0 15f\n14:\ns_waitcnt lgkmcnt(7)\n" \
"v_mfma_f32_32x32x16_bf16 v[112:127],v[152:155],v[64:67],v[128:143]\nds_read_b128 v[184:187],v218 offset:8192\nv_exp_f32 v103,v103\n" \
"v_add_f32 v216,v216,v102\ns_waitcnt lgkmcnt(6)\nv_mfma_f32_32x32x16_bf16 v[112:127],v[156:159],v[68:71],v[112:127]\n" \
"ds_read_b128 v[188:191],v218 offset:9216\nv_exp_f32 v104,v104\nv_add_f32 v216,v216,v103\nv_cvt_pk_bf16_f32 v147,v102,v103\ns_sub_u32 s43,s40,1\n" \
"v_mfma_f32_32x32x16_bf16 v[112:127],v[160:163],v[72:75],v[112:127]\nds_read_b128 v[192:195],v218 offset:10240\nv_exp_f32 v105,v105\n" \
"v_add_f32 v216,v216,v104\ns_and_b32 s43,s43,3\ns_waitcnt lgkmcnt(6)\nv_mfma_f32_32x32x16_bf16 v[112:127],v[164:167],v[76:79],v[112:127]\n" \
"ds_read_b128 v[196:199],v218 offset:11264\nv_exp_f32 v106,v106\nv_add_f32 v216,v216,v105\nv_cvt_pk_bf16_f32 v148,v104,v105\ns_lshl_b32 s43,s43,15\n" \
"v_mfma_f32_32x32x16_bf16 v[112:127],v[168:171],v[80:83],v[112:127]\nds_read_b128 v[200:203],v218 offset:12288\nv_exp_f32 v107,v107\n" \
"v_add_f32 v216,v216,v106\ns_sub_u32 s48,s40,s70\ns_waitcnt lgkmcnt(6)\nv_mfma_f32_32x32x16_bf16 v[112:127],v[172:175],v[84:87],v[112:127]\n" \
"ds_read_b128 v[204:207],v218 offset:13312\nv_exp_f32 v108,v108\nv_add_f32 v216,v216,v107\nv_cvt_pk_bf16_f32 v149,v106,v107\ns_lshl_b32 s44,s48,15\n" \
"v_mfma_f32_32x32x16_bf16 v[112:127],v[176:179],v[88:91],v[112:127]\nds_read_b128 v[208:211],v218 offset:14336\nv_exp_f32 v109,v109\n" \
"v_add_f32 v216,v216,v108\ns_cmp_lg_u32 s48,15\ns_cbranch_scc1 732f\nv_mov_b32 v220,0\ns_mov_b32 s49,0x400000\n731:\n" \
"global_load_dword v221,v220,s[78:79] offset:64 sc1\ns_waitcnt vmcnt(0)\nv_readfirstlane_b32 s50,v221\ns_sub_u32 s49,s49,1\ns_cbranch_scc1 732f\n" \
"s_cmp_ge_u32 s50,4\ns_cbranch_scc1 732f\ns_sleep 8\ns_branch 731b\n732:\ns_sub_u32 s49,s81,s44\ns_cmp_lt_i32 s48,0\ns_cselect_b32 s44,s49,s44\n" \
"s_waitcnt lgkmcnt(6)\nv_mfma_f32_32x32x16_bf16 v[112:127],v[180:183],v[92:95],v[112:127]\nds_read_b128 v[212:215],v218 offset:15360\n" \
"v_exp_f32 v110,v110\nv_add_f32 v216,v216,v109\nv_cvt_pk_bf16_f32 v150,v108,v109\ns_add_u32 s45,s44,4096\n" \
"v_mfma_f32_32x32x16_bf16 v[0:15],v[184:187],v[144:147],v[0:15]\nds_read_b128 v[152:155],v217 offset:0\nv_exp_f32 v111,v111\nv_add_f32 v216,v216,v110\n" \
"s_xor_b32 s46,s43,0x10000\ns_waitcnt lgkmcnt(6)\nv_mfma_f32_32x32x16_bf16 v[16:31],v[188:191],v[144:147],v[16:31]\n" \
"ds_read_b128 v[156:159],v217 offset:1024\nv_exp_f32 v112,v112\nv_add_f32 v216,v216,v111\nv_cvt_pk_bf16_f32 v151,v110,v111\ns_add_u32 s46,s46,s47\n" \
"v_mfma_f32_32x32x16_bf16 v[32:47],v[192:195],v[144:147],v[32:47]\nds_read_b128 v[160:163],v217 offset:2048\nv_exp_f32 v113,v113\n" \
"v_add_f32 v216,v216,v112\nv_add_u32 v219,s43,v253\ns_waitcnt lgkmcnt(6)\nv_mfma_f32_32x32x16_bf16 v[48:63],v[196:199],v[144:147],v[48:63]\n" \
"ds_read_b128 v[164:167],v217 offset:3072\nv_exp_f32 v114,v114\nv_add_f32 v216,v216,v113\nv_cvt_pk_bf16_f32 v144,v112,v113\n" \
"v_mfma_f32_32x32x16_bf16 v[0:15],v[200:203],v[148:151],v[0:15]\nds_read_b128 v[168:171],v217 offset:4096\nv_exp_f32 v115,v115\n" \
"v_add_f32 v216,v216,v114\ns_waitcnt lgkmcnt(6)\nv_mfma_f32_32x32x16_bf16 v[16:31],v[204:207],v[148:151],v[16:31]\n" \
"ds_read_b128 v[172:175],v217 offset:5120\nv_exp_f32 v116,v116\nv_add_f32 v216,v216,v115\nv_cvt_pk_bf16_f32 v145,v114,v115\n" \
"v_mfma_f32_32x32x16_bf16 v[32:47],v[208:211],v[148:151],v[32:47]\nds_read_b128 v[176:179],v217 offset:6144\nv_exp_f32 v117,v117\n" \
"v_add_f32 v216,v216,v116\ns_waitcnt lgkmcnt(6)\nv_mfma_f32_32x32x16_bf16 v[48:63],v[212:215],v[148:151],v[48:63]\n" \
"ds_read_b128 v[180:183],v217 offset:7168\nv_exp_f32 v118,v118\nv_add_f32 v216,v216,v117\nv_cvt_pk_bf16_f32 v146,v116,v117\n" \
"v_mfma_f32_32x32x16_bf16 v[96:111],v[152:155],v[64:67],v[128:143]\nds_read_b128 v[184:187],v217 offset:24576\nv_exp_f32 v119,v119\n" \
"v_add_f32 v216,v216,v118\ns_waitcnt lgkmcnt(6)\nv_mfma_f32_32x32x16_bf16 v[96:111],v[156:159],v[68:71],v[96:111]\n" \
"ds_read_b128 v[188:191],v217 offset:25600\nv_exp_f32 v120,v120\nv_add_f32 v216,v216,v119\nv_cvt_pk_bf16_f32 v147,v118,v119\n" \
"v_mfma_f32_32x32x16_bf16 v[96:111],v[160:163],v[72:75],v[96:111]\nds_read_b128 v[192:195],v217 offset:26624\nv_exp_f32 v121,v121\n" \
"v_add_f32 v216,v216,v120\ns_waitcnt vmcnt(8)\ns_barrier\ns_mov_b32 m0,s46\ns_waitcnt lgkmcnt(6)\n" \
"v_mfma_f32_32x32x16_bf16 v[96:111],v[164:167],v[76:79],v[96:111]\nds_read_b128 v[196:199],v217 offset:27648\nv_exp_f32 v122,v122\n" \
"v_add_f32 v216,v216,v121\nv_cvt_pk_bf16_f32 v148,v120,v121\nbuffer_load_dwordx4 v254,%[srd],s44 offen offset:0 sc1 lds\n" \
"v_mfma_f32_32x32x16_bf16 v[96:111],v[168:171],v[80:83],v[96:111]\nds_read_b128 v[200:203],v217 offset:28672\nv_exp_f32 v123,v123\n" \
"v_add_f32 v216,v216,v122\nbuffer_load_dwordx4 v254,%[srd],s44 offen offset:1024 sc1 lds\ns_waitcnt lgkmcnt(6)\n" \
"v_mfma_f32_32x32x16_bf16 v[96:111],v[172:175],v[84:87],v[96:111]\nds_read_b128 v[204:207],v217 offset:29696\nv_exp_f32 v124,v124\n" \
"v_add_f32 v216,v216,v123\nv_cvt_pk_bf16_f32 v149,v122,v123\nbuffer_load_dwordx4 v254,%[srd],s44 offen offset:2048 sc1 lds\n" \
"v_mfma_f32_32x32x16_bf16 v[96:111],v[176:179],v[88:91],v[96:111]\nds_read_b128 v[208:211],v217 offset:30720\nv_exp_f32 v125,v125\n" \
"v_add_f32 v216,v216,v124\nbuffer_load_dwordx4 v254,%[srd],s44 offen offset:3072 sc1 lds\ns_add_u32 m0,s46,4096\ns_waitcnt lgkmcnt(6)\n" \
"v_mfma_f32_32x32x16_bf16 v[96:111],v[180:183],v[92:95],v[96:111]\nds_read_b128 v[212:215],v217 offset:31744\nv_exp_f32 v126,v126\n" \
"v_add_f32 v216,v216,v125\nv_cvt_pk_bf16_f32 v150,v124,v125\nbuffer_load_dwordx4 v254,%[srd],s45 offen offset:0 sc1 lds\n" \
"v_mfma_f32_32x32x16_bf16 v[0:15],v[184:187],v[144:147],v[0:15]\nds_read_b128 v[152:155],v219 offset:16384\nv_exp_f32 v127,v127\n" \
"v_add_f32 v216,v216,v126\nbuffer_load_dwordx4 v254,%[srd],s45 offen offset:1024 sc1 lds\ns_waitcnt lgkmcnt(6)\n" \
"v_mfma_f32_32x32x16_bf16 v[16:31],v[188:191],v[144:147],v[16:31]\nds_read_b128 v[156:159],v219 offset:17408\nv_exp_f32 v96,v96\n" \
"v_add_f32 v216,v216,v127\nv_cvt_pk_bf16_f32 v151,v126,v127\nbuffer_load_dwordx4 v254,%[srd],s45 offen offset:2048 sc1 lds\n" \
"v_mfma_f32_32x32x16_bf16 v[32:47],v[192:195],v[144:147],v[32:47]\nds_read_b128 v[160:163],v219 offset:18432\nv_exp_f32 v97,v97\n" \
"v_add_f32 v216,v216,v96\nbuffer_load_dwordx4 v254,%[srd],s45 offen offset:3072 sc1 lds\ns_waitcnt lgkmcnt(6)\n" \
"v_mfma_f32_32x32x16_bf16 v[48:63],v[196:199],v[144:147],v[48:63]\nds_read_b128 v[164:167],v219 offset:19456\nv_exp_f32 v98,v98\n" \
"v_add_f32 v216,v216,v97\nv_cvt_pk_bf16_f32 v144,v96,v97\nv_mfma_f32_32x32x16_bf16 v[0:15],v[200:203],v[148:151],v[0:15]\n" \
"ds_read_b128 v[168:171],v219 offset:20480\nv_exp_f32 v99,v99\nv_add_f32 v216,v216,v98\nv_mov_b32 v218,v217\ns_waitcnt lgkmcnt(6)\n" \
"v_mfma_f32_32x32x16_bf16 v[16:31],v[204:207],v[148:151],v[16:31]\nds_read_b128 v[172:175],v219 offset:21504\nv_exp_f32 v100,v100\n" \
"v_add_f32 v216,v216,v99\nv_cvt_pk_bf16_f32 v145,v98,v99\ns_sub_u32 s40,s40,1\nv_mfma_f32_32x32x16_bf16 v[32:47],v[208:211],v[148:151],v[32:47]\n" \
"ds_read_b128 v[176:179],v219 offset:22528\nv_exp_f32 v101,v101\nv_add_f32 v216,v216,v100\ns_cmp_ge_i32 s40,s69\ns_waitcnt lgkmcnt(7)\n" \
"v_mfma_f32_32x32x16_bf16 v[48:63],v[212:215],v[148:151],v[48:63]\nds_read_b128 v[180:183],v219 offset:23552\nv_exp_f32 v102,v102\n" \
"v_add_f32 v216,v216,v101\nv_cvt_pk_bf16_f32 v146,v100,v101\nv_mov_b32 v217,v219\ns_cbranch_scc1 14b\n15:\nv_lshrrev_b32 v114,2,v248\n" \
"v_and_b32 v114,0x7c,v114\ns_lshl_b32 s49,s73,7\ns_add_u32 s49,s49,73662464\nbuffer_load_dword v112,v114,%[sws],s49 offen\n" \
"buffer_load_dword v113,v114,%[sws],s82 offen\nds_read_b128 v[184:187],v218 offset:8192\nv_exp_f32 v103,v103\nv_add_f32 v216,v216,v102\n" \
"ds_read_b128 v[188:191],v218 offset:9216\nv_exp_f32 v104,v104\nv_add_f32 v216,v216,v103\nv_cvt_pk_bf16_f32 v147,v102,v103\n" \
"ds_read_b128 v[192:195],v218 offset:10240\nv_exp_f32 v105,v105\nv_add_f32 v216,v216,v104\nds_read_b128 v[196:199],v218 offset:11264\n" \
"v_exp_f32 v106,v106\nv_add_f32 v216,v216,v105\nv_cvt_pk_bf16_f32 v148,v104,v105\nds_read_b128 v[200:203],v218 offset:12288\nv_exp_f32 v107,v107\n" \
"v_add_f32 v216,v216,v106\nds_read_b128 v[204:207],v218 offset:13312\nv_exp_f32 v108,v108\nv_add_f32 v216,v216,v107\nv_cvt_pk_bf16_f32 v149,v106,v107\n" \
"ds_read_b128 v[208:211],v218 offset:14336\nv_exp_f32 v109,v109\nv_add_f32 v216,v216,v108\nds_read_b128 v[212:215],v218 offset:15360\n" \
"v_exp_f32 v110,v110\nv_add_f32 v216,v216,v109\nv_cvt_pk_bf16_f32 v150,v108,v109\ns_waitcnt lgkmcnt(7)\n" \
"v_mfma_f32_32x32x16_bf16 v[0:15],v[184:187],v[144:147],v[0:15]\nv_exp_f32 v111,v111\nv_add_f32 v216,v216,v110\ns_waitcnt lgkmcnt(5)\n" \
"v_mfma_f32_32x32x16_bf16 v[16:31],v[188:191],v[144:147],v[16:31]\nv_add_f32 v216,v216,v111\nv_cvt_pk_bf16_f32 v151,v110,v111\n" \
"v_mfma_f32_32x32x16_bf16 v[32:47],v[192:195],v[144:147],v[32:47]\ns_waitcnt lgkmcnt(3)\n" \
"v_mfma_f32_32x32x16_bf16 v[48:63],v[196:199],v[144:147],v[48:63]\nv_mfma_f32_32x32x16_bf16 v[0:15],v[200:203],v[148:151],v[0:15]\ns_waitcnt lgkmcnt(1)\n" \
"v_mfma_f32_32x32x16_bf16 v[16:31],v[204:207],v[148:151],v[16:31]\nv_mfma_f32_32x32x16_bf16 v[32:47],v[208:211],v[148:151],v[32:47]\n" \
"s_waitcnt lgkmcnt(0)\nv_mfma_f32_32x32x16_bf16 v[48:63],v[212:215],v[148:151],v[48:63]\ns_waitcnt vmcnt(0) lgkmcnt(0)\ns_nop 7\ns_nop 7\n"

#define K12B_LONG \
"s_mov_b32 s59,s66\ns_mov_b32 s52,s64\ns_add_u32 s53,s64,8192\ns_add_u32 s54,s64,16384\ns_add_u32 s55,s64,24576\ns_mov_b32 s56,s65\n" \
"s_add_u32 s57,s65,32768\ns_mov_b32 m0,s59\ns_nop 0\nbuffer_load_dwordx4 v228,%[sx],s52 offen lds\ns_add_u32 m0,m0,1024\ns_nop 0\n" \
"buffer_load_dwordx4 v228,%[sx],s53 offen lds\ns_add_u32 m0,m0,1024\ns_nop 0\nbuffer_load_dwordx4 v229,%[sx],s54 offen lds\ns_add_u32 m0,m0,1024\n" \
"s_nop 0\nbuffer_load_dwordx4 v229,%[sx],s55 offen lds\ns_add_u32 m0,m0,29696\ns_nop 0\nbuffer_load_dwordx4 v230,%[sk],s56 offen lds\n" \
"s_add_u32 m0,m0,1024\ns_nop 0\nbuffer_load_dwordx4 v230,%[sv],s56 offen lds\ns_add_u32 m0,m0,1024\ns_nop 0\n" \
"buffer_load_dwordx4 v230,%[sk],s57 offen lds\ns_add_u32 m0,m0,1024\ns_nop 0\nbuffer_load_dwordx4 v230,%[sv],s57 offen lds\nv_add_u32 v228,128,v228\n" \
"v_add_u32 v229,128,v229\nv_add_u32 v230,0x80000,v230\ns_xor_b32 s59,s59,0x10000\ns_mov_b32 m0,s59\ns_nop 0\n" \
"buffer_load_dwordx4 v228,%[sx],s52 offen lds\ns_add_u32 m0,m0,1024\ns_nop 0\nbuffer_load_dwordx4 v228,%[sx],s53 offen lds\ns_add_u32 m0,m0,1024\n" \
"s_nop 0\nbuffer_load_dwordx4 v229,%[sx],s54 offen lds\ns_add_u32 m0,m0,1024\ns_nop 0\nbuffer_load_dwordx4 v229,%[sx],s55 offen lds\n" \
"s_add_u32 m0,m0,29696\ns_nop 0\nbuffer_load_dwordx4 v230,%[sk],s56 offen lds\ns_add_u32 m0,m0,1024\ns_nop 0\n" \
"buffer_load_dwordx4 v230,%[sv],s56 offen lds\ns_add_u32 m0,m0,1024\ns_nop 0\nbuffer_load_dwordx4 v230,%[sk],s57 offen lds\ns_add_u32 m0,m0,1024\n" \
"s_nop 0\nbuffer_load_dwordx4 v230,%[sv],s57 offen lds\nv_add_u32 v228,128,v228\nv_add_u32 v229,128,v229\nv_add_u32 v230,0x80000,v230\n" \
"s_xor_b32 s59,s59,0x10000\ns_waitcnt vmcnt(8)\ns_barrier\nds_read_b64_tr_b16 v[160:161],v226 offset:0\nds_read_b64_tr_b16 v[162:163],v226 offset:2048\n" \
"ds_read_b64_tr_b16 v[164:165],v227 offset:0\nds_read_b64_tr_b16 v[166:167],v227 offset:2048\nds_read_b64_tr_b16 v[168:169],v226 offset:256\n" \
"ds_read_b64_tr_b16 v[170:171],v226 offset:2304\nds_read_b64_tr_b16 v[172:173],v227 offset:256\nds_read_b64_tr_b16 v[174:175],v227 offset:2304\n" \
"ds_read_b128 v[128:131],v224 offset:0\nds_read_b128 v[132:135],v224 offset:1024\nds_read_b128 v[136:139],v224 offset:4096\n" \
"ds_read_b128 v[140:143],v224 offset:5120\nds_read_b128 v[144:147],v224 offset:8192\nds_read_b128 v[148:151],v224 offset:9216\n" \
"ds_read_b128 v[152:155],v224 offset:12288\nds_read_b128 v[156:159],v224 offset:13312\ns_waitcnt lgkmcnt(7)\n" \
"v_mfma_f32_16x16x32_bf16 v[0:3],v[160:163],v[128:131],0\nds_read_b64_tr_b16 v[176:177],v226 offset:16384\n" \
"v_mfma_f32_16x16x32_bf16 v[4:7],v[164:167],v[128:131],0\nv_mfma_f32_16x16x32_bf16 v[8:11],v[168:171],v[128:131],0\n" \
"v_mfma_f32_16x16x32_bf16 v[12:15],v[172:175],v[128:131],0\nds_read_b128 v[128:131],v225 offset:0\nds_read_b64_tr_b16 v[178:179],v226 offset:18432\n" \
"s_waitcnt lgkmcnt(9)\nv_mfma_f32_16x16x32_bf16 v[16:19],v[160:163],v[132:135],0\nv_mfma_f32_16x16x32_bf16 v[20:23],v[164:167],v[132:135],0\n" \
"v_mfma_f32_16x16x32_bf16 v[24:27],v[168:171],v[132:135],0\nds_read_b64_tr_b16 v[180:181],v227 offset:16384\n" \
"v_mfma_f32_16x16x32_bf16 v[28:31],v[172:175],v[132:135],0\nds_read_b128 v[132:135],v225 offset:1024\ns_waitcnt lgkmcnt(10)\n" \
"v_mfma_f32_16x16x32_bf16 v[32:35],v[160:163],v[136:139],0\nv_mfma_f32_16x16x32_bf16 v[36:39],v[164:167],v[136:139],0\n" \
"ds_read_b64_tr_b16 v[182:183],v227 offset:18432\nv_mfma_f32_16x16x32_bf16 v[40:43],v[168:171],v[136:139],0\n" \
"v_mfma_f32_16x16x32_bf16 v[44:47],v[172:175],v[136:139],0\nds_read_b128 v[136:139],v225 offset:4096\ns_waitcnt lgkmcnt(11)\n" \
"v_mfma_f32_16x16x32_bf16 v[48:51],v[160:163],v[140:143],0\nds_read_b64_tr_b16 v[184:185],v226 offset:16640\n" \
"v_mfma_f32_16x16x32_bf16 v[52:55],v[164:167],v[140:143],0\nv_mfma_f32_16x16x32_bf16 v[56:59],v[168:171],v[140:143],0\n" \
"v_mfma_f32_16x16x32_bf16 v[60:63],v[172:175],v[140:143],0\nds_read_b128 v[140:143],v225 offset:5120\nds_read_b64_tr_b16 v[186:187],v226 offset:18688\n" \
"s_waitcnt lgkmcnt(13)\nv_mfma_f32_16x16x32_bf16 v[64:67],v[160:163],v[144:147],0\nv_mfma_f32_16x16x32_bf16 v[68:71],v[164:167],v[144:147],0\n" \
"v_mfma_f32_16x16x32_bf16 v[72:75],v[168:171],v[144:147],0\nds_read_b64_tr_b16 v[188:189],v227 offset:16640\n" \
"v_mfma_f32_16x16x32_bf16 v[76:79],v[172:175],v[144:147],0\nds_read_b128 v[144:147],v225 offset:8192\ns_waitcnt lgkmcnt(14)\n" \
"v_mfma_f32_16x16x32_bf16 v[80:83],v[160:163],v[148:151],0\nv_mfma_f32_16x16x32_bf16 v[84:87],v[164:167],v[148:151],0\n" \
"ds_read_b64_tr_b16 v[190:191],v227 offset:18688\nv_mfma_f32_16x16x32_bf16 v[88:91],v[168:171],v[148:151],0\n" \
"v_mfma_f32_16x16x32_bf16 v[92:95],v[172:175],v[148:151],0\nds_read_b128 v[148:151],v225 offset:9216\ns_waitcnt lgkmcnt(15)\n" \
"v_mfma_f32_16x16x32_bf16 v[96:99],v[160:163],v[152:155],0\nv_mfma_f32_16x16x32_bf16 v[100:103],v[164:167],v[152:155],0\n" \
"v_mfma_f32_16x16x32_bf16 v[104:107],v[168:171],v[152:155],0\nv_mfma_f32_16x16x32_bf16 v[108:111],v[172:175],v[152:155],0\n" \
"ds_read_b128 v[152:155],v225 offset:12288\ns_waitcnt lgkmcnt(15)\nv_mfma_f32_16x16x32_bf16 v[112:115],v[160:163],v[156:159],0\n" \
"v_mfma_f32_16x16x32_bf16 v[116:119],v[164:167],v[156:159],0\nv_mfma_f32_16x16x32_bf16 v[120:123],v[168:171],v[156:159],0\n" \
"v_mfma_f32_16x16x32_bf16 v[124:127],v[172:175],v[156:159],0\nds_read_b128 v[156:159],v225 offset:13312\ns_waitcnt lgkmcnt(13)\n" \
"v_mfma_f32_16x16x32_bf16 v[0:3],v[176:179],v[128:131],v[0:3]\ns_waitcnt lgkmcnt(10)\nv_mfma_f32_16x16x32_bf16 v[4:7],v[180:183],v[128:131],v[4:7]\n" \
"s_waitcnt lgkmcnt(6)\nv_mfma_f32_16x16x32_bf16 v[8:11],v[184:187],v[128:131],v[8:11]\ns_waitcnt lgkmcnt(3)\n" \
"v_mfma_f32_16x16x32_bf16 v[12:15],v[188:191],v[128:131],v[12:15]\ns_waitcnt vmcnt(0) lgkmcnt(0)\ns_barrier\nv_xor_b32 v224,0x10000,v224\n" \
"v_xor_b32 v225,0x10000,v225\nv_xor_b32 v226,0x10000,v226\nv_xor_b32 v227,0x10000,v227\n" \
"v_mfma_f32_16x16x32_bf16 v[16:19],v[176:179],v[132:135],v[16:19]\nds_read_b128 v[128:131],v224 offset:0\n" \
"v_mfma_f32_16x16x32_bf16 v[20:23],v[180:183],v[132:135],v[20:23]\nds_read_b64_tr_b16 v[160:161],v226 offset:0\ns_mov_b32 m0,s59\ns_nop 0\n" \
"buffer_load_dwordx4 v228,%[sx],s52 offen lds\nv_mfma_f32_16x16x32_bf16 v[24:27],v[184:187],v[132:135],v[24:27]\n" \
"v_mfma_f32_16x16x32_bf16 v[28:31],v[188:191],v[132:135],v[28:31]\nds_read_b128 v[132:135],v224 offset:1024\n" \
"v_mfma_f32_16x16x32_bf16 v[32:35],v[176:179],v[136:139],v[32:35]\nds_read_b64_tr_b16 v[162:163],v226 offset:2048\ns_add_u32 m0,m0,1024\ns_nop 0\n" \
"buffer_load_dwordx4 v228,%[sx],s53 offen lds\nv_mfma_f32_16x16x32_bf16 v[36:39],v[180:183],v[136:139],v[36:39]\n" \
"v_mfma_f32_16x16x32_bf16 v[40:43],v[184:187],v[136:139],v[40:43]\nv_mfma_f32_16x16x32_bf16 v[44:47],v[188:191],v[136:139],v[44:47]\n" \
"ds_read_b128 v[136:139],v224 offset:4096\nds_read_b64_tr_b16 v[164:165],v227 offset:0\ns_add_u32 m0,m0,1024\ns_nop 0\n" \
"buffer_load_dwordx4 v229,%[sx],s54 offen lds\nv_mfma_f32_16x16x32_bf16 v[48:51],v[176:179],v[140:143],v[48:51]\n" \
"v_mfma_f32_16x16x32_bf16 v[52:55],v[180:183],v[140:143],v[52:55]\nv_mfma_f32_16x16x32_bf16 v[56:59],v[184:187],v[140:143],v[56:59]\n" \
"ds_read_b64_tr_b16 v[166:167],v227 offset:2048\ns_add_u32 m0,m0,1024\ns_nop 0\nbuffer_load_dwordx4 v229,%[sx],s55 offen lds\n" \
"v_mfma_f32_16x16x32_bf16 v[60:63],v[188:191],v[140:143],v[60:63]\nds_read_b128 v[140:143],v224 offset:5120\n" \
"v_mfma_f32_16x16x32_bf16 v[64:67],v[176:179],v[144:147],v[64:67]\nv_mfma_f32_16x16x32_bf16 v[68:71],v[180:183],v[144:147],v[68:71]\n" \
"ds_read_b64_tr_b16 v[168:169],v226 offset:256\ns_add_u32 m0,m0,29696\ns_nop 0\nbuffer_load_dwordx4 v230,%[sk],s56 offen lds\n" \
"v_mfma_f32_16x16x32_bf16 v[72:75],v[184:187],v[144:147],v[72:75]\nv_mfma_f32_16x16x32_bf16 v[76:79],v[188:191],v[144:147],v[76:79]\n" \
"ds_read_b128 v[144:147],v224 offset:8192\nv_mfma_f32_16x16x32_bf16 v[80:83],v[176:179],v[148:151],v[80:83]\n" \
"ds_read_b64_tr_b16 v[170:171],v226 offset:2304\ns_add_u32 m0,m0,1024\ns_nop 0\nbuffer_load_dwordx4 v230,%[sv],s56 offen lds\n" \
"v_mfma_f32_16x16x32_bf16 v[84:87],v[180:183],v[148:151],v[84:87]\nv_mfma_f32_16x16x32_bf16 v[88:91],v[184:187],v[148:151],v[88:91]\n" \
"v_mfma_f32_16x16x32_bf16 v[92:95],v[188:191],v[148:151],v[92:95]\nds_read_b128 v[148:151],v224 offset:9216\n" \
"ds_read_b64_tr_b16 v[172:173],v227 offset:256\ns_add_u32 m0,m0,1024\ns_nop 0\nbuffer_load_dwordx4 v230,%[sk],s57 offen lds\n" \
"v_mfma_f32_16x16x32_bf16 v[96:99],v[176:179],v[152:155],v[96:99]\nv_mfma_f32_16x16x32_bf16 v[100:103],v[180:183],v[152:155],v[100:103]\n" \
"v_mfma_f32_16x16x32_bf16 v[104:107],v[184:187],v[152:155],v[104:107]\nds_read_b64_tr_b16 v[174:175],v227 offset:2304\ns_add_u32 m0,m0,1024\ns_nop 0\n" \
"buffer_load_dwordx4 v230,%[sv],s57 offen lds\nv_add_u32 v228,128,v228\nv_add_u32 v229,128,v229\nv_add_u32 v230,0x80000,v230\ns_xor_b32 s59,s59,0x10000\n" \
"v_mfma_f32_16x16x32_bf16 v[108:111],v[188:191],v[152:155],v[108:111]\nds_read_b128 v[152:155],v224 offset:12288\n" \
"v_mfma_f32_16x16x32_bf16 v[112:115],v[176:179],v[156:159],v[112:115]\nv_mfma_f32_16x16x32_bf16 v[116:119],v[180:183],v[156:159],v[116:119]\n" \
"v_mfma_f32_16x16x32_bf16 v[120:123],v[184:187],v[156:159],v[120:123]\nv_mfma_f32_16x16x32_bf16 v[124:127],v[188:191],v[156:159],v[124:127]\n" \
"ds_read_b128 v[156:159],v224 offset:13312\ns_mov_b32 s60,4\n1:\ns_waitcnt lgkmcnt(12)\nv_mfma_f32_16x16x32_bf16 v[0:3],v[160:163],v[128:131],v[0:3]\n" \
"ds_read_b64_tr_b16 v[176:177],v226 offset:16384\ns_waitcnt lgkmcnt(10)\nv_mfma_f32_16x16x32_bf16 v[4:7],v[164:167],v[128:131],v[4:7]\n" \
"s_waitcnt lgkmcnt(6)\nv_mfma_f32_16x16x32_bf16 v[8:11],v[168:171],v[128:131],v[8:11]\ns_waitcnt lgkmcnt(3)\n" \
"v_mfma_f32_16x16x32_bf16 v[12:15],v[172:175],v[128:131],v[12:15]\nds_read_b128 v[128:131],v225 offset:0\n" \
"ds_read_b64_tr_b16 v[178:179],v226 offset:18432\nv_mfma_f32_16x16x32_bf16 v[16:19],v[160:163],v[132:135],v[16:19]\n" \
"v_mfma_f32_16x16x32_bf16 v[20:23],v[164:167],v[132:135],v[20:23]\nv_mfma_f32_16x16x32_bf16 v[24:27],v[168:171],v[132:135],v[24:27]\n" \
"ds_read_b64_tr_b16 v[180:181],v227 offset:16384\nv_mfma_f32_16x16x32_bf16 v[28:31],v[172:175],v[132:135],v[28:31]\n" \
"ds_read_b128 v[132:135],v225 offset:1024\nv_mfma_f32_16x16x32_bf16 v[32:35],v[160:163],v[136:139],v[32:35]\n" \
"v_mfma_f32_16x16x32_bf16 v[36:39],v[164:167],v[136:139],v[36:39]\nds_read_b64_tr_b16 v[182:183],v227 offset:18432\n" \
"v_mfma_f32_16x16x32_bf16 v[40:43],v[168:171],v[136:139],v[40:43]\nv_mfma_f32_16x16x32_bf16 v[44:47],v[172:175],v[136:139],v[44:47]\n" \
"ds_read_b128 v[136:139],v225 offset:4096\nv_mfma_f32_16x16x32_bf16 v[48:51],v[160:163],v[140:143],v[48:51]\n" \
"ds_read_b64_tr_b16 v[184:185],v226 offset:16640\nv_mfma_f32_16x16x32_bf16 v[52:55],v[164:167],v[140:143],v[52:55]\n" \
"v_mfma_f32_16x16x32_bf16 v[56:59],v[168:171],v[140:143],v[56:59]\nv_mfma_f32_16x16x32_bf16 v[60:63],v[172:175],v[140:143],v[60:63]\n" \
"ds_read_b128 v[140:143],v225 offset:5120\nds_read_b64_tr_b16 v[186:187],v226 offset:18688\n" \
"v_mfma_f32_16x16x32_bf16 v[64:67],v[160:163],v[144:147],v[64:67]\nv_mfma_f32_16x16x32_bf16 v[68:71],v[164:167],v[144:147],v[68:71]\n" \
"v_mfma_f32_16x16x32_bf16 v[72:75],v[168:171],v[144:147],v[72:75]\nds_read_b64_tr_b16 v[188:189],v227 offset:16640\n" \
"v_mfma_f32_16x16x32_bf16 v[76:79],v[172:175],v[144:147],v[76:79]\nds_read_b128 v[144:147],v225 offset:8192\n" \
"v_mfma_f32_16x16x32_bf16 v[80:83],v[160:163],v[148:151],v[80:83]\nv_mfma_f32_16x16x32_bf16 v[84:87],v[164:167],v[148:151],v[84:87]\n" \
"ds_read_b64_tr_b16 v[190:191],v227 offset:18688\nv_mfma_f32_16x16x32_bf16 v[88:91],v[168:171],v[148:151],v[88:91]\n" \
"v_mfma_f32_16x16x32_bf16 v[92:95],v[172:175],v[148:151],v[92:95]\nds_read_b128 v[148:151],v225 offset:9216\ns_waitcnt lgkmcnt(15)\n" \
"v_mfma_f32_16x16x32_bf16 v[96:99],v[160:163],v[152:155],v[96:99]\nv_mfma_f32_16x16x32_bf16 v[100:103],v[164:167],v[152:155],v[100:103]\n" \
"v_mfma_f32_16x16x32_bf16 v[104:107],v[168:171],v[152:155],v[104:107]\nv_mfma_f32_16x16x32_bf16 v[108:111],v[172:175],v[152:155],v[108:111]\n" \
"ds_read_b128 v[152:155],v225 offset:12288\ns_waitcnt lgkmcnt(15)\nv_mfma_f32_16x16x32_bf16 v[112:115],v[160:163],v[156:159],v[112:115]\n" \
"v_mfma_f32_16x16x32_bf16 v[116:119],v[164:167],v[156:159],v[116:119]\nv_mfma_f32_16x16x32_bf16 v[120:123],v[168:171],v[156:159],v[120:123]\n" \
"v_mfma_f32_16x16x32_bf16 v[124:127],v[172:175],v[156:159],v[124:127]\nds_read_b128 v[156:159],v225 offset:13312\ns_waitcnt lgkmcnt(13)\n" \
"v_mfma_f32_16x16x32_bf16 v[0:3],v[176:179],v[128:131],v[0:3]\ns_waitcnt lgkmcnt(10)\nv_mfma_f32_16x16x32_bf16 v[4:7],v[180:183],v[128:131],v[4:7]\n" \
"s_waitcnt lgkmcnt(6)\nv_mfma_f32_16x16x32_bf16 v[8:11],v[184:187],v[128:131],v[8:11]\ns_waitcnt lgkmcnt(3)\n" \
"v_mfma_f32_16x16x32_bf16 v[12:15],v[188:191],v[128:131],v[12:15]\ns_waitcnt vmcnt(0) lgkmcnt(0)\ns_barrier\nv_xor_b32 v224,0x10000,v224\n" \
"v_xor_b32 v225,0x10000,v225\nv_xor_b32 v226,0x10000,v226\nv_xor_b32 v227,0x10000,v227\n" \
"v_mfma_f32_16x16x32_bf16 v[16:19],v[176:179],v[132:135],v[16:19]\nds_read_b128 v[128:131],v224 offset:0\n" \
"v_mfma_f32_16x16x32_bf16 v[20:23],v[180:183],v[132:135],v[20:23]\nds_read_b64_tr_b16 v[160:161],v226 offset:0\ns_mov_b32 m0,s59\ns_nop 0\n" \
"buffer_load_dwordx4 v228,%[sx],s52 offen lds\nv_mfma_f32_16x16x32_bf16 v[24:27],v[184:187],v[132:135],v[24:27]\n" \
"v_mfma_f32_16x16x32_bf16 v[28:31],v[188:191],v[132:135],v[28:31]\nds_read_b128 v[132:135],v224 offset:1024\n" \
"v_mfma_f32_16x16x32_bf16 v[32:35],v[176:179],v[136:139],v[32:35]\nds_read_b64_tr_b16 v[162:163],v226 offset:2048\ns_add_u32 m0,m0,1024\ns_nop 0\n" \
"buffer_load_dwordx4 v228,%[sx],s53 offen lds\nv_mfma_f32_16x16x32_bf16 v[36:39],v[180:183],v[136:139],v[36:39]\n" \
"v_mfma_f32_16x16x32_bf16 v[40:43],v[184:187],v[136:139],v[40:43]\nv_mfma_f32_16x16x32_bf16 v[44:47],v[188:191],v[136:139],v[44:47]\n" \
"ds_read_b128 v[136:139],v224 offset:4096\nds_read_b64_tr_b16 v[164:165],v227 offset:0\ns_add_u32 m0,m0,1024\ns_nop 0\n" \
"buffer_load_dwordx4 v229,%[sx],s54 offen lds\nv_mfma_f32_16x16x32_bf16 v[48:51],v[176:179],v[140:143],v[48:51]\n" \
"v_mfma_f32_16x16x32_bf16 v[52:55],v[180:183],v[140:143],v[52:55]\nv_mfma_f32_16x16x32_bf16 v[56:59],v[184:187],v[140:143],v[56:59]\n" \
"ds_read_b64_tr_b16 v[166:167],v227 offset:2048\ns_add_u32 m0,m0,1024\ns_nop 0\nbuffer_load_dwordx4 v229,%[sx],s55 offen lds\n" \
"v_mfma_f32_16x16x32_bf16 v[60:63],v[188:191],v[140:143],v[60:63]\nds_read_b128 v[140:143],v224 offset:5120\n" \
"v_mfma_f32_16x16x32_bf16 v[64:67],v[176:179],v[144:147],v[64:67]\nv_mfma_f32_16x16x32_bf16 v[68:71],v[180:183],v[144:147],v[68:71]\n" \
"ds_read_b64_tr_b16 v[168:169],v226 offset:256\ns_add_u32 m0,m0,29696\ns_nop 0\nbuffer_load_dwordx4 v230,%[sk],s56 offen lds\n" \
"v_mfma_f32_16x16x32_bf16 v[72:75],v[184:187],v[144:147],v[72:75]\nv_mfma_f32_16x16x32_bf16 v[76:79],v[188:191],v[144:147],v[76:79]\n" \
"ds_read_b128 v[144:147],v224 offset:8192\nv_mfma_f32_16x16x32_bf16 v[80:83],v[176:179],v[148:151],v[80:83]\n" \
"ds_read_b64_tr_b16 v[170:171],v226 offset:2304\ns_add_u32 m0,m0,1024\ns_nop 0\nbuffer_load_dwordx4 v230,%[sv],s56 offen lds\n" \
"v_mfma_f32_16x16x32_bf16 v[84:87],v[180:183],v[148:151],v[84:87]\nv_mfma_f32_16x16x32_bf16 v[88:91],v[184:187],v[148:151],v[88:91]\n" \
"v_mfma_f32_16x16x32_bf16 v[92:95],v[188:191],v[148:151],v[92:95]\nds_read_b128 v[148:151],v224 offset:9216\n" \
"ds_read_b64_tr_b16 v[172:173],v227 offset:256\ns_add_u32 m0,m0,1024\ns_nop 0\nbuffer_load_dwordx4 v230,%[sk],s57 offen lds\n" \
"v_mfma_f32_16x16x32_bf16 v[96:99],v[176:179],v[152:155],v[96:99]\nv_mfma_f32_16x16x32_bf16 v[100:103],v[180:183],v[152:155],v[100:103]\n" \
"v_mfma_f32_16x16x32_bf16 v[104:107],v[184:187],v[152:155],v[104:107]\nds_read_b64_tr_b16 v[174:175],v227 offset:2304\ns_add_u32 m0,m0,1024\ns_nop 0\n" \
"buffer_load_dwordx4 v230,%[sv],s57 offen lds\nv_add_u32 v228,128,v228\nv_add_u32 v229,128,v229\nv_add_u32 v230,0x80000,v230\ns_xor_b32 s59,s59,0x10000\n" \
"v_mfma_f32_16x16x32_bf16 v[108:111],v[188:191],v[152:155],v[108:111]\nds_read_b128 v[152:155],v224 offset:12288\n" \
"v_mfma_f32_16x16x32_bf16 v[112:115],v[176:179],v[156:159],v[112:115]\nv_mfma_f32_16x16x32_bf16 v[116:119],v[180:183],v[156:159],v[116:119]\n" \
"v_mfma_f32_16x16x32_bf16 v[120:123],v[184:187],v[156:159],v[120:123]\nv_mfma_f32_16x16x32_bf16 v[124:127],v[188:191],v[156:159],v[124:127]\n" \
"ds_read_b128 v[156:159],v224 offset:13312\ns_sub_u32 s60,s60,1\ns_cmp_lg_u32 s60,0\ns_cbranch_scc1 1b\ns_waitcnt lgkmcnt(12)\n" \
"v_mfma_f32_16x16x32_bf16 v[0:3],v[160:163],v[128:131],v[0:3]\nds_read_b64_tr_b16 v[176:177],v226 offset:16384\ns_waitcnt lgkmcnt(10)\n" \
"v_mfma_f32_16x16x32_bf16 v[4:7],v[164:167],v[128:131],v[4:7]\ns_waitcnt lgkmcnt(6)\nv_mfma_f32_16x16x32_bf16 v[8:11],v[168:171],v[128:131],v[8:11]\n" \
"s_waitcnt lgkmcnt(3)\nv_mfma_f32_16x16x32_bf16 v[12:15],v[172:175],v[128:131],v[12:15]\nds_read_b128 v[128:131],v225 offset:0\n" \
"ds_read_b64_tr_b16 v[178:179],v226 offset:18432\nv_mfma_f32_16x16x32_bf16 v[16:19],v[160:163],v[132:135],v[16:19]\n" \
"v_mfma_f32_16x16x32_bf16 v[20:23],v[164:167],v[132:135],v[20:23]\nv_mfma_f32_16x16x32_bf16 v[24:27],v[168:171],v[132:135],v[24:27]\n" \
"ds_read_b64_tr_b16 v[180:181],v227 offset:16384\nv_mfma_f32_16x16x32_bf16 v[28:31],v[172:175],v[132:135],v[28:31]\n" \
"ds_read_b128 v[132:135],v225 offset:1024\nv_mfma_f32_16x16x32_bf16 v[32:35],v[160:163],v[136:139],v[32:35]\n" \
"v_mfma_f32_16x16x32_bf16 v[36:39],v[164:167],v[136:139],v[36:39]\nds_read_b64_tr_b16 v[182:183],v227 offset:18432\n" \
"v_mfma_f32_16x16x32_bf16 v[40:43],v[168:171],v[136:139],v[40:43]\nv_mfma_f32_16x16x32_bf16 v[44:47],v[172:175],v[136:139],v[44:47]\n" \
"ds_read_b128 v[136:139],v225 offset:4096\nv_mfma_f32_16x16x32_bf16 v[48:51],v[160:163],v[140:143],v[48:51]\n" \
"ds_read_b64_tr_b16 v[184:185],v226 offset:16640\nv_mfma_f32_16x16x32_bf16 v[52:55],v[164:167],v[140:143],v[52:55]\n" \
"v_mfma_f32_16x16x32_bf16 v[56:59],v[168:171],v[140:143],v[56:59]\nv_mfma_f32_16x16x32_bf16 v[60:63],v[172:175],v[140:143],v[60:63]\n" \
"ds_read_b128 v[140:143],v225 offset:5120\nds_read_b64_tr_b16 v[186:187],v226 offset:18688\n" \
"v_mfma_f32_16x16x32_bf16 v[64:67],v[160:163],v[144:147],v[64:67]\nv_mfma_f32_16x16x32_bf16 v[68:71],v[164:167],v[144:147],v[68:71]\n" \
"v_mfma_f32_16x16x32_bf16 v[72:75],v[168:171],v[144:147],v[72:75]\nds_read_b64_tr_b16 v[188:189],v227 offset:16640\n" \
"v_mfma_f32_16x16x32_bf16 v[76:79],v[172:175],v[144:147],v[76:79]\nds_read_b128 v[144:147],v225 offset:8192\n" \
"v_mfma_f32_16x16x32_bf16 v[80:83],v[160:163],v[148:151],v[80:83]\nv_mfma_f32_16x16x32_bf16 v[84:87],v[164:167],v[148:151],v[84:87]\n" \
"ds_read_b64_tr_b16 v[190:191],v227 offset:18688\nv_mfma_f32_16x16x32_bf16 v[88:91],v[168:171],v[148:151],v[88:91]\n" \
"v_mfma_f32_16x16x32_bf16 v[92:95],v[172:175],v[148:151],v[92:95]\nds_read_b128 v[148:151],v225 offset:9216\ns_waitcnt lgkmcnt(15)\n" \
"v_mfma_f32_16x16x32_bf16 v[96:99],v[160:163],v[152:155],v[96:99]\nv_mfma_f32_16x16x32_bf16 v[100:103],v[164:167],v[152:155],v[100:103]\n" \
"v_mfma_f32_16x16x32_bf16 v[104:107],v[168:171],v[152:155],v[104:107]\nv_mfma_f32_16x16x32_bf16 v[108:111],v[172:175],v[152:155],v[108:111]\n" \
"ds_read_b128 v[152:155],v225 offset:12288\ns_waitcnt lgkmcnt(15)\nv_mfma_f32_16x16x32_bf16 v[112:115],v[160:163],v[156:159],v[112:115]\n" \
"v_mfma_f32_16x16x32_bf16 v[116:119],v[164:167],v[156:159],v[116:119]\nv_mfma_f32_16x16x32_bf16 v[120:123],v[168:171],v[156:159],v[120:123]\n" \
"v_mfma_f32_16x16x32_bf16 v[124:127],v[172:175],v[156:159],v[124:127]\nds_read_b128 v[156:159],v225 offset:13312\ns_waitcnt lgkmcnt(13)\n" \
"v_mfma_f32_16x16x32_bf16 v[0:3],v[176:179],v[128:131],v[0:3]\ns_waitcnt lgkmcnt(10)\nv_mfma_f32_16x16x32_bf16 v[4:7],v[180:183],v[128:131],v[4:7]\n" \
"s_waitcnt lgkmcnt(6)\nv_mfma_f32_16x16x32_bf16 v[8:11],v[184:187],v[128:131],v[8:11]\ns_waitcnt lgkmcnt(3)\n" \
"v_mfma_f32_16x16x32_bf16 v[12:15],v[188:191],v[128:131],v[12:15]\ns_waitcnt vmcnt(0) lgkmcnt(0)\ns_barrier\nv_xor_b32 v224,0x10000,v224\n" \
"v_xor_b32 v225,0x10000,v225\nv_xor_b32 v226,0x10000,v226\nv_xor_b32 v227,0x10000,v227\n" \
"v_mfma_f32_16x16x32_bf16 v[16:19],v[176:179],v[132:135],v[16:19]\nds_read_b128 v[128:131],v224 offset:0\n" \
"v_mfma_f32_16x16x32_bf16 v[20:23],v[180:183],v[132:135],v[20:23]\nds_read_b64_tr_b16 v[160:161],v226 offset:0\ns_mov_b32 m0,s59\ns_nop 0\n" \
"buffer_load_dwordx4 v228,%[sx],s52 offen lds\nv_mfma_f32_16x16x32_bf16 v[24:27],v[184:187],v[132:135],v[24:27]\n" \
"v_mfma_f32_16x16x32_bf16 v[28:31],v[188:191],v[132:135],v[28:31]\nds_read_b128 v[132:135],v224 offset:1024\n" \
"v_mfma_f32_16x16x32_bf16 v[32:35],v[176:179],v[136:139],v[32:35]\nds_read_b64_tr_b16 v[162:163],v226 offset:2048\ns_add_u32 m0,m0,1024\ns_nop 0\n" \
"buffer_load_dwordx4 v228,%[sx],s53 offen lds\nv_mfma_f32_16x16x32_bf16 v[36:39],v[180:183],v[136:139],v[36:39]\n" \
"v_mfma_f32_16x16x32_bf16 v[40:43],v[184:187],v[136:139],v[40:43]\nv_mfma_f32_16x16x32_bf16 v[44:47],v[188:191],v[136:139],v[44:47]\n" \
"ds_read_b128 v[136:139],v224 offset:4096\nds_read_b64_tr_b16 v[164:165],v227 offset:0\ns_add_u32 m0,m0,1024\ns_nop 0\n" \
"buffer_load_dwordx4 v229,%[sx],s54 offen lds\nv_mfma_f32_16x16x32_bf16 v[48:51],v[176:179],v[140:143],v[48:51]\n" \
"v_mfma_f32_16x16x32_bf16 v[52:55],v[180:183],v[140:143],v[52:55]\nv_mfma_f32_16x16x32_bf16 v[56:59],v[184:187],v[140:143],v[56:59]\n" \
"ds_read_b64_tr_b16 v[166:167],v227 offset:2048\ns_add_u32 m0,m0,1024\ns_nop 0\nbuffer_load_dwordx4 v229,%[sx],s55 offen lds\n" \
"v_mfma_f32_16x16x32_bf16 v[60:63],v[188:191],v[140:143],v[60:63]\nds_read_b128 v[140:143],v224 offset:5120\n" \
"v_mfma_f32_16x16x32_bf16 v[64:67],v[176:179],v[144:147],v[64:67]\nv_mfma_f32_16x16x32_bf16 v[68:71],v[180:183],v[144:147],v[68:71]\n" \
"ds_read_b64_tr_b16 v[168:169],v226 offset:256\ns_add_u32 m0,m0,29696\ns_nop 0\nbuffer_load_dwordx4 v230,%[sk],s56 offen lds\n" \
"v_mfma_f32_16x16x32_bf16 v[72:75],v[184:187],v[144:147],v[72:75]\nv_mfma_f32_16x16x32_bf16 v[76:79],v[188:191],v[144:147],v[76:79]\n" \
"ds_read_b128 v[144:147],v224 offset:8192\nv_mfma_f32_16x16x32_bf16 v[80:83],v[176:179],v[148:151],v[80:83]\n" \
"ds_read_b64_tr_b16 v[170:171],v226 offset:2304\ns_add_u32 m0,m0,1024\ns_nop 0\nbuffer_load_dwordx4 v230,%[sv],s56 offen lds\n" \
"v_mfma_f32_16x16x32_bf16 v[84:87],v[180:183],v[148:151],v[84:87]\nv_mfma_f32_16x16x32_bf16 v[88:91],v[184:187],v[148:151],v[88:91]\n" \
"v_mfma_f32_16x16x32_bf16 v[92:95],v[188:191],v[148:151],v[92:95]\nds_read_b128 v[148:151],v224 offset:9216\n" \
"ds_read_b64_tr_b16 v[172:173],v227 offset:256\ns_add_u32 m0,m0,1024\ns_nop 0\nbuffer_load_dwordx4 v230,%[sk],s57 offen lds\n" \
"v_mfma_f32_16x16x32_bf16 v[96:99],v[176:179],v[152:155],v[96:99]\nv_mfma_f32_16x16x32_bf16 v[100:103],v[180:183],v[152:155],v[100:103]\n" \
"v_mfma_f32_16x16x32_bf16 v[104:107],v[184:187],v[152:155],v[104:107]\nds_read_b64_tr_b16 v[174:175],v227 offset:2304\ns_add_u32 m0,m0,1024\ns_nop 0\n" \
"buffer_load_dwordx4 v230,%[sv],s57 offen lds\nv_add_u32 v228,128,v228\nv_add_u32 v229,128,v229\nv_add_u32 v230,0x80000,v230\ns_xor_b32 s59,s59,0x10000\n" \
"v_mfma_f32_16x16x32_bf16 v[108:111],v[188:191],v[152:155],v[108:111]\nds_read_b128 v[152:155],v224 offset:12288\n" \
"v_mfma_f32_16x16x32_bf16 v[112:115],v[176:179],v[156:159],v[112:115]\nv_mfma_f32_16x16x32_bf16 v[116:119],v[180:183],v[156:159],v[116:119]\n" \
"v_mfma_f32_16x16x32_bf16 v[120:123],v[184:187],v[156:159],v[120:123]\nv_mfma_f32_16x16x32_bf16 v[124:127],v[188:191],v[156:159],v[124:127]\n" \
"ds_read_b128 v[156:159],v224 offset:13312\ns_and_b32 s50,s66,0xffff\ns_add_u32 m0,s50,131072\ns_add_u32 s49,s67,0\n" \
"buffer_load_dwordx4 v249,%[sq],s49 offen lds\ns_add_u32 m0,m0,1024\ns_add_u32 s49,s49,32768\nbuffer_load_dwordx4 v249,%[sq],s49 offen lds\n" \
"s_add_u32 m0,m0,1024\ns_add_u32 s49,s49,32768\nbuffer_load_dwordx4 v249,%[sq],s49 offen lds\ns_add_u32 m0,m0,1024\ns_add_u32 s49,s49,32768\n" \
"buffer_load_dwordx4 v249,%[sq],s49 offen lds\ns_add_u32 s58,s76,0\nbuffer_load_dwordx4 v[192:195],v248,%[sws],s58 offen offset:0\n" \
"buffer_load_dwordx4 v[196:199],v248,%[sws],s58 offen offset:1024\nbuffer_load_dwordx4 v[200:203],v248,%[sws],s58 offen offset:2048\n" \
"buffer_load_dwordx4 v[204:207],v248,%[sws],s58 offen offset:3072\ns_add_u32 s58,s76,4096\n" \
"buffer_load_dwordx4 v[208:211],v248,%[sws],s58 offen offset:0\nbuffer_load_dwordx4 v[212:215],v248,%[sws],s58 offen offset:1024\n" \
"buffer_load_dwordx4 v[216:219],v248,%[sws],s58 offen offset:2048\nbuffer_load_dwordx4 v[220:223],v248,%[sws],s58 offen offset:3072\n" \
"s_waitcnt lgkmcnt(12)\nv_mfma_f32_16x16x32_bf16 v[0:3],v[160:163],v[128:131],v[0:3]\nds_read_b64_tr_b16 v[176:177],v226 offset:16384\n" \
"s_waitcnt lgkmcnt(10)\nv_mfma_f32_16x16x32_bf16 v[4:7],v[164:167],v[128:131],v[4:7]\ns_waitcnt lgkmcnt(6)\n" \
"v_mfma_f32_16x16x32_bf16 v[8:11],v[168:171],v[128:131],v[8:11]\ns_waitcnt lgkmcnt(3)\nv_mfma_f32_16x16x32_bf16 v[12:15],v[172:175],v[128:131],v[12:15]\n" \
"ds_read_b128 v[128:131],v225 offset:0\nds_read_b64_tr_b16 v[178:179],v226 offset:18432\n" \
"v_mfma_f32_16x16x32_bf16 v[16:19],v[160:163],v[132:135],v[16:19]\nv_mfma_f32_16x16x32_bf16 v[20:23],v[164:167],v[132:135],v[20:23]\n" \
"v_mfma_f32_16x16x32_bf16 v[24:27],v[168:171],v[132:135],v[24:27]\nds_read_b64_tr_b16 v[180:181],v227 offset:16384\n" \
"v_mfma_f32_16x16x32_bf16 v[28:31],v[172:175],v[132:135],v[28:31]\nds_read_b128 v[132:135],v225 offset:1024\n" \
"v_mfma_f32_16x16x32_bf16 v[32:35],v[160:163],v[136:139],v[32:35]\nv_mfma_f32_16x16x32_bf16 v[36:39],v[164:167],v[136:139],v[36:39]\n" \
"ds_read_b64_tr_b16 v[182:183],v227 offset:18432\nv_mfma_f32_16x16x32_bf16 v[40:43],v[168:171],v[136:139],v[40:43]\n" \
"v_mfma_f32_16x16x32_bf16 v[44:47],v[172:175],v[136:139],v[44:47]\nds_read_b128 v[136:139],v225 offset:4096\n" \
"v_mfma_f32_16x16x32_bf16 v[48:51],v[160:163],v[140:143],v[48:51]\nds_read_b64_tr_b16 v[184:185],v226 offset:16640\n" \
"v_mfma_f32_16x16x32_bf16 v[52:55],v[164:167],v[140:143],v[52:55]\nv_mfma_f32_16x16x32_bf16 v[56:59],v[168:171],v[140:143],v[56:59]\n" \
"v_mfma_f32_16x16x32_bf16 v[60:63],v[172:175],v[140:143],v[60:63]\nds_read_b128 v[140:143],v225 offset:5120\n" \
"ds_read_b64_tr_b16 v[186:187],v226 offset:18688\nv_mfma_f32_16x16x32_bf16 v[64:67],v[160:163],v[144:147],v[64:67]\n" \
"v_mfma_f32_16x16x32_bf16 v[68:71],v[164:167],v[144:147],v[68:71]\nv_mfma_f32_16x16x32_bf16 v[72:75],v[168:171],v[144:147],v[72:75]\n" \
"ds_read_b64_tr_b16 v[188:189],v227 offset:16640\nv_mfma_f32_16x16x32_bf16 v[76:79],v[172:175],v[144:147],v[76:79]\n" \
"ds_read_b128 v[144:147],v225 offset:8192\nv_mfma_f32_16x16x32_bf16 v[80:83],v[160:163],v[148:151],v[80:83]\n" \
"v_mfma_f32_16x16x32_bf16 v[84:87],v[164:167],v[148:151],v[84:87]\nds_read_b64_tr_b16 v[190:191],v227 offset:18688\n" \
"v_mfma_f32_16x16x32_bf16 v[88:91],v[168:171],v[148:151],v[88:91]\nv_mfma_f32_16x16x32_bf16 v[92:95],v[172:175],v[148:151],v[92:95]\n" \
"ds_read_b128 v[148:151],v225 offset:9216\ns_waitcnt lgkmcnt(15)\nv_mfma_f32_16x16x32_bf16 v[96:99],v[160:163],v[152:155],v[96:99]\n" \
"v_mfma_f32_16x16x32_bf16 v[100:103],v[164:167],v[152:155],v[100:103]\nv_mfma_f32_16x16x32_bf16 v[104:107],v[168:171],v[152:155],v[104:107]\n" \
"v_mfma_f32_16x16x32_bf16 v[108:111],v[172:175],v[152:155],v[108:111]\nds_read_b128 v[152:155],v225 offset:12288\ns_waitcnt lgkmcnt(15)\n" \
"v_mfma_f32_16x16x32_bf16 v[112:115],v[160:163],v[156:159],v[112:115]\nv_mfma_f32_16x16x32_bf16 v[116:119],v[164:167],v[156:159],v[116:119]\n" \
"v_mfma_f32_16x16x32_bf16 v[120:123],v[168:171],v[156:159],v[120:123]\nv_mfma_f32_16x16x32_bf16 v[124:127],v[172:175],v[156:159],v[124:127]\n" \
"ds_read_b128 v[156:159],v225 offset:13312\ns_waitcnt lgkmcnt(13)\nv_mfma_f32_16x16x32_bf16 v[0:3],v[176:179],v[128:131],v[0:3]\ns_waitcnt lgkmcnt(10)\n" \
"v_mfma_f32_16x16x32_bf16 v[4:7],v[180:183],v[128:131],v[4:7]\ns_waitcnt lgkmcnt(6)\nv_mfma_f32_16x16x32_bf16 v[8:11],v[184:187],v[128:131],v[8:11]\n" \
"s_waitcnt lgkmcnt(3)\nv_mfma_f32_16x16x32_bf16 v[12:15],v[188:191],v[128:131],v[12:15]\ns_waitcnt vmcnt(0) lgkmcnt(0)\ns_barrier\n" \
"v_xor_b32 v224,0x10000,v224\nv_xor_b32 v225,0x10000,v225\nv_xor_b32 v226,0x10000,v226\nv_xor_b32 v227,0x10000,v227\n" \
"v_mfma_f32_16x16x32_bf16 v[16:19],v[176:179],v[132:135],v[16:19]\nds_read_b128 v[128:131],v224 offset:0\n" \
"v_mfma_f32_16x16x32_bf16 v[20:23],v[180:183],v[132:135],v[20:23]\nds_read_b64_tr_b16 v[160:161],v226 offset:0\ns_add_u32 m0,s80,65536\n" \
"s_add_u32 s49,s68,0\nbuffer_load_dwordx4 v248,%[sws],s49 offen offset:0 lds\nv_mfma_f32_16x16x32_bf16 v[24:27],v[184:187],v[132:135],v[24:27]\n" \
"v_mfma_f32_16x16x32_bf16 v[28:31],v[188:191],v[132:135],v[28:31]\nds_read_b128 v[132:135],v224 offset:1024\n" \
"v_mfma_f32_16x16x32_bf16 v[32:35],v[176:179],v[136:139],v[32:35]\nds_read_b64_tr_b16 v[162:163],v226 offset:2048\n" \
"buffer_load_dwordx4 v248,%[sws],s49 offen offset:1024 lds\nv_mfma_f32_16x16x32_bf16 v[36:39],v[180:183],v[136:139],v[36:39]\n" \
"v_mfma_f32_16x16x32_bf16 v[40:43],v[184:187],v[136:139],v[40:43]\nv_mfma_f32_16x16x32_bf16 v[44:47],v[188:191],v[136:139],v[44:47]\n" \
"ds_read_b128 v[136:139],v224 offset:4096\nds_read_b64_tr_b16 v[164:165],v227 offset:0\nbuffer_load_dwordx4 v248,%[sws],s49 offen offset:2048 lds\n" \
"v_mfma_f32_16x16x32_bf16 v[48:51],v[176:179],v[140:143],v[48:51]\nv_mfma_f32_16x16x32_bf16 v[52:55],v[180:183],v[140:143],v[52:55]\n" \
"v_mfma_f32_16x16x32_bf16 v[56:59],v[184:187],v[140:143],v[56:59]\nds_read_b64_tr_b16 v[166:167],v227 offset:2048\n" \
"buffer_load_dwordx4 v248,%[sws],s49 offen offset:3072 lds\nv_mfma_f32_16x16x32_bf16 v[60:63],v[188:191],v[140:143],v[60:63]\n" \
"ds_read_b128 v[140:143],v224 offset:5120\nv_mfma_f32_16x16x32_bf16 v[64:67],v[176:179],v[144:147],v[64:67]\n" \
"v_mfma_f32_16x16x32_bf16 v[68:71],v[180:183],v[144:147],v[68:71]\nds_read_b64_tr_b16 v[168:169],v226 offset:256\ns_add_u32 m0,s80,69632\n" \
"s_add_u32 s49,s68,4096\nbuffer_load_dwordx4 v248,%[sws],s49 offen offset:0 lds\nv_mfma_f32_16x16x32_bf16 v[72:75],v[184:187],v[144:147],v[72:75]\n" \
"v_mfma_f32_16x16x32_bf16 v[76:79],v[188:191],v[144:147],v[76:79]\nds_read_b128 v[144:147],v224 offset:8192\n" \
"v_mfma_f32_16x16x32_bf16 v[80:83],v[176:179],v[148:151],v[80:83]\nds_read_b64_tr_b16 v[170:171],v226 offset:2304\n" \
"buffer_load_dwordx4 v248,%[sws],s49 offen offset:1024 lds\nv_mfma_f32_16x16x32_bf16 v[84:87],v[180:183],v[148:151],v[84:87]\n" \
"v_mfma_f32_16x16x32_bf16 v[88:91],v[184:187],v[148:151],v[88:91]\nv_mfma_f32_16x16x32_bf16 v[92:95],v[188:191],v[148:151],v[92:95]\n" \
"ds_read_b128 v[148:151],v224 offset:9216\nds_read_b64_tr_b16 v[172:173],v227 offset:256\n" \
"v_mfma_f32_16x16x32_bf16 v[96:99],v[176:179],v[152:155],v[96:99]\nv_mfma_f32_16x16x32_bf16 v[100:103],v[180:183],v[152:155],v[100:103]\n" \
"v_mfma_f32_16x16x32_bf16 v[104:107],v[184:187],v[152:155],v[104:107]\nds_read_b64_tr_b16 v[174:175],v227 offset:2304\n" \
"v_mfma_f32_16x16x32_bf16 v[108:111],v[188:191],v[152:155],v[108:111]\nds_read_b128 v[152:155],v224 offset:12288\n" \
"v_mfma_f32_16x16x32_bf16 v[112:115],v[176:179],v[156:159],v[112:115]\nv_mfma_f32_16x16x32_bf16 v[116:119],v[180:183],v[156:159],v[116:119]\n" \
"v_mfma_f32_16x16x32_bf16 v[120:123],v[184:187],v[156:159],v[120:123]\nv_mfma_f32_16x16x32_bf16 v[124:127],v[188:191],v[156:159],v[124:127]\n" \
"ds_read_b128 v[156:159],v224 offset:13312\ns_waitcnt lgkmcnt(12)\nv_mfma_f32_16x16x32_bf16 v[0:3],v[160:163],v[128:131],v[0:3]\n" \
"ds_read_b64_tr_b16 v[176:177],v226 offset:16384\ns_waitcnt lgkmcnt(10)\nv_mfma_f32_16x16x32_bf16 v[4:7],v[164:167],v[128:131],v[4:7]\n" \
"s_waitcnt lgkmcnt(6)\nv_mfma_f32_16x16x32_bf16 v[8:11],v[168:171],v[128:131],v[8:11]\ns_waitcnt lgkmcnt(3)\n" \
"v_mfma_f32_16x16x32_bf16 v[12:15],v[172:175],v[128:131],v[12:15]\nds_read_b128 v[128:131],v225 offset:0\n" \
"ds_read_b64_tr_b16 v[178:179],v226 offset:18432\nv_mfma_f32_16x16x32_bf16 v[16:19],v[160:163],v[132:135],v[16:19]\n" \
"v_mfma_f32_16x16x32_bf16 v[20:23],v[164:167],v[132:135],v[20:23]\nv_mfma_f32_16x16x32_bf16 v[24:27],v[168:171],v[132:135],v[24:27]\n" \
"ds_read_b64_tr_b16 v[180:181],v227 offset:16384\nv_mfma_f32_16x16x32_bf16 v[28:31],v[172:175],v[132:135],v[28:31]\n" \
"ds_read_b128 v[132:135],v225 offset:1024\nv_mfma_f32_16x16x32_bf16 v[32:35],v[160:163],v[136:139],v[32:35]\n" \
"v_mfma_f32_16x16x32_bf16 v[36:39],v[164:167],v[136:139],v[36:39]\nds_read_b64_tr_b16 v[182:183],v227 offset:18432\n" \
"v_mfma_f32_16x16x32_bf16 v[40:43],v[168:171],v[136:139],v[40:43]\nv_mfma_f32_16x16x32_bf16 v[44:47],v[172:175],v[136:139],v[44:47]\n" \
"ds_read_b128 v[136:139],v225 offset:4096\nv_mfma_f32_16x16x32_bf16 v[48:51],v[160:163],v[140:143],v[48:51]\n" \
"ds_read_b64_tr_b16 v[184:185],v226 offset:16640\nv_mfma_f32_16x16x32_bf16 v[52:55],v[164:167],v[140:143],v[52:55]\n" \
"v_mfma_f32_16x16x32_bf16 v[56:59],v[168:171],v[140:143],v[56:59]\nv_mfma_f32_16x16x32_bf16 v[60:63],v[172:175],v[140:143],v[60:63]\n" \
"ds_read_b128 v[140:143],v225 offset:5120\nds_read_b64_tr_b16 v[186:187],v226 offset:18688\n" \
"v_mfma_f32_16x16x32_bf16 v[64:67],v[160:163],v[144:147],v[64:67]\nv_mfma_f32_16x16x32_bf16 v[68:71],v[164:167],v[144:147],v[68:71]\n" \
"v_mfma_f32_16x16x32_bf16 v[72:75],v[168:171],v[144:147],v[72:75]\nds_read_b64_tr_b16 v[188:189],v227 offset:16640\n" \
"v_mfma_f32_16x16x32_bf16 v[76:79],v[172:175],v[144:147],v[76:79]\nds_read_b128 v[144:147],v225 offset:8192\n" \
"v_mfma_f32_16x16x32_bf16 v[80:83],v[160:163],v[148:151],v[80:83]\nv_mfma_f32_16x16x32_bf16 v[84:87],v[164:167],v[148:151],v[84:87]\n" \
"ds_read_b64_tr_b16 v[190:191],v227 offset:18688\nv_mfma_f32_16x16x32_bf16 v[88:91],v[168:171],v[148:151],v[88:91]\n" \
"v_mfma_f32_16x16x32_bf16 v[92:95],v[172:175],v[148:151],v[92:95]\nds_read_b128 v[148:151],v225 offset:9216\ns_waitcnt lgkmcnt(15)\n" \
"v_mfma_f32_16x16x32_bf16 v[96:99],v[160:163],v[152:155],v[96:99]\nv_mfma_f32_16x16x32_bf16 v[100:103],v[164:167],v[152:155],v[100:103]\n" \
"v_mfma_f32_16x16x32_bf16 v[104:107],v[168:171],v[152:155],v[104:107]\nv_mfma_f32_16x16x32_bf16 v[108:111],v[172:175],v[152:155],v[108:111]\n" \
"ds_read_b128 v[152:155],v225 offset:12288\ns_waitcnt lgkmcnt(15)\nv_mfma_f32_16x16x32_bf16 v[112:115],v[160:163],v[156:159],v[112:115]\n" \
"v_mfma_f32_16x16x32_bf16 v[116:119],v[164:167],v[156:159],v[116:119]\nv_mfma_f32_16x16x32_bf16 v[120:123],v[168:171],v[156:159],v[120:123]\n" \
"v_mfma_f32_16x16x32_bf16 v[124:127],v[172:175],v[156:159],v[124:127]\nds_read_b128 v[156:159],v225 offset:13312\ns_waitcnt lgkmcnt(13)\n" \
"v_mfma_f32_16x16x32_bf16 v[0:3],v[176:179],v[128:131],v[0:3]\ns_waitcnt lgkmcnt(10)\nv_mfma_f32_16x16x32_bf16 v[4:7],v[180:183],v[128:131],v[4:7]\n" \
"s_waitcnt lgkmcnt(6)\nv_mfma_f32_16x16x32_bf16 v[8:11],v[184:187],v[128:131],v[8:11]\ns_waitcnt lgkmcnt(3)\n" \
"v_mfma_f32_16x16x32_bf16 v[12:15],v[188:191],v[128:131],v[12:15]\ns_waitcnt vmcnt(0) lgkmcnt(0)\ns_barrier\n" \
"v_mfma_f32_16x16x32_bf16 v[16:19],v[176:179],v[132:135],v[16:19]\nv_mfma_f32_16x16x32_bf16 v[20:23],v[180:183],v[132:135],v[20:23]\n" \
"v_mfma_f32_16x16x32_bf16 v[24:27],v[184:187],v[132:135],v[24:27]\nv_mfma_f32_16x16x32_bf16 v[28:31],v[188:191],v[132:135],v[28:31]\n" \
"v_mfma_f32_16x16x32_bf16 v[32:35],v[176:179],v[136:139],v[32:35]\nbuffer_load_dwordx4 v[128:131],v248,%[sws],s71 offen offset:0\n" \
"v_mfma_f32_16x16x32_bf16 v[36:39],v[180:183],v[136:139],v[36:39]\nv_mfma_f32_16x16x32_bf16 v[40:43],v[184:187],v[136:139],v[40:43]\n" \
"v_mfma_f32_16x16x32_bf16 v[44:47],v[188:191],v[136:139],v[44:47]\nbuffer_load_dwordx4 v[132:135],v248,%[sws],s71 offen offset:1024\n" \
"v_mfma_f32_16x16x32_bf16 v[48:51],v[176:179],v[140:143],v[48:51]\nv_mfma_f32_16x16x32_bf16 v[52:55],v[180:183],v[140:143],v[52:55]\n" \
"v_mfma_f32_16x16x32_bf16 v[56:59],v[184:187],v[140:143],v[56:59]\ns_add_u32 m0,s80,0\ns_add_u32 s49,s68,24576\n" \
"buffer_load_dwordx4 v248,%[sws],s49 offen offset:0 lds\nv_mfma_f32_16x16x32_bf16 v[60:63],v[188:191],v[140:143],v[60:63]\n" \
"v_mfma_f32_16x16x32_bf16 v[64:67],v[176:179],v[144:147],v[64:67]\nv_mfma_f32_16x16x32_bf16 v[68:71],v[180:183],v[144:147],v[68:71]\n" \
"buffer_load_dwordx4 v248,%[sws],s49 offen offset:1024 lds\nv_mfma_f32_16x16x32_bf16 v[72:75],v[184:187],v[144:147],v[72:75]\n" \
"v_mfma_f32_16x16x32_bf16 v[76:79],v[188:191],v[144:147],v[76:79]\nv_mfma_f32_16x16x32_bf16 v[80:83],v[176:179],v[148:151],v[80:83]\n" \
"buffer_load_dwordx4 v248,%[sws],s49 offen offset:2048 lds\nv_mfma_f32_16x16x32_bf16 v[84:87],v[180:183],v[148:151],v[84:87]\n" \
"v_mfma_f32_16x16x32_bf16 v[88:91],v[184:187],v[148:151],v[88:91]\nv_mfma_f32_16x16x32_bf16 v[92:95],v[188:191],v[148:151],v[92:95]\n" \
"buffer_load_dwordx4 v248,%[sws],s49 offen offset:3072 lds\nv_mfma_f32_16x16x32_bf16 v[96:99],v[176:179],v[152:155],v[96:99]\n" \
"v_mfma_f32_16x16x32_bf16 v[100:103],v[180:183],v[152:155],v[100:103]\nv_mfma_f32_16x16x32_bf16 v[104:107],v[184:187],v[152:155],v[104:107]\n" \
"s_add_u32 m0,s80,4096\ns_add_u32 s49,s68,28672\nbuffer_load_dwordx4 v248,%[sws],s49 offen offset:0 lds\n" \
"v_mfma_f32_16x16x32_bf16 v[108:111],v[188:191],v[152:155],v[108:111]\nv_mfma_f32_16x16x32_bf16 v[112:115],v[176:179],v[156:159],v[112:115]\n" \
"v_mfma_f32_16x16x32_bf16 v[116:119],v[180:183],v[156:159],v[116:119]\nbuffer_load_dwordx4 v248,%[sws],s49 offen offset:1024 lds\n" \
"v_mfma_f32_16x16x32_bf16 v[120:123],v[184:187],v[156:159],v[120:123]\nv_mfma_f32_16x16x32_bf16 v[124:127],v[188:191],v[156:159],v[124:127]\ns_nop 7\n" \
"v_cvt_pk_bf16_f32 v136,v0,v1\nv_cvt_pk_bf16_f32 v137,v2,v3\nv_cvt_pk_bf16_f32 v138,v4,v5\nv_cvt_pk_bf16_f32 v139,v6,v7\nv_cvt_pk_bf16_f32 v140,v16,v17\n" \
"v_cvt_pk_bf16_f32 v141,v18,v19\nv_cvt_pk_bf16_f32 v142,v20,v21\nv_cvt_pk_bf16_f32 v143,v22,v23\nv_cvt_pk_bf16_f32 v144,v8,v9\n" \
"v_cvt_pk_bf16_f32 v145,v10,v11\nv_cvt_pk_bf16_f32 v146,v12,v13\nv_cvt_pk_bf16_f32 v147,v14,v15\nv_cvt_pk_bf16_f32 v148,v24,v25\n" \
"v_cvt_pk_bf16_f32 v149,v26,v27\nv_cvt_pk_bf16_f32 v150,v28,v29\nv_cvt_pk_bf16_f32 v151,v30,v31\nv_cvt_pk_bf16_f32 v152,v32,v33\n" \
"v_cvt_pk_bf16_f32 v153,v34,v35\nv_cvt_pk_bf16_f32 v154,v36,v37\nv_cvt_pk_bf16_f32 v155,v38,v39\nv_cvt_pk_bf16_f32 v156,v48,v49\n" \
"v_cvt_pk_bf16_f32 v157,v50,v51\nv_cvt_pk_bf16_f32 v158,v52,v53\nv_cvt_pk_bf16_f32 v159,v54,v55\nv_cvt_pk_bf16_f32 v160,v40,v41\n" \
"v_cvt_pk_bf16_f32 v161,v42,v43\nv_cvt_pk_bf16_f32 v162,v44,v45\nv_cvt_pk_bf16_f32 v163,v46,v47\nv_cvt_pk_bf16_f32 v164,v56,v57\n" \
"v_cvt_pk_bf16_f32 v165,v58,v59\nv_cvt_pk_bf16_f32 v166,v60,v61\nv_cvt_pk_bf16_f32 v167,v62,v63\nv_cvt_pk_bf16_f32 v168,v64,v65\n" \
"v_cvt_pk_bf16_f32 v169,v66,v67\nv_cvt_pk_bf16_f32 v170,v68,v69\nv_cvt_pk_bf16_f32 v171,v70,v71\nv_cvt_pk_bf16_f32 v172,v80,v81\n" \
"v_cvt_pk_bf16_f32 v173,v82,v83\nv_cvt_pk_bf16_f32 v174,v84,v85\nv_cvt_pk_bf16_f32 v175,v86,v87\nv_cvt_pk_bf16_f32 v176,v72,v73\n" \
"v_cvt_pk_bf16_f32 v177,v74,v75\nv_cvt_pk_bf16_f32 v178,v76,v77\nv_cvt_pk_bf16_f32 v179,v78,v79\nv_cvt_pk_bf16_f32 v180,v88,v89\n" \
"v_cvt_pk_bf16_f32 v181,v90,v91\nv_cvt_pk_bf16_f32 v182,v92,v93\nv_cvt_pk_bf16_f32 v183,v94,v95\nv_cvt_pk_bf16_f32 v184,v96,v97\n" \
"v_cvt_pk_bf16_f32 v185,v98,v99\nv_cvt_pk_bf16_f32 v186,v100,v101\nv_cvt_pk_bf16_f32 v187,v102,v103\nv_cvt_pk_bf16_f32 v188,v112,v113\n" \
"v_cvt_pk_bf16_f32 v189,v114,v115\nv_cvt_pk_bf16_f32 v190,v116,v117\nv_cvt_pk_bf16_f32 v191,v118,v119\nv_cvt_pk_bf16_f32 v224,v104,v105\n" \
"v_cvt_pk_bf16_f32 v225,v106,v107\nv_cvt_pk_bf16_f32 v226,v108,v109\nv_cvt_pk_bf16_f32 v227,v110,v111\nv_cvt_pk_bf16_f32 v228,v120,v121\n" \
"v_cvt_pk_bf16_f32 v229,v122,v123\nv_cvt_pk_bf16_f32 v230,v124,v125\nv_cvt_pk_bf16_f32 v231,v126,v127\ns_nop 1\nv_mov_b32 v0,0\nv_mov_b32 v1,0\n" \
"v_mov_b32 v2,0\nv_mov_b32 v3,0\nv_mov_b32 v4,0\nv_mov_b32 v5,0\nv_mov_b32 v6,0\nv_mov_b32 v7,0\nv_mov_b32 v8,0\nv_mov_b32 v9,0\nv_mov_b32 v10,0\n" \
"v_mov_b32 v11,0\nv_mov_b32 v12,0\nv_mov_b32 v13,0\nv_mov_b32 v14,0\nv_mov_b32 v15,0\nv_mov_b32 v16,0\nv_mov_b32 v17,0\nv_mov_b32 v18,0\n" \
"v_mov_b32 v19,0\nv_mov_b32 v20,0\nv_mov_b32 v21,0\nv_mov_b32 v22,0\nv_mov_b32 v23,0\nv_mov_b32 v24,0\nv_mov_b32 v25,0\nv_mov_b32 v26,0\n" \
"v_mov_b32 v27,0\nv_mov_b32 v28,0\nv_mov_b32 v29,0\nv_mov_b32 v30,0\nv_mov_b32 v31,0\nv_add_u32 v252,65536,v253\nv_add_u32 v255,65536,v253\n" \
"ds_read_b128 v[116:119],v255\ns_waitcnt vmcnt(6)\ns_waitcnt lgkmcnt(0)\nv_mfma_f32_32x32x16_bf16 v[32:47],v[116:119],v[128:131],0\n" \
"v_mfma_f32_32x32x16_bf16 v[48:63],v[116:119],v[132:135],0\ns_nop 7\ns_nop 7\nv_max3_f32 v120,v32,v33,v34\nv_max3_f32 v121,v35,v36,v37\n" \
"v_max3_f32 v120,v120,v38,v39\nv_max3_f32 v121,v121,v40,v41\nv_max3_f32 v120,v120,v42,v43\nv_max3_f32 v121,v121,v44,v45\nv_max3_f32 v120,v120,v46,v47\n" \
"v_max_f32 v120,v120,v121\nv_mov_b32 v121,v120\ns_nop 1\nv_permlane32_swap_b32 v120,v121\ns_nop 1\nv_max_f32 v120,v120,v121\nv_sub_f32 v64,0,v120\n" \
"v_sub_f32 v65,0,v120\nv_sub_f32 v66,0,v120\nv_sub_f32 v67,0,v120\nv_sub_f32 v68,0,v120\nv_sub_f32 v69,0,v120\nv_sub_f32 v70,0,v120\n" \
"v_sub_f32 v71,0,v120\nv_sub_f32 v72,0,v120\nv_sub_f32 v73,0,v120\nv_sub_f32 v74,0,v120\nv_sub_f32 v75,0,v120\nv_sub_f32 v76,0,v120\n" \
"v_sub_f32 v77,0,v120\nv_sub_f32 v78,0,v120\nv_sub_f32 v79,0,v120\nv_max3_f32 v122,v48,v49,v50\nv_max3_f32 v123,v51,v52,v53\n" \
"v_max3_f32 v122,v122,v54,v55\nv_max3_f32 v123,v123,v56,v57\nv_max3_f32 v122,v122,v58,v59\nv_max3_f32 v123,v123,v60,v61\nv_max3_f32 v122,v122,v62,v63\n" \
"v_max_f32 v122,v122,v123\nv_mov_b32 v123,v122\ns_nop 1\nv_permlane32_swap_b32 v122,v123\ns_nop 1\nv_max_f32 v122,v122,v123\nv_sub_f32 v80,0,v122\n" \
"v_sub_f32 v81,0,v122\nv_sub_f32 v82,0,v122\nv_sub_f32 v83,0,v122\nv_sub_f32 v84,0,v122\nv_sub_f32 v85,0,v122\nv_sub_f32 v86,0,v122\n" \
"v_sub_f32 v87,0,v122\nv_sub_f32 v88,0,v122\nv_sub_f32 v89,0,v122\nv_sub_f32 v90,0,v122\nv_sub_f32 v91,0,v122\nv_sub_f32 v92,0,v122\n" \
"v_sub_f32 v93,0,v122\nv_sub_f32 v94,0,v122\nv_sub_f32 v95,0,v122\nv_mov_b32 v100,0\nv_mov_b32 v101,0\nv_mov_b32 v102,0\nv_mov_b32 v103,0\n" \
"v_mov_b32 v60,0\nv_mov_b32 v61,0\nv_mov_b32 v62,0\nv_mov_b32 v63,0\nv_mov_b32 v124,0\nv_mov_b32 v125,0\nv_mov_b32 v126,0\nv_mov_b32 v127,0\n" \
"ds_read_b128 v[104:107],v252 offset:0\nds_read_b128 v[108:111],v252 offset:1024\nds_read_b128 v[112:115],v252 offset:2048\ns_waitcnt lgkmcnt(0)\n" \
"v_mfma_f32_32x32x16_bf16 v[32:47],v[104:107],v[128:131],v[64:79]\ns_nop 7\ns_nop 7\nv_exp_f32 v32,v32\nv_cvt_pk_bf16_f32 v102,v60,v61\n" \
"v_exp_f32 v33,v33\nv_exp_f32 v34,v34\nv_cvt_pk_bf16_f32 v103,v62,v63\nv_mfma_f32_32x32x16_bf16 v[48:63],v[104:107],v[132:135],v[80:95]\n" \
"v_exp_f32 v35,v35\nv_cvt_pk_bf16_f32 v96,v32,v33\nv_exp_f32 v36,v36\nds_read_b128 v[116:119],v252 offset:3072\nv_exp_f32 v37,v37\n" \
"v_cvt_pk_bf16_f32 v97,v34,v35\nv_exp_f32 v38,v38\nv_mfma_f32_32x32x16_bf16 v[16:31],v[124:127],v[100:103],v[16:31]\nv_exp_f32 v39,v39\n" \
"v_cvt_pk_bf16_f32 v98,v36,v37\nv_exp_f32 v40,v40\nds_read_b128 v[120:123],v252 offset:4096\nv_exp_f32 v41,v41\nv_cvt_pk_bf16_f32 v99,v38,v39\n" \
"v_exp_f32 v42,v42\nds_read_b128 v[124:127],v252 offset:5120\nv_exp_f32 v43,v43\nv_cvt_pk_bf16_f32 v100,v40,v41\nv_exp_f32 v44,v44\n" \
"v_mfma_f32_32x32x16_bf16 v[0:15],v[108:111],v[96:99],v[0:15]\nv_exp_f32 v45,v45\nv_cvt_pk_bf16_f32 v101,v42,v43\nv_exp_f32 v46,v46\nv_exp_f32 v47,v47\n" \
"v_exp_f32 v48,v48\nv_cvt_pk_bf16_f32 v102,v44,v45\nv_exp_f32 v49,v49\nv_exp_f32 v50,v50\nv_cvt_pk_bf16_f32 v103,v46,v47\ns_waitcnt lgkmcnt(2)\n" \
"v_mfma_f32_32x32x16_bf16 v[32:47],v[116:119],v[128:131],v[64:79]\nv_exp_f32 v51,v51\nv_cvt_pk_bf16_f32 v96,v48,v49\nv_exp_f32 v52,v52\n" \
"v_exp_f32 v53,v53\nv_cvt_pk_bf16_f32 v97,v50,v51\nv_exp_f32 v54,v54\nv_mfma_f32_32x32x16_bf16 v[0:15],v[112:115],v[100:103],v[0:15]\n" \
"s_waitcnt vmcnt(0)\ns_add_u32 s51,s75,0\nbuffer_store_dwordx4 v[136:139],v251,%[sws],s51 offen offset:0 sc1\nv_exp_f32 v55,v55\n" \
"v_cvt_pk_bf16_f32 v98,v52,v53\nv_exp_f32 v56,v56\nv_exp_f32 v57,v57\nv_cvt_pk_bf16_f32 v99,v54,v55\nv_exp_f32 v58,v58\nv_exp_f32 v59,v59\n" \
"v_cvt_pk_bf16_f32 v100,v56,v57\nv_exp_f32 v60,v60\nv_mfma_f32_32x32x16_bf16 v[16:31],v[108:111],v[96:99],v[16:31]\nv_exp_f32 v61,v61\n" \
"v_cvt_pk_bf16_f32 v101,v58,v59\nv_exp_f32 v62,v62\nv_exp_f32 v63,v63\ns_waitcnt lgkmcnt(0)\nv_exp_f32 v32,v32\nv_cvt_pk_bf16_f32 v102,v60,v61\n" \
"v_exp_f32 v33,v33\nv_exp_f32 v34,v34\nv_cvt_pk_bf16_f32 v103,v62,v63\nv_mfma_f32_32x32x16_bf16 v[48:63],v[116:119],v[132:135],v[80:95]\n" \
"buffer_store_dwordx4 v[140:143],v251,%[sws],s51 offen offset:128 sc1\nv_exp_f32 v35,v35\nv_cvt_pk_bf16_f32 v96,v32,v33\nv_exp_f32 v36,v36\n" \
"ds_read_b128 v[104:107],v252 offset:6144\nv_exp_f32 v37,v37\nv_cvt_pk_bf16_f32 v97,v34,v35\nv_exp_f32 v38,v38\n" \
"v_mfma_f32_32x32x16_bf16 v[16:31],v[112:115],v[100:103],v[16:31]\nv_exp_f32 v39,v39\nv_cvt_pk_bf16_f32 v98,v36,v37\nv_exp_f32 v40,v40\n" \
"ds_read_b128 v[108:111],v252 offset:7168\nv_exp_f32 v41,v41\nv_cvt_pk_bf16_f32 v99,v38,v39\nv_exp_f32 v42,v42\n" \
"ds_read_b128 v[112:115],v252 offset:8192\nv_exp_f32 v43,v43\nv_cvt_pk_bf16_f32 v100,v40,v41\nv_exp_f32 v44,v44\n" \
"v_mfma_f32_32x32x16_bf16 v[0:15],v[120:123],v[96:99],v[0:15]\nv_exp_f32 v45,v45\nv_cvt_pk_bf16_f32 v101,v42,v43\nv_exp_f32 v46,v46\nv_exp_f32 v47,v47\n" \
"v_exp_f32 v48,v48\nv_cvt_pk_bf16_f32 v102,v44,v45\nv_exp_f32 v49,v49\nv_exp_f32 v50,v50\nv_cvt_pk_bf16_f32 v103,v46,v47\ns_waitcnt lgkmcnt(2)\n" \
"v_mfma_f32_32x32x16_bf16 v[32:47],v[104:107],v[128:131],v[64:79]\nbuffer_store_dwordx4 v[144:147],v251,%[sws],s51 offen offset:2048 sc1\n" \
"v_exp_f32 v51,v51\nv_cvt_pk_bf16_f32 v96,v48,v49\nv_exp_f32 v52,v52\nv_exp_f32 v53,v53\nv_cvt_pk_bf16_f32 v97,v50,v51\nv_exp_f32 v54,v54\n" \
"v_mfma_f32_32x32x16_bf16 v[0:15],v[124:127],v[100:103],v[0:15]\nv_exp_f32 v55,v55\nv_cvt_pk_bf16_f32 v98,v52,v53\nv_exp_f32 v56,v56\nv_exp_f32 v57,v57\n" \
"v_cvt_pk_bf16_f32 v99,v54,v55\nv_exp_f32 v58,v58\nv_exp_f32 v59,v59\nv_cvt_pk_bf16_f32 v100,v56,v57\nv_exp_f32 v60,v60\n" \
"v_mfma_f32_32x32x16_bf16 v[16:31],v[120:123],v[96:99],v[16:31]\nbuffer_store_dwordx4 v[148:151],v251,%[sws],s51 offen offset:2176 sc1\n" \
"v_exp_f32 v61,v61\nv_cvt_pk_bf16_f32 v101,v58,v59\nv_exp_f32 v62,v62\nv_exp_f32 v63,v63\ns_waitcnt lgkmcnt(0)\nv_exp_f32 v32,v32\n" \
"v_cvt_pk_bf16_f32 v102,v60,v61\nv_exp_f32 v33,v33\nv_exp_f32 v34,v34\nv_cvt_pk_bf16_f32 v103,v62,v63\n" \
"v_mfma_f32_32x32x16_bf16 v[48:63],v[104:107],v[132:135],v[80:95]\nv_exp_f32 v35,v35\nv_cvt_pk_bf16_f32 v96,v32,v33\nv_exp_f32 v36,v36\n" \
"ds_read_b128 v[116:119],v252 offset:9216\nv_exp_f32 v37,v37\nv_cvt_pk_bf16_f32 v97,v34,v35\nv_exp_f32 v38,v38\n" \
"v_mfma_f32_32x32x16_bf16 v[16:31],v[124:127],v[100:103],v[16:31]\nv_exp_f32 v39,v39\nv_cvt_pk_bf16_f32 v98,v36,v37\nv_exp_f32 v40,v40\n" \
"ds_read_b128 v[120:123],v252 offset:10240\nv_exp_f32 v41,v41\nv_cvt_pk_bf16_f32 v99,v38,v39\nv_exp_f32 v42,v42\n" \
"ds_read_b128 v[124:127],v252 offset:11264\nv_exp_f32 v43,v43\nv_cvt_pk_bf16_f32 v100,v40,v41\nv_exp_f32 v44,v44\n" \
"v_mfma_f32_32x32x16_bf16 v[0:15],v[108:111],v[96:99],v[0:15]\ns_add_u32 s51,s75,16384\n" \
"buffer_store_dwordx4 v[152:155],v251,%[sws],s51 offen offset:0 sc1\nv_exp_f32 v45,v45\nv_cvt_pk_bf16_f32 v101,v42,v43\nv_exp_f32 v46,v46\n" \
"v_exp_f32 v47,v47\nv_exp_f32 v48,v48\nv_cvt_pk_bf16_f32 v102,v44,v45\nv_exp_f32 v49,v49\nv_exp_f32 v50,v50\nv_cvt_pk_bf16_f32 v103,v46,v47\n" \
"s_waitcnt lgkmcnt(2)\nv_mfma_f32_32x32x16_bf16 v[32:47],v[116:119],v[128:131],v[64:79]\nv_exp_f32 v51,v51\nv_cvt_pk_bf16_f32 v96,v48,v49\n" \
"v_exp_f32 v52,v52\nv_exp_f32 v53,v53\nv_cvt_pk_bf16_f32 v97,v50,v51\nv_exp_f32 v54,v54\nv_mfma_f32_32x32x16_bf16 v[0:15],v[112:115],v[100:103],v[0:15]\n" \
"v_exp_f32 v55,v55\nv_cvt_pk_bf16_f32 v98,v52,v53\nv_exp_f32 v56,v56\nv_exp_f32 v57,v57\nv_cvt_pk_bf16_f32 v99,v54,v55\nv_exp_f32 v58,v58\n" \
"buffer_store_dwordx4 v[156:159],v251,%[sws],s51 offen offset:128 sc1\nv_exp_f32 v59,v59\nv_cvt_pk_bf16_f32 v100,v56,v57\nv_exp_f32 v60,v60\n" \
"v_mfma_f32_32x32x16_bf16 v[16:31],v[108:111],v[96:99],v[16:31]\nv_exp_f32 v61,v61\nv_cvt_pk_bf16_f32 v101,v58,v59\nv_exp_f32 v62,v62\n" \
"v_exp_f32 v63,v63\ns_waitcnt lgkmcnt(0)\nv_exp_f32 v32,v32\nv_cvt_pk_bf16_f32 v102,v60,v61\nv_exp_f32 v33,v33\nv_exp_f32 v34,v34\n" \
"v_cvt_pk_bf16_f32 v103,v62,v63\nv_mfma_f32_32x32x16_bf16 v[48:63],v[116:119],v[132:135],v[80:95]\nv_exp_f32 v35,v35\nv_cvt_pk_bf16_f32 v96,v32,v33\n" \
"v_exp_f32 v36,v36\nds_read_b128 v[104:107],v252 offset:12288\nv_exp_f32 v37,v37\nv_cvt_pk_bf16_f32 v97,v34,v35\nv_exp_f32 v38,v38\n" \
"v_mfma_f32_32x32x16_bf16 v[16:31],v[112:115],v[100:103],v[16:31]\nv_exp_f32 v39,v39\nv_cvt_pk_bf16_f32 v98,v36,v37\nv_exp_f32 v40,v40\n" \
"ds_read_b128 v[108:111],v252 offset:13312\nv_exp_f32 v41,v41\nv_cvt_pk_bf16_f32 v99,v38,v39\nv_exp_f32 v42,v42\n" \
"ds_read_b128 v[112:115],v252 offset:14336\nbuffer_store_dwordx4 v[160:163],v251,%[sws],s51 offen offset:2048 sc1\nv_exp_f32 v43,v43\n" \
"v_cvt_pk_bf16_f32 v100,v40,v41\nv_exp_f32 v44,v44\nv_mfma_f32_32x32x16_bf16 v[0:15],v[120:123],v[96:99],v[0:15]\nv_exp_f32 v45,v45\n" \
"v_cvt_pk_bf16_f32 v101,v42,v43\nv_exp_f32 v46,v46\nv_exp_f32 v47,v47\nv_exp_f32 v48,v48\nv_cvt_pk_bf16_f32 v102,v44,v45\nv_exp_f32 v49,v49\n" \
"v_exp_f32 v50,v50\nv_cvt_pk_bf16_f32 v103,v46,v47\ns_waitcnt lgkmcnt(2)\nv_mfma_f32_32x32x16_bf16 v[32:47],v[104:107],v[128:131],v[64:79]\n" \
"v_exp_f32 v51,v51\nv_cvt_pk_bf16_f32 v96,v48,v49\nv_exp_f32 v52,v52\nv_exp_f32 v53,v53\nv_cvt_pk_bf16_f32 v97,v50,v51\nv_exp_f32 v54,v54\n" \
"v_mfma_f32_32x32x16_bf16 v[0:15],v[124:127],v[100:103],v[0:15]\nbuffer_store_dwordx4 v[164:167],v251,%[sws],s51 offen offset:2176 sc1\n" \
"v_exp_f32 v55,v55\nv_cvt_pk_bf16_f32 v98,v52,v53\nv_exp_f32 v56,v56\nv_exp_f32 v57,v57\nv_cvt_pk_bf16_f32 v99,v54,v55\nv_exp_f32 v58,v58\n" \
"v_exp_f32 v59,v59\nv_cvt_pk_bf16_f32 v100,v56,v57\nv_exp_f32 v60,v60\nv_mfma_f32_32x32x16_bf16 v[16:31],v[120:123],v[96:99],v[16:31]\n" \
"v_exp_f32 v61,v61\nv_cvt_pk_bf16_f32 v101,v58,v59\nv_exp_f32 v62,v62\nv_exp_f32 v63,v63\ns_waitcnt lgkmcnt(0)\nv_exp_f32 v32,v32\n" \
"v_cvt_pk_bf16_f32 v102,v60,v61\nv_exp_f32 v33,v33\nv_exp_f32 v34,v34\nv_cvt_pk_bf16_f32 v103,v62,v63\n" \
"v_mfma_f32_32x32x16_bf16 v[48:63],v[104:107],v[132:135],v[80:95]\nv_exp_f32 v35,v35\nv_cvt_pk_bf16_f32 v96,v32,v33\nv_exp_f32 v36,v36\n" \
"ds_read_b128 v[116:119],v252 offset:15360\nv_exp_f32 v37,v37\nv_cvt_pk_bf16_f32 v97,v34,v35\nv_exp_f32 v38,v38\n" \
"v_mfma_f32_32x32x16_bf16 v[16:31],v[124:127],v[100:103],v[16:31]\ns_add_u32 s51,s75,32768\n" \
"buffer_store_dwordx4 v[168:171],v251,%[sws],s51 offen offset:0 sc1\nv_exp_f32 v39,v39\nv_cvt_pk_bf16_f32 v98,v36,v37\nv_exp_f32 v40,v40\n" \
"ds_read_b128 v[120:123],v252 offset:16384\nv_exp_f32 v41,v41\nv_cvt_pk_bf16_f32 v99,v38,v39\nv_exp_f32 v42,v42\n" \
"ds_read_b128 v[124:127],v252 offset:17408\nv_exp_f32 v43,v43\nv_cvt_pk_bf16_f32 v100,v40,v41\nv_exp_f32 v44,v44\n" \
"v_mfma_f32_32x32x16_bf16 v[0:15],v[108:111],v[96:99],v[0:15]\nv_exp_f32 v45,v45\nv_cvt_pk_bf16_f32 v101,v42,v43\nv_exp_f32 v46,v46\nv_exp_f32 v47,v47\n" \
"v_exp_f32 v48,v48\nv_cvt_pk_bf16_f32 v102,v44,v45\nv_exp_f32 v49,v49\nv_exp_f32 v50,v50\nv_cvt_pk_bf16_f32 v103,v46,v47\ns_waitcnt lgkmcnt(2)\n" \
"v_mfma_f32_32x32x16_bf16 v[32:47],v[116:119],v[128:131],v[64:79]\nbuffer_store_dwordx4 v[172:175],v251,%[sws],s51 offen offset:128 sc1\n" \
"v_exp_f32 v51,v51\nv_cvt_pk_bf16_f32 v96,v48,v49\nv_exp_f32 v52,v52\nv_exp_f32 v53,v53\nv_cvt_pk_bf16_f32 v97,v50,v51\nv_exp_f32 v54,v54\n" \
"v_mfma_f32_32x32x16_bf16 v[0:15],v[112:115],v[100:103],v[0:15]\nv_exp_f32 v55,v55\nv_cvt_pk_bf16_f32 v98,v52,v53\nv_exp_f32 v56,v56\nv_exp_f32 v57,v57\n" \
"v_cvt_pk_bf16_f32 v99,v54,v55\nv_exp_f32 v58,v58\nv_exp_f32 v59,v59\nv_cvt_pk_bf16_f32 v100,v56,v57\nv_exp_f32 v60,v60\n" \
"v_mfma_f32_32x32x16_bf16 v[16:31],v[108:111],v[96:99],v[16:31]\nv_exp_f32 v61,v61\nv_cvt_pk_bf16_f32 v101,v58,v59\nv_exp_f32 v62,v62\n" \
"v_exp_f32 v63,v63\ns_waitcnt lgkmcnt(0)\nv_exp_f32 v32,v32\nv_cvt_pk_bf16_f32 v102,v60,v61\nv_exp_f32 v33,v33\nv_exp_f32 v34,v34\n" \
"v_cvt_pk_bf16_f32 v103,v62,v63\nv_mfma_f32_32x32x16_bf16 v[48:63],v[116:119],v[132:135],v[80:95]\n" \
"buffer_store_dwordx4 v[176:179],v251,%[sws],s51 offen offset:2048 sc1\nv_exp_f32 v35,v35\nv_cvt_pk_bf16_f32 v96,v32,v33\nv_exp_f32 v36,v36\n" \
"ds_read_b128 v[104:107],v252 offset:18432\nv_exp_f32 v37,v37\nv_cvt_pk_bf16_f32 v97,v34,v35\nv_exp_f32 v38,v38\n" \
"v_mfma_f32_32x32x16_bf16 v[16:31],v[112:115],v[100:103],v[16:31]\nv_exp_f32 v39,v39\nv_cvt_pk_bf16_f32 v98,v36,v37\nv_exp_f32 v40,v40\n" \
"ds_read_b128 v[108:111],v252 offset:19456\nv_exp_f32 v41,v41\nv_cvt_pk_bf16_f32 v99,v38,v39\nv_exp_f32 v42,v42\n" \
"ds_read_b128 v[112:115],v252 offset:20480\nv_exp_f32 v43,v43\nv_cvt_pk_bf16_f32 v100,v40,v41\nv_exp_f32 v44,v44\n" \
"v_mfma_f32_32x32x16_bf16 v[0:15],v[120:123],v[96:99],v[0:15]\nbuffer_store_dwordx4 v[180:183],v251,%[sws],s51 offen offset:2176 sc1\nv_exp_f32 v45,v45\n" \
"v_cvt_pk_bf16_f32 v101,v42,v43\nv_exp_f32 v46,v46\nv_exp_f32 v47,v47\nv_exp_f32 v48,v48\nv_cvt_pk_bf16_f32 v102,v44,v45\nv_exp_f32 v49,v49\n" \
"v_exp_f32 v50,v50\nv_cvt_pk_bf16_f32 v103,v46,v47\ns_waitcnt lgkmcnt(2)\nv_mfma_f32_32x32x16_bf16 v[32:47],v[104:107],v[128:131],v[64:79]\n" \
"v_exp_f32 v51,v51\nv_cvt_pk_bf16_f32 v96,v48,v49\nv_exp_f32 v52,v52\nv_exp_f32 v53,v53\nv_cvt_pk_bf16_f32 v97,v50,v51\nv_exp_f32 v54,v54\n" \
"v_mfma_f32_32x32x16_bf16 v[0:15],v[124:127],v[100:103],v[0:15]\nv_exp_f32 v55,v55\nv_cvt_pk_bf16_f32 v98,v52,v53\nv_exp_f32 v56,v56\nv_exp_f32 v57,v57\n" \
"v_cvt_pk_bf16_f32 v99,v54,v55\nv_exp_f32 v58,v58\nv_exp_f32 v59,v59\nv_cvt_pk_bf16_f32 v100,v56,v57\nv_exp_f32 v60,v60\n" \
"v_mfma_f32_32x32x16_bf16 v[16:31],v[120:123],v[96:99],v[16:31]\ns_add_u32 s51,s75,49152\n" \
"buffer_store_dwordx4 v[184:187],v251,%[sws],s51 offen offset:0 sc1\nv_exp_f32 v61,v61\nv_cvt_pk_bf16_f32 v101,v58,v59\nv_exp_f32 v62,v62\n" \
"v_exp_f32 v63,v63\ns_waitcnt lgkmcnt(0)\nv_exp_f32 v32,v32\nv_cvt_pk_bf16_f32 v102,v60,v61\nv_exp_f32 v33,v33\nv_exp_f32 v34,v34\n" \
"v_cvt_pk_bf16_f32 v103,v62,v63\nv_mfma_f32_32x32x16_bf16 v[48:63],v[104:107],v[132:135],v[80:95]\nv_exp_f32 v35,v35\nv_cvt_pk_bf16_f32 v96,v32,v33\n" \
"v_exp_f32 v36,v36\nds_read_b128 v[116:119],v252 offset:21504\nv_exp_f32 v37,v37\nv_cvt_pk_bf16_f32 v97,v34,v35\nv_exp_f32 v38,v38\n" \
"v_mfma_f32_32x32x16_bf16 v[16:31],v[124:127],v[100:103],v[16:31]\nv_exp_f32 v39,v39\nv_cvt_pk_bf16_f32 v98,v36,v37\nv_exp_f32 v40,v40\n" \
"ds_read_b128 v[120:123],v252 offset:22528\nv_exp_f32 v41,v41\nv_cvt_pk_bf16_f32 v99,v38,v39\nv_exp_f32 v42,v42\n" \
"ds_read_b128 v[124:127],v252 offset:23552\nbuffer_store_dwordx4 v[188:191],v251,%[sws],s51 offen offset:128 sc1\nv_exp_f32 v43,v43\n" \
"v_cvt_pk_bf16_f32 v100,v40,v41\nv_exp_f32 v44,v44\nv_mfma_f32_32x32x16_bf16 v[0:15],v[108:111],v[96:99],v[0:15]\nv_exp_f32 v45,v45\n" \
"v_cvt_pk_bf16_f32 v101,v42,v43\nv_exp_f32 v46,v46\nv_exp_f32 v47,v47\nv_exp_f32 v48,v48\nv_cvt_pk_bf16_f32 v102,v44,v45\nv_exp_f32 v49,v49\n" \
"v_exp_f32 v50,v50\nv_cvt_pk_bf16_f32 v103,v46,v47\ns_waitcnt lgkmcnt(2)\nv_mfma_f32_32x32x16_bf16 v[32:47],v[116:119],v[128:131],v[64:79]\n" \
"v_exp_f32 v51,v51\nv_cvt_pk_bf16_f32 v96,v48,v49\nv_exp_f32 v52,v52\nv_exp_f32 v53,v53\nv_cvt_pk_bf16_f32 v97,v50,v51\nv_exp_f32 v54,v54\n" \
"v_mfma_f32_32x32x16_bf16 v[0:15],v[112:115],v[100:103],v[0:15]\nv_exp_f32 v55,v55\nv_cvt_pk_bf16_f32 v98,v52,v53\nv_exp_f32 v56,v56\nv_exp_f32 v57,v57\n" \
"v_cvt_pk_bf16_f32 v99,v54,v55\nv_exp_f32 v58,v58\nbuffer_store_dwordx4 v[224:227],v251,%[sws],s51 offen offset:2048 sc1\nv_exp_f32 v59,v59\n" \
"v_cvt_pk_bf16_f32 v100,v56,v57\nv_exp_f32 v60,v60\nv_mfma_f32_32x32x16_bf16 v[16:31],v[108:111],v[96:99],v[16:31]\ns_barrier\nv_add_u32 v252,0,v253\n" \
"v_exp_f32 v61,v61\nv_cvt_pk_bf16_f32 v101,v58,v59\nv_exp_f32 v62,v62\nv_exp_f32 v63,v63\ns_waitcnt lgkmcnt(0)\nv_exp_f32 v32,v32\n" \
"v_cvt_pk_bf16_f32 v102,v60,v61\nv_exp_f32 v33,v33\nv_exp_f32 v34,v34\nv_cvt_pk_bf16_f32 v103,v62,v63\n" \
"v_mfma_f32_32x32x16_bf16 v[48:63],v[116:119],v[132:135],v[80:95]\nv_exp_f32 v35,v35\nv_cvt_pk_bf16_f32 v96,v32,v33\nv_exp_f32 v36,v36\n" \
"ds_read_b128 v[104:107],v252 offset:0\nv_exp_f32 v37,v37\nv_cvt_pk_bf16_f32 v97,v34,v35\nv_exp_f32 v38,v38\n" \
"v_mfma_f32_32x32x16_bf16 v[16:31],v[112:115],v[100:103],v[16:31]\nbuffer_store_dwordx4 v[228:231],v251,%[sws],s51 offen offset:2176 sc1\n" \
"v_exp_f32 v39,v39\nv_cvt_pk_bf16_f32 v98,v36,v37\nv_exp_f32 v40,v40\nds_read_b128 v[108:111],v252 offset:1024\nv_exp_f32 v41,v41\n" \
"v_cvt_pk_bf16_f32 v99,v38,v39\nv_exp_f32 v42,v42\nds_read_b128 v[112:115],v252 offset:2048\nv_exp_f32 v43,v43\nv_cvt_pk_bf16_f32 v100,v40,v41\n" \
"v_exp_f32 v44,v44\nv_mfma_f32_32x32x16_bf16 v[0:15],v[120:123],v[96:99],v[0:15]\nv_exp_f32 v45,v45\nv_cvt_pk_bf16_f32 v101,v42,v43\nv_exp_f32 v46,v46\n" \
"v_exp_f32 v47,v47\nv_exp_f32 v48,v48\nv_cvt_pk_bf16_f32 v102,v44,v45\nv_exp_f32 v49,v49\nv_exp_f32 v50,v50\nv_cvt_pk_bf16_f32 v103,v46,v47\n" \
"s_waitcnt lgkmcnt(2)\nv_mfma_f32_32x32x16_bf16 v[32:47],v[104:107],v[128:131],v[64:79]\nv_exp_f32 v51,v51\nv_cvt_pk_bf16_f32 v96,v48,v49\n" \
"v_exp_f32 v52,v52\nv_exp_f32 v53,v53\nv_cvt_pk_bf16_f32 v97,v50,v51\nv_exp_f32 v54,v54\nv_mfma_f32_32x32x16_bf16 v[0:15],v[124:127],v[100:103],v[0:15]\n" \
"v_exp_f32 v55,v55\nv_cvt_pk_bf16_f32 v98,v52,v53\nv_exp_f32 v56,v56\nv_exp_f32 v57,v57\nv_cvt_pk_bf16_f32 v99,v54,v55\nv_exp_f32 v58,v58\n" \
"v_exp_f32 v59,v59\nv_cvt_pk_bf16_f32 v100,v56,v57\nv_exp_f32 v60,v60\nv_mfma_f32_32x32x16_bf16 v[16:31],v[120:123],v[96:99],v[16:31]\n" \
"v_exp_f32 v61,v61\nv_cvt_pk_bf16_f32 v101,v58,v59\nv_exp_f32 v62,v62\nv_exp_f32 v63,v63\ns_waitcnt lgkmcnt(0)\nv_exp_f32 v32,v32\n" \
"v_cvt_pk_bf16_f32 v102,v60,v61\nv_exp_f32 v33,v33\nv_exp_f32 v34,v34\nv_cvt_pk_bf16_f32 v103,v62,v63\n" \
"v_mfma_f32_32x32x16_bf16 v[48:63],v[104:107],v[132:135],v[80:95]\nv_exp_f32 v35,v35\nv_cvt_pk_bf16_f32 v96,v32,v33\nv_exp_f32 v36,v36\n" \
"ds_read_b128 v[116:119],v252 offset:3072\nv_exp_f32 v37,v37\nv_cvt_pk_bf16_f32 v97,v34,v35\nv_exp_f32 v38,v38\n" \
"v_mfma_f32_32x32x16_bf16 v[16:31],v[124:127],v[100:103],v[16:31]\ns_barrier\nv_exp_f32 v39,v39\nv_cvt_pk_bf16_f32 v98,v36,v37\nv_exp_f32 v40,v40\n" \
"ds_read_b128 v[120:123],v252 offset:4096\nv_exp_f32 v41,v41\nv_cvt_pk_bf16_f32 v99,v38,v39\nv_exp_f32 v42,v42\n" \
"ds_read_b128 v[124:127],v252 offset:5120\ns_and_b32 s50,s66,0xffff\ns_add_u32 m0,s50,65536\ns_add_u32 s49,s67,1048576\n" \
"buffer_load_dwordx4 v249,%[sq],s49 offen lds\nv_exp_f32 v43,v43\nv_cvt_pk_bf16_f32 v100,v40,v41\nv_exp_f32 v44,v44\n" \
"v_mfma_f32_32x32x16_bf16 v[0:15],v[108:111],v[96:99],v[0:15]\nv_exp_f32 v45,v45\nv_cvt_pk_bf16_f32 v101,v42,v43\nv_exp_f32 v46,v46\nv_exp_f32 v47,v47\n" \
"v_exp_f32 v48,v48\nv_cvt_pk_bf16_f32 v102,v44,v45\nv_exp_f32 v49,v49\nv_exp_f32 v50,v50\nv_cvt_pk_bf16_f32 v103,v46,v47\ns_waitcnt lgkmcnt(2)\n" \
"v_mfma_f32_32x32x16_bf16 v[32:47],v[116:119],v[128:131],v[64:79]\ns_add_u32 m0,m0,1024\ns_add_u32 s49,s49,32768\n" \
"buffer_load_dwordx4 v249,%[sq],s49 offen lds\nv_exp_f32 v51,v51\nv_cvt_pk_bf16_f32 v96,v48,v49\nv_exp_f32 v52,v52\nv_exp_f32 v53,v53\n" \
"v_cvt_pk_bf16_f32 v97,v50,v51\nv_exp_f32 v54,v54\nv_mfma_f32_32x32x16_bf16 v[0:15],v[112:115],v[100:103],v[0:15]\nv_exp_f32 v55,v55\n" \
"v_cvt_pk_bf16_f32 v98,v52,v53\nv_exp_f32 v56,v56\nv_exp_f32 v57,v57\nv_cvt_pk_bf16_f32 v99,v54,v55\nv_exp_f32 v58,v58\ns_add_u32 m0,m0,1024\n" \
"s_add_u32 s49,s49,32768\nbuffer_load_dwordx4 v249,%[sq],s49 offen lds\nv_exp_f32 v59,v59\nv_cvt_pk_bf16_f32 v100,v56,v57\nv_exp_f32 v60,v60\n" \
"v_mfma_f32_32x32x16_bf16 v[16:31],v[108:111],v[96:99],v[16:31]\nv_exp_f32 v61,v61\nv_cvt_pk_bf16_f32 v101,v58,v59\nv_exp_f32 v62,v62\n" \
"v_exp_f32 v63,v63\ns_waitcnt lgkmcnt(0)\nv_exp_f32 v32,v32\nv_cvt_pk_bf16_f32 v102,v60,v61\nv_exp_f32 v33,v33\nv_exp_f32 v34,v34\n" \
"v_cvt_pk_bf16_f32 v103,v62,v63\nv_mfma_f32_32x32x16_bf16 v[48:63],v[116:119],v[132:135],v[80:95]\ns_add_u32 m0,m0,1024\ns_add_u32 s49,s49,32768\n" \
"buffer_load_dwordx4 v249,%[sq],s49 offen lds\nv_exp_f32 v35,v35\nv_cvt_pk_bf16_f32 v96,v32,v33\nv_exp_f32 v36,v36\n" \
"ds_read_b128 v[104:107],v252 offset:6144\nv_exp_f32 v37,v37\nv_cvt_pk_bf16_f32 v97,v34,v35\nv_exp_f32 v38,v38\n" \
"v_mfma_f32_32x32x16_bf16 v[16:31],v[112:115],v[100:103],v[16:31]\nv_exp_f32 v39,v39\nv_cvt_pk_bf16_f32 v98,v36,v37\nv_exp_f32 v40,v40\n" \
"ds_read_b128 v[108:111],v252 offset:7168\nv_exp_f32 v41,v41\nv_cvt_pk_bf16_f32 v99,v38,v39\nv_exp_f32 v42,v42\n" \
"ds_read_b128 v[112:115],v252 offset:8192\ns_and_b32 s50,s66,0xffff\ns_add_u32 m0,s50,98304\ns_add_u32 s49,s67,2097152\n" \
"buffer_load_dwordx4 v249,%[sq],s49 offen lds\nv_exp_f32 v43,v43\nv_cvt_pk_bf16_f32 v100,v40,v41\nv_exp_f32 v44,v44\n" \
"v_mfma_f32_32x32x16_bf16 v[0:15],v[120:123],v[96:99],v[0:15]\nv_exp_f32 v45,v45\nv_cvt_pk_bf16_f32 v101,v42,v43\nv_exp_f32 v46,v46\nv_exp_f32 v47,v47\n" \
"v_exp_f32 v48,v48\nv_cvt_pk_bf16_f32 v102,v44,v45\nv_exp_f32 v49,v49\nv_exp_f32 v50,v50\nv_cvt_pk_bf16_f32 v103,v46,v47\ns_waitcnt lgkmcnt(2)\n" \
"v_mfma_f32_32x32x16_bf16 v[32:47],v[104:107],v[128:131],v[64:79]\ns_add_u32 m0,m0,1024\ns_add_u32 s49,s49,32768\n" \
"buffer_load_dwordx4 v249,%[sq],s49 offen lds\nv_exp_f32 v51,v51\nv_cvt_pk_bf16_f32 v96,v48,v49\nv_exp_f32 v52,v52\nv_exp_f32 v53,v53\n" \
"v_cvt_pk_bf16_f32 v97,v50,v51\nv_exp_f32 v54,v54\nv_mfma_f32_32x32x16_bf16 v[0:15],v[124:127],v[100:103],v[0:15]\nv_exp_f32 v55,v55\n" \
"v_cvt_pk_bf16_f32 v98,v52,v53\nv_exp_f32 v56,v56\nv_exp_f32 v57,v57\nv_cvt_pk_bf16_f32 v99,v54,v55\nv_exp_f32 v58,v58\ns_add_u32 m0,m0,1024\n" \
"s_add_u32 s49,s49,32768\nbuffer_load_dwordx4 v249,%[sq],s49 offen lds\nv_exp_f32 v59,v59\nv_cvt_pk_bf16_f32 v100,v56,v57\nv_exp_f32 v60,v60\n" \
"v_mfma_f32_32x32x16_bf16 v[16:31],v[120:123],v[96:99],v[16:31]\nv_exp_f32 v61,v61\nv_cvt_pk_bf16_f32 v101,v58,v59\nv_exp_f32 v62,v62\n" \
"v_exp_f32 v63,v63\ns_waitcnt lgkmcnt(0)\nv_exp_f32 v32,v32\nv_cvt_pk_bf16_f32 v102,v60,v61\nv_exp_f32 v33,v33\nv_exp_f32 v34,v34\n" \
"v_cvt_pk_bf16_f32 v103,v62,v63\nv_mfma_f32_32x32x16_bf16 v[48:63],v[104:107],v[132:135],v[80:95]\ns_add_u32 m0,m0,1024\ns_add_u32 s49,s49,32768\n" \
"buffer_load_dwordx4 v249,%[sq],s49 offen lds\nv_exp_f32 v35,v35\nv_cvt_pk_bf16_f32 v96,v32,v33\nv_exp_f32 v36,v36\n" \
"ds_read_b128 v[116:119],v252 offset:9216\nv_exp_f32 v37,v37\nv_cvt_pk_bf16_f32 v97,v34,v35\nv_exp_f32 v38,v38\n" \
"v_mfma_f32_32x32x16_bf16 v[16:31],v[124:127],v[100:103],v[16:31]\nv_exp_f32 v39,v39\nv_cvt_pk_bf16_f32 v98,v36,v37\nv_exp_f32 v40,v40\n" \
"ds_read_b128 v[120:123],v252 offset:10240\nv_exp_f32 v41,v41\nv_cvt_pk_bf16_f32 v99,v38,v39\nv_exp_f32 v42,v42\n" \
"ds_read_b128 v[124:127],v252 offset:11264\nv_exp_f32 v43,v43\nv_cvt_pk_bf16_f32 v100,v40,v41\nv_exp_f32 v44,v44\n" \
"v_mfma_f32_32x32x16_bf16 v[0:15],v[108:111],v[96:99],v[0:15]\nv_exp_f32 v45,v45\nv_cvt_pk_bf16_f32 v101,v42,v43\nv_exp_f32 v46,v46\nv_exp_f32 v47,v47\n" \
"v_exp_f32 v48,v48\nv_cvt_pk_bf16_f32 v102,v44,v45\nv_exp_f32 v49,v49\nv_exp_f32 v50,v50\nv_cvt_pk_bf16_f32 v103,v46,v47\ns_waitcnt lgkmcnt(2)\n" \
"v_mfma_f32_32x32x16_bf16 v[32:47],v[116:119],v[128:131],v[64:79]\ns_waitcnt vmcnt(0)\ns_barrier\nv_exp_f32 v51,v51\nv_cvt_pk_bf16_f32 v96,v48,v49\n" \
"v_exp_f32 v52,v52\nv_exp_f32 v53,v53\nv_cvt_pk_bf16_f32 v97,v50,v51\nv_exp_f32 v54,v54\nv_mfma_f32_32x32x16_bf16 v[0:15],v[112:115],v[100:103],v[0:15]\n" \
"s_add_u32 s58,s76,8192\nbuffer_load_dwordx4 v[136:139],v248,%[sws],s58 offen offset:0\nv_exp_f32 v55,v55\nv_cvt_pk_bf16_f32 v98,v52,v53\n" \
"v_exp_f32 v56,v56\nv_exp_f32 v57,v57\nv_cvt_pk_bf16_f32 v99,v54,v55\nv_exp_f32 v58,v58\n" \
"buffer_load_dwordx4 v[140:143],v248,%[sws],s58 offen offset:1024\nv_exp_f32 v59,v59\nv_cvt_pk_bf16_f32 v100,v56,v57\nv_exp_f32 v60,v60\n" \
"v_mfma_f32_32x32x16_bf16 v[16:31],v[108:111],v[96:99],v[16:31]\nbuffer_load_dwordx4 v[144:147],v248,%[sws],s58 offen offset:2048\nv_exp_f32 v61,v61\n" \
"v_cvt_pk_bf16_f32 v101,v58,v59\nv_exp_f32 v62,v62\nv_exp_f32 v63,v63\ns_waitcnt lgkmcnt(0)\nv_exp_f32 v32,v32\nv_cvt_pk_bf16_f32 v102,v60,v61\n" \
"v_exp_f32 v33,v33\nv_exp_f32 v34,v34\nv_cvt_pk_bf16_f32 v103,v62,v63\nv_mfma_f32_32x32x16_bf16 v[48:63],v[116:119],v[132:135],v[80:95]\n" \
"buffer_load_dwordx4 v[148:151],v248,%[sws],s58 offen offset:3072\nv_exp_f32 v35,v35\nv_cvt_pk_bf16_f32 v96,v32,v33\nv_exp_f32 v36,v36\n" \
"ds_read_b128 v[104:107],v252 offset:12288\nv_exp_f32 v37,v37\nv_cvt_pk_bf16_f32 v97,v34,v35\nv_exp_f32 v38,v38\n" \
"v_mfma_f32_32x32x16_bf16 v[16:31],v[112:115],v[100:103],v[16:31]\ns_add_u32 s58,s76,12288\n" \
"buffer_load_dwordx4 v[152:155],v248,%[sws],s58 offen offset:0\nv_exp_f32 v39,v39\nv_cvt_pk_bf16_f32 v98,v36,v37\nv_exp_f32 v40,v40\n" \
"ds_read_b128 v[108:111],v252 offset:13312\nv_exp_f32 v41,v41\nv_cvt_pk_bf16_f32 v99,v38,v39\nv_exp_f32 v42,v42\n" \
"ds_read_b128 v[112:115],v252 offset:14336\nbuffer_load_dwordx4 v[156:159],v248,%[sws],s58 offen offset:1024\nv_exp_f32 v43,v43\n" \
"v_cvt_pk_bf16_f32 v100,v40,v41\nv_exp_f32 v44,v44\nv_mfma_f32_32x32x16_bf16 v[0:15],v[120:123],v[96:99],v[0:15]\n" \
"buffer_load_dwordx4 v[160:163],v248,%[sws],s58 offen offset:2048\nv_exp_f32 v45,v45\nv_cvt_pk_bf16_f32 v101,v42,v43\nv_exp_f32 v46,v46\n" \
"v_exp_f32 v47,v47\nv_exp_f32 v48,v48\nv_cvt_pk_bf16_f32 v102,v44,v45\nv_exp_f32 v49,v49\nv_exp_f32 v50,v50\nv_cvt_pk_bf16_f32 v103,v46,v47\n" \
"s_waitcnt lgkmcnt(2)\nv_mfma_f32_32x32x16_bf16 v[32:47],v[104:107],v[128:131],v[64:79]\n" \
"buffer_load_dwordx4 v[164:167],v248,%[sws],s58 offen offset:3072\nv_exp_f32 v51,v51\nv_cvt_pk_bf16_f32 v96,v48,v49\nv_exp_f32 v52,v52\n" \
"v_exp_f32 v53,v53\nv_cvt_pk_bf16_f32 v97,v50,v51\nv_exp_f32 v54,v54\nv_mfma_f32_32x32x16_bf16 v[0:15],v[124:127],v[100:103],v[0:15]\n" \
"s_add_u32 s58,s76,16384\nbuffer_load_dwordx4 v[168:171],v248,%[sws],s58 offen offset:0\nv_exp_f32 v55,v55\nv_cvt_pk_bf16_f32 v98,v52,v53\n" \
"v_exp_f32 v56,v56\nv_exp_f32 v57,v57\nv_cvt_pk_bf16_f32 v99,v54,v55\nv_exp_f32 v58,v58\n" \
"buffer_load_dwordx4 v[172:175],v248,%[sws],s58 offen offset:1024\nv_exp_f32 v59,v59\nv_cvt_pk_bf16_f32 v100,v56,v57\nv_exp_f32 v60,v60\n" \
"v_mfma_f32_32x32x16_bf16 v[16:31],v[120:123],v[96:99],v[16:31]\nbuffer_load_dwordx4 v[176:179],v248,%[sws],s58 offen offset:2048\nv_exp_f32 v61,v61\n" \
"v_cvt_pk_bf16_f32 v101,v58,v59\nv_exp_f32 v62,v62\nv_exp_f32 v63,v63\ns_waitcnt lgkmcnt(0)\nv_exp_f32 v32,v32\nv_cvt_pk_bf16_f32 v102,v60,v61\n" \
"v_exp_f32 v33,v33\nv_exp_f32 v34,v34\nv_cvt_pk_bf16_f32 v103,v62,v63\nv_mfma_f32_32x32x16_bf16 v[48:63],v[104:107],v[132:135],v[80:95]\n" \
"buffer_load_dwordx4 v[180:183],v248,%[sws],s58 offen offset:3072\nv_exp_f32 v35,v35\nv_cvt_pk_bf16_f32 v96,v32,v33\nv_exp_f32 v36,v36\n" \
"ds_read_b128 v[116:119],v252 offset:15360\nv_exp_f32 v37,v37\nv_cvt_pk_bf16_f32 v97,v34,v35\nv_exp_f32 v38,v38\n" \
"v_mfma_f32_32x32x16_bf16 v[16:31],v[124:127],v[100:103],v[16:31]\ns_add_u32 s58,s76,20480\n" \
"buffer_load_dwordx4 v[184:187],v248,%[sws],s58 offen offset:0\nv_exp_f32 v39,v39\nv_cvt_pk_bf16_f32 v98,v36,v37\nv_exp_f32 v40,v40\n" \
"ds_read_b128 v[120:123],v252 offset:16384\nv_exp_f32 v41,v41\nv_cvt_pk_bf16_f32 v99,v38,v39\nv_exp_f32 v42,v42\n" \
"ds_read_b128 v[124:127],v252 offset:17408\nbuffer_load_dwordx4 v[188:191],v248,%[sws],s58 offen offset:1024\nv_exp_f32 v43,v43\n" \
"v_cvt_pk_bf16_f32 v100,v40,v41\nv_exp_f32 v44,v44\nv_mfma_f32_32x32x16_bf16 v[0:15],v[108:111],v[96:99],v[0:15]\nv_exp_f32 v45,v45\n" \
"v_cvt_pk_bf16_f32 v101,v42,v43\nv_exp_f32 v46,v46\nv_exp_f32 v47,v47\nv_exp_f32 v48,v48\nv_cvt_pk_bf16_f32 v102,v44,v45\nv_exp_f32 v49,v49\n" \
"v_exp_f32 v50,v50\nv_cvt_pk_bf16_f32 v103,v46,v47\ns_waitcnt lgkmcnt(2)\nv_mfma_f32_32x32x16_bf16 v[32:47],v[116:119],v[128:131],v[64:79]\n" \
"v_exp_f32 v51,v51\nv_cvt_pk_bf16_f32 v96,v48,v49\nv_exp_f32 v52,v52\nv_exp_f32 v53,v53\nv_cvt_pk_bf16_f32 v97,v50,v51\nv_exp_f32 v54,v54\n" \
"v_mfma_f32_32x32x16_bf16 v[0:15],v[112:115],v[100:103],v[0:15]\nv_exp_f32 v55,v55\nv_cvt_pk_bf16_f32 v98,v52,v53\nv_exp_f32 v56,v56\nv_exp_f32 v57,v57\n" \
"v_cvt_pk_bf16_f32 v99,v54,v55\nv_exp_f32 v58,v58\nv_exp_f32 v59,v59\nv_cvt_pk_bf16_f32 v100,v56,v57\nv_exp_f32 v60,v60\n" \
"v_mfma_f32_32x32x16_bf16 v[16:31],v[108:111],v[96:99],v[16:31]\nv_exp_f32 v61,v61\nv_cvt_pk_bf16_f32 v101,v58,v59\nv_exp_f32 v62,v62\n" \
"v_exp_f32 v63,v63\ns_waitcnt lgkmcnt(0)\nv_exp_f32 v32,v32\nv_cvt_pk_bf16_f32 v102,v60,v61\nv_exp_f32 v33,v33\nv_exp_f32 v34,v34\n" \
"v_cvt_pk_bf16_f32 v103,v62,v63\nv_mfma_f32_32x32x16_bf16 v[48:63],v[116:119],v[132:135],v[80:95]\nv_exp_f32 v35,v35\nv_cvt_pk_bf16_f32 v96,v32,v33\n" \
"v_exp_f32 v36,v36\nds_read_b128 v[104:107],v252 offset:18432\nv_exp_f32 v37,v37\nv_cvt_pk_bf16_f32 v97,v34,v35\nv_exp_f32 v38,v38\n" \
"v_mfma_f32_32x32x16_bf16 v[16:31],v[112:115],v[100:103],v[16:31]\nv_exp_f32 v39,v39\nv_cvt_pk_bf16_f32 v98,v36,v37\nv_exp_f32 v40,v40\n" \
"ds_read_b128 v[108:111],v252 offset:19456\nv_exp_f32 v41,v41\nv_cvt_pk_bf16_f32 v99,v38,v39\nv_exp_f32 v42,v42\n" \
"ds_read_b128 v[112:115],v252 offset:20480\nv_exp_f32 v43,v43\nv_cvt_pk_bf16_f32 v100,v40,v41\nv_exp_f32 v44,v44\n" \
"v_mfma_f32_32x32x16_bf16 v[0:15],v[120:123],v[96:99],v[0:15]\nv_exp_f32 v45,v45\nv_cvt_pk_bf16_f32 v101,v42,v43\nv_exp_f32 v46,v46\nv_exp_f32 v47,v47\n" \
"v_exp_f32 v48,v48\nv_cvt_pk_bf16_f32 v102,v44,v45\nv_exp_f32 v49,v49\nv_exp_f32 v50,v50\nv_cvt_pk_bf16_f32 v103,v46,v47\ns_waitcnt lgkmcnt(2)\n" \
"v_mfma_f32_32x32x16_bf16 v[32:47],v[104:107],v[128:131],v[64:79]\nv_exp_f32 v51,v51\nv_cvt_pk_bf16_f32 v96,v48,v49\nv_exp_f32 v52,v52\n" \
"v_exp_f32 v53,v53\nv_cvt_pk_bf16_f32 v97,v50,v51\nv_exp_f32 v54,v54\nv_mfma_f32_32x32x16_bf16 v[0:15],v[124:127],v[100:103],v[0:15]\nv_exp_f32 v55,v55\n" \
"v_cvt_pk_bf16_f32 v98,v52,v53\nv_exp_f32 v56,v56\nv_exp_f32 v57,v57\nv_cvt_pk_bf16_f32 v99,v54,v55\nv_exp_f32 v58,v58\nv_exp_f32 v59,v59\n" \
"v_cvt_pk_bf16_f32 v100,v56,v57\nv_exp_f32 v60,v60\nv_mfma_f32_32x32x16_bf16 v[16:31],v[120:123],v[96:99],v[16:31]\nv_exp_f32 v61,v61\n" \
"v_cvt_pk_bf16_f32 v101,v58,v59\nv_exp_f32 v62,v62\nv_exp_f32 v63,v63\ns_waitcnt lgkmcnt(0)\nv_exp_f32 v32,v32\nv_cvt_pk_bf16_f32 v102,v60,v61\n" \
"v_exp_f32 v33,v33\nv_exp_f32 v34,v34\nv_cvt_pk_bf16_f32 v103,v62,v63\nv_mfma_f32_32x32x16_bf16 v[48:63],v[104:107],v[132:135],v[80:95]\n" \
"v_exp_f32 v35,v35\nv_cvt_pk_bf16_f32 v96,v32,v33\nv_exp_f32 v36,v36\nds_read_b128 v[116:119],v252 offset:21504\nv_exp_f32 v37,v37\n" \
"v_cvt_pk_bf16_f32 v97,v34,v35\nv_exp_f32 v38,v38\nv_mfma_f32_32x32x16_bf16 v[16:31],v[124:127],v[100:103],v[16:31]\nv_exp_f32 v39,v39\n" \
"v_cvt_pk_bf16_f32 v98,v36,v37\nv_exp_f32 v40,v40\nds_read_b128 v[120:123],v252 offset:22528\nv_exp_f32 v41,v41\nv_cvt_pk_bf16_f32 v99,v38,v39\n" \
"v_exp_f32 v42,v42\nds_read_b128 v[124:127],v252 offset:23552\nv_exp_f32 v43,v43\nv_cvt_pk_bf16_f32 v100,v40,v41\nv_exp_f32 v44,v44\n" \
"v_mfma_f32_32x32x16_bf16 v[0:15],v[108:111],v[96:99],v[0:15]\nv_exp_f32 v45,v45\nv_cvt_pk_bf16_f32 v101,v42,v43\nv_exp_f32 v46,v46\nv_exp_f32 v47,v47\n" \
"v_exp_f32 v48,v48\nv_cvt_pk_bf16_f32 v102,v44,v45\nv_exp_f32 v49,v49\nv_exp_f32 v50,v50\nv_cvt_pk_bf16_f32 v103,v46,v47\ns_waitcnt lgkmcnt(2)\n" \
"v_mfma_f32_32x32x16_bf16 v[32:47],v[116:119],v[128:131],v[64:79]\nv_exp_f32 v51,v51\nv_cvt_pk_bf16_f32 v96,v48,v49\nv_exp_f32 v52,v52\n" \
"v_exp_f32 v53,v53\nv_cvt_pk_bf16_f32 v97,v50,v51\nv_exp_f32 v54,v54\nv_mfma_f32_32x32x16_bf16 v[0:15],v[112:115],v[100:103],v[0:15]\nv_exp_f32 v55,v55\n" \
"v_cvt_pk_bf16_f32 v98,v52,v53\nv_exp_f32 v56,v56\nv_exp_f32 v57,v57\nv_cvt_pk_bf16_f32 v99,v54,v55\nv_exp_f32 v58,v58\nv_exp_f32 v59,v59\n" \
"v_cvt_pk_bf16_f32 v100,v56,v57\nv_exp_f32 v60,v60\nv_mfma_f32_32x32x16_bf16 v[16:31],v[108:111],v[96:99],v[16:31]\nv_exp_f32 v61,v61\n" \
"v_cvt_pk_bf16_f32 v101,v58,v59\nv_exp_f32 v62,v62\nv_exp_f32 v63,v63\ns_waitcnt lgkmcnt(0)\nv_exp_f32 v32,v32\nv_cvt_pk_bf16_f32 v102,v60,v61\n" \
"v_exp_f32 v33,v33\nv_exp_f32 v34,v34\nv_cvt_pk_bf16_f32 v103,v62,v63\nv_mfma_f32_32x32x16_bf16 v[48:63],v[116:119],v[132:135],v[80:95]\n" \
"v_exp_f32 v35,v35\nv_cvt_pk_bf16_f32 v96,v32,v33\nv_exp_f32 v36,v36\nv_exp_f32 v37,v37\nv_cvt_pk_bf16_f32 v97,v34,v35\nv_exp_f32 v38,v38\n" \
"v_mfma_f32_32x32x16_bf16 v[16:31],v[112:115],v[100:103],v[16:31]\nv_exp_f32 v39,v39\nv_cvt_pk_bf16_f32 v98,v36,v37\nv_exp_f32 v40,v40\n" \
"v_exp_f32 v41,v41\nv_cvt_pk_bf16_f32 v99,v38,v39\nv_exp_f32 v42,v42\nv_exp_f32 v43,v43\nv_cvt_pk_bf16_f32 v100,v40,v41\nv_exp_f32 v44,v44\n" \
"v_mfma_f32_32x32x16_bf16 v[0:15],v[120:123],v[96:99],v[0:15]\nv_exp_f32 v45,v45\nv_cvt_pk_bf16_f32 v101,v42,v43\nv_exp_f32 v46,v46\nv_exp_f32 v47,v47\n" \
"v_exp_f32 v48,v48\nv_cvt_pk_bf16_f32 v102,v44,v45\nv_exp_f32 v49,v49\nv_exp_f32 v50,v50\nv_cvt_pk_bf16_f32 v103,v46,v47\nv_exp_f32 v51,v51\n" \
"v_cvt_pk_bf16_f32 v96,v48,v49\nv_exp_f32 v52,v52\nv_exp_f32 v53,v53\nv_cvt_pk_bf16_f32 v97,v50,v51\nv_exp_f32 v54,v54\n" \
"v_mfma_f32_32x32x16_bf16 v[0:15],v[124:127],v[100:103],v[0:15]\nv_exp_f32 v55,v55\nv_cvt_pk_bf16_f32 v98,v52,v53\nv_exp_f32 v56,v56\nv_exp_f32 v57,v57\n" \
"v_cvt_pk_bf16_f32 v99,v54,v55\nv_exp_f32 v58,v58\nv_exp_f32 v59,v59\nv_cvt_pk_bf16_f32 v100,v56,v57\nv_exp_f32 v60,v60\n" \
"v_mfma_f32_32x32x16_bf16 v[16:31],v[120:123],v[96:99],v[16:31]\nv_exp_f32 v61,v61\nv_cvt_pk_bf16_f32 v101,v58,v59\nv_exp_f32 v62,v62\n" \
"v_exp_f32 v63,v63\ns_nop 1\nv_cvt_pk_bf16_f32 v102,v60,v61\nv_cvt_pk_bf16_f32 v103,v62,v63\ns_nop 1\n" \
"v_mfma_f32_32x32x16_bf16 v[16:31],v[124:127],v[100:103],v[16:31]\ns_waitcnt lgkmcnt(0)\ns_nop 7\ns_nop 7\nv_mov_b32 v104,v0\nv_mov_b32 v105,v1\n" \
"v_mov_b32 v106,v2\nv_mov_b32 v107,v3\nv_mov_b32 v108,v4\nv_mov_b32 v109,v5\nv_mov_b32 v110,v6\nv_mov_b32 v111,v7\nv_mov_b32 v112,v8\n" \
"v_mov_b32 v113,v16\nv_mov_b32 v114,v17\nv_mov_b32 v115,v18\nv_mov_b32 v116,v19\nv_mov_b32 v117,v20\nv_mov_b32 v118,v21\nv_mov_b32 v119,v22\n" \
"v_mov_b32 v120,v23\nv_mov_b32 v121,v24\nv_mov_b32 v122,v64\nv_mov_b32 v123,v80\nv_mov_b32 v124,v128\nv_mov_b32 v125,v129\nv_mov_b32 v126,v130\n" \
"v_mov_b32 v127,v131\nv_mov_b32 v128,v132\nv_mov_b32 v129,v133\nv_mov_b32 v130,v134\nv_mov_b32 v131,v135\nv_add_u32 v224,131072,v250\n" \
"v_add_u32 v225,65536,v250\nds_read_b64_tr_b16 v[64:65],v224 offset:0\nds_read_b64_tr_b16 v[66:67],v224 offset:1024\n" \
"ds_read_b64_tr_b16 v[68:69],v224 offset:256\nds_read_b64_tr_b16 v[70:71],v224 offset:1280\nds_read_b64_tr_b16 v[72:73],v224 offset:512\n" \
"ds_read_b64_tr_b16 v[74:75],v224 offset:1536\nds_read_b64_tr_b16 v[76:77],v224 offset:768\nds_read_b64_tr_b16 v[78:79],v224 offset:1792\n" \
"ds_read_b64_tr_b16 v[80:81],v224 offset:4096\nds_read_b64_tr_b16 v[82:83],v224 offset:5120\nds_read_b64_tr_b16 v[84:85],v224 offset:4352\n" \
"ds_read_b64_tr_b16 v[86:87],v224 offset:5376\ns_add_u32 s58,s76,20480\ns_waitcnt lgkmcnt(10)\nv_mfma_f32_32x32x16_bf16 v[0:15],v[64:67],v[192:195],0\n" \
"ds_read_b64_tr_b16 v[88:89],v224 offset:4608\nds_read_b64_tr_b16 v[90:91],v224 offset:5632\n" \
"buffer_load_dwordx4 v[96:99],v248,%[sws],s58 offen offset:2048\ns_waitcnt lgkmcnt(8)\nv_mfma_f32_32x32x16_bf16 v[16:31],v[68:71],v[192:195],0\n" \
"ds_read_b64_tr_b16 v[92:93],v224 offset:4864\nds_read_b64_tr_b16 v[94:95],v224 offset:5888\n" \
"buffer_load_dwordx4 v[100:103],v248,%[sws],s58 offen offset:3072\nv_mfma_f32_32x32x16_bf16 v[32:47],v[72:75],v[192:195],0\n" \
"ds_read_b64_tr_b16 v[64:65],v224 offset:8192\nds_read_b64_tr_b16 v[66:67],v224 offset:9216\ns_waitcnt lgkmcnt(8)\n" \
"v_mfma_f32_32x32x16_bf16 v[48:63],v[76:79],v[192:195],0\nds_read_b64_tr_b16 v[68:69],v224 offset:8448\nds_read_b64_tr_b16 v[70:71],v224 offset:9472\n" \
"s_add_u32 s58,s76,24576\nbuffer_load_dwordx4 v[192:195],v248,%[sws],s58 offen offset:0\nv_mfma_f32_32x32x16_bf16 v[0:15],v[80:83],v[196:199],v[0:15]\n" \
"ds_read_b64_tr_b16 v[72:73],v224 offset:8704\nds_read_b64_tr_b16 v[74:75],v224 offset:9728\ns_waitcnt lgkmcnt(8)\n" \
"v_mfma_f32_32x32x16_bf16 v[16:31],v[84:87],v[196:199],v[16:31]\nds_read_b64_tr_b16 v[76:77],v224 offset:8960\n" \
"ds_read_b64_tr_b16 v[78:79],v224 offset:9984\nv_mfma_f32_32x32x16_bf16 v[32:47],v[88:91],v[196:199],v[32:47]\n" \
"ds_read_b64_tr_b16 v[80:81],v224 offset:12288\nds_read_b64_tr_b16 v[82:83],v224 offset:13312\ns_waitcnt lgkmcnt(8)\n" \
"v_mfma_f32_32x32x16_bf16 v[48:63],v[92:95],v[196:199],v[48:63]\nds_read_b64_tr_b16 v[84:85],v224 offset:12544\n" \
"ds_read_b64_tr_b16 v[86:87],v224 offset:13568\nbuffer_load_dwordx4 v[196:199],v248,%[sws],s58 offen offset:1024\n" \
"v_mfma_f32_32x32x16_bf16 v[0:15],v[64:67],v[200:203],v[0:15]\nds_read_b64_tr_b16 v[88:89],v224 offset:12800\n" \
"ds_read_b64_tr_b16 v[90:91],v224 offset:13824\ns_waitcnt lgkmcnt(8)\nv_mfma_f32_32x32x16_bf16 v[16:31],v[68:71],v[200:203],v[16:31]\n" \
"ds_read_b64_tr_b16 v[92:93],v224 offset:13056\nds_read_b64_tr_b16 v[94:95],v224 offset:14080\n" \
"v_mfma_f32_32x32x16_bf16 v[32:47],v[72:75],v[200:203],v[32:47]\nds_read_b64_tr_b16 v[64:65],v224 offset:16384\n" \
"ds_read_b64_tr_b16 v[66:67],v224 offset:17408\ns_waitcnt lgkmcnt(8)\nv_mfma_f32_32x32x16_bf16 v[48:63],v[76:79],v[200:203],v[48:63]\n" \
"ds_read_b64_tr_b16 v[68:69],v224 offset:16640\nds_read_b64_tr_b16 v[70:71],v224 offset:17664\n" \
"buffer_load_dwordx4 v[200:203],v248,%[sws],s58 offen offset:2048\nv_mfma_f32_32x32x16_bf16 v[0:15],v[80:83],v[204:207],v[0:15]\n" \
"ds_read_b64_tr_b16 v[72:73],v224 offset:16896\nds_read_b64_tr_b16 v[74:75],v224 offset:17920\ns_waitcnt lgkmcnt(8)\n" \
"v_mfma_f32_32x32x16_bf16 v[16:31],v[84:87],v[204:207],v[16:31]\nds_read_b64_tr_b16 v[76:77],v224 offset:17152\n" \
"ds_read_b64_tr_b16 v[78:79],v224 offset:18176\nv_mfma_f32_32x32x16_bf16 v[32:47],v[88:91],v[204:207],v[32:47]\n" \
"ds_read_b64_tr_b16 v[80:81],v224 offset:20480\nds_read_b64_tr_b16 v[82:83],v224 offset:21504\ns_waitcnt lgkmcnt(8)\n" \
"v_mfma_f32_32x32x16_bf16 v[48:63],v[92:95],v[204:207],v[48:63]\nds_read_b64_tr_b16 v[84:85],v224 offset:20736\n" \
"ds_read_b64_tr_b16 v[86:87],v224 offset:21760\nbuffer_load_dwordx4 v[204:207],v248,%[sws],s58 offen offset:3072\n" \
"v_mfma_f32_32x32x16_bf16 v[0:15],v[64:67],v[208:211],v[0:15]\nds_read_b64_tr_b16 v[88:89],v224 offset:20992\n" \
"ds_read_b64_tr_b16 v[90:91],v224 offset:22016\ns_waitcnt lgkmcnt(8)\nv_mfma_f32_32x32x16_bf16 v[16:31],v[68:71],v[208:211],v[16:31]\n" \
"ds_read_b64_tr_b16 v[92:93],v224 offset:21248\nds_read_b64_tr_b16 v[94:95],v224 offset:22272\n" \
"v_mfma_f32_32x32x16_bf16 v[32:47],v[72:75],v[208:211],v[32:47]\nds_read_b64_tr_b16 v[64:65],v224 offset:24576\n" \
"ds_read_b64_tr_b16 v[66:67],v224 offset:25600\ns_waitcnt lgkmcnt(8)\nv_mfma_f32_32x32x16_bf16 v[48:63],v[76:79],v[208:211],v[48:63]\n" \
"ds_read_b64_tr_b16 v[68:69],v224 offset:24832\nds_read_b64_tr_b16 v[70:71],v224 offset:25856\ns_add_u32 s58,s76,28672\n" \
"buffer_load_dwordx4 v[208:211],v248,%[sws],s58 offen offset:0\nv_mfma_f32_32x32x16_bf16 v[0:15],v[80:83],v[212:215],v[0:15]\n" \
"ds_read_b64_tr_b16 v[72:73],v224 offset:25088\nds_read_b64_tr_b16 v[74:75],v224 offset:26112\ns_waitcnt lgkmcnt(8)\n" \
"v_mfma_f32_32x32x16_bf16 v[16:31],v[84:87],v[212:215],v[16:31]\nds_read_b64_tr_b16 v[76:77],v224 offset:25344\n" \
"ds_read_b64_tr_b16 v[78:79],v224 offset:26368\nv_mfma_f32_32x32x16_bf16 v[32:47],v[88:91],v[212:215],v[32:47]\n" \
"ds_read_b64_tr_b16 v[80:81],v224 offset:28672\nds_read_b64_tr_b16 v[82:83],v224 offset:29696\ns_waitcnt lgkmcnt(8)\n" \
"v_mfma_f32_32x32x16_bf16 v[48:63],v[92:95],v[212:215],v[48:63]\nds_read_b64_tr_b16 v[84:85],v224 offset:28928\n" \
"ds_read_b64_tr_b16 v[86:87],v224 offset:29952\nbuffer_load_dwordx4 v[212:215],v248,%[sws],s58 offen offset:1024\n" \
"v_mfma_f32_32x32x16_bf16 v[0:15],v[64:67],v[216:219],v[0:15]\nds_read_b64_tr_b16 v[88:89],v224 offset:29184\n" \
"ds_read_b64_tr_b16 v[90:91],v224 offset:30208\ns_waitcnt lgkmcnt(8)\nv_mfma_f32_32x32x16_bf16 v[16:31],v[68:71],v[216:219],v[16:31]\n" \
"ds_read_b64_tr_b16 v[92:93],v224 offset:29440\nds_read_b64_tr_b16 v[94:95],v224 offset:30464\n" \
"v_mfma_f32_32x32x16_bf16 v[32:47],v[72:75],v[216:219],v[32:47]\nds_read_b64_tr_b16 v[64:65],v225 offset:0\n" \
"ds_read_b64_tr_b16 v[66:67],v225 offset:1024\ns_waitcnt lgkmcnt(8)\nv_mfma_f32_32x32x16_bf16 v[48:63],v[76:79],v[216:219],v[48:63]\n" \
"ds_read_b64_tr_b16 v[68:69],v225 offset:256\nds_read_b64_tr_b16 v[70:71],v225 offset:1280\nv_add_u32 v224,98304,v250\n" \
"buffer_load_dwordx4 v[216:219],v248,%[sws],s58 offen offset:2048\nv_mfma_f32_32x32x16_bf16 v[0:15],v[80:83],v[220:223],v[0:15]\n" \
"ds_read_b64_tr_b16 v[72:73],v225 offset:512\nds_read_b64_tr_b16 v[74:75],v225 offset:1536\ns_waitcnt lgkmcnt(8)\n" \
"v_mfma_f32_32x32x16_bf16 v[16:31],v[84:87],v[220:223],v[16:31]\nds_read_b64_tr_b16 v[76:77],v225 offset:768\n" \
"ds_read_b64_tr_b16 v[78:79],v225 offset:1792\nv_mfma_f32_32x32x16_bf16 v[32:47],v[88:91],v[220:223],v[32:47]\n" \
"ds_read_b64_tr_b16 v[80:81],v225 offset:4096\nds_read_b64_tr_b16 v[82:83],v225 offset:5120\ns_waitcnt lgkmcnt(8)\n" \
"v_mfma_f32_32x32x16_bf16 v[48:63],v[92:95],v[220:223],v[48:63]\nds_read_b64_tr_b16 v[84:85],v225 offset:4352\n" \
"ds_read_b64_tr_b16 v[86:87],v225 offset:5376\nbuffer_load_dwordx4 v[220:223],v248,%[sws],s58 offen offset:3072\ns_waitcnt vmcnt(16)\n" \
"v_mfma_f32_32x32x16_bf16 v[0:15],v[64:67],v[136:139],v[0:15]\nds_read_b64_tr_b16 v[88:89],v225 offset:4608\n" \
"ds_read_b64_tr_b16 v[90:91],v225 offset:5632\ns_waitcnt lgkmcnt(8)\nv_mfma_f32_32x32x16_bf16 v[16:31],v[68:71],v[136:139],v[16:31]\n" \
"ds_read_b64_tr_b16 v[92:93],v225 offset:4864\nds_read_b64_tr_b16 v[94:95],v225 offset:5888\ns_barrier\ns_and_b32 s50,s66,0xffff\n" \
"s_add_u32 m0,s50,131072\ns_add_u32 s49,s67,3145728\nbuffer_load_dwordx4 v249,%[sq],s49 offen lds\n" \
"v_mfma_f32_32x32x16_bf16 v[32:47],v[72:75],v[136:139],v[32:47]\nds_read_b64_tr_b16 v[64:65],v225 offset:8192\n" \
"ds_read_b64_tr_b16 v[66:67],v225 offset:9216\ns_add_u32 m0,m0,1024\ns_add_u32 s49,s49,32768\nbuffer_load_dwordx4 v249,%[sq],s49 offen lds\n" \
"s_waitcnt lgkmcnt(8)\nv_mfma_f32_32x32x16_bf16 v[48:63],v[76:79],v[136:139],v[48:63]\nds_read_b64_tr_b16 v[68:69],v225 offset:8448\n" \
"ds_read_b64_tr_b16 v[70:71],v225 offset:9472\ns_add_u32 m0,m0,1024\ns_add_u32 s49,s49,32768\nbuffer_load_dwordx4 v249,%[sq],s49 offen lds\n" \
"v_mfma_f32_32x32x16_bf16 v[0:15],v[80:83],v[140:143],v[0:15]\nds_read_b64_tr_b16 v[72:73],v225 offset:8704\n" \
"ds_read_b64_tr_b16 v[74:75],v225 offset:9728\ns_add_u32 m0,m0,1024\ns_add_u32 s49,s49,32768\nbuffer_load_dwordx4 v249,%[sq],s49 offen lds\n" \
"s_waitcnt lgkmcnt(8)\nv_mfma_f32_32x32x16_bf16 v[16:31],v[84:87],v[140:143],v[16:31]\nds_read_b64_tr_b16 v[76:77],v225 offset:8960\n" \
"ds_read_b64_tr_b16 v[78:79],v225 offset:9984\nv_mfma_f32_32x32x16_bf16 v[32:47],v[88:91],v[140:143],v[32:47]\n" \
"ds_read_b64_tr_b16 v[80:81],v225 offset:12288\nds_read_b64_tr_b16 v[82:83],v225 offset:13312\ns_waitcnt lgkmcnt(8)\n" \
"v_mfma_f32_32x32x16_bf16 v[48:63],v[92:95],v[140:143],v[48:63]\nds_read_b64_tr_b16 v[84:85],v225 offset:12544\n" \
"ds_read_b64_tr_b16 v[86:87],v225 offset:13568\nv_mfma_f32_32x32x16_bf16 v[0:15],v[64:67],v[144:147],v[0:15]\n" \
"ds_read_b64_tr_b16 v[88:89],v225 offset:12800\nds_read_b64_tr_b16 v[90:91],v225 offset:13824\ns_waitcnt lgkmcnt(8)\n" \
"v_mfma_f32_32x32x16_bf16 v[16:31],v[68:71],v[144:147],v[16:31]\nds_read_b64_tr_b16 v[92:93],v225 offset:13056\n" \
"ds_read_b64_tr_b16 v[94:95],v225 offset:14080\nv_mfma_f32_32x32x16_bf16 v[32:47],v[72:75],v[144:147],v[32:47]\n" \
"ds_read_b64_tr_b16 v[64:65],v225 offset:16384\nds_read_b64_tr_b16 v[66:67],v225 offset:17408\ns_waitcnt lgkmcnt(8)\n" \
"v_mfma_f32_32x32x16_bf16 v[48:63],v[76:79],v[144:147],v[48:63]\nds_read_b64_tr_b16 v[68:69],v225 offset:16640\n" \
"ds_read_b64_tr_b16 v[70:71],v225 offset:17664\nv_mfma_f32_32x32x16_bf16 v[0:15],v[80:83],v[148:151],v[0:15]\n" \
"ds_read_b64_tr_b16 v[72:73],v225 offset:16896\nds_read_b64_tr_b16 v[74:75],v225 offset:17920\ns_waitcnt lgkmcnt(8)\n" \
"v_mfma_f32_32x32x16_bf16 v[16:31],v[84:87],v[148:151],v[16:31]\nds_read_b64_tr_b16 v[76:77],v225 offset:17152\n" \
"ds_read_b64_tr_b16 v[78:79],v225 offset:18176\nv_mfma_f32_32x32x16_bf16 v[32:47],v[88:91],v[148:151],v[32:47]\n" \
"ds_read_b64_tr_b16 v[80:81],v225 offset:20480\nds_read_b64_tr_b16 v[82:83],v225 offset:21504\ns_waitcnt lgkmcnt(8)\n" \
"v_mfma_f32_32x32x16_bf16 v[48:63],v[92:95],v[148:151],v[48:63]\nds_read_b64_tr_b16 v[84:85],v225 offset:20736\n" \
"ds_read_b64_tr_b16 v[86:87],v225 offset:21760\nv_mfma_f32_32x32x16_bf16 v[0:15],v[64:67],v[152:155],v[0:15]\n" \
"ds_read_b64_tr_b16 v[88:89],v225 offset:20992\nds_read_b64_tr_b16 v[90:91],v225 offset:22016\ns_waitcnt lgkmcnt(8)\n" \
"v_mfma_f32_32x32x16_bf16 v[16:31],v[68:71],v[152:155],v[16:31]\nds_read_b64_tr_b16 v[92:93],v225 offset:21248\n" \
"ds_read_b64_tr_b16 v[94:95],v225 offset:22272\nv_mfma_f32_32x32x16_bf16 v[32:47],v[72:75],v[152:155],v[32:47]\n" \
"ds_read_b64_tr_b16 v[64:65],v225 offset:24576\nds_read_b64_tr_b16 v[66:67],v225 offset:25600\ns_waitcnt lgkmcnt(8)\n" \
"v_mfma_f32_32x32x16_bf16 v[48:63],v[76:79],v[152:155],v[48:63]\nds_read_b64_tr_b16 v[68:69],v225 offset:24832\n" \
"ds_read_b64_tr_b16 v[70:71],v225 offset:25856\nv_mfma_f32_32x32x16_bf16 v[0:15],v[80:83],v[156:159],v[0:15]\n" \
"ds_read_b64_tr_b16 v[72:73],v225 offset:25088\nds_read_b64_tr_b16 v[74:75],v225 offset:26112\ns_waitcnt lgkmcnt(8)\n" \
"v_mfma_f32_32x32x16_bf16 v[16:31],v[84:87],v[156:159],v[16:31]\nds_read_b64_tr_b16 v[76:77],v225 offset:25344\n" \
"ds_read_b64_tr_b16 v[78:79],v225 offset:26368\nv_mfma_f32_32x32x16_bf16 v[32:47],v[88:91],v[156:159],v[32:47]\n" \
"ds_read_b64_tr_b16 v[80:81],v225 offset:28672\nds_read_b64_tr_b16 v[82:83],v225 offset:29696\ns_waitcnt lgkmcnt(8)\n" \
"v_mfma_f32_32x32x16_bf16 v[48:63],v[92:95],v[156:159],v[48:63]\nds_read_b64_tr_b16 v[84:85],v225 offset:28928\n" \
"ds_read_b64_tr_b16 v[86:87],v225 offset:29952\nv_mfma_f32_32x32x16_bf16 v[0:15],v[64:67],v[160:163],v[0:15]\n" \
"ds_read_b64_tr_b16 v[88:89],v225 offset:29184\nds_read_b64_tr_b16 v[90:91],v225 offset:30208\ns_waitcnt lgkmcnt(8)\n" \
"v_mfma_f32_32x32x16_bf16 v[16:31],v[68:71],v[160:163],v[16:31]\nds_read_b64_tr_b16 v[92:93],v225 offset:29440\n" \
"ds_read_b64_tr_b16 v[94:95],v225 offset:30464\nv_mfma_f32_32x32x16_bf16 v[32:47],v[72:75],v[160:163],v[32:47]\n" \
"ds_read_b64_tr_b16 v[64:65],v224 offset:0\nds_read_b64_tr_b16 v[66:67],v224 offset:1024\ns_waitcnt lgkmcnt(8)\n" \
"v_mfma_f32_32x32x16_bf16 v[48:63],v[76:79],v[160:163],v[48:63]\nds_read_b64_tr_b16 v[68:69],v224 offset:256\n" \
"ds_read_b64_tr_b16 v[70:71],v224 offset:1280\nv_add_u32 v225,131072,v250\nv_mfma_f32_32x32x16_bf16 v[0:15],v[80:83],v[164:167],v[0:15]\n" \
"ds_read_b64_tr_b16 v[72:73],v224 offset:512\nds_read_b64_tr_b16 v[74:75],v224 offset:1536\ns_waitcnt lgkmcnt(8)\n" \
"v_mfma_f32_32x32x16_bf16 v[16:31],v[84:87],v[164:167],v[16:31]\nds_read_b64_tr_b16 v[76:77],v224 offset:768\n" \
"ds_read_b64_tr_b16 v[78:79],v224 offset:1792\nv_mfma_f32_32x32x16_bf16 v[32:47],v[88:91],v[164:167],v[32:47]\n" \
"ds_read_b64_tr_b16 v[80:81],v224 offset:4096\nds_read_b64_tr_b16 v[82:83],v224 offset:5120\ns_waitcnt lgkmcnt(8)\n" \
"v_mfma_f32_32x32x16_bf16 v[48:63],v[92:95],v[164:167],v[48:63]\nds_read_b64_tr_b16 v[84:85],v224 offset:4352\n" \
"ds_read_b64_tr_b16 v[86:87],v224 offset:5376\ns_waitcnt vmcnt(12)\nv_mfma_f32_32x32x16_bf16 v[0:15],v[64:67],v[168:171],v[0:15]\n" \
"ds_read_b64_tr_b16 v[88:89],v224 offset:4608\nds_read_b64_tr_b16 v[90:91],v224 offset:5632\ns_waitcnt lgkmcnt(8)\n" \
"v_mfma_f32_32x32x16_bf16 v[16:31],v[68:71],v[168:171],v[16:31]\nds_read_b64_tr_b16 v[92:93],v224 offset:4864\n" \
"ds_read_b64_tr_b16 v[94:95],v224 offset:5888\nv_mfma_f32_32x32x16_bf16 v[32:47],v[72:75],v[168:171],v[32:47]\n" \
"ds_read_b64_tr_b16 v[64:65],v224 offset:8192\nds_read_b64_tr_b16 v[66:67],v224 offset:9216\ns_waitcnt lgkmcnt(8)\n" \
"v_mfma_f32_32x32x16_bf16 v[48:63],v[76:79],v[168:171],v[48:63]\nds_read_b64_tr_b16 v[68:69],v224 offset:8448\n" \
"ds_read_b64_tr_b16 v[70:71],v224 offset:9472\nv_mfma_f32_32x32x16_bf16 v[0:15],v[80:83],v[172:175],v[0:15]\n" \
"ds_read_b64_tr_b16 v[72:73],v224 offset:8704\nds_read_b64_tr_b16 v[74:75],v224 offset:9728\ns_waitcnt lgkmcnt(8)\n" \
"v_mfma_f32_32x32x16_bf16 v[16:31],v[84:87],v[172:175],v[16:31]\nds_read_b64_tr_b16 v[76:77],v224 offset:8960\n" \
"ds_read_b64_tr_b16 v[78:79],v224 offset:9984\nv_mfma_f32_32x32x16_bf16 v[32:47],v[88:91],v[172:175],v[32:47]\n" \
"ds_read_b64_tr_b16 v[80:81],v224 offset:12288\nds_read_b64_tr_b16 v[82:83],v224 offset:13312\ns_waitcnt lgkmcnt(8)\n" \
"v_mfma_f32_32x32x16_bf16 v[48:63],v[92:95],v[172:175],v[48:63]\nds_read_b64_tr_b16 v[84:85],v224 offset:12544\n" \
"ds_read_b64_tr_b16 v[86:87],v224 offset:13568\nv_mfma_f32_32x32x16_bf16 v[0:15],v[64:67],v[176:179],v[0:15]\n" \
"ds_read_b64_tr_b16 v[88:89],v224 offset:12800\nds_read_b64_tr_b16 v[90:91],v224 offset:13824\ns_waitcnt lgkmcnt(8)\n" \
"v_mfma_f32_32x32x16_bf16 v[16:31],v[68:71],v[176:179],v[16:31]\nds_read_b64_tr_b16 v[92:93],v224 offset:13056\n" \
"ds_read_b64_tr_b16 v[94:95],v224 offset:14080\nv_mfma_f32_32x32x16_bf16 v[32:47],v[72:75],v[176:179],v[32:47]\n" \
"ds_read_b64_tr_b16 v[64:65],v224 offset:16384\nds_read_b64_tr_b16 v[66:67],v224 offset:17408\ns_waitcnt lgkmcnt(8)\n" \
"v_mfma_f32_32x32x16_bf16 v[48:63],v[76:79],v[176:179],v[48:63]\nds_read_b64_tr_b16 v[68:69],v224 offset:16640\n" \
"ds_read_b64_tr_b16 v[70:71],v224 offset:17664\nv_mfma_f32_32x32x16_bf16 v[0:15],v[80:83],v[180:183],v[0:15]\n" \
"ds_read_b64_tr_b16 v[72:73],v224 offset:16896\nds_read_b64_tr_b16 v[74:75],v224 offset:17920\ns_waitcnt lgkmcnt(8)\n" \
"v_mfma_f32_32x32x16_bf16 v[16:31],v[84:87],v[180:183],v[16:31]\nds_read_b64_tr_b16 v[76:77],v224 offset:17152\n" \
"ds_read_b64_tr_b16 v[78:79],v224 offset:18176\nv_mfma_f32_32x32x16_bf16 v[32:47],v[88:91],v[180:183],v[32:47]\n" \
"ds_read_b64_tr_b16 v[80:81],v224 offset:20480\nds_read_b64_tr_b16 v[82:83],v224 offset:21504\ns_waitcnt lgkmcnt(8)\n" \
"v_mfma_f32_32x32x16_bf16 v[48:63],v[92:95],v[180:183],v[48:63]\nds_read_b64_tr_b16 v[84:85],v224 offset:20736\n" \
"ds_read_b64_tr_b16 v[86:87],v224 offset:21760\nv_mfma_f32_32x32x16_bf16 v[0:15],v[64:67],v[184:187],v[0:15]\n" \
"ds_read_b64_tr_b16 v[88:89],v224 offset:20992\nds_read_b64_tr_b16 v[90:91],v224 offset:22016\ns_waitcnt lgkmcnt(8)\n" \
"v_mfma_f32_32x32x16_bf16 v[16:31],v[68:71],v[184:187],v[16:31]\nds_read_b64_tr_b16 v[92:93],v224 offset:21248\n" \
"ds_read_b64_tr_b16 v[94:95],v224 offset:22272\nv_mfma_f32_32x32x16_bf16 v[32:47],v[72:75],v[184:187],v[32:47]\n" \
"ds_read_b64_tr_b16 v[64:65],v224 offset:24576\nds_read_b64_tr_b16 v[66:67],v224 offset:25600\ns_waitcnt lgkmcnt(8)\n" \
"v_mfma_f32_32x32x16_bf16 v[48:63],v[76:79],v[184:187],v[48:63]\nds_read_b64_tr_b16 v[68:69],v224 offset:24832\n" \
"ds_read_b64_tr_b16 v[70:71],v224 offset:25856\nv_mfma_f32_32x32x16_bf16 v[0:15],v[80:83],v[188:191],v[0:15]\n" \
"ds_read_b64_tr_b16 v[72:73],v224 offset:25088\nds_read_b64_tr_b16 v[74:75],v224 offset:26112\ns_waitcnt lgkmcnt(8)\n" \
"v_mfma_f32_32x32x16_bf16 v[16:31],v[84:87],v[188:191],v[16:31]\nds_read_b64_tr_b16 v[76:77],v224 offset:25344\n" \
"ds_read_b64_tr_b16 v[78:79],v224 offset:26368\nv_mfma_f32_32x32x16_bf16 v[32:47],v[88:91],v[188:191],v[32:47]\n" \
"ds_read_b64_tr_b16 v[80:81],v224 offset:28672\nds_read_b64_tr_b16 v[82:83],v224 offset:29696\ns_waitcnt lgkmcnt(8)\n" \
"v_mfma_f32_32x32x16_bf16 v[48:63],v[92:95],v[188:191],v[48:63]\nds_read_b64_tr_b16 v[84:85],v224 offset:28928\n" \
"ds_read_b64_tr_b16 v[86:87],v224 offset:29952\nv_mfma_f32_32x32x16_bf16 v[0:15],v[64:67],v[96:99],v[0:15]\n" \
"ds_read_b64_tr_b16 v[88:89],v224 offset:29184\nds_read_b64_tr_b16 v[90:91],v224 offset:30208\ns_waitcnt vmcnt(0)\ns_barrier\ns_waitcnt lgkmcnt(8)\n" \
"v_mfma_f32_32x32x16_bf16 v[16:31],v[68:71],v[96:99],v[16:31]\nds_read_b64_tr_b16 v[92:93],v224 offset:29440\n" \
"ds_read_b64_tr_b16 v[94:95],v224 offset:30464\nv_mfma_f32_32x32x16_bf16 v[32:47],v[72:75],v[96:99],v[32:47]\nds_read_b64_tr_b16 v[64:65],v225 offset:0\n" \
"ds_read_b64_tr_b16 v[66:67],v225 offset:1024\ns_waitcnt lgkmcnt(8)\nv_mfma_f32_32x32x16_bf16 v[48:63],v[76:79],v[96:99],v[48:63]\n" \
"ds_read_b64_tr_b16 v[68:69],v225 offset:256\nds_read_b64_tr_b16 v[70:71],v225 offset:1280\n" \
"v_mfma_f32_32x32x16_bf16 v[0:15],v[80:83],v[100:103],v[0:15]\nds_read_b64_tr_b16 v[72:73],v225 offset:512\n" \
"ds_read_b64_tr_b16 v[74:75],v225 offset:1536\ns_waitcnt lgkmcnt(8)\nv_mfma_f32_32x32x16_bf16 v[16:31],v[84:87],v[100:103],v[16:31]\n" \
"ds_read_b64_tr_b16 v[76:77],v225 offset:768\nds_read_b64_tr_b16 v[78:79],v225 offset:1792\n" \
"v_mfma_f32_32x32x16_bf16 v[32:47],v[88:91],v[100:103],v[32:47]\nds_read_b64_tr_b16 v[80:81],v225 offset:4096\n" \
"ds_read_b64_tr_b16 v[82:83],v225 offset:5120\ns_waitcnt lgkmcnt(8)\nv_mfma_f32_32x32x16_bf16 v[48:63],v[92:95],v[100:103],v[48:63]\n" \
"ds_read_b64_tr_b16 v[84:85],v225 offset:4352\nds_read_b64_tr_b16 v[86:87],v225 offset:5376\n" \
"v_mfma_f32_32x32x16_bf16 v[0:15],v[64:67],v[192:195],v[0:15]\nds_read_b64_tr_b16 v[88:89],v225 offset:4608\n" \
"ds_read_b64_tr_b16 v[90:91],v225 offset:5632\ns_waitcnt lgkmcnt(8)\nv_mfma_f32_32x32x16_bf16 v[16:31],v[68:71],v[192:195],v[16:31]\n" \
"ds_read_b64_tr_b16 v[92:93],v225 offset:4864\nds_read_b64_tr_b16 v[94:95],v225 offset:5888\n" \
"v_mfma_f32_32x32x16_bf16 v[32:47],v[72:75],v[192:195],v[32:47]\nds_read_b64_tr_b16 v[64:65],v225 offset:8192\n" \
"ds_read_b64_tr_b16 v[66:67],v225 offset:9216\ns_waitcnt lgkmcnt(8)\nv_mfma_f32_32x32x16_bf16 v[48:63],v[76:79],v[192:195],v[48:63]\n" \
"ds_read_b64_tr_b16 v[68:69],v225 offset:8448\nds_read_b64_tr_b16 v[70:71],v225 offset:9472\n" \
"v_mfma_f32_32x32x16_bf16 v[0:15],v[80:83],v[196:199],v[0:15]\nds_read_b64_tr_b16 v[72:73],v225 offset:8704\n" \
"ds_read_b64_tr_b16 v[74:75],v225 offset:9728\ns_waitcnt lgkmcnt(8)\nv_mfma_f32_32x32x16_bf16 v[16:31],v[84:87],v[196:199],v[16:31]\n" \
"ds_read_b64_tr_b16 v[76:77],v225 offset:8960\nds_read_b64_tr_b16 v[78:79],v225 offset:9984\n" \
"v_mfma_f32_32x32x16_bf16 v[32:47],v[88:91],v[196:199],v[32:47]\nds_read_b64_tr_b16 v[80:81],v225 offset:12288\n" \
"ds_read_b64_tr_b16 v[82:83],v225 offset:13312\ns_waitcnt lgkmcnt(8)\nv_mfma_f32_32x32x16_bf16 v[48:63],v[92:95],v[196:199],v[48:63]\n" \
"ds_read_b64_tr_b16 v[84:85],v225 offset:12544\nds_read_b64_tr_b16 v[86:87],v225 offset:13568\n" \
"v_mfma_f32_32x32x16_bf16 v[0:15],v[64:67],v[200:203],v[0:15]\nds_read_b64_tr_b16 v[88:89],v225 offset:12800\n" \
"ds_read_b64_tr_b16 v[90:91],v225 offset:13824\ns_waitcnt lgkmcnt(8)\nv_mfma_f32_32x32x16_bf16 v[16:31],v[68:71],v[200:203],v[16:31]\n" \
"ds_read_b64_tr_b16 v[92:93],v225 offset:13056\nds_read_b64_tr_b16 v[94:95],v225 offset:14080\n" \
"v_mfma_f32_32x32x16_bf16 v[32:47],v[72:75],v[200:203],v[32:47]\nds_read_b64_tr_b16 v[64:65],v225 offset:16384\n" \
"ds_read_b64_tr_b16 v[66:67],v225 offset:17408\ns_waitcnt lgkmcnt(8)\nv_mfma_f32_32x32x16_bf16 v[48:63],v[76:79],v[200:203],v[48:63]\n" \
"ds_read_b64_tr_b16 v[68:69],v225 offset:16640\nds_read_b64_tr_b16 v[70:71],v225 offset:17664\n" \
"v_mfma_f32_32x32x16_bf16 v[0:15],v[80:83],v[204:207],v[0:15]\nds_read_b64_tr_b16 v[72:73],v225 offset:16896\n" \
"ds_read_b64_tr_b16 v[74:75],v225 offset:17920\ns_waitcnt lgkmcnt(8)\nv_mfma_f32_32x32x16_bf16 v[16:31],v[84:87],v[204:207],v[16:31]\n" \
"ds_read_b64_tr_b16 v[76:77],v225 offset:17152\nds_read_b64_tr_b16 v[78:79],v225 offset:18176\n" \
"v_mfma_f32_32x32x16_bf16 v[32:47],v[88:91],v[204:207],v[32:47]\nds_read_b64_tr_b16 v[80:81],v225 offset:20480\n" \
"ds_read_b64_tr_b16 v[82:83],v225 offset:21504\ns_waitcnt lgkmcnt(8)\nv_mfma_f32_32x32x16_bf16 v[48:63],v[92:95],v[204:207],v[48:63]\n" \
"ds_read_b64_tr_b16 v[84:85],v225 offset:20736\nds_read_b64_tr_b16 v[86:87],v225 offset:21760\n" \
"v_mfma_f32_32x32x16_bf16 v[0:15],v[64:67],v[208:211],v[0:15]\nds_read_b64_tr_b16 v[88:89],v225 offset:20992\n" \
"ds_read_b64_tr_b16 v[90:91],v225 offset:22016\ns_waitcnt lgkmcnt(8)\nv_mfma_f32_32x32x16_bf16 v[16:31],v[68:71],v[208:211],v[16:31]\n" \
"ds_read_b64_tr_b16 v[92:93],v225 offset:21248\nds_read_b64_tr_b16 v[94:95],v225 offset:22272\n" \
"v_mfma_f32_32x32x16_bf16 v[32:47],v[72:75],v[208:211],v[32:47]\nds_read_b64_tr_b16 v[64:65],v225 offset:24576\n" \
"ds_read_b64_tr_b16 v[66:67],v225 offset:25600\ns_waitcnt lgkmcnt(8)\nv_mfma_f32_32x32x16_bf16 v[48:63],v[76:79],v[208:211],v[48:63]\n" \
"ds_read_b64_tr_b16 v[68:69],v225 offset:24832\nds_read_b64_tr_b16 v[70:71],v225 offset:25856\n" \
"v_mfma_f32_32x32x16_bf16 v[0:15],v[80:83],v[212:215],v[0:15]\nds_read_b64_tr_b16 v[72:73],v225 offset:25088\n" \
"ds_read_b64_tr_b16 v[74:75],v225 offset:26112\ns_waitcnt lgkmcnt(8)\nv_mfma_f32_32x32x16_bf16 v[16:31],v[84:87],v[212:215],v[16:31]\n" \
"ds_read_b64_tr_b16 v[76:77],v225 offset:25344\nds_read_b64_tr_b16 v[78:79],v225 offset:26368\n" \
"v_mfma_f32_32x32x16_bf16 v[32:47],v[88:91],v[212:215],v[32:47]\nds_read_b64_tr_b16 v[80:81],v225 offset:28672\n" \
"ds_read_b64_tr_b16 v[82:83],v225 offset:29696\ns_waitcnt lgkmcnt(8)\nv_mfma_f32_32x32x16_bf16 v[48:63],v[92:95],v[212:215],v[48:63]\n" \
"ds_read_b64_tr_b16 v[84:85],v225 offset:28928\nds_read_b64_tr_b16 v[86:87],v225 offset:29952\n" \
"v_mfma_f32_32x32x16_bf16 v[0:15],v[64:67],v[216:219],v[0:15]\nds_read_b64_tr_b16 v[88:89],v225 offset:29184\n" \
"ds_read_b64_tr_b16 v[90:91],v225 offset:30208\ns_waitcnt lgkmcnt(8)\nv_mfma_f32_32x32x16_bf16 v[16:31],v[68:71],v[216:219],v[16:31]\n" \
"ds_read_b64_tr_b16 v[92:93],v225 offset:29440\nds_read_b64_tr_b16 v[94:95],v225 offset:30464\n" \
"v_mfma_f32_32x32x16_bf16 v[32:47],v[72:75],v[216:219],v[32:47]\ns_waitcnt lgkmcnt(6)\nv_mfma_f32_32x32x16_bf16 v[48:63],v[76:79],v[216:219],v[48:63]\n" \
"v_mfma_f32_32x32x16_bf16 v[0:15],v[80:83],v[220:223],v[0:15]\ns_waitcnt lgkmcnt(2)\nv_mfma_f32_32x32x16_bf16 v[16:31],v[84:87],v[220:223],v[16:31]\n" \
"v_mfma_f32_32x32x16_bf16 v[32:47],v[88:91],v[220:223],v[32:47]\ns_waitcnt lgkmcnt(0)\nv_mfma_f32_32x32x16_bf16 v[48:63],v[92:95],v[220:223],v[48:63]\n" \
"s_nop 7\ns_nop 7\ns_mov_b32 s49,0x3e0293ee\nv_mul_f32 v0,s49,v0\nv_mul_f32 v1,s49,v1\nv_mul_f32 v2,s49,v2\nv_mul_f32 v3,s49,v3\nv_mul_f32 v4,s49,v4\n" \
"v_mul_f32 v5,s49,v5\nv_mul_f32 v6,s49,v6\nv_mul_f32 v7,s49,v7\nv_mul_f32 v8,s49,v8\nv_mul_f32 v9,s49,v9\nv_mul_f32 v10,s49,v10\nv_mul_f32 v11,s49,v11\n" \
"v_mul_f32 v12,s49,v12\nv_mul_f32 v13,s49,v13\nv_mul_f32 v14,s49,v14\nv_mul_f32 v15,s49,v15\nv_mul_f32 v16,s49,v16\nv_mul_f32 v17,s49,v17\n" \
"v_mul_f32 v18,s49,v18\nv_mul_f32 v19,s49,v19\nv_mul_f32 v20,s49,v20\nv_mul_f32 v21,s49,v21\nv_mul_f32 v22,s49,v22\nv_mul_f32 v23,s49,v23\n" \
"v_mul_f32 v24,s49,v24\nv_mul_f32 v25,s49,v25\nv_mul_f32 v26,s49,v26\nv_mul_f32 v27,s49,v27\nv_mul_f32 v28,s49,v28\nv_mul_f32 v29,s49,v29\n" \
"v_mul_f32 v30,s49,v30\nv_mul_f32 v31,s49,v31\nv_mul_f32 v32,s49,v32\nv_mul_f32 v33,s49,v33\nv_mul_f32 v34,s49,v34\nv_mul_f32 v35,s49,v35\n" \
"v_mul_f32 v36,s49,v36\nv_mul_f32 v37,s49,v37\nv_mul_f32 v38,s49,v38\nv_mul_f32 v39,s49,v39\nv_mul_f32 v40,s49,v40\nv_mul_f32 v41,s49,v41\n" \
"v_mul_f32 v42,s49,v42\nv_mul_f32 v43,s49,v43\nv_mul_f32 v44,s49,v44\nv_mul_f32 v45,s49,v45\nv_mul_f32 v46,s49,v46\nv_mul_f32 v47,s49,v47\n" \
"v_mul_f32 v48,s49,v48\nv_mul_f32 v49,s49,v49\nv_mul_f32 v50,s49,v50\nv_mul_f32 v51,s49,v51\nv_mul_f32 v52,s49,v52\nv_mul_f32 v53,s49,v53\n" \
"v_mul_f32 v54,s49,v54\nv_mul_f32 v55,s49,v55\nv_mul_f32 v56,s49,v56\nv_mul_f32 v57,s49,v57\nv_mul_f32 v58,s49,v58\nv_mul_f32 v59,s49,v59\n" \
"v_mul_f32 v60,s49,v60\nv_mul_f32 v61,s49,v61\nv_mul_f32 v62,s49,v62\nv_mul_f32 v63,s49,v63\nv_cvt_pk_bf16_f32 v64,v0,v1\nv_cvt_pk_bf16_f32 v65,v2,v3\n" \
"v_cvt_pk_bf16_f32 v66,v4,v5\nv_cvt_pk_bf16_f32 v67,v6,v7\nv_cvt_pk_bf16_f32 v68,v8,v9\nv_cvt_pk_bf16_f32 v69,v10,v11\nv_cvt_pk_bf16_f32 v70,v12,v13\n" \
"v_cvt_pk_bf16_f32 v71,v14,v15\nv_cvt_pk_bf16_f32 v72,v16,v17\nv_cvt_pk_bf16_f32 v73,v18,v19\nv_cvt_pk_bf16_f32 v74,v20,v21\n" \
"v_cvt_pk_bf16_f32 v75,v22,v23\nv_cvt_pk_bf16_f32 v76,v24,v25\nv_cvt_pk_bf16_f32 v77,v26,v27\nv_cvt_pk_bf16_f32 v78,v28,v29\n" \
"v_cvt_pk_bf16_f32 v79,v30,v31\nv_cvt_pk_bf16_f32 v80,v32,v33\nv_cvt_pk_bf16_f32 v81,v34,v35\nv_cvt_pk_bf16_f32 v82,v36,v37\n" \
"v_cvt_pk_bf16_f32 v83,v38,v39\nv_cvt_pk_bf16_f32 v84,v40,v41\nv_cvt_pk_bf16_f32 v85,v42,v43\nv_cvt_pk_bf16_f32 v86,v44,v45\n" \
"v_cvt_pk_bf16_f32 v87,v46,v47\nv_cvt_pk_bf16_f32 v88,v48,v49\nv_cvt_pk_bf16_f32 v89,v50,v51\nv_cvt_pk_bf16_f32 v90,v52,v53\n" \
"v_cvt_pk_bf16_f32 v91,v54,v55\nv_cvt_pk_bf16_f32 v92,v56,v57\nv_cvt_pk_bf16_f32 v93,v58,v59\nv_cvt_pk_bf16_f32 v94,v60,v61\n" \
"v_cvt_pk_bf16_f32 v95,v62,v63\nv_mov_b32 v0,0\nv_mov_b32 v1,0\nv_mov_b32 v2,0\nv_mov_b32 v3,0\nv_mov_b32 v4,0\nv_mov_b32 v5,0\nv_mov_b32 v6,0\n" \
"v_mov_b32 v7,0\nv_mov_b32 v8,0\nv_mov_b32 v9,0\nv_mov_b32 v10,0\nv_mov_b32 v11,0\nv_mov_b32 v12,0\nv_mov_b32 v13,0\nv_mov_b32 v14,0\nv_mov_b32 v15,0\n" \
"v_mov_b32 v16,0\nv_mov_b32 v17,0\nv_mov_b32 v18,0\nv_mov_b32 v19,0\nv_mov_b32 v20,0\nv_mov_b32 v21,0\nv_mov_b32 v22,0\nv_mov_b32 v23,0\n" \
"v_mov_b32 v24,0\nv_mov_b32 v25,0\nv_mov_b32 v26,0\nv_mov_b32 v27,0\nv_mov_b32 v28,0\nv_mov_b32 v29,0\nv_mov_b32 v30,0\nv_mov_b32 v31,0\n" \
"v_mov_b32 v32,0\nv_mov_b32 v33,0\nv_mov_b32 v34,0\nv_mov_b32 v35,0\nv_mov_b32 v36,0\nv_mov_b32 v37,0\nv_mov_b32 v38,0\nv_mov_b32 v39,0\n" \
"v_mov_b32 v40,0\nv_mov_b32 v41,0\nv_mov_b32 v42,0\nv_mov_b32 v43,0\nv_mov_b32 v44,0\nv_mov_b32 v45,0\nv_mov_b32 v46,0\nv_mov_b32 v47,0\n" \
"v_mov_b32 v48,0\nv_mov_b32 v49,0\nv_mov_b32 v50,0\nv_mov_b32 v51,0\nv_mov_b32 v52,0\nv_mov_b32 v53,0\nv_mov_b32 v54,0\nv_mov_b32 v55,0\n" \
"v_mov_b32 v56,0\nv_mov_b32 v57,0\nv_mov_b32 v58,0\nv_mov_b32 v59,0\nv_mov_b32 v60,0\nv_mov_b32 v61,0\nv_mov_b32 v62,0\nv_mov_b32 v63,0\n" \
"v_mov_b32 v230,v124\nv_mov_b32 v231,v125\nv_mov_b32 v249,v126\nv_mov_b32 v250,v127\nv_mov_b32 v251,v128\nv_mov_b32 v252,v129\nv_mov_b32 v255,v130\n" \
"v_mov_b32 v222,v131\nv_mov_b32 v128,v232\nv_mov_b32 v129,v233\nv_mov_b32 v130,v234\nv_mov_b32 v131,v235\nv_mov_b32 v132,v236\nv_mov_b32 v133,v237\n" \
"v_mov_b32 v134,v238\nv_mov_b32 v135,v239\nv_mov_b32 v136,v240\nv_mov_b32 v137,v241\nv_mov_b32 v138,v242\nv_mov_b32 v139,v243\nv_mov_b32 v140,v244\n" \
"v_mov_b32 v141,v245\nv_mov_b32 v142,v246\nv_mov_b32 v143,v247\nv_mov_b32 v216,0\nv_mov_b32 v232,v104\nv_mov_b32 v233,v105\nv_mov_b32 v234,v106\n" \
"v_mov_b32 v235,v107\nv_mov_b32 v236,v108\nv_mov_b32 v237,v109\nv_mov_b32 v238,v110\nv_mov_b32 v239,v111\nv_mov_b32 v240,v112\nv_mov_b32 v241,v113\n" \
"v_mov_b32 v242,v114\nv_mov_b32 v243,v115\nv_mov_b32 v244,v116\nv_mov_b32 v245,v117\nv_mov_b32 v246,v118\nv_mov_b32 v247,v119\nv_mov_b32 v226,v120\n" \
"v_mov_b32 v227,v121\nv_mov_b32 v228,v122\nv_mov_b32 v229,v123\ns_barrier\ns_mov_b32 s40,s72\ns_lshr_b32 s41,s73,1\ns_add_u32 s41,s41,s69\n" \
"s_mov_b32 s47,s74\nv_lshlrev_b32 v220,2,v248\nglobal_load_dword v221,v220,s[84:85]\nglobal_load_dword v221,v220,s[86:87]\n10:\ns_cmp_le_i32 s40,s41\n" \
"s_cbranch_scc1 11f\ns_barrier\ns_sub_u32 s40,s40,1\ns_branch 10b\n11:\ns_and_b32 s43,s40,3\ns_lshl_b32 s43,s43,15\nv_add_u32 v217,s43,v253\n" \
"v_mov_b32 v218,v217\ns_bitcmp1_b32 s73,0\ns_cbranch_scc1 12f\ns_sub_u32 s43,s40,1\ns_and_b32 s43,s43,3\ns_lshl_b32 s43,s43,15\n" \
"ds_read_b128 v[152:155],v217 offset:0\nds_read_b128 v[156:159],v217 offset:1024\nds_read_b128 v[160:163],v217 offset:2048\nv_add_u32 v219,s43,v253\n" \
"ds_read_b128 v[164:167],v217 offset:3072\nds_read_b128 v[168:171],v217 offset:4096\nds_read_b128 v[172:175],v217 offset:5120\n" \
"ds_read_b128 v[176:179],v217 offset:6144\nds_read_b128 v[180:183],v217 offset:7168\ns_waitcnt lgkmcnt(7)\n" \
"v_mfma_f32_32x32x16_bf16 v[96:111],v[152:155],v[64:67],v[128:143]\ns_waitcnt lgkmcnt(5)\n" \
"v_mfma_f32_32x32x16_bf16 v[96:111],v[156:159],v[68:71],v[96:111]\nv_mfma_f32_32x32x16_bf16 v[96:111],v[160:163],v[72:75],v[96:111]\ns_barrier\n" \
"s_waitcnt lgkmcnt(3)\nv_mfma_f32_32x32x16_bf16 v[96:111],v[164:167],v[76:79],v[96:111]\n" \
"v_mfma_f32_32x32x16_bf16 v[96:111],v[168:171],v[80:83],v[96:111]\ns_waitcnt lgkmcnt(1)\n" \
"v_mfma_f32_32x32x16_bf16 v[96:111],v[172:175],v[84:87],v[96:111]\nv_mfma_f32_32x32x16_bf16 v[96:111],v[176:179],v[88:91],v[96:111]\n" \
"s_waitcnt lgkmcnt(0)\nv_mfma_f32_32x32x16_bf16 v[96:111],v[180:183],v[92:95],v[96:111]\nds_read_b128 v[152:155],v219 offset:16384\n" \
"ds_read_b128 v[156:159],v219 offset:17408\ns_nop 7\ns_nop 7\nv_max3_f32 v223,v96,v97,v98\nv_max3_f32 v224,v99,v100,v101\n" \
"v_max3_f32 v223,v223,v102,v103\nv_max3_f32 v224,v224,v104,v105\nv_max3_f32 v223,v223,v106,v107\nv_max3_f32 v224,v224,v108,v109\n" \
"v_max3_f32 v223,v223,v110,v111\nv_max3_f32 v223,v223,v224,v224\nv_mov_b32 v224,v223\ns_nop 1\nv_permlane32_swap_b32 v223,v224\ns_nop 1\n" \
"v_max_f32 v223,v223,v224\nv_sub_f32 v96,v96,v223\nv_sub_f32 v97,v97,v223\nv_sub_f32 v98,v98,v223\nv_sub_f32 v99,v99,v223\nv_sub_f32 v100,v100,v223\n" \
"v_sub_f32 v101,v101,v223\nv_sub_f32 v102,v102,v223\nv_sub_f32 v103,v103,v223\nv_sub_f32 v104,v104,v223\nv_sub_f32 v105,v105,v223\n" \
"v_sub_f32 v106,v106,v223\nv_sub_f32 v107,v107,v223\nv_sub_f32 v108,v108,v223\nv_sub_f32 v109,v109,v223\nv_sub_f32 v110,v110,v223\n" \
"v_sub_f32 v111,v111,v223\nv_sub_f32 v128,0,v223\nv_sub_f32 v129,0,v223\nv_sub_f32 v130,0,v223\nv_sub_f32 v131,0,v223\nv_sub_f32 v132,0,v223\n" \
"v_sub_f32 v133,0,v223\nv_sub_f32 v134,0,v223\nv_sub_f32 v135,0,v223\nv_sub_f32 v136,0,v223\nv_sub_f32 v137,0,v223\nv_sub_f32 v138,0,v223\n" \
"v_sub_f32 v139,0,v223\nv_sub_f32 v140,0,v223\nv_sub_f32 v141,0,v223\nv_sub_f32 v142,0,v223\nv_sub_f32 v143,0,v223\ns_nop 1\nv_exp_f32 v96,v96\n" \
"ds_read_b128 v[160:163],v219 offset:18432\nv_exp_f32 v97,v97\nv_add_f32 v216,v216,v96\nds_read_b128 v[164:167],v219 offset:19456\nv_exp_f32 v98,v98\n" \
"v_add_f32 v216,v216,v97\nv_cvt_pk_bf16_f32 v144,v96,v97\nds_read_b128 v[168:171],v219 offset:20480\nv_exp_f32 v99,v99\nv_add_f32 v216,v216,v98\n" \
"v_mov_b32 v218,v217\nds_read_b128 v[172:175],v219 offset:21504\nv_exp_f32 v100,v100\nv_add_f32 v216,v216,v99\nv_cvt_pk_bf16_f32 v145,v98,v99\n" \
"s_sub_u32 s40,s40,1\nds_read_b128 v[176:179],v219 offset:22528\nv_exp_f32 v101,v101\nv_add_f32 v216,v216,v100\ns_cmp_ge_i32 s40,s69\n" \
"ds_read_b128 v[180:183],v219 offset:23552\nv_exp_f32 v102,v102\nv_add_f32 v216,v216,v101\nv_cvt_pk_bf16_f32 v146,v100,v101\nv_mov_b32 v217,v219\n" \
"s_branch 13f\n12:\nds_read_b128 v[152:155],v217 offset:16384\nds_read_b128 v[156:159],v217 offset:17408\nds_read_b128 v[160:163],v217 offset:18432\n" \
"ds_read_b128 v[164:167],v217 offset:19456\nds_read_b128 v[168:171],v217 offset:20480\nds_read_b128 v[172:175],v217 offset:21504\n" \
"ds_read_b128 v[176:179],v217 offset:22528\nds_read_b128 v[180:183],v217 offset:23552\ns_waitcnt lgkmcnt(7)\n" \
"v_mfma_f32_32x32x16_bf16 v[112:127],v[152:155],v[64:67],v[128:143]\ns_waitcnt lgkmcnt(5)\n" \
"v_mfma_f32_32x32x16_bf16 v[112:127],v[156:159],v[68:71],v[112:127]\ns_sub_u32 s43,s40,1\n" \
"v_mfma_f32_32x32x16_bf16 v[112:127],v[160:163],v[72:75],v[112:127]\ns_and_b32 s43,s43,3\ns_waitcnt lgkmcnt(3)\n" \
"v_mfma_f32_32x32x16_bf16 v[112:127],v[164:167],v[76:79],v[112:127]\ns_lshl_b32 s43,s43,15\n" \
"v_mfma_f32_32x32x16_bf16 v[112:127],v[168:171],v[80:83],v[112:127]\ns_waitcnt lgkmcnt(1)\n" \
"v_mfma_f32_32x32x16_bf16 v[112:127],v[172:175],v[84:87],v[112:127]\nv_mfma_f32_32x32x16_bf16 v[112:127],v[176:179],v[88:91],v[112:127]\n" \
"s_waitcnt lgkmcnt(0)\nv_mfma_f32_32x32x16_bf16 v[112:127],v[180:183],v[92:95],v[112:127]\nds_read_b128 v[152:155],v217 offset:0\n" \
"ds_read_b128 v[156:159],v217 offset:1024\ns_nop 7\ns_nop 7\nv_max3_f32 v223,v112,v113,v114\nv_max3_f32 v224,v115,v116,v117\n" \
"v_max3_f32 v223,v223,v118,v119\nv_max3_f32 v224,v224,v120,v121\nv_max3_f32 v223,v223,v122,v123\nv_max3_f32 v224,v224,v124,v125\n" \
"v_max3_f32 v223,v223,v126,v127\nv_max3_f32 v223,v223,v224,v224\nv_mov_b32 v224,v223\ns_nop 1\nv_permlane32_swap_b32 v223,v224\ns_nop 1\n" \
"v_max_f32 v223,v223,v224\nv_sub_f32 v112,v112,v223\nv_sub_f32 v113,v113,v223\nv_sub_f32 v114,v114,v223\nv_sub_f32 v115,v115,v223\n" \
"v_sub_f32 v116,v116,v223\nv_sub_f32 v117,v117,v223\nv_sub_f32 v118,v118,v223\nv_sub_f32 v119,v119,v223\nv_sub_f32 v120,v120,v223\n" \
"v_sub_f32 v121,v121,v223\nv_sub_f32 v122,v122,v223\nv_sub_f32 v123,v123,v223\nv_sub_f32 v124,v124,v223\nv_sub_f32 v125,v125,v223\n" \
"v_sub_f32 v126,v126,v223\nv_sub_f32 v127,v127,v223\nv_sub_f32 v128,0,v223\nv_sub_f32 v129,0,v223\nv_sub_f32 v130,0,v223\nv_sub_f32 v131,0,v223\n" \
"v_sub_f32 v132,0,v223\nv_sub_f32 v133,0,v223\nv_sub_f32 v134,0,v223\nv_sub_f32 v135,0,v223\nv_sub_f32 v136,0,v223\nv_sub_f32 v137,0,v223\n" \
"v_sub_f32 v138,0,v223\nv_sub_f32 v139,0,v223\nv_sub_f32 v140,0,v223\nv_sub_f32 v141,0,v223\nv_sub_f32 v142,0,v223\nv_sub_f32 v143,0,v223\ns_nop 1\n" \
"v_exp_f32 v112,v112\nds_read_b128 v[160:163],v217 offset:2048\nv_exp_f32 v113,v113\nv_add_f32 v216,v216,v112\nv_add_u32 v219,s43,v253\n" \
"ds_read_b128 v[164:167],v217 offset:3072\nv_exp_f32 v114,v114\nv_add_f32 v216,v216,v113\nv_cvt_pk_bf16_f32 v144,v112,v113\n" \
"ds_read_b128 v[168:171],v217 offset:4096\nv_exp_f32 v115,v115\nv_add_f32 v216,v216,v114\nds_read_b128 v[172:175],v217 offset:5120\nv_exp_f32 v116,v116\n" \
"v_add_f32 v216,v216,v115\nv_cvt_pk_bf16_f32 v145,v114,v115\nds_read_b128 v[176:179],v217 offset:6144\nv_exp_f32 v117,v117\nv_add_f32 v216,v216,v116\n" \
"ds_read_b128 v[180:183],v217 offset:7168\nv_exp_f32 v118,v118\nv_add_f32 v216,v216,v117\nv_cvt_pk_bf16_f32 v146,v116,v117\ns_waitcnt lgkmcnt(7)\n" \
"v_mfma_f32_32x32x16_bf16 v[96:111],v[152:155],v[64:67],v[128:143]\nds_read_b128 v[184:187],v217 offset:24576\nv_exp_f32 v119,v119\n" \
"v_add_f32 v216,v216,v118\ns_waitcnt lgkmcnt(6)\nv_mfma_f32_32x32x16_bf16 v[96:111],v[156:159],v[68:71],v[96:111]\n" \
"ds_read_b128 v[188:191],v217 offset:25600\nv_exp_f32 v120,v120\nv_add_f32 v216,v216,v119\nv_cvt_pk_bf16_f32 v147,v118,v119\n" \
"v_mfma_f32_32x32x16_bf16 v[96:111],v[160:163],v[72:75],v[96:111]\nds_read_b128 v[192:195],v217 offset:26624\nv_exp_f32 v121,v121\n" \
"v_add_f32 v216,v216,v120\ns_barrier\ns_waitcnt lgkmcnt(6)\nv_mfma_f32_32x32x16_bf16 v[96:111],v[164:167],v[76:79],v[96:111]\n" \
"ds_read_b128 v[196:199],v217 offset:27648\nv_exp_f32 v122,v122\nv_add_f32 v216,v216,v121\nv_cvt_pk_bf16_f32 v148,v120,v121\n" \
"v_mfma_f32_32x32x16_bf16 v[96:111],v[168:171],v[80:83],v[96:111]\nds_read_b128 v[200:203],v217 offset:28672\nv_exp_f32 v123,v123\n" \
"v_add_f32 v216,v216,v122\ns_waitcnt lgkmcnt(6)\nv_mfma_f32_32x32x16_bf16 v[96:111],v[172:175],v[84:87],v[96:111]\n" \
"ds_read_b128 v[204:207],v217 offset:29696\nv_exp_f32 v124,v124\nv_add_f32 v216,v216,v123\nv_cvt_pk_bf16_f32 v149,v122,v123\n" \
"v_mfma_f32_32x32x16_bf16 v[96:111],v[176:179],v[88:91],v[96:111]\nds_read_b128 v[208:211],v217 offset:30720\nv_exp_f32 v125,v125\n" \
"v_add_f32 v216,v216,v124\ns_waitcnt lgkmcnt(6)\nv_mfma_f32_32x32x16_bf16 v[96:111],v[180:183],v[92:95],v[96:111]\n" \
"ds_read_b128 v[212:215],v217 offset:31744\nv_exp_f32 v126,v126\nv_add_f32 v216,v216,v125\nv_cvt_pk_bf16_f32 v150,v124,v125\n" \
"v_mfma_f32_32x32x16_bf16 v[0:15],v[184:187],v[144:147],v[0:15]\nds_read_b128 v[152:155],v219 offset:16384\nv_exp_f32 v127,v127\n" \
"v_add_f32 v216,v216,v126\ns_waitcnt lgkmcnt(6)\nv_mfma_f32_32x32x16_bf16 v[16:31],v[188:191],v[144:147],v[16:31]\n" \
"ds_read_b128 v[156:159],v219 offset:17408\nv_exp_f32 v96,v96\nv_add_f32 v216,v216,v127\nv_cvt_pk_bf16_f32 v151,v126,v127\n" \
"v_mfma_f32_32x32x16_bf16 v[32:47],v[192:195],v[144:147],v[32:47]\nds_read_b128 v[160:163],v219 offset:18432\nv_exp_f32 v97,v97\n" \
"v_add_f32 v216,v216,v96\ns_waitcnt lgkmcnt(6)\nv_mfma_f32_32x32x16_bf16 v[48:63],v[196:199],v[144:147],v[48:63]\n" \
"ds_read_b128 v[164:167],v219 offset:19456\nv_exp_f32 v98,v98\nv_add_f32 v216,v216,v97\nv_cvt_pk_bf16_f32 v144,v96,v97\n" \
"v_mfma_f32_32x32x16_bf16 v[0:15],v[200:203],v[148:151],v[0:15]\nds_read_b128 v[168:171],v219 offset:20480\nv_exp_f32 v99,v99\nv_add_f32 v216,v216,v98\n" \
"v_mov_b32 v218,v217\ns_waitcnt lgkmcnt(6)\nv_mfma_f32_32x32x16_bf16 v[16:31],v[204:207],v[148:151],v[16:31]\nds_read_b128 v[172:175],v219 offset:21504\n" \
"v_exp_f32 v100,v100\nv_add_f32 v216,v216,v99\nv_cvt_pk_bf16_f32 v145,v98,v99\ns_sub_u32 s40,s40,1\n" \
"v_mfma_f32_32x32x16_bf16 v[32:47],v[208:211],v[148:151],v[32:47]\nds_read_b128 v[176:179],v219 offset:22528\nv_exp_f32 v101,v101\n" \
"v_add_f32 v216,v216,v100\ns_cmp_ge_i32 s40,s69\ns_waitcnt lgkmcnt(7)\nv_mfma_f32_32x32x16_bf16 v[48:63],v[212:215],v[148:151],v[48:63]\n" \
"ds_read_b128 v[180:183],v219 offset:23552\nv_exp_f32 v102,v102\nv_add_f32 v216,v216,v101\nv_cvt_pk_bf16_f32 v146,v100,v101\nv_mov_b32 v217,v219\n13:\n" \
"s_cbranch_scc0 15f\n14:\ns_waitcnt lgkmcnt(7)\nv_mfma_f32_32x32x16_bf16 v[112:127],v[152:155],v[64:67],v[128:143]\n" \
"ds_read_b128 v[184:187],v218 offset:8192\nv_exp_f32 v103,v103\nv_add_f32 v216,v216,v102\ns_waitcnt lgkmcnt(6)\n" \
"v_mfma_f32_32x32x16_bf16 v[112:127],v[156:159],v[68:71],v[112:127]\nds_read_b128 v[188:191],v218 offset:9216\nv_exp_f32 v104,v104\n" \
"v_add_f32 v216,v216,v103\nv_cvt_pk_bf16_f32 v147,v102,v103\ns_sub_u32 s43,s40,1\nv_mfma_f32_32x32x16_bf16 v[112:127],v[160:163],v[72:75],v[112:127]\n" \
"ds_read_b128 v[192:195],v218 offset:10240\nv_exp_f32 v105,v105\nv_add_f32 v216,v216,v104\ns_and_b32 s43,s43,3\ns_waitcnt lgkmcnt(6)\n" \
"v_mfma_f32_32x32x16_bf16 v[112:127],v[164:167],v[76:79],v[112:127]\nds_read_b128 v[196:199],v218 offset:11264\nv_exp_f32 v106,v106\n" \
"v_add_f32 v216,v216,v105\nv_cvt_pk_bf16_f32 v148,v104,v105\ns_lshl_b32 s43,s43,15\nv_mfma_f32_32x32x16_bf16 v[112:127],v[168:171],v[80:83],v[112:127]\n" \
"ds_read_b128 v[200:203],v218 offset:12288\nv_exp_f32 v107,v107\nv_add_f32 v216,v216,v106\ns_waitcnt lgkmcnt(6)\n" \
"v_mfma_f32_32x32x16_bf16 v[112:127],v[172:175],v[84:87],v[112:127]\nds_read_b128 v[204:207],v218 offset:13312\nv_exp_f32 v108,v108\n" \
"v_add_f32 v216,v216,v107\nv_cvt_pk_bf16_f32 v149,v106,v107\nv_mfma_f32_32x32x16_bf16 v[112:127],v[176:179],v[88:91],v[112:127]\n" \
"ds_read_b128 v[208:211],v218 offset:14336\nv_exp_f32 v109,v109\nv_add_f32 v216,v216,v108\ns_waitcnt lgkmcnt(6)\n" \
"v_mfma_f32_32x32x16_bf16 v[112:127],v[180:183],v[92:95],v[112:127]\nds_read_b128 v[212:215],v218 offset:15360\nv_exp_f32 v110,v110\n" \
"v_add_f32 v216,v216,v109\nv_cvt_pk_bf16_f32 v150,v108,v109\nv_mfma_f32_32x32x16_bf16 v[0:15],v[184:187],v[144:147],v[0:15]\n" \
"ds_read_b128 v[152:155],v217 offset:0\nv_exp_f32 v111,v111\nv_add_f32 v216,v216,v110\ns_waitcnt lgkmcnt(6)\n" \
"v_mfma_f32_32x32x16_bf16 v[16:31],v[188:191],v[144:147],v[16:31]\nds_read_b128 v[156:159],v217 offset:1024\nv_exp_f32 v112,v112\n" \
"v_add_f32 v216,v216,v111\nv_cvt_pk_bf16_f32 v151,v110,v111\nv_mfma_f32_32x32x16_bf16 v[32:47],v[192:195],v[144:147],v[32:47]\n" \
"ds_read_b128 v[160:163],v217 offset:2048\nv_exp_f32 v113,v113\nv_add_f32 v216,v216,v112\nv_add_u32 v219,s43,v253\ns_waitcnt lgkmcnt(6)\n" \
"v_mfma_f32_32x32x16_bf16 v[48:63],v[196:199],v[144:147],v[48:63]\nds_read_b128 v[164:167],v217 offset:3072\nv_exp_f32 v114,v114\n" \
"v_add_f32 v216,v216,v113\nv_cvt_pk_bf16_f32 v144,v112,v113\nv_mfma_f32_32x32x16_bf16 v[0:15],v[200:203],v[148:151],v[0:15]\n" \
"ds_read_b128 v[168:171],v217 offset:4096\nv_exp_f32 v115,v115\nv_add_f32 v216,v216,v114\ns_waitcnt lgkmcnt(6)\n" \
"v_mfma_f32_32x32x16_bf16 v[16:31],v[204:207],v[148:151],v[16:31]\nds_read_b128 v[172:175],v217 offset:5120\nv_exp_f32 v116,v116\n" \
"v_add_f32 v216,v216,v115\nv_cvt_pk_bf16_f32 v145,v114,v115\nv_mfma_f32_32x32x16_bf16 v[32:47],v[208:211],v[148:151],v[32:47]\n" \
"ds_read_b128 v[176:179],v217 offset:6144\nv_exp_f32 v117,v117\nv_add_f32 v216,v216,v116\ns_waitcnt lgkmcnt(6)\n" \
"v_mfma_f32_32x32x16_bf16 v[48:63],v[212:215],v[148:151],v[48:63]\nds_read_b128 v[180:183],v217 offset:7168\nv_exp_f32 v118,v118\n" \
"v_add_f32 v216,v216,v117\nv_cvt_pk_bf16_f32 v146,v116,v117\nv_mfma_f32_32x32x16_bf16 v[96:111],v[152:155],v[64:67],v[128:143]\n" \
"ds_read_b128 v[184:187],v217 offset:24576\nv_exp_f32 v119,v119\nv_add_f32 v216,v216,v118\ns_waitcnt lgkmcnt(6)\n" \
"v_mfma_f32_32x32x16_bf16 v[96:111],v[156:159],v[68:71],v[96:111]\nds_read_b128 v[188:191],v217 offset:25600\nv_exp_f32 v120,v120\n" \
"v_add_f32 v216,v216,v119\nv_cvt_pk_bf16_f32 v147,v118,v119\nv_mfma_f32_32x32x16_bf16 v[96:111],v[160:163],v[72:75],v[96:111]\n" \
"ds_read_b128 v[192:195],v217 offset:26624\nv_exp_f32 v121,v121\nv_add_f32 v216,v216,v120\ns_barrier\ns_waitcnt lgkmcnt(6)\n" \
"v_mfma_f32_32x32x16_bf16 v[96:111],v[164:167],v[76:79],v[96:111]\nds_read_b128 v[196:199],v217 offset:27648\nv_exp_f32 v122,v122\n" \
"v_add_f32 v216,v216,v121\nv_cvt_pk_bf16_f32 v148,v120,v121\nv_mfma_f32_32x32x16_bf16 v[96:111],v[168:171],v[80:83],v[96:111]\n" \
"ds_read_b128 v[200:203],v217 offset:28672\nv_exp_f32 v123,v123\nv_add_f32 v216,v216,v122\ns_waitcnt lgkmcnt(6)\n" \
"v_mfma_f32_32x32x16_bf16 v[96:111],v[172:175],v[84:87],v[96:111]\nds_read_b128 v[204:207],v217 offset:29696\nv_exp_f32 v124,v124\n" \
"v_add_f32 v216,v216,v123\nv_cvt_pk_bf16_f32 v149,v122,v123\nv_mfma_f32_32x32x16_bf16 v[96:111],v[176:179],v[88:91],v[96:111]\n" \
"ds_read_b128 v[208:211],v217 offset:30720\nv_exp_f32 v125,v125\nv_add_f32 v216,v216,v124\ns_waitcnt lgkmcnt(6)\n" \
"v_mfma_f32_32x32x16_bf16 v[96:111],v[180:183],v[92:95],v[96:111]\nds_read_b128 v[212:215],v217 offset:31744\nv_exp_f32 v126,v126\n" \
"v_add_f32 v216,v216,v125\nv_cvt_pk_bf16_f32 v150,v124,v125\nv_mfma_f32_32x32x16_bf16 v[0:15],v[184:187],v[144:147],v[0:15]\n" \
"ds_read_b128 v[152:155],v219 offset:16384\nv_exp_f32 v127,v127\nv_add_f32 v216,v216,v126\ns_waitcnt lgkmcnt(6)\n" \
"v_mfma_f32_32x32x16_bf16 v[16:31],v[188:191],v[144:147],v[16:31]\nds_read_b128 v[156:159],v219 offset:17408\nv_exp_f32 v96,v96\n" \
"v_add_f32 v216,v216,v127\nv_cvt_pk_bf16_f32 v151,v126,v127\nv_mfma_f32_32x32x16_bf16 v[32:47],v[192:195],v[144:147],v[32:47]\n" \
"ds_read_b128 v[160:163],v219 offset:18432\nv_exp_f32 v97,v97\nv_add_f32 v216,v216,v96\ns_waitcnt lgkmcnt(6)\n" \
"v_mfma_f32_32x32x16_bf16 v[48:63],v[196:199],v[144:147],v[48:63]\nds_read_b128 v[164:167],v219 offset:19456\nv_exp_f32 v98,v98\n" \
"v_add_f32 v216,v216,v97\nv_cvt_pk_bf16_f32 v144,v96,v97\nv_mfma_f32_32x32x16_bf16 v[0:15],v[200:203],v[148:151],v[0:15]\n" \
"ds_read_b128 v[168:171],v219 offset:20480\nv_exp_f32 v99,v99\nv_add_f32 v216,v216,v98\nv_mov_b32 v218,v217\ns_waitcnt lgkmcnt(6)\n" \
"v_mfma_f32_32x32x16_bf16 v[16:31],v[204:207],v[148:151],v[16:31]\nds_read_b128 v[172:175],v219 offset:21504\nv_exp_f32 v100,v100\n" \
"v_add_f32 v216,v216,v99\nv_cvt_pk_bf16_f32 v145,v98,v99\ns_sub_u32 s40,s40,1\nv_mfma_f32_32x32x16_bf16 v[32:47],v[208:211],v[148:151],v[32:47]\n" \
"ds_read_b128 v[176:179],v219 offset:22528\nv_exp_f32 v101,v101\nv_add_f32 v216,v216,v100\ns_cmp_ge_i32 s40,s69\ns_waitcnt lgkmcnt(7)\n" \
"v_mfma_f32_32x32x16_bf16 v[48:63],v[212:215],v[148:151],v[48:63]\nds_read_b128 v[180:183],v219 offset:23552\nv_exp_f32 v102,v102\n" \
"v_add_f32 v216,v216,v101\nv_cvt_pk_bf16_f32 v146,v100,v101\nv_mov_b32 v217,v219\ns_cbranch_scc1 14b\n15:\nv_lshrrev_b32 v114,2,v248\n" \
"v_and_b32 v114,0x7c,v114\ns_lshl_b32 s49,s73,7\ns_add_u32 s49,s49,73662464\nbuffer_load_dword v112,v114,%[sws],s49 offen\n" \
"buffer_load_dword v113,v114,%[sws],s82 offen\nds_read_b128 v[184:187],v218 offset:8192\nv_exp_f32 v103,v103\nv_add_f32 v216,v216,v102\n" \
"ds_read_b128 v[188:191],v218 offset:9216\nv_exp_f32 v104,v104\nv_add_f32 v216,v216,v103\nv_cvt_pk_bf16_f32 v147,v102,v103\n" \
"ds_read_b128 v[192:195],v218 offset:10240\nv_exp_f32 v105,v105\nv_add_f32 v216,v216,v104\nds_read_b128 v[196:199],v218 offset:11264\n" \
"v_exp_f32 v106,v106\nv_add_f32 v216,v216,v105\nv_cvt_pk_bf16_f32 v148,v104,v105\nds_read_b128 v[200:203],v218 offset:12288\nv_exp_f32 v107,v107\n" \
"v_add_f32 v216,v216,v106\nds_read_b128 v[204:207],v218 offset:13312\nv_exp_f32 v108,v108\nv_add_f32 v216,v216,v107\nv_cvt_pk_bf16_f32 v149,v106,v107\n" \
"ds_read_b128 v[208:211],v218 offset:14336\nv_exp_f32 v109,v109\nv_add_f32 v216,v216,v108\nds_read_b128 v[212:215],v218 offset:15360\n" \
"v_exp_f32 v110,v110\nv_add_f32 v216,v216,v109\nv_cvt_pk_bf16_f32 v150,v108,v109\ns_waitcnt lgkmcnt(7)\n" \
"v_mfma_f32_32x32x16_bf16 v[0:15],v[184:187],v[144:147],v[0:15]\nv_exp_f32 v111,v111\nv_add_f32 v216,v216,v110\ns_waitcnt lgkmcnt(5)\n" \
"v_mfma_f32_32x32x16_bf16 v[16:31],v[188:191],v[144:147],v[16:31]\nv_add_f32 v216,v216,v111\nv_cvt_pk_bf16_f32 v151,v110,v111\n" \
"v_mfma_f32_32x32x16_bf16 v[32:47],v[192:195],v[144:147],v[32:47]\ns_waitcnt lgkmcnt(3)\n" \
"v_mfma_f32_32x32x16_bf16 v[48:63],v[196:199],v[144:147],v[48:63]\nv_mfma_f32_32x32x16_bf16 v[0:15],v[200:203],v[148:151],v[0:15]\ns_waitcnt lgkmcnt(1)\n" \
"v_mfma_f32_32x32x16_bf16 v[16:31],v[204:207],v[148:151],v[16:31]\nv_mfma_f32_32x32x16_bf16 v[32:47],v[208:211],v[148:151],v[32:47]\n" \
"s_waitcnt lgkmcnt(0)\nv_mfma_f32_32x32x16_bf16 v[48:63],v[212:215],v[148:151],v[48:63]\ns_waitcnt vmcnt(0) lgkmcnt(0)\ns_nop 7\ns_nop 7\n"

#define K12B_LOADER \
"s_mov_b32 s59,s66\ns_mov_b32 s52,s64\ns_add_u32 s53,s64,8192\ns_add_u32 s54,s64,16384\ns_add_u32 s55,s64,24576\ns_mov_b32 s56,s65\n" \
"s_add_u32 s57,s65,32768\ns_mov_b32 m0,s59\ns_nop 0\nbuffer_load_dwordx4 v228,%[sx],s52 offen lds\ns_add_u32 m0,m0,1024\ns_nop 0\n" \
"buffer_load_dwordx4 v228,%[sx],s53 offen lds\ns_add_u32 m0,m0,1024\ns_nop 0\nbuffer_load_dwordx4 v229,%[sx],s54 offen lds\ns_add_u32 m0,m0,1024\n" \
"s_nop 0\nbuffer_load_dwordx4 v229,%[sx],s55 offen lds\ns_add_u32 m0,m0,29696\ns_nop 0\nbuffer_load_dwordx4 v230,%[sk],s56 offen lds\n" \
"s_add_u32 m0,m0,1024\ns_nop 0\nbuffer_load_dwordx4 v230,%[sv],s56 offen lds\ns_add_u32 m0,m0,1024\ns_nop 0\n" \
"buffer_load_dwordx4 v230,%[sk],s57 offen lds\ns_add_u32 m0,m0,1024\ns_nop 0\nbuffer_load_dwordx4 v230,%[sv],s57 offen lds\nv_add_u32 v228,128,v228\n" \
"v_add_u32 v229,128,v229\nv_add_u32 v230,0x80000,v230\ns_xor_b32 s59,s59,0x10000\ns_mov_b32 m0,s59\ns_nop 0\n" \
"buffer_load_dwordx4 v228,%[sx],s52 offen lds\ns_add_u32 m0,m0,1024\ns_nop 0\nbuffer_load_dwordx4 v228,%[sx],s53 offen lds\ns_add_u32 m0,m0,1024\n" \
"s_nop 0\nbuffer_load_dwordx4 v229,%[sx],s54 offen lds\ns_add_u32 m0,m0,1024\ns_nop 0\nbuffer_load_dwordx4 v229,%[sx],s55 offen lds\n" \
"s_add_u32 m0,m0,29696\ns_nop 0\nbuffer_load_dwordx4 v230,%[sk],s56 offen lds\ns_add_u32 m0,m0,1024\ns_nop 0\n" \
"buffer_load_dwordx4 v230,%[sv],s56 offen lds\ns_add_u32 m0,m0,1024\ns_nop 0\nbuffer_load_dwordx4 v230,%[sk],s57 offen lds\ns_add_u32 m0,m0,1024\n" \
"s_nop 0\nbuffer_load_dwordx4 v230,%[sv],s57 offen lds\nv_add_u32 v228,128,v228\nv_add_u32 v229,128,v229\nv_add_u32 v230,0x80000,v230\n" \
"s_xor_b32 s59,s59,0x10000\ns_waitcnt vmcnt(8)\ns_barrier\nds_read_b64_tr_b16 v[160:161],v226 offset:0\nds_read_b64_tr_b16 v[162:163],v226 offset:2048\n" \
"ds_read_b64_tr_b16 v[164:165],v227 offset:0\nds_read_b64_tr_b16 v[166:167],v227 offset:2048\nds_read_b64_tr_b16 v[168:169],v226 offset:256\n" \
"ds_read_b64_tr_b16 v[170:171],v226 offset:2304\nds_read_b64_tr_b16 v[172:173],v227 offset:256\nds_read_b64_tr_b16 v[174:175],v227 offset:2304\n" \
"ds_read_b128 v[128:131],v224 offset:0\nds_read_b128 v[132:135],v224 offset:1024\nds_read_b128 v[136:139],v224 offset:4096\n" \
"ds_read_b128 v[140:143],v224 offset:5120\nds_read_b128 v[144:147],v224 offset:8192\nds_read_b128 v[148:151],v224 offset:9216\n" \
"ds_read_b128 v[152:155],v224 offset:12288\nds_read_b128 v[156:159],v224 offset:13312\ns_waitcnt lgkmcnt(7)\n" \
"v_mfma_f32_16x16x32_bf16 v[0:3],v[128:131],v[160:163],0\nds_read_b64_tr_b16 v[176:177],v226 offset:16384\n" \
"v_mfma_f32_16x16x32_bf16 v[4:7],v[128:131],v[164:167],0\nv_mfma_f32_16x16x32_bf16 v[8:11],v[128:131],v[168:171],0\n" \
"v_mfma_f32_16x16x32_bf16 v[12:15],v[128:131],v[172:175],0\nds_read_b128 v[128:131],v225 offset:0\nds_read_b64_tr_b16 v[178:179],v226 offset:18432\n" \
"s_waitcnt lgkmcnt(9)\nv_mfma_f32_16x16x32_bf16 v[16:19],v[132:135],v[160:163],0\nv_mfma_f32_16x16x32_bf16 v[20:23],v[132:135],v[164:167],0\n" \
"v_mfma_f32_16x16x32_bf16 v[24:27],v[132:135],v[168:171],0\nds_read_b64_tr_b16 v[180:181],v227 offset:16384\n" \
"v_mfma_f32_16x16x32_bf16 v[28:31],v[132:135],v[172:175],0\nds_read_b128 v[132:135],v225 offset:1024\ns_waitcnt lgkmcnt(10)\n" \
"v_mfma_f32_16x16x32_bf16 v[32:35],v[136:139],v[160:163],0\nv_mfma_f32_16x16x32_bf16 v[36:39],v[136:139],v[164:167],0\n" \
"ds_read_b64_tr_b16 v[182:183],v227 offset:18432\nv_mfma_f32_16x16x32_bf16 v[40:43],v[136:139],v[168:171],0\n" \
"v_mfma_f32_16x16x32_bf16 v[44:47],v[136:139],v[172:175],0\nds_read_b128 v[136:139],v225 offset:4096\ns_waitcnt lgkmcnt(11)\n" \
"v_mfma_f32_16x16x32_bf16 v[48:51],v[140:143],v[160:163],0\nds_read_b64_tr_b16 v[184:185],v226 offset:16640\n" \
"v_mfma_f32_16x16x32_bf16 v[52:55],v[140:143],v[164:167],0\nv_mfma_f32_16x16x32_bf16 v[56:59],v[140:143],v[168:171],0\n" \
"v_mfma_f32_16x16x32_bf16 v[60:63],v[140:143],v[172:175],0\nds_read_b128 v[140:143],v225 offset:5120\nds_read_b64_tr_b16 v[186:187],v226 offset:18688\n" \
"s_waitcnt lgkmcnt(13)\nv_mfma_f32_16x16x32_bf16 v[64:67],v[144:147],v[160:163],0\nv_mfma_f32_16x16x32_bf16 v[68:71],v[144:147],v[164:167],0\n" \
"v_mfma_f32_16x16x32_bf16 v[72:75],v[144:147],v[168:171],0\nds_read_b64_tr_b16 v[188:189],v227 offset:16640\n" \
"v_mfma_f32_16x16x32_bf16 v[76:79],v[144:147],v[172:175],0\nds_read_b128 v[144:147],v225 offset:8192\ns_waitcnt lgkmcnt(14)\n" \
"v_mfma_f32_16x16x32_bf16 v[80:83],v[148:151],v[160:163],0\nv_mfma_f32_16x16x32_bf16 v[84:87],v[148:151],v[164:167],0\n" \
"ds_read_b64_tr_b16 v[190:191],v227 offset:18688\nv_mfma_f32_16x16x32_bf16 v[88:91],v[148:151],v[168:171],0\n" \
"v_mfma_f32_16x16x32_bf16 v[92:95],v[148:151],v[172:175],0\nds_read_b128 v[148:151],v225 offset:9216\ns_waitcnt lgkmcnt(15)\n" \
"v_mfma_f32_16x16x32_bf16 v[96:99],v[152:155],v[160:163],0\nv_mfma_f32_16x16x32_bf16 v[100:103],v[152:155],v[164:167],0\n" \
"v_mfma_f32_16x16x32_bf16 v[104:107],v[152:155],v[168:171],0\nv_mfma_f32_16x16x32_bf16 v[108:111],v[152:155],v[172:175],0\n" \
"ds_read_b128 v[152:155],v225 offset:12288\ns_waitcnt lgkmcnt(15)\nv_mfma_f32_16x16x32_bf16 v[112:115],v[156:159],v[160:163],0\n" \
"v_mfma_f32_16x16x32_bf16 v[116:119],v[156:159],v[164:167],0\nv_mfma_f32_16x16x32_bf16 v[120:123],v[156:159],v[168:171],0\n" \
"v_mfma_f32_16x16x32_bf16 v[124:127],v[156:159],v[172:175],0\nds_read_b128 v[156:159],v225 offset:13312\ns_waitcnt lgkmcnt(13)\n" \
"v_mfma_f32_16x16x32_bf16 v[0:3],v[128:131],v[176:179],v[0:3]\ns_waitcnt lgkmcnt(10)\nv_mfma_f32_16x16x32_bf16 v[4:7],v[128:131],v[180:183],v[4:7]\n" \
"s_waitcnt lgkmcnt(6)\nv_mfma_f32_16x16x32_bf16 v[8:11],v[128:131],v[184:187],v[8:11]\ns_waitcnt lgkmcnt(3)\n" \
"v_mfma_f32_16x16x32_bf16 v[12:15],v[128:131],v[188:191],v[12:15]\ns_waitcnt vmcnt(0) lgkmcnt(0)\ns_barrier\nv_xor_b32 v224,0x10000,v224\n" \
"v_xor_b32 v225,0x10000,v225\nv_xor_b32 v226,0x10000,v226\nv_xor_b32 v227,0x10000,v227\n" \
"v_mfma_f32_16x16x32_bf16 v[16:19],v[132:135],v[176:179],v[16:19]\nds_read_b128 v[128:131],v224 offset:0\n" \
"v_mfma_f32_16x16x32_bf16 v[20:23],v[132:135],v[180:183],v[20:23]\nds_read_b64_tr_b16 v[160:161],v226 offset:0\ns_mov_b32 m0,s59\ns_nop 0\n" \
"buffer_load_dwordx4 v228,%[sx],s52 offen lds\nv_mfma_f32_16x16x32_bf16 v[24:27],v[132:135],v[184:187],v[24:27]\n" \
"v_mfma_f32_16x16x32_bf16 v[28:31],v[132:135],v[188:191],v[28:31]\nds_read_b128 v[132:135],v224 offset:1024\n" \
"v_mfma_f32_16x16x32_bf16 v[32:35],v[136:139],v[176:179],v[32:35]\nds_read_b64_tr_b16 v[162:163],v226 offset:2048\ns_add_u32 m0,m0,1024\ns_nop 0\n" \
"buffer_load_dwordx4 v228,%[sx],s53 offen lds\nv_mfma_f32_16x16x32_bf16 v[36:39],v[136:139],v[180:183],v[36:39]\n" \
"v_mfma_f32_16x16x32_bf16 v[40:43],v[136:139],v[184:187],v[40:43]\nv_mfma_f32_16x16x32_bf16 v[44:47],v[136:139],v[188:191],v[44:47]\n" \
"ds_read_b128 v[136:139],v224 offset:4096\nds_read_b64_tr_b16 v[164:165],v227 offset:0\ns_add_u32 m0,m0,1024\ns_nop 0\n" \
"buffer_load_dwordx4 v229,%[sx],s54 offen lds\nv_mfma_f32_16x16x32_bf16 v[48:51],v[140:143],v[176:179],v[48:51]\n" \
"v_mfma_f32_16x16x32_bf16 v[52:55],v[140:143],v[180:183],v[52:55]\nv_mfma_f32_16x16x32_bf16 v[56:59],v[140:143],v[184:187],v[56:59]\n" \
"ds_read_b64_tr_b16 v[166:167],v227 offset:2048\ns_add_u32 m0,m0,1024\ns_nop 0\nbuffer_load_dwordx4 v229,%[sx],s55 offen lds\n" \
"v_mfma_f32_16x16x32_bf16 v[60:63],v[140:143],v[188:191],v[60:63]\nds_read_b128 v[140:143],v224 offset:5120\n" \
"v_mfma_f32_16x16x32_bf16 v[64:67],v[144:147],v[176:179],v[64:67]\nv_mfma_f32_16x16x32_bf16 v[68:71],v[144:147],v[180:183],v[68:71]\n" \
"ds_read_b64_tr_b16 v[168:169],v226 offset:256\ns_add_u32 m0,m0,29696\ns_nop 0\nbuffer_load_dwordx4 v230,%[sk],s56 offen lds\n" \
"v_mfma_f32_16x16x32_bf16 v[72:75],v[144:147],v[184:187],v[72:75]\nv_mfma_f32_16x16x32_bf16 v[76:79],v[144:147],v[188:191],v[76:79]\n" \
"ds_read_b128 v[144:147],v224 offset:8192\nv_mfma_f32_16x16x32_bf16 v[80:83],v[148:151],v[176:179],v[80:83]\n" \
"ds_read_b64_tr_b16 v[170:171],v226 offset:2304\ns_add_u32 m0,m0,1024\ns_nop 0\nbuffer_load_dwordx4 v230,%[sv],s56 offen lds\n" \
"v_mfma_f32_16x16x32_bf16 v[84:87],v[148:151],v[180:183],v[84:87]\nv_mfma_f32_16x16x32_bf16 v[88:91],v[148:151],v[184:187],v[88:91]\n" \
"v_mfma_f32_16x16x32_bf16 v[92:95],v[148:151],v[188:191],v[92:95]\nds_read_b128 v[148:151],v224 offset:9216\n" \
"ds_read_b64_tr_b16 v[172:173],v227 offset:256\ns_add_u32 m0,m0,1024\ns_nop 0\nbuffer_load_dwordx4 v230,%[sk],s57 offen lds\n" \
"v_mfma_f32_16x16x32_bf16 v[96:99],v[152:155],v[176:179],v[96:99]\nv_mfma_f32_16x16x32_bf16 v[100:103],v[152:155],v[180:183],v[100:103]\n" \
"v_mfma_f32_16x16x32_bf16 v[104:107],v[152:155],v[184:187],v[104:107]\nds_read_b64_tr_b16 v[174:175],v227 offset:2304\ns_add_u32 m0,m0,1024\ns_nop 0\n" \
"buffer_load_dwordx4 v230,%[sv],s57 offen lds\nv_add_u32 v228,128,v228\nv_add_u32 v229,128,v229\nv_add_u32 v230,0x80000,v230\ns_xor_b32 s59,s59,0x10000\n" \
"v_mfma_f32_16x16x32_bf16 v[108:111],v[152:155],v[188:191],v[108:111]\nds_read_b128 v[152:155],v224 offset:12288\n" \
"v_mfma_f32_16x16x32_bf16 v[112:115],v[156:159],v[176:179],v[112:115]\nv_mfma_f32_16x16x32_bf16 v[116:119],v[156:159],v[180:183],v[116:119]\n" \
"v_mfma_f32_16x16x32_bf16 v[120:123],v[156:159],v[184:187],v[120:123]\nv_mfma_f32_16x16x32_bf16 v[124:127],v[156:159],v[188:191],v[124:127]\n" \
"ds_read_b128 v[156:159],v224 offset:13312\ns_mov_b32 s60,4\n1:\ns_waitcnt lgkmcnt(12)\nv_mfma_f32_16x16x32_bf16 v[0:3],v[128:131],v[160:163],v[0:3]\n" \
"ds_read_b64_tr_b16 v[176:177],v226 offset:16384\ns_waitcnt lgkmcnt(10)\nv_mfma_f32_16x16x32_bf16 v[4:7],v[128:131],v[164:167],v[4:7]\n" \
"s_waitcnt lgkmcnt(6)\nv_mfma_f32_16x16x32_bf16 v[8:11],v[128:131],v[168:171],v[8:11]\ns_waitcnt lgkmcnt(3)\n" \
"v_mfma_f32_16x16x32_bf16 v[12:15],v[128:131],v[172:175],v[12:15]\nds_read_b128 v[128:131],v225 offset:0\n" \
"ds_read_b64_tr_b16 v[178:179],v226 offset:18432\nv_mfma_f32_16x16x32_bf16 v[16:19],v[132:135],v[160:163],v[16:19]\n" \
"v_mfma_f32_16x16x32_bf16 v[20:23],v[132:135],v[164:167],v[20:23]\nv_mfma_f32_16x16x32_bf16 v[24:27],v[132:135],v[168:171],v[24:27]\n" \
"ds_read_b64_tr_b16 v[180:181],v227 offset:16384\nv_mfma_f32_16x16x32_bf16 v[28:31],v[132:135],v[172:175],v[28:31]\n" \
"ds_read_b128 v[132:135],v225 offset:1024\nv_mfma_f32_16x16x32_bf16 v[32:35],v[136:139],v[160:163],v[32:35]\n" \
"v_mfma_f32_16x16x32_bf16 v[36:39],v[136:139],v[164:167],v[36:39]\nds_read_b64_tr_b16 v[182:183],v227 offset:18432\n" \
"v_mfma_f32_16x16x32_bf16 v[40:43],v[136:139],v[168:171],v[40:43]\nv_mfma_f32_16x16x32_bf16 v[44:47],v[136:139],v[172:175],v[44:47]\n" \
"ds_read_b128 v[136:139],v225 offset:4096\nv_mfma_f32_16x16x32_bf16 v[48:51],v[140:143],v[160:163],v[48:51]\n" \
"ds_read_b64_tr_b16 v[184:185],v226 offset:16640\nv_mfma_f32_16x16x32_bf16 v[52:55],v[140:143],v[164:167],v[52:55]\n" \
"v_mfma_f32_16x16x32_bf16 v[56:59],v[140:143],v[168:171],v[56:59]\nv_mfma_f32_16x16x32_bf16 v[60:63],v[140:143],v[172:175],v[60:63]\n" \
"ds_read_b128 v[140:143],v225 offset:5120\nds_read_b64_tr_b16 v[186:187],v226 offset:18688\n" \
"v_mfma_f32_16x16x32_bf16 v[64:67],v[144:147],v[160:163],v[64:67]\nv_mfma_f32_16x16x32_bf16 v[68:71],v[144:147],v[164:167],v[68:71]\n" \
"v_mfma_f32_16x16x32_bf16 v[72:75],v[144:147],v[168:171],v[72:75]\nds_read_b64_tr_b16 v[188:189],v227 offset:16640\n" \
"v_mfma_f32_16x16x32_bf16 v[76:79],v[144:147],v[172:175],v[76:79]\nds_read_b128 v[144:147],v225 offset:8192\n" \
"v_mfma_f32_16x16x32_bf16 v[80:83],v[148:151],v[160:163],v[80:83]\nv_mfma_f32_16x16x32_bf16 v[84:87],v[148:151],v[164:167],v[84:87]\n" \
"ds_read_b64_tr_b16 v[190:191],v227 offset:18688\nv_mfma_f32_16x16x32_bf16 v[88:91],v[148:151],v[168:171],v[88:91]\n" \
"v_mfma_f32_16x16x32_bf16 v[92:95],v[148:151],v[172:175],v[92:95]\nds_read_b128 v[148:151],v225 offset:9216\ns_waitcnt lgkmcnt(15)\n" \
"v_mfma_f32_16x16x32_bf16 v[96:99],v[152:155],v[160:163],v[96:99]\nv_mfma_f32_16x16x32_bf16 v[100:103],v[152:155],v[164:167],v[100:103]\n" \
"v_mfma_f32_16x16x32_bf16 v[104:107],v[152:155],v[168:171],v[104:107]\nv_mfma_f32_16x16x32_bf16 v[108:111],v[152:155],v[172:175],v[108:111]\n" \
"ds_read_b128 v[152:155],v225 offset:12288\ns_waitcnt lgkmcnt(15)\nv_mfma_f32_16x16x32_bf16 v[112:115],v[156:159],v[160:163],v[112:115]\n" \
"v_mfma_f32_16x16x32_bf16 v[116:119],v[156:159],v[164:167],v[116:119]\nv_mfma_f32_16x16x32_bf16 v[120:123],v[156:159],v[168:171],v[120:123]\n" \
"v_mfma_f32_16x16x32_bf16 v[124:127],v[156:159],v[172:175],v[124:127]\nds_read_b128 v[156:159],v225 offset:13312\ns_waitcnt lgkmcnt(13)\n" \
"v_mfma_f32_16x16x32_bf16 v[0:3],v[128:131],v[176:179],v[0:3]\ns_waitcnt lgkmcnt(10)\nv_mfma_f32_16x16x32_bf16 v[4:7],v[128:131],v[180:183],v[4:7]\n" \
"s_waitcnt lgkmcnt(6)\nv_mfma_f32_16x16x32_bf16 v[8:11],v[128:131],v[184:187],v[8:11]\ns_waitcnt lgkmcnt(3)\n" \
"v_mfma_f32_16x16x32_bf16 v[12:15],v[128:131],v[188:191],v[12:15]\ns_waitcnt vmcnt(0) lgkmcnt(0)\ns_barrier\nv_xor_b32 v224,0x10000,v224\n" \
"v_xor_b32 v225,0x10000,v225\nv_xor_b32 v226,0x10000,v226\nv_xor_b32 v227,0x10000,v227\n" \
"v_mfma_f32_16x16x32_bf16 v[16:19],v[132:135],v[176:179],v[16:19]\nds_read_b128 v[128:131],v224 offset:0\n" \
"v_mfma_f32_16x16x32_bf16 v[20:23],v[132:135],v[180:183],v[20:23]\nds_read_b64_tr_b16 v[160:161],v226 offset:0\ns_mov_b32 m0,s59\ns_nop 0\n" \
"buffer_load_dwordx4 v228,%[sx],s52 offen lds\nv_mfma_f32_16x16x32_bf16 v[24:27],v[132:135],v[184:187],v[24:27]\n" \
"v_mfma_f32_16x16x32_bf16 v[28:31],v[132:135],v[188:191],v[28:31]\nds_read_b128 v[132:135],v224 offset:1024\n" \
"v_mfma_f32_16x16x32_bf16 v[32:35],v[136:139],v[176:179],v[32:35]\nds_read_b64_tr_b16 v[162:163],v226 offset:2048\ns_add_u32 m0,m0,1024\ns_nop 0\n" \
"buffer_load_dwordx4 v228,%[sx],s53 offen lds\nv_mfma_f32_16x16x32_bf16 v[36:39],v[136:139],v[180:183],v[36:39]\n" \
"v_mfma_f32_16x16x32_bf16 v[40:43],v[136:139],v[184:187],v[40:43]\nv_mfma_f32_16x16x32_bf16 v[44:47],v[136:139],v[188:191],v[44:47]\n" \
"ds_read_b128 v[136:139],v224 offset:4096\nds_read_b64_tr_b16 v[164:165],v227 offset:0\ns_add_u32 m0,m0,1024\ns_nop 0\n" \
"buffer_load_dwordx4 v229,%[sx],s54 offen lds\nv_mfma_f32_16x16x32_bf16 v[48:51],v[140:143],v[176:179],v[48:51]\n" \
"v_mfma_f32_16x16x32_bf16 v[52:55],v[140:143],v[180:183],v[52:55]\nv_mfma_f32_16x16x32_bf16 v[56:59],v[140:143],v[184:187],v[56:59]\n" \
"ds_read_b64_tr_b16 v[166:167],v227 offset:2048\ns_add_u32 m0,m0,1024\ns_nop 0\nbuffer_load_dwordx4 v229,%[sx],s55 offen lds\n" \
"v_mfma_f32_16x16x32_bf16 v[60:63],v[140:143],v[188:191],v[60:63]\nds_read_b128 v[140:143],v224 offset:5120\n" \
"v_mfma_f32_16x16x32_bf16 v[64:67],v[144:147],v[176:179],v[64:67]\nv_mfma_f32_16x16x32_bf16 v[68:71],v[144:147],v[180:183],v[68:71]\n" \
"ds_read_b64_tr_b16 v[168:169],v226 offset:256\ns_add_u32 m0,m0,29696\ns_nop 0\nbuffer_load_dwordx4 v230,%[sk],s56 offen lds\n" \
"v_mfma_f32_16x16x32_bf16 v[72:75],v[144:147],v[184:187],v[72:75]\nv_mfma_f32_16x16x32_bf16 v[76:79],v[144:147],v[188:191],v[76:79]\n" \
"ds_read_b128 v[144:147],v224 offset:8192\nv_mfma_f32_16x16x32_bf16 v[80:83],v[148:151],v[176:179],v[80:83]\n" \
"ds_read_b64_tr_b16 v[170:171],v226 offset:2304\ns_add_u32 m0,m0,1024\ns_nop 0\nbuffer_load_dwordx4 v230,%[sv],s56 offen lds\n" \
"v_mfma_f32_16x16x32_bf16 v[84:87],v[148:151],v[180:183],v[84:87]\nv_mfma_f32_16x16x32_bf16 v[88:91],v[148:151],v[184:187],v[88:91]\n" \
"v_mfma_f32_16x16x32_bf16 v[92:95],v[148:151],v[188:191],v[92:95]\nds_read_b128 v[148:151],v224 offset:9216\n" \
"ds_read_b64_tr_b16 v[172:173],v227 offset:256\ns_add_u32 m0,m0,1024\ns_nop 0\nbuffer_load_dwordx4 v230,%[sk],s57 offen lds\n" \
"v_mfma_f32_16x16x32_bf16 v[96:99],v[152:155],v[176:179],v[96:99]\nv_mfma_f32_16x16x32_bf16 v[100:103],v[152:155],v[180:183],v[100:103]\n" \
"v_mfma_f32_16x16x32_bf16 v[104:107],v[152:155],v[184:187],v[104:107]\nds_read_b64_tr_b16 v[174:175],v227 offset:2304\ns_add_u32 m0,m0,1024\ns_nop 0\n" \
"buffer_load_dwordx4 v230,%[sv],s57 offen lds\nv_add_u32 v228,128,v228\nv_add_u32 v229,128,v229\nv_add_u32 v230,0x80000,v230\ns_xor_b32 s59,s59,0x10000\n" \
"v_mfma_f32_16x16x32_bf16 v[108:111],v[152:155],v[188:191],v[108:111]\nds_read_b128 v[152:155],v224 offset:12288\n" \
"v_mfma_f32_16x16x32_bf16 v[112:115],v[156:159],v[176:179],v[112:115]\nv_mfma_f32_16x16x32_bf16 v[116:119],v[156:159],v[180:183],v[116:119]\n" \
"v_mfma_f32_16x16x32_bf16 v[120:123],v[156:159],v[184:187],v[120:123]\nv_mfma_f32_16x16x32_bf16 v[124:127],v[156:159],v[188:191],v[124:127]\n" \
"ds_read_b128 v[156:159],v224 offset:13312\ns_sub_u32 s60,s60,1\ns_cmp_lg_u32 s60,0\ns_cbranch_scc1 1b\ns_waitcnt lgkmcnt(12)\n" \
"v_mfma_f32_16x16x32_bf16 v[0:3],v[128:131],v[160:163],v[0:3]\nds_read_b64_tr_b16 v[176:177],v226 offset:16384\ns_waitcnt lgkmcnt(10)\n" \
"v_mfma_f32_16x16x32_bf16 v[4:7],v[128:131],v[164:167],v[4:7]\ns_waitcnt lgkmcnt(6)\nv_mfma_f32_16x16x32_bf16 v[8:11],v[128:131],v[168:171],v[8:11]\n" \
"s_waitcnt lgkmcnt(3)\nv_mfma_f32_16x16x32_bf16 v[12:15],v[128:131],v[172:175],v[12:15]\nds_read_b128 v[128:131],v225 offset:0\n" \
"ds_read_b64_tr_b16 v[178:179],v226 offset:18432\nv_mfma_f32_16x16x32_bf16 v[16:19],v[132:135],v[160:163],v[16:19]\n" \
"v_mfma_f32_16x16x32_bf16 v[20:23],v[132:135],v[164:167],v[20:23]\nv_mfma_f32_16x16x32_bf16 v[24:27],v[132:135],v[168:171],v[24:27]\n" \
"ds_read_b64_tr_b16 v[180:181],v227 offset:16384\nv_mfma_f32_16x16x32_bf16 v[28:31],v[132:135],v[172:175],v[28:31]\n" \
"ds_read_b128 v[132:135],v225 offset:1024\nv_mfma_f32_16x16x32_bf16 v[32:35],v[136:139],v[160:163],v[32:35]\n" \
"v_mfma_f32_16x16x32_bf16 v[36:39],v[136:139],v[164:167],v[36:39]\nds_read_b64_tr_b16 v[182:183],v227 offset:18432\n" \
"v_mfma_f32_16x16x32_bf16 v[40:43],v[136:139],v[168:171],v[40:43]\nv_mfma_f32_16x16x32_bf16 v[44:47],v[136:139],v[172:175],v[44:47]\n" \
"ds_read_b128 v[136:139],v225 offset:4096\nv_mfma_f32_16x16x32_bf16 v[48:51],v[140:143],v[160:163],v[48:51]\n" \
"ds_read_b64_tr_b16 v[184:185],v226 offset:16640\nv_mfma_f32_16x16x32_bf16 v[52:55],v[140:143],v[164:167],v[52:55]\n" \
"v_mfma_f32_16x16x32_bf16 v[56:59],v[140:143],v[168:171],v[56:59]\nv_mfma_f32_16x16x32_bf16 v[60:63],v[140:143],v[172:175],v[60:63]\n" \
"ds_read_b128 v[140:143],v225 offset:5120\nds_read_b64_tr_b16 v[186:187],v226 offset:18688\n" \
"v_mfma_f32_16x16x32_bf16 v[64:67],v[144:147],v[160:163],v[64:67]\nv_mfma_f32_16x16x32_bf16 v[68:71],v[144:147],v[164:167],v[68:71]\n" \
"v_mfma_f32_16x16x32_bf16 v[72:75],v[144:147],v[168:171],v[72:75]\nds_read_b64_tr_b16 v[188:189],v227 offset:16640\n" \
"v_mfma_f32_16x16x32_bf16 v[76:79],v[144:147],v[172:175],v[76:79]\nds_read_b128 v[144:147],v225 offset:8192\n" \
"v_mfma_f32_16x16x32_bf16 v[80:83],v[148:151],v[160:163],v[80:83]\nv_mfma_f32_16x16x32_bf16 v[84:87],v[148:151],v[164:167],v[84:87]\n" \
"ds_read_b64_tr_b16 v[190:191],v227 offset:18688\nv_mfma_f32_16x16x32_bf16 v[88:91],v[148:151],v[168:171],v[88:91]\n" \
"v_mfma_f32_16x16x32_bf16 v[92:95],v[148:151],v[172:175],v[92:95]\nds_read_b128 v[148:151],v225 offset:9216\ns_waitcnt lgkmcnt(15)\n" \
"v_mfma_f32_16x16x32_bf16 v[96:99],v[152:155],v[160:163],v[96:99]\nv_mfma_f32_16x16x32_bf16 v[100:103],v[152:155],v[164:167],v[100:103]\n" \
"v_mfma_f32_16x16x32_bf16 v[104:107],v[152:155],v[168:171],v[104:107]\nv_mfma_f32_16x16x32_bf16 v[108:111],v[152:155],v[172:175],v[108:111]\n" \
"ds_read_b128 v[152:155],v225 offset:12288\ns_waitcnt lgkmcnt(15)\nv_mfma_f32_16x16x32_bf16 v[112:115],v[156:159],v[160:163],v[112:115]\n" \
"v_mfma_f32_16x16x32_bf16 v[116:119],v[156:159],v[164:167],v[116:119]\nv_mfma_f32_16x16x32_bf16 v[120:123],v[156:159],v[168:171],v[120:123]\n" \
"v_mfma_f32_16x16x32_bf16 v[124:127],v[156:159],v[172:175],v[124:127]\nds_read_b128 v[156:159],v225 offset:13312\ns_waitcnt lgkmcnt(13)\n" \
"v_mfma_f32_16x16x32_bf16 v[0:3],v[128:131],v[176:179],v[0:3]\ns_waitcnt lgkmcnt(10)\nv_mfma_f32_16x16x32_bf16 v[4:7],v[128:131],v[180:183],v[4:7]\n" \
"s_waitcnt lgkmcnt(6)\nv_mfma_f32_16x16x32_bf16 v[8:11],v[128:131],v[184:187],v[8:11]\ns_waitcnt lgkmcnt(3)\n" \
"v_mfma_f32_16x16x32_bf16 v[12:15],v[128:131],v[188:191],v[12:15]\ns_waitcnt vmcnt(0) lgkmcnt(0)\ns_barrier\nv_xor_b32 v224,0x10000,v224\n" \
"v_xor_b32 v225,0x10000,v225\nv_xor_b32 v226,0x10000,v226\nv_xor_b32 v227,0x10000,v227\n" \
"v_mfma_f32_16x16x32_bf16 v[16:19],v[132:135],v[176:179],v[16:19]\nds_read_b128 v[128:131],v224 offset:0\n" \
"v_mfma_f32_16x16x32_bf16 v[20:23],v[132:135],v[180:183],v[20:23]\nds_read_b64_tr_b16 v[160:161],v226 offset:0\ns_mov_b32 m0,s59\ns_nop 0\n" \
"buffer_load_dwordx4 v228,%[sx],s52 offen lds\nv_mfma_f32_16x16x32_bf16 v[24:27],v[132:135],v[184:187],v[24:27]\n" \
"v_mfma_f32_16x16x32_bf16 v[28:31],v[132:135],v[188:191],v[28:31]\nds_read_b128 v[132:135],v224 offset:1024\n" \
"v_mfma_f32_16x16x32_bf16 v[32:35],v[136:139],v[176:179],v[32:35]\nds_read_b64_tr_b16 v[162:163],v226 offset:2048\ns_add_u32 m0,m0,1024\ns_nop 0\n" \
"buffer_load_dwordx4 v228,%[sx],s53 offen lds\nv_mfma_f32_16x16x32_bf16 v[36:39],v[136:139],v[180:183],v[36:39]\n" \
"v_mfma_f32_16x16x32_bf16 v[40:43],v[136:139],v[184:187],v[40:43]\nv_mfma_f32_16x16x32_bf16 v[44:47],v[136:139],v[188:191],v[44:47]\n" \
"ds_read_b128 v[136:139],v224 offset:4096\nds_read_b64_tr_b16 v[164:165],v227 offset:0\ns_add_u32 m0,m0,1024\ns_nop 0\n" \
"buffer_load_dwordx4 v229,%[sx],s54 offen lds\nv_mfma_f32_16x16x32_bf16 v[48:51],v[140:143],v[176:179],v[48:51]\n" \
"v_mfma_f32_16x16x32_bf16 v[52:55],v[140:143],v[180:183],v[52:55]\nv_mfma_f32_16x16x32_bf16 v[56:59],v[140:143],v[184:187],v[56:59]\n" \
"ds_read_b64_tr_b16 v[166:167],v227 offset:2048\ns_add_u32 m0,m0,1024\ns_nop 0\nbuffer_load_dwordx4 v229,%[sx],s55 offen lds\n" \
"v_mfma_f32_16x16x32_bf16 v[60:63],v[140:143],v[188:191],v[60:63]\nds_read_b128 v[140:143],v224 offset:5120\n" \
"v_mfma_f32_16x16x32_bf16 v[64:67],v[144:147],v[176:179],v[64:67]\nv_mfma_f32_16x16x32_bf16 v[68:71],v[144:147],v[180:183],v[68:71]\n" \
"ds_read_b64_tr_b16 v[168:169],v226 offset:256\ns_add_u32 m0,m0,29696\ns_nop 0\nbuffer_load_dwordx4 v230,%[sk],s56 offen lds\n" \
"v_mfma_f32_16x16x32_bf16 v[72:75],v[144:147],v[184:187],v[72:75]\nv_mfma_f32_16x16x32_bf16 v[76:79],v[144:147],v[188:191],v[76:79]\n" \
"ds_read_b128 v[144:147],v224 offset:8192\nv_mfma_f32_16x16x32_bf16 v[80:83],v[148:151],v[176:179],v[80:83]\n" \
"ds_read_b64_tr_b16 v[170:171],v226 offset:2304\ns_add_u32 m0,m0,1024\ns_nop 0\nbuffer_load_dwordx4 v230,%[sv],s56 offen lds\n" \
"v_mfma_f32_16x16x32_bf16 v[84:87],v[148:151],v[180:183],v[84:87]\nv_mfma_f32_16x16x32_bf16 v[88:91],v[148:151],v[184:187],v[88:91]\n" \
"v_mfma_f32_16x16x32_bf16 v[92:95],v[148:151],v[188:191],v[92:95]\nds_read_b128 v[148:151],v224 offset:9216\n" \
"ds_read_b64_tr_b16 v[172:173],v227 offset:256\ns_add_u32 m0,m0,1024\ns_nop 0\nbuffer_load_dwordx4 v230,%[sk],s57 offen lds\n" \
"v_mfma_f32_16x16x32_bf16 v[96:99],v[152:155],v[176:179],v[96:99]\nv_mfma_f32_16x16x32_bf16 v[100:103],v[152:155],v[180:183],v[100:103]\n" \
"v_mfma_f32_16x16x32_bf16 v[104:107],v[152:155],v[184:187],v[104:107]\nds_read_b64_tr_b16 v[174:175],v227 offset:2304\ns_add_u32 m0,m0,1024\ns_nop 0\n" \
"buffer_load_dwordx4 v230,%[sv],s57 offen lds\nv_add_u32 v228,128,v228\nv_add_u32 v229,128,v229\nv_add_u32 v230,0x80000,v230\ns_xor_b32 s59,s59,0x10000\n" \
"v_mfma_f32_16x16x32_bf16 v[108:111],v[152:155],v[188:191],v[108:111]\nds_read_b128 v[152:155],v224 offset:12288\n" \
"v_mfma_f32_16x16x32_bf16 v[112:115],v[156:159],v[176:179],v[112:115]\nv_mfma_f32_16x16x32_bf16 v[116:119],v[156:159],v[180:183],v[116:119]\n" \
"v_mfma_f32_16x16x32_bf16 v[120:123],v[156:159],v[184:187],v[120:123]\nv_mfma_f32_16x16x32_bf16 v[124:127],v[156:159],v[188:191],v[124:127]\n" \
"ds_read_b128 v[156:159],v224 offset:13312\ns_and_b32 s50,s66,0xffff\ns_add_u32 m0,s50,131072\ns_add_u32 s49,s67,0\n" \
"buffer_load_dwordx4 v249,%[sq],s49 offen lds\ns_add_u32 m0,m0,1024\ns_add_u32 s49,s49,32768\nbuffer_load_dwordx4 v249,%[sq],s49 offen lds\n" \
"s_add_u32 m0,m0,1024\ns_add_u32 s49,s49,32768\nbuffer_load_dwordx4 v249,%[sq],s49 offen lds\ns_add_u32 m0,m0,1024\ns_add_u32 s49,s49,32768\n" \
"buffer_load_dwordx4 v249,%[sq],s49 offen lds\ns_add_u32 s58,s76,0\nbuffer_load_dwordx4 v[192:195],v248,%[sws],s58 offen offset:0\n" \
"buffer_load_dwordx4 v[196:199],v248,%[sws],s58 offen offset:1024\nbuffer_load_dwordx4 v[200:203],v248,%[sws],s58 offen offset:2048\n" \
"buffer_load_dwordx4 v[204:207],v248,%[sws],s58 offen offset:3072\ns_add_u32 s58,s76,4096\n" \
"buffer_load_dwordx4 v[208:211],v248,%[sws],s58 offen offset:0\nbuffer_load_dwordx4 v[212:215],v248,%[sws],s58 offen offset:1024\n" \
"buffer_load_dwordx4 v[216:219],v248,%[sws],s58 offen offset:2048\nbuffer_load_dwordx4 v[220:223],v248,%[sws],s58 offen offset:3072\n" \
"s_waitcnt lgkmcnt(12)\nv_mfma_f32_16x16x32_bf16 v[0:3],v[128:131],v[160:163],v[0:3]\nds_read_b64_tr_b16 v[176:177],v226 offset:16384\n" \
"s_waitcnt lgkmcnt(10)\nv_mfma_f32_16x16x32_bf16 v[4:7],v[128:131],v[164:167],v[4:7]\ns_waitcnt lgkmcnt(6)\n" \
"v_mfma_f32_16x16x32_bf16 v[8:11],v[128:131],v[168:171],v[8:11]\ns_waitcnt lgkmcnt(3)\nv_mfma_f32_16x16x32_bf16 v[12:15],v[128:131],v[172:175],v[12:15]\n" \
"ds_read_b128 v[128:131],v225 offset:0\nds_read_b64_tr_b16 v[178:179],v226 offset:18432\n" \
"v_mfma_f32_16x16x32_bf16 v[16:19],v[132:135],v[160:163],v[16:19]\nv_mfma_f32_16x16x32_bf16 v[20:23],v[132:135],v[164:167],v[20:23]\n" \
"v_mfma_f32_16x16x32_bf16 v[24:27],v[132:135],v[168:171],v[24:27]\nds_read_b64_tr_b16 v[180:181],v227 offset:16384\n" \
"v_mfma_f32_16x16x32_bf16 v[28:31],v[132:135],v[172:175],v[28:31]\nds_read_b128 v[132:135],v225 offset:1024\n" \
"v_mfma_f32_16x16x32_bf16 v[32:35],v[136:139],v[160:163],v[32:35]\nv_mfma_f32_16x16x32_bf16 v[36:39],v[136:139],v[164:167],v[36:39]\n" \
"ds_read_b64_tr_b16 v[182:183],v227 offset:18432\nv_mfma_f32_16x16x32_bf16 v[40:43],v[136:139],v[168:171],v[40:43]\n" \
"v_mfma_f32_16x16x32_bf16 v[44:47],v[136:139],v[172:175],v[44:47]\nds_read_b128 v[136:139],v225 offset:4096\n" \
"v_mfma_f32_16x16x32_bf16 v[48:51],v[140:143],v[160:163],v[48:51]\nds_read_b64_tr_b16 v[184:185],v226 offset:16640\n" \
"v_mfma_f32_16x16x32_bf16 v[52:55],v[140:143],v[164:167],v[52:55]\nv_mfma_f32_16x16x32_bf16 v[56:59],v[140:143],v[168:171],v[56:59]\n" \
"v_mfma_f32_16x16x32_bf16 v[60:63],v[140:143],v[172:175],v[60:63]\nds_read_b128 v[140:143],v225 offset:5120\n" \
"ds_read_b64_tr_b16 v[186:187],v226 offset:18688\nv_mfma_f32_16x16x32_bf16 v[64:67],v[144:147],v[160:163],v[64:67]\n" \
"v_mfma_f32_16x16x32_bf16 v[68:71],v[144:147],v[164:167],v[68:71]\nv_mfma_f32_16x16x32_bf16 v[72:75],v[144:147],v[168:171],v[72:75]\n" \
"ds_read_b64_tr_b16 v[188:189],v227 offset:16640\nv_mfma_f32_16x16x32_bf16 v[76:79],v[144:147],v[172:175],v[76:79]\n" \
"ds_read_b128 v[144:147],v225 offset:8192\nv_mfma_f32_16x16x32_bf16 v[80:83],v[148:151],v[160:163],v[80:83]\n" \
"v_mfma_f32_16x16x32_bf16 v[84:87],v[148:151],v[164:167],v[84:87]\nds_read_b64_tr_b16 v[190:191],v227 offset:18688\n" \
"v_mfma_f32_16x16x32_bf16 v[88:91],v[148:151],v[168:171],v[88:91]\nv_mfma_f32_16x16x32_bf16 v[92:95],v[148:151],v[172:175],v[92:95]\n" \
"ds_read_b128 v[148:151],v225 offset:9216\ns_waitcnt lgkmcnt(15)\nv_mfma_f32_16x16x32_bf16 v[96:99],v[152:155],v[160:163],v[96:99]\n" \
"v_mfma_f32_16x16x32_bf16 v[100:103],v[152:155],v[164:167],v[100:103]\nv_mfma_f32_16x16x32_bf16 v[104:107],v[152:155],v[168:171],v[104:107]\n" \
"v_mfma_f32_16x16x32_bf16 v[108:111],v[152:155],v[172:175],v[108:111]\nds_read_b128 v[152:155],v225 offset:12288\ns_waitcnt lgkmcnt(15)\n" \
"v_mfma_f32_16x16x32_bf16 v[112:115],v[156:159],v[160:163],v[112:115]\nv_mfma_f32_16x16x32_bf16 v[116:119],v[156:159],v[164:167],v[116:119]\n" \
"v_mfma_f32_16x16x32_bf16 v[120:123],v[156:159],v[168:171],v[120:123]\nv_mfma_f32_16x16x32_bf16 v[124:127],v[156:159],v[172:175],v[124:127]\n" \
"ds_read_b128 v[156:159],v225 offset:13312\ns_waitcnt lgkmcnt(13)\nv_mfma_f32_16x16x32_bf16 v[0:3],v[128:131],v[176:179],v[0:3]\ns_waitcnt lgkmcnt(10)\n" \
"v_mfma_f32_16x16x32_bf16 v[4:7],v[128:131],v[180:183],v[4:7]\ns_waitcnt lgkmcnt(6)\nv_mfma_f32_16x16x32_bf16 v[8:11],v[128:131],v[184:187],v[8:11]\n" \
"s_waitcnt lgkmcnt(3)\nv_mfma_f32_16x16x32_bf16 v[12:15],v[128:131],v[188:191],v[12:15]\ns_waitcnt vmcnt(0) lgkmcnt(0)\ns_barrier\n" \
"v_xor_b32 v224,0x10000,v224\nv_xor_b32 v225,0x10000,v225\nv_xor_b32 v226,0x10000,v226\nv_xor_b32 v227,0x10000,v227\n" \
"v_mfma_f32_16x16x32_bf16 v[16:19],v[132:135],v[176:179],v[16:19]\nds_read_b128 v[128:131],v224 offset:0\n" \
"v_mfma_f32_16x16x32_bf16 v[20:23],v[132:135],v[180:183],v[20:23]\nds_read_b64_tr_b16 v[160:161],v226 offset:0\ns_add_u32 m0,s80,65536\n" \
"s_add_u32 s49,s68,0\nbuffer_load_dwordx4 v248,%[sws],s49 offen offset:0 lds\nv_mfma_f32_16x16x32_bf16 v[24:27],v[132:135],v[184:187],v[24:27]\n" \
"v_mfma_f32_16x16x32_bf16 v[28:31],v[132:135],v[188:191],v[28:31]\nds_read_b128 v[132:135],v224 offset:1024\n" \
"v_mfma_f32_16x16x32_bf16 v[32:35],v[136:139],v[176:179],v[32:35]\nds_read_b64_tr_b16 v[162:163],v226 offset:2048\n" \
"buffer_load_dwordx4 v248,%[sws],s49 offen offset:1024 lds\nv_mfma_f32_16x16x32_bf16 v[36:39],v[136:139],v[180:183],v[36:39]\n" \
"v_mfma_f32_16x16x32_bf16 v[40:43],v[136:139],v[184:187],v[40:43]\nv_mfma_f32_16x16x32_bf16 v[44:47],v[136:139],v[188:191],v[44:47]\n" \
"ds_read_b128 v[136:139],v224 offset:4096\nds_read_b64_tr_b16 v[164:165],v227 offset:0\nbuffer_load_dwordx4 v248,%[sws],s49 offen offset:2048 lds\n" \
"v_mfma_f32_16x16x32_bf16 v[48:51],v[140:143],v[176:179],v[48:51]\nv_mfma_f32_16x16x32_bf16 v[52:55],v[140:143],v[180:183],v[52:55]\n" \
"v_mfma_f32_16x16x32_bf16 v[56:59],v[140:143],v[184:187],v[56:59]\nds_read_b64_tr_b16 v[166:167],v227 offset:2048\n" \
"buffer_load_dwordx4 v248,%[sws],s49 offen offset:3072 lds\nv_mfma_f32_16x16x32_bf16 v[60:63],v[140:143],v[188:191],v[60:63]\n" \
"ds_read_b128 v[140:143],v224 offset:5120\nv_mfma_f32_16x16x32_bf16 v[64:67],v[144:147],v[176:179],v[64:67]\n" \
"v_mfma_f32_16x16x32_bf16 v[68:71],v[144:147],v[180:183],v[68:71]\nds_read_b64_tr_b16 v[168:169],v226 offset:256\ns_add_u32 m0,s80,69632\n" \
"s_add_u32 s49,s68,4096\nbuffer_load_dwordx4 v248,%[sws],s49 offen offset:0 lds\nv_mfma_f32_16x16x32_bf16 v[72:75],v[144:147],v[184:187],v[72:75]\n" \
"v_mfma_f32_16x16x32_bf16 v[76:79],v[144:147],v[188:191],v[76:79]\nds_read_b128 v[144:147],v224 offset:8192\n" \
"v_mfma_f32_16x16x32_bf16 v[80:83],v[148:151],v[176:179],v[80:83]\nds_read_b64_tr_b16 v[170:171],v226 offset:2304\n" \
"buffer_load_dwordx4 v248,%[sws],s49 offen offset:1024 lds\nv_mfma_f32_16x16x32_bf16 v[84:87],v[148:151],v[180:183],v[84:87]\n" \
"v_mfma_f32_16x16x32_bf16 v[88:91],v[148:151],v[184:187],v[88:91]\nv_mfma_f32_16x16x32_bf16 v[92:95],v[148:151],v[188:191],v[92:95]\n" \
"ds_read_b128 v[148:151],v224 offset:9216\nds_read_b64_tr_b16 v[172:173],v227 offset:256\n" \
"v_mfma_f32_16x16x32_bf16 v[96:99],v[152:155],v[176:179],v[96:99]\nv_mfma_f32_16x16x32_bf16 v[100:103],v[152:155],v[180:183],v[100:103]\n" \
"v_mfma_f32_16x16x32_bf16 v[104:107],v[152:155],v[184:187],v[104:107]\nds_read_b64_tr_b16 v[174:175],v227 offset:2304\n" \
"v_mfma_f32_16x16x32_bf16 v[108:111],v[152:155],v[188:191],v[108:111]\nds_read_b128 v[152:155],v224 offset:12288\n" \
"v_mfma_f32_16x16x32_bf16 v[112:115],v[156:159],v[176:179],v[112:115]\nv_mfma_f32_16x16x32_bf16 v[116:119],v[156:159],v[180:183],v[116:119]\n" \
"v_mfma_f32_16x16x32_bf16 v[120:123],v[156:159],v[184:187],v[120:123]\nv_mfma_f32_16x16x32_bf16 v[124:127],v[156:159],v[188:191],v[124:127]\n" \
"ds_read_b128 v[156:159],v224 offset:13312\ns_waitcnt lgkmcnt(12)\nv_mfma_f32_16x16x32_bf16 v[0:3],v[128:131],v[160:163],v[0:3]\n" \
"ds_read_b64_tr_b16 v[176:177],v226 offset:16384\ns_waitcnt lgkmcnt(10)\nv_mfma_f32_16x16x32_bf16 v[4:7],v[128:131],v[164:167],v[4:7]\n" \
"s_waitcnt lgkmcnt(6)\nv_mfma_f32_16x16x32_bf16 v[8:11],v[128:131],v[168:171],v[8:11]\ns_waitcnt lgkmcnt(3)\n" \
"v_mfma_f32_16x16x32_bf16 v[12:15],v[128:131],v[172:175],v[12:15]\nds_read_b128 v[128:131],v225 offset:0\n" \
"ds_read_b64_tr_b16 v[178:179],v226 offset:18432\nv_mfma_f32_16x16x32_bf16 v[16:19],v[132:135],v[160:163],v[16:19]\n" \
"v_mfma_f32_16x16x32_bf16 v[20:23],v[132:135],v[164:167],v[20:23]\nv_mfma_f32_16x16x32_bf16 v[24:27],v[132:135],v[168:171],v[24:27]\n" \
"ds_read_b64_tr_b16 v[180:181],v227 offset:16384\nv_mfma_f32_16x16x32_bf16 v[28:31],v[132:135],v[172:175],v[28:31]\n" \
"ds_read_b128 v[132:135],v225 offset:1024\nv_mfma_f32_16x16x32_bf16 v[32:35],v[136:139],v[160:163],v[32:35]\n" \
"v_mfma_f32_16x16x32_bf16 v[36:39],v[136:139],v[164:167],v[36:39]\nds_read_b64_tr_b16 v[182:183],v227 offset:18432\n" \
"v_mfma_f32_16x16x32_bf16 v[40:43],v[136:139],v[168:171],v[40:43]\nv_mfma_f32_16x16x32_bf16 v[44:47],v[136:139],v[172:175],v[44:47]\n" \
"ds_read_b128 v[136:139],v225 offset:4096\nv_mfma_f32_16x16x32_bf16 v[48:51],v[140:143],v[160:163],v[48:51]\n" \
"ds_read_b64_tr_b16 v[184:185],v226 offset:16640\nv_mfma_f32_16x16x32_bf16 v[52:55],v[140:143],v[164:167],v[52:55]\n" \
"v_mfma_f32_16x16x32_bf16 v[56:59],v[140:143],v[168:171],v[56:59]\nv_mfma_f32_16x16x32_bf16 v[60:63],v[140:143],v[172:175],v[60:63]\n" \
"ds_read_b128 v[140:143],v225 offset:5120\nds_read_b64_tr_b16 v[186:187],v226 offset:18688\n" \
"v_mfma_f32_16x16x32_bf16 v[64:67],v[144:147],v[160:163],v[64:67]\nv_mfma_f32_16x16x32_bf16 v[68:71],v[144:147],v[164:167],v[68:71]\n" \
"v_mfma_f32_16x16x32_bf16 v[72:75],v[144:147],v[168:171],v[72:75]\nds_read_b64_tr_b16 v[188:189],v227 offset:16640\n" \
"v_mfma_f32_16x16x32_bf16 v[76:79],v[144:147],v[172:175],v[76:79]\nds_read_b128 v[144:147],v225 offset:8192\n" \
"v_mfma_f32_16x16x32_bf16 v[80:83],v[148:151],v[160:163],v[80:83]\nv_mfma_f32_16x16x32_bf16 v[84:87],v[148:151],v[164:167],v[84:87]\n" \
"ds_read_b64_tr_b16 v[190:191],v227 offset:18688\nv_mfma_f32_16x16x32_bf16 v[88:91],v[148:151],v[168:171],v[88:91]\n" \
"v_mfma_f32_16x16x32_bf16 v[92:95],v[148:151],v[172:175],v[92:95]\nds_read_b128 v[148:151],v225 offset:9216\ns_waitcnt lgkmcnt(15)\n" \
"v_mfma_f32_16x16x32_bf16 v[96:99],v[152:155],v[160:163],v[96:99]\nv_mfma_f32_16x16x32_bf16 v[100:103],v[152:155],v[164:167],v[100:103]\n" \
"v_mfma_f32_16x16x32_bf16 v[104:107],v[152:155],v[168:171],v[104:107]\nv_mfma_f32_16x16x32_bf16 v[108:111],v[152:155],v[172:175],v[108:111]\n" \
"ds_read_b128 v[152:155],v225 offset:12288\ns_waitcnt lgkmcnt(15)\nv_mfma_f32_16x16x32_bf16 v[112:115],v[156:159],v[160:163],v[112:115]\n" \
"v_mfma_f32_16x16x32_bf16 v[116:119],v[156:159],v[164:167],v[116:119]\nv_mfma_f32_16x16x32_bf16 v[120:123],v[156:159],v[168:171],v[120:123]\n" \
"v_mfma_f32_16x16x32_bf16 v[124:127],v[156:159],v[172:175],v[124:127]\nds_read_b128 v[156:159],v225 offset:13312\ns_waitcnt lgkmcnt(13)\n" \
"v_mfma_f32_16x16x32_bf16 v[0:3],v[128:131],v[176:179],v[0:3]\ns_waitcnt lgkmcnt(10)\nv_mfma_f32_16x16x32_bf16 v[4:7],v[128:131],v[180:183],v[4:7]\n" \
"s_waitcnt lgkmcnt(6)\nv_mfma_f32_16x16x32_bf16 v[8:11],v[128:131],v[184:187],v[8:11]\ns_waitcnt lgkmcnt(3)\n" \
"v_mfma_f32_16x16x32_bf16 v[12:15],v[128:131],v[188:191],v[12:15]\ns_waitcnt vmcnt(0) lgkmcnt(0)\ns_barrier\n" \
"v_mfma_f32_16x16x32_bf16 v[16:19],v[132:135],v[176:179],v[16:19]\nv_mfma_f32_16x16x32_bf16 v[20:23],v[132:135],v[180:183],v[20:23]\n" \
"v_mfma_f32_16x16x32_bf16 v[24:27],v[132:135],v[184:187],v[24:27]\nv_mfma_f32_16x16x32_bf16 v[28:31],v[132:135],v[188:191],v[28:31]\n" \
"v_mfma_f32_16x16x32_bf16 v[32:35],v[136:139],v[176:179],v[32:35]\nbuffer_load_dwordx4 v[128:131],v248,%[sws],s71 offen offset:0\n" \
"v_mfma_f32_16x16x32_bf16 v[36:39],v[136:139],v[180:183],v[36:39]\nv_mfma_f32_16x16x32_bf16 v[40:43],v[136:139],v[184:187],v[40:43]\n" \
"v_mfma_f32_16x16x32_bf16 v[44:47],v[136:139],v[188:191],v[44:47]\nbuffer_load_dwordx4 v[132:135],v248,%[sws],s71 offen offset:1024\n" \
"v_mfma_f32_16x16x32_bf16 v[48:51],v[140:143],v[176:179],v[48:51]\nv_mfma_f32_16x16x32_bf16 v[52:55],v[140:143],v[180:183],v[52:55]\n" \
"v_mfma_f32_16x16x32_bf16 v[56:59],v[140:143],v[184:187],v[56:59]\ns_add_u32 m0,s80,0\ns_add_u32 s49,s68,24576\n" \
"buffer_load_dwordx4 v248,%[sws],s49 offen offset:0 lds\nv_mfma_f32_16x16x32_bf16 v[60:63],v[140:143],v[188:191],v[60:63]\n" \
"v_mfma_f32_16x16x32_bf16 v[64:67],v[144:147],v[176:179],v[64:67]\nv_mfma_f32_16x16x32_bf16 v[68:71],v[144:147],v[180:183],v[68:71]\n" \
"buffer_load_dwordx4 v248,%[sws],s49 offen offset:1024 lds\nv_mfma_f32_16x16x32_bf16 v[72:75],v[144:147],v[184:187],v[72:75]\n" \
"v_mfma_f32_16x16x32_bf16 v[76:79],v[144:147],v[188:191],v[76:79]\nv_mfma_f32_16x16x32_bf16 v[80:83],v[148:151],v[176:179],v[80:83]\n" \
"buffer_load_dwordx4 v248,%[sws],s49 offen offset:2048 lds\nv_mfma_f32_16x16x32_bf16 v[84:87],v[148:151],v[180:183],v[84:87]\n" \
"v_mfma_f32_16x16x32_bf16 v[88:91],v[148:151],v[184:187],v[88:91]\nv_mfma_f32_16x16x32_bf16 v[92:95],v[148:151],v[188:191],v[92:95]\n" \
"buffer_load_dwordx4 v248,%[sws],s49 offen offset:3072 lds\nv_mfma_f32_16x16x32_bf16 v[96:99],v[152:155],v[176:179],v[96:99]\n" \
"v_mfma_f32_16x16x32_bf16 v[100:103],v[152:155],v[180:183],v[100:103]\nv_mfma_f32_16x16x32_bf16 v[104:107],v[152:155],v[184:187],v[104:107]\n" \
"s_add_u32 m0,s80,4096\ns_add_u32 s49,s68,28672\nbuffer_load_dwordx4 v248,%[sws],s49 offen offset:0 lds\n" \
"v_mfma_f32_16x16x32_bf16 v[108:111],v[152:155],v[188:191],v[108:111]\nv_mfma_f32_16x16x32_bf16 v[112:115],v[156:159],v[176:179],v[112:115]\n" \
"v_mfma_f32_16x16x32_bf16 v[116:119],v[156:159],v[180:183],v[116:119]\nbuffer_load_dwordx4 v248,%[sws],s49 offen offset:1024 lds\n" \
"v_mfma_f32_16x16x32_bf16 v[120:123],v[156:159],v[184:187],v[120:123]\nv_mfma_f32_16x16x32_bf16 v[124:127],v[156:159],v[188:191],v[124:127]\ns_nop 7\n" \
"v_cvt_pk_bf16_f32 v136,v0,v1\nv_cvt_pk_bf16_f32 v137,v2,v3\nv_cvt_pk_bf16_f32 v138,v16,v17\nv_cvt_pk_bf16_f32 v139,v18,v19\n" \
"v_cvt_pk_bf16_f32 v140,v4,v5\nv_cvt_pk_bf16_f32 v141,v6,v7\nv_cvt_pk_bf16_f32 v142,v20,v21\nv_cvt_pk_bf16_f32 v143,v22,v23\n" \
"v_cvt_pk_bf16_f32 v144,v8,v9\nv_cvt_pk_bf16_f32 v145,v10,v11\nv_cvt_pk_bf16_f32 v146,v24,v25\nv_cvt_pk_bf16_f32 v147,v26,v27\n" \
"v_cvt_pk_bf16_f32 v148,v12,v13\nv_cvt_pk_bf16_f32 v149,v14,v15\nv_cvt_pk_bf16_f32 v150,v28,v29\nv_cvt_pk_bf16_f32 v151,v30,v31\n" \
"v_cvt_pk_bf16_f32 v152,v32,v33\nv_cvt_pk_bf16_f32 v153,v34,v35\nv_cvt_pk_bf16_f32 v154,v48,v49\nv_cvt_pk_bf16_f32 v155,v50,v51\n" \
"v_cvt_pk_bf16_f32 v156,v36,v37\nv_cvt_pk_bf16_f32 v157,v38,v39\nv_cvt_pk_bf16_f32 v158,v52,v53\nv_cvt_pk_bf16_f32 v159,v54,v55\n" \
"v_cvt_pk_bf16_f32 v160,v40,v41\nv_cvt_pk_bf16_f32 v161,v42,v43\nv_cvt_pk_bf16_f32 v162,v56,v57\nv_cvt_pk_bf16_f32 v163,v58,v59\n" \
"v_cvt_pk_bf16_f32 v164,v44,v45\nv_cvt_pk_bf16_f32 v165,v46,v47\nv_cvt_pk_bf16_f32 v166,v60,v61\nv_cvt_pk_bf16_f32 v167,v62,v63\n" \
"v_cvt_pk_bf16_f32 v168,v64,v65\nv_cvt_pk_bf16_f32 v169,v66,v67\nv_cvt_pk_bf16_f32 v170,v80,v81\nv_cvt_pk_bf16_f32 v171,v82,v83\n" \
"v_cvt_pk_bf16_f32 v172,v68,v69\nv_cvt_pk_bf16_f32 v173,v70,v71\nv_cvt_pk_bf16_f32 v174,v84,v85\nv_cvt_pk_bf16_f32 v175,v86,v87\n" \
"v_cvt_pk_bf16_f32 v176,v72,v73\nv_cvt_pk_bf16_f32 v177,v74,v75\nv_cvt_pk_bf16_f32 v178,v88,v89\nv_cvt_pk_bf16_f32 v179,v90,v91\n" \
"v_cvt_pk_bf16_f32 v180,v76,v77\nv_cvt_pk_bf16_f32 v181,v78,v79\nv_cvt_pk_bf16_f32 v182,v92,v93\nv_cvt_pk_bf16_f32 v183,v94,v95\n" \
"v_cvt_pk_bf16_f32 v184,v96,v97\nv_cvt_pk_bf16_f32 v185,v98,v99\nv_cvt_pk_bf16_f32 v186,v112,v113\nv_cvt_pk_bf16_f32 v187,v114,v115\n" \
"v_cvt_pk_bf16_f32 v188,v100,v101\nv_cvt_pk_bf16_f32 v189,v102,v103\nv_cvt_pk_bf16_f32 v190,v116,v117\nv_cvt_pk_bf16_f32 v191,v118,v119\n" \
"v_cvt_pk_bf16_f32 v224,v104,v105\nv_cvt_pk_bf16_f32 v225,v106,v107\nv_cvt_pk_bf16_f32 v226,v120,v121\nv_cvt_pk_bf16_f32 v227,v122,v123\n" \
"v_cvt_pk_bf16_f32 v228,v108,v109\nv_cvt_pk_bf16_f32 v229,v110,v111\nv_cvt_pk_bf16_f32 v230,v124,v125\nv_cvt_pk_bf16_f32 v231,v126,v127\ns_nop 1\n" \
"v_mov_b32 v0,0\nv_mov_b32 v1,0\nv_mov_b32 v2,0\nv_mov_b32 v3,0\nv_mov_b32 v4,0\nv_mov_b32 v5,0\nv_mov_b32 v6,0\nv_mov_b32 v7,0\nv_mov_b32 v8,0\n" \
"v_mov_b32 v9,0\nv_mov_b32 v10,0\nv_mov_b32 v11,0\nv_mov_b32 v12,0\nv_mov_b32 v13,0\nv_mov_b32 v14,0\nv_mov_b32 v15,0\nv_mov_b32 v16,0\nv_mov_b32 v17,0\n" \
"v_mov_b32 v18,0\nv_mov_b32 v19,0\nv_mov_b32 v20,0\nv_mov_b32 v21,0\nv_mov_b32 v22,0\nv_mov_b32 v23,0\nv_mov_b32 v24,0\nv_mov_b32 v25,0\n" \
"v_mov_b32 v26,0\nv_mov_b32 v27,0\nv_mov_b32 v28,0\nv_mov_b32 v29,0\nv_mov_b32 v30,0\nv_mov_b32 v31,0\nv_add_u32 v252,90112,v253\n" \
"v_add_u32 v255,65536,v253\nds_read_b128 v[116:119],v255\ns_waitcnt vmcnt(6)\ns_waitcnt lgkmcnt(0)\n" \
"v_mfma_f32_32x32x16_bf16 v[32:47],v[116:119],v[128:131],0\nv_mfma_f32_32x32x16_bf16 v[48:63],v[116:119],v[132:135],0\ns_nop 7\ns_nop 7\n" \
"v_max3_f32 v120,v32,v33,v34\nv_max3_f32 v121,v35,v36,v37\nv_max3_f32 v120,v120,v38,v39\nv_max3_f32 v121,v121,v40,v41\nv_max3_f32 v120,v120,v42,v43\n" \
"v_max3_f32 v121,v121,v44,v45\nv_max3_f32 v120,v120,v46,v47\nv_max_f32 v120,v120,v121\nv_mov_b32 v121,v120\ns_nop 1\nv_permlane32_swap_b32 v120,v121\n" \
"s_nop 1\nv_max_f32 v120,v120,v121\nv_sub_f32 v64,0,v120\nv_sub_f32 v65,0,v120\nv_sub_f32 v66,0,v120\nv_sub_f32 v67,0,v120\nv_sub_f32 v68,0,v120\n" \
"v_sub_f32 v69,0,v120\nv_sub_f32 v70,0,v120\nv_sub_f32 v71,0,v120\nv_sub_f32 v72,0,v120\nv_sub_f32 v73,0,v120\nv_sub_f32 v74,0,v120\n" \
"v_sub_f32 v75,0,v120\nv_sub_f32 v76,0,v120\nv_sub_f32 v77,0,v120\nv_sub_f32 v78,0,v120\nv_sub_f32 v79,0,v120\nv_max3_f32 v122,v48,v49,v50\n" \
"v_max3_f32 v123,v51,v52,v53\nv_max3_f32 v122,v122,v54,v55\nv_max3_f32 v123,v123,v56,v57\nv_max3_f32 v122,v122,v58,v59\nv_max3_f32 v123,v123,v60,v61\n" \
"v_max3_f32 v122,v122,v62,v63\nv_max_f32 v122,v122,v123\nv_mov_b32 v123,v122\ns_nop 1\nv_permlane32_swap_b32 v122,v123\ns_nop 1\n" \
"v_max_f32 v122,v122,v123\nv_sub_f32 v80,0,v122\nv_sub_f32 v81,0,v122\nv_sub_f32 v82,0,v122\nv_sub_f32 v83,0,v122\nv_sub_f32 v84,0,v122\n" \
"v_sub_f32 v85,0,v122\nv_sub_f32 v86,0,v122\nv_sub_f32 v87,0,v122\nv_sub_f32 v88,0,v122\nv_sub_f32 v89,0,v122\nv_sub_f32 v90,0,v122\n" \
"v_sub_f32 v91,0,v122\nv_sub_f32 v92,0,v122\nv_sub_f32 v93,0,v122\nv_sub_f32 v94,0,v122\nv_sub_f32 v95,0,v122\nv_mov_b32 v100,0\nv_mov_b32 v101,0\n" \
"v_mov_b32 v102,0\nv_mov_b32 v103,0\nv_mov_b32 v60,0\nv_mov_b32 v61,0\nv_mov_b32 v62,0\nv_mov_b32 v63,0\nv_mov_b32 v124,0\nv_mov_b32 v125,0\n" \
"v_mov_b32 v126,0\nv_mov_b32 v127,0\nds_read_b128 v[104:107],v252 offset:0\nds_read_b128 v[108:111],v252 offset:1024\n" \
"ds_read_b128 v[112:115],v252 offset:2048\ns_waitcnt lgkmcnt(0)\nv_mfma_f32_32x32x16_bf16 v[32:47],v[104:107],v[128:131],v[64:79]\ns_nop 7\ns_nop 7\n" \
"v_exp_f32 v32,v32\nv_cvt_pk_bf16_f32 v102,v60,v61\nv_exp_f32 v33,v33\nv_exp_f32 v34,v34\nv_cvt_pk_bf16_f32 v103,v62,v63\n" \
"v_mfma_f32_32x32x16_bf16 v[48:63],v[104:107],v[132:135],v[80:95]\ns_setprio 1\nv_exp_f32 v35,v35\nv_cvt_pk_bf16_f32 v96,v32,v33\nv_exp_f32 v36,v36\n" \
"ds_read_b128 v[116:119],v252 offset:3072\nv_exp_f32 v37,v37\nv_cvt_pk_bf16_f32 v97,v34,v35\nv_exp_f32 v38,v38\n" \
"v_mfma_f32_32x32x16_bf16 v[16:31],v[124:127],v[100:103],v[16:31]\nv_exp_f32 v39,v39\nv_cvt_pk_bf16_f32 v98,v36,v37\nv_exp_f32 v40,v40\n" \
"ds_read_b128 v[120:123],v252 offset:4096\nv_exp_f32 v41,v41\nv_cvt_pk_bf16_f32 v99,v38,v39\nv_exp_f32 v42,v42\n" \
"ds_read_b128 v[124:127],v252 offset:5120\nv_exp_f32 v43,v43\nv_cvt_pk_bf16_f32 v100,v40,v41\nv_exp_f32 v44,v44\n" \
"v_mfma_f32_32x32x16_bf16 v[0:15],v[108:111],v[96:99],v[0:15]\nv_exp_f32 v45,v45\nv_cvt_pk_bf16_f32 v101,v42,v43\nv_exp_f32 v46,v46\nv_exp_f32 v47,v47\n" \
"v_exp_f32 v48,v48\nv_cvt_pk_bf16_f32 v102,v44,v45\nv_exp_f32 v49,v49\nv_exp_f32 v50,v50\nv_cvt_pk_bf16_f32 v103,v46,v47\ns_waitcnt lgkmcnt(2)\n" \
"v_mfma_f32_32x32x16_bf16 v[32:47],v[116:119],v[128:131],v[64:79]\nv_exp_f32 v51,v51\nv_cvt_pk_bf16_f32 v96,v48,v49\nv_exp_f32 v52,v52\n" \
"v_exp_f32 v53,v53\nv_cvt_pk_bf16_f32 v97,v50,v51\nv_exp_f32 v54,v54\nv_mfma_f32_32x32x16_bf16 v[0:15],v[112:115],v[100:103],v[0:15]\n" \
"s_waitcnt vmcnt(0)\ns_add_u32 s51,s75,0\nbuffer_store_dwordx4 v[136:139],v251,%[sws],s51 offen offset:0 sc1\nv_exp_f32 v55,v55\n" \
"v_cvt_pk_bf16_f32 v98,v52,v53\nv_exp_f32 v56,v56\nv_exp_f32 v57,v57\nv_cvt_pk_bf16_f32 v99,v54,v55\nv_exp_f32 v58,v58\nv_exp_f32 v59,v59\n" \
"v_cvt_pk_bf16_f32 v100,v56,v57\nv_exp_f32 v60,v60\nv_mfma_f32_32x32x16_bf16 v[16:31],v[108:111],v[96:99],v[16:31]\nv_exp_f32 v61,v61\n" \
"v_cvt_pk_bf16_f32 v101,v58,v59\nv_exp_f32 v62,v62\nv_exp_f32 v63,v63\ns_waitcnt lgkmcnt(0)\nv_exp_f32 v32,v32\nv_cvt_pk_bf16_f32 v102,v60,v61\n" \
"v_exp_f32 v33,v33\nv_exp_f32 v34,v34\nv_cvt_pk_bf16_f32 v103,v62,v63\nv_mfma_f32_32x32x16_bf16 v[48:63],v[116:119],v[132:135],v[80:95]\n" \
"buffer_store_dwordx4 v[140:143],v251,%[sws],s51 offen offset:128 sc1\nv_exp_f32 v35,v35\nv_cvt_pk_bf16_f32 v96,v32,v33\nv_exp_f32 v36,v36\n" \
"ds_read_b128 v[104:107],v252 offset:6144\nv_exp_f32 v37,v37\nv_cvt_pk_bf16_f32 v97,v34,v35\nv_exp_f32 v38,v38\n" \
"v_mfma_f32_32x32x16_bf16 v[16:31],v[112:115],v[100:103],v[16:31]\nv_exp_f32 v39,v39\nv_cvt_pk_bf16_f32 v98,v36,v37\nv_exp_f32 v40,v40\n" \
"ds_read_b128 v[108:111],v252 offset:7168\nv_exp_f32 v41,v41\nv_cvt_pk_bf16_f32 v99,v38,v39\nv_exp_f32 v42,v42\n" \
"ds_read_b128 v[112:115],v252 offset:8192\nv_exp_f32 v43,v43\nv_cvt_pk_bf16_f32 v100,v40,v41\nv_exp_f32 v44,v44\n" \
"v_mfma_f32_32x32x16_bf16 v[0:15],v[120:123],v[96:99],v[0:15]\nv_exp_f32 v45,v45\nv_cvt_pk_bf16_f32 v101,v42,v43\nv_exp_f32 v46,v46\nv_exp_f32 v47,v47\n" \
"v_exp_f32 v48,v48\nv_cvt_pk_bf16_f32 v102,v44,v45\nv_exp_f32 v49,v49\nv_exp_f32 v50,v50\nv_cvt_pk_bf16_f32 v103,v46,v47\ns_waitcnt lgkmcnt(2)\n" \
"v_mfma_f32_32x32x16_bf16 v[32:47],v[104:107],v[128:131],v[64:79]\nbuffer_store_dwordx4 v[144:147],v251,%[sws],s51 offen offset:1024 sc1\n" \
"v_exp_f32 v51,v51\nv_cvt_pk_bf16_f32 v96,v48,v49\nv_exp_f32 v52,v52\nv_exp_f32 v53,v53\nv_cvt_pk_bf16_f32 v97,v50,v51\nv_exp_f32 v54,v54\n" \
"v_mfma_f32_32x32x16_bf16 v[0:15],v[124:127],v[100:103],v[0:15]\nv_exp_f32 v55,v55\nv_cvt_pk_bf16_f32 v98,v52,v53\nv_exp_f32 v56,v56\nv_exp_f32 v57,v57\n" \
"v_cvt_pk_bf16_f32 v99,v54,v55\nv_exp_f32 v58,v58\nv_exp_f32 v59,v59\nv_cvt_pk_bf16_f32 v100,v56,v57\nv_exp_f32 v60,v60\n" \
"v_mfma_f32_32x32x16_bf16 v[16:31],v[120:123],v[96:99],v[16:31]\nbuffer_store_dwordx4 v[148:151],v251,%[sws],s51 offen offset:1152 sc1\n" \
"v_exp_f32 v61,v61\nv_cvt_pk_bf16_f32 v101,v58,v59\nv_exp_f32 v62,v62\nv_exp_f32 v63,v63\ns_waitcnt lgkmcnt(0)\nv_exp_f32 v32,v32\n" \
"v_cvt_pk_bf16_f32 v102,v60,v61\nv_exp_f32 v33,v33\nv_exp_f32 v34,v34\nv_cvt_pk_bf16_f32 v103,v62,v63\n" \
"v_mfma_f32_32x32x16_bf16 v[48:63],v[104:107],v[132:135],v[80:95]\nv_exp_f32 v35,v35\nv_cvt_pk_bf16_f32 v96,v32,v33\nv_exp_f32 v36,v36\n" \
"ds_read_b128 v[116:119],v252 offset:9216\nv_exp_f32 v37,v37\nv_cvt_pk_bf16_f32 v97,v34,v35\nv_exp_f32 v38,v38\n" \
"v_mfma_f32_32x32x16_bf16 v[16:31],v[124:127],v[100:103],v[16:31]\nv_exp_f32 v39,v39\nv_cvt_pk_bf16_f32 v98,v36,v37\nv_exp_f32 v40,v40\n" \
"ds_read_b128 v[120:123],v252 offset:10240\nv_exp_f32 v41,v41\nv_cvt_pk_bf16_f32 v99,v38,v39\nv_exp_f32 v42,v42\n" \
"ds_read_b128 v[124:127],v252 offset:11264\nv_exp_f32 v43,v43\nv_cvt_pk_bf16_f32 v100,v40,v41\nv_exp_f32 v44,v44\n" \
"v_mfma_f32_32x32x16_bf16 v[0:15],v[108:111],v[96:99],v[0:15]\ns_add_u32 s51,s75,16384\n" \
"buffer_store_dwordx4 v[152:155],v251,%[sws],s51 offen offset:0 sc1\nv_exp_f32 v45,v45\nv_cvt_pk_bf16_f32 v101,v42,v43\nv_exp_f32 v46,v46\n" \
"v_exp_f32 v47,v47\nv_exp_f32 v48,v48\nv_cvt_pk_bf16_f32 v102,v44,v45\nv_exp_f32 v49,v49\nv_exp_f32 v50,v50\nv_cvt_pk_bf16_f32 v103,v46,v47\n" \
"s_waitcnt lgkmcnt(2)\nv_mfma_f32_32x32x16_bf16 v[32:47],v[116:119],v[128:131],v[64:79]\nv_exp_f32 v51,v51\nv_cvt_pk_bf16_f32 v96,v48,v49\n" \
"v_exp_f32 v52,v52\nv_exp_f32 v53,v53\nv_cvt_pk_bf16_f32 v97,v50,v51\nv_exp_f32 v54,v54\nv_mfma_f32_32x32x16_bf16 v[0:15],v[112:115],v[100:103],v[0:15]\n" \
"v_exp_f32 v55,v55\nv_cvt_pk_bf16_f32 v98,v52,v53\nv_exp_f32 v56,v56\nv_exp_f32 v57,v57\nv_cvt_pk_bf16_f32 v99,v54,v55\nv_exp_f32 v58,v58\n" \
"buffer_store_dwordx4 v[156:159],v251,%[sws],s51 offen offset:128 sc1\nv_exp_f32 v59,v59\nv_cvt_pk_bf16_f32 v100,v56,v57\nv_exp_f32 v60,v60\n" \
"v_mfma_f32_32x32x16_bf16 v[16:31],v[108:111],v[96:99],v[16:31]\nv_exp_f32 v61,v61\nv_cvt_pk_bf16_f32 v101,v58,v59\nv_exp_f32 v62,v62\n" \
"v_exp_f32 v63,v63\ns_waitcnt lgkmcnt(0)\nv_exp_f32 v32,v32\nv_cvt_pk_bf16_f32 v102,v60,v61\nv_exp_f32 v33,v33\nv_exp_f32 v34,v34\n" \
"v_cvt_pk_bf16_f32 v103,v62,v63\nv_mfma_f32_32x32x16_bf16 v[48:63],v[116:119],v[132:135],v[80:95]\nv_exp_f32 v35,v35\nv_cvt_pk_bf16_f32 v96,v32,v33\n" \
"v_exp_f32 v36,v36\nds_read_b128 v[104:107],v252 offset:12288\nv_exp_f32 v37,v37\nv_cvt_pk_bf16_f32 v97,v34,v35\nv_exp_f32 v38,v38\n" \
"v_mfma_f32_32x32x16_bf16 v[16:31],v[112:115],v[100:103],v[16:31]\nv_exp_f32 v39,v39\nv_cvt_pk_bf16_f32 v98,v36,v37\nv_exp_f32 v40,v40\n" \
"ds_read_b128 v[108:111],v252 offset:13312\nv_exp_f32 v41,v41\nv_cvt_pk_bf16_f32 v99,v38,v39\nv_exp_f32 v42,v42\n" \
"ds_read_b128 v[112:115],v252 offset:14336\nbuffer_store_dwordx4 v[160:163],v251,%[sws],s51 offen offset:1024 sc1\nv_exp_f32 v43,v43\n" \
"v_cvt_pk_bf16_f32 v100,v40,v41\nv_exp_f32 v44,v44\nv_mfma_f32_32x32x16_bf16 v[0:15],v[120:123],v[96:99],v[0:15]\nv_exp_f32 v45,v45\n" \
"v_cvt_pk_bf16_f32 v101,v42,v43\nv_exp_f32 v46,v46\nv_exp_f32 v47,v47\nv_exp_f32 v48,v48\nv_cvt_pk_bf16_f32 v102,v44,v45\nv_exp_f32 v49,v49\n" \
"v_exp_f32 v50,v50\nv_cvt_pk_bf16_f32 v103,v46,v47\ns_waitcnt lgkmcnt(2)\nv_mfma_f32_32x32x16_bf16 v[32:47],v[104:107],v[128:131],v[64:79]\n" \
"v_exp_f32 v51,v51\nv_cvt_pk_bf16_f32 v96,v48,v49\nv_exp_f32 v52,v52\nv_exp_f32 v53,v53\nv_cvt_pk_bf16_f32 v97,v50,v51\nv_exp_f32 v54,v54\n" \
"v_mfma_f32_32x32x16_bf16 v[0:15],v[124:127],v[100:103],v[0:15]\nbuffer_store_dwordx4 v[164:167],v251,%[sws],s51 offen offset:1152 sc1\n" \
"v_exp_f32 v55,v55\nv_cvt_pk_bf16_f32 v98,v52,v53\nv_exp_f32 v56,v56\nv_exp_f32 v57,v57\nv_cvt_pk_bf16_f32 v99,v54,v55\nv_exp_f32 v58,v58\n" \
"v_exp_f32 v59,v59\nv_cvt_pk_bf16_f32 v100,v56,v57\nv_exp_f32 v60,v60\nv_mfma_f32_32x32x16_bf16 v[16:31],v[120:123],v[96:99],v[16:31]\n" \
"v_exp_f32 v61,v61\nv_cvt_pk_bf16_f32 v101,v58,v59\nv_exp_f32 v62,v62\nv_exp_f32 v63,v63\ns_waitcnt lgkmcnt(0)\nv_exp_f32 v32,v32\n" \
"v_cvt_pk_bf16_f32 v102,v60,v61\nv_exp_f32 v33,v33\nv_exp_f32 v34,v34\nv_cvt_pk_bf16_f32 v103,v62,v63\n" \
"v_mfma_f32_32x32x16_bf16 v[48:63],v[104:107],v[132:135],v[80:95]\nv_exp_f32 v35,v35\nv_cvt_pk_bf16_f32 v96,v32,v33\nv_exp_f32 v36,v36\n" \
"ds_read_b128 v[116:119],v252 offset:15360\nv_exp_f32 v37,v37\nv_cvt_pk_bf16_f32 v97,v34,v35\nv_exp_f32 v38,v38\n" \
"v_mfma_f32_32x32x16_bf16 v[16:31],v[124:127],v[100:103],v[16:31]\ns_add_u32 s51,s75,32768\n" \
"buffer_store_dwordx4 v[168:171],v251,%[sws],s51 offen offset:0 sc1\nv_exp_f32 v39,v39\nv_cvt_pk_bf16_f32 v98,v36,v37\nv_exp_f32 v40,v40\n" \
"ds_read_b128 v[120:123],v252 offset:16384\nv_exp_f32 v41,v41\nv_cvt_pk_bf16_f32 v99,v38,v39\nv_exp_f32 v42,v42\n" \
"ds_read_b128 v[124:127],v252 offset:17408\nv_exp_f32 v43,v43\nv_cvt_pk_bf16_f32 v100,v40,v41\nv_exp_f32 v44,v44\n" \
"v_mfma_f32_32x32x16_bf16 v[0:15],v[108:111],v[96:99],v[0:15]\nv_exp_f32 v45,v45\nv_cvt_pk_bf16_f32 v101,v42,v43\nv_exp_f32 v46,v46\nv_exp_f32 v47,v47\n" \
"v_exp_f32 v48,v48\nv_cvt_pk_bf16_f32 v102,v44,v45\nv_exp_f32 v49,v49\nv_exp_f32 v50,v50\nv_cvt_pk_bf16_f32 v103,v46,v47\ns_waitcnt lgkmcnt(2)\n" \
"v_mfma_f32_32x32x16_bf16 v[32:47],v[116:119],v[128:131],v[64:79]\nbuffer_store_dwordx4 v[172:175],v251,%[sws],s51 offen offset:128 sc1\n" \
"v_exp_f32 v51,v51\nv_cvt_pk_bf16_f32 v96,v48,v49\nv_exp_f32 v52,v52\nv_exp_f32 v53,v53\nv_cvt_pk_bf16_f32 v97,v50,v51\nv_exp_f32 v54,v54\n" \
"v_mfma_f32_32x32x16_bf16 v[0:15],v[112:115],v[100:103],v[0:15]\nv_exp_f32 v55,v55\nv_cvt_pk_bf16_f32 v98,v52,v53\nv_exp_f32 v56,v56\nv_exp_f32 v57,v57\n" \
"v_cvt_pk_bf16_f32 v99,v54,v55\nv_exp_f32 v58,v58\nv_exp_f32 v59,v59\nv_cvt_pk_bf16_f32 v100,v56,v57\nv_exp_f32 v60,v60\n" \
"v_mfma_f32_32x32x16_bf16 v[16:31],v[108:111],v[96:99],v[16:31]\nv_exp_f32 v61,v61\nv_cvt_pk_bf16_f32 v101,v58,v59\nv_exp_f32 v62,v62\n" \
"v_exp_f32 v63,v63\ns_waitcnt lgkmcnt(0)\nv_exp_f32 v32,v32\nv_cvt_pk_bf16_f32 v102,v60,v61\nv_exp_f32 v33,v33\nv_exp_f32 v34,v34\n" \
"v_cvt_pk_bf16_f32 v103,v62,v63\nv_mfma_f32_32x32x16_bf16 v[48:63],v[116:119],v[132:135],v[80:95]\n" \
"buffer_store_dwordx4 v[176:179],v251,%[sws],s51 offen offset:1024 sc1\nv_exp_f32 v35,v35\nv_cvt_pk_bf16_f32 v96,v32,v33\nv_exp_f32 v36,v36\n" \
"ds_read_b128 v[104:107],v252 offset:18432\nv_exp_f32 v37,v37\nv_cvt_pk_bf16_f32 v97,v34,v35\nv_exp_f32 v38,v38\n" \
"v_mfma_f32_32x32x16_bf16 v[16:31],v[112:115],v[100:103],v[16:31]\nv_exp_f32 v39,v39\nv_cvt_pk_bf16_f32 v98,v36,v37\nv_exp_f32 v40,v40\n" \
"ds_read_b128 v[108:111],v252 offset:19456\nv_exp_f32 v41,v41\nv_cvt_pk_bf16_f32 v99,v38,v39\nv_exp_f32 v42,v42\n" \
"ds_read_b128 v[112:115],v252 offset:20480\nv_exp_f32 v43,v43\nv_cvt_pk_bf16_f32 v100,v40,v41\nv_exp_f32 v44,v44\n" \
"v_mfma_f32_32x32x16_bf16 v[0:15],v[120:123],v[96:99],v[0:15]\nbuffer_store_dwordx4 v[180:183],v251,%[sws],s51 offen offset:1152 sc1\nv_exp_f32 v45,v45\n" \
"v_cvt_pk_bf16_f32 v101,v42,v43\nv_exp_f32 v46,v46\nv_exp_f32 v47,v47\nv_exp_f32 v48,v48\nv_cvt_pk_bf16_f32 v102,v44,v45\nv_exp_f32 v49,v49\n" \
"v_exp_f32 v50,v50\nv_cvt_pk_bf16_f32 v103,v46,v47\ns_waitcnt lgkmcnt(2)\nv_mfma_f32_32x32x16_bf16 v[32:47],v[104:107],v[128:131],v[64:79]\ns_setprio 0\n" \
"v_exp_f32 v51,v51\nv_cvt_pk_bf16_f32 v96,v48,v49\nv_exp_f32 v52,v52\nv_exp_f32 v53,v53\nv_cvt_pk_bf16_f32 v97,v50,v51\nv_exp_f32 v54,v54\n" \
"v_mfma_f32_32x32x16_bf16 v[0:15],v[124:127],v[100:103],v[0:15]\nv_exp_f32 v55,v55\nv_cvt_pk_bf16_f32 v98,v52,v53\nv_exp_f32 v56,v56\nv_exp_f32 v57,v57\n" \
"v_cvt_pk_bf16_f32 v99,v54,v55\nv_exp_f32 v58,v58\nv_exp_f32 v59,v59\nv_cvt_pk_bf16_f32 v100,v56,v57\nv_exp_f32 v60,v60\n" \
"v_mfma_f32_32x32x16_bf16 v[16:31],v[120:123],v[96:99],v[16:31]\ns_add_u32 s51,s75,49152\n" \
"buffer_store_dwordx4 v[184:187],v251,%[sws],s51 offen offset:0 sc1\nv_exp_f32 v61,v61\nv_cvt_pk_bf16_f32 v101,v58,v59\nv_exp_f32 v62,v62\n" \
"v_exp_f32 v63,v63\ns_waitcnt lgkmcnt(0)\nv_exp_f32 v32,v32\nv_cvt_pk_bf16_f32 v102,v60,v61\nv_exp_f32 v33,v33\nv_exp_f32 v34,v34\n" \
"v_cvt_pk_bf16_f32 v103,v62,v63\nv_mfma_f32_32x32x16_bf16 v[48:63],v[104:107],v[132:135],v[80:95]\nv_exp_f32 v35,v35\nv_cvt_pk_bf16_f32 v96,v32,v33\n" \
"v_exp_f32 v36,v36\nds_read_b128 v[116:119],v252 offset:21504\nv_exp_f32 v37,v37\nv_cvt_pk_bf16_f32 v97,v34,v35\nv_exp_f32 v38,v38\n" \
"v_mfma_f32_32x32x16_bf16 v[16:31],v[124:127],v[100:103],v[16:31]\nv_exp_f32 v39,v39\nv_cvt_pk_bf16_f32 v98,v36,v37\nv_exp_f32 v40,v40\n" \
"ds_read_b128 v[120:123],v252 offset:22528\nv_exp_f32 v41,v41\nv_cvt_pk_bf16_f32 v99,v38,v39\nv_exp_f32 v42,v42\n" \
"ds_read_b128 v[124:127],v252 offset:23552\nbuffer_store_dwordx4 v[188:191],v251,%[sws],s51 offen offset:128 sc1\nv_exp_f32 v43,v43\n" \
"v_cvt_pk_bf16_f32 v100,v40,v41\nv_exp_f32 v44,v44\nv_mfma_f32_32x32x16_bf16 v[0:15],v[108:111],v[96:99],v[0:15]\nv_exp_f32 v45,v45\n" \
"v_cvt_pk_bf16_f32 v101,v42,v43\nv_exp_f32 v46,v46\nv_exp_f32 v47,v47\nv_exp_f32 v48,v48\nv_cvt_pk_bf16_f32 v102,v44,v45\nv_exp_f32 v49,v49\n" \
"v_exp_f32 v50,v50\nv_cvt_pk_bf16_f32 v103,v46,v47\ns_waitcnt lgkmcnt(2)\nv_mfma_f32_32x32x16_bf16 v[32:47],v[116:119],v[128:131],v[64:79]\n" \
"v_exp_f32 v51,v51\nv_cvt_pk_bf16_f32 v96,v48,v49\nv_exp_f32 v52,v52\nv_exp_f32 v53,v53\nv_cvt_pk_bf16_f32 v97,v50,v51\nv_exp_f32 v54,v54\n" \
"v_mfma_f32_32x32x16_bf16 v[0:15],v[112:115],v[100:103],v[0:15]\nv_exp_f32 v55,v55\nv_cvt_pk_bf16_f32 v98,v52,v53\nv_exp_f32 v56,v56\nv_exp_f32 v57,v57\n" \
"v_cvt_pk_bf16_f32 v99,v54,v55\nv_exp_f32 v58,v58\nbuffer_store_dwordx4 v[224:227],v251,%[sws],s51 offen offset:1024 sc1\nv_exp_f32 v59,v59\n" \
"v_cvt_pk_bf16_f32 v100,v56,v57\nv_exp_f32 v60,v60\nv_mfma_f32_32x32x16_bf16 v[16:31],v[108:111],v[96:99],v[16:31]\ns_barrier\n" \
"v_add_u32 v252,24576,v253\ns_setprio 1\nv_exp_f32 v61,v61\nv_cvt_pk_bf16_f32 v101,v58,v59\nv_exp_f32 v62,v62\nv_exp_f32 v63,v63\ns_waitcnt lgkmcnt(0)\n" \
"v_exp_f32 v32,v32\nv_cvt_pk_bf16_f32 v102,v60,v61\nv_exp_f32 v33,v33\nv_exp_f32 v34,v34\nv_cvt_pk_bf16_f32 v103,v62,v63\n" \
"v_mfma_f32_32x32x16_bf16 v[48:63],v[116:119],v[132:135],v[80:95]\nv_exp_f32 v35,v35\nv_cvt_pk_bf16_f32 v96,v32,v33\nv_exp_f32 v36,v36\n" \
"ds_read_b128 v[104:107],v252 offset:0\nv_exp_f32 v37,v37\nv_cvt_pk_bf16_f32 v97,v34,v35\nv_exp_f32 v38,v38\n" \
"v_mfma_f32_32x32x16_bf16 v[16:31],v[112:115],v[100:103],v[16:31]\nbuffer_store_dwordx4 v[228:231],v251,%[sws],s51 offen offset:1152 sc1\n" \
"v_exp_f32 v39,v39\nv_cvt_pk_bf16_f32 v98,v36,v37\nv_exp_f32 v40,v40\nds_read_b128 v[108:111],v252 offset:1024\nv_exp_f32 v41,v41\n" \
"v_cvt_pk_bf16_f32 v99,v38,v39\nv_exp_f32 v42,v42\nds_read_b128 v[112:115],v252 offset:2048\nv_exp_f32 v43,v43\nv_cvt_pk_bf16_f32 v100,v40,v41\n" \
"v_exp_f32 v44,v44\nv_mfma_f32_32x32x16_bf16 v[0:15],v[120:123],v[96:99],v[0:15]\nv_exp_f32 v45,v45\nv_cvt_pk_bf16_f32 v101,v42,v43\nv_exp_f32 v46,v46\n" \
"v_exp_f32 v47,v47\nv_exp_f32 v48,v48\nv_cvt_pk_bf16_f32 v102,v44,v45\nv_exp_f32 v49,v49\nv_exp_f32 v50,v50\nv_cvt_pk_bf16_f32 v103,v46,v47\n" \
"s_waitcnt lgkmcnt(2)\nv_mfma_f32_32x32x16_bf16 v[32:47],v[104:107],v[128:131],v[64:79]\nv_exp_f32 v51,v51\nv_cvt_pk_bf16_f32 v96,v48,v49\n" \
"v_exp_f32 v52,v52\nv_exp_f32 v53,v53\nv_cvt_pk_bf16_f32 v97,v50,v51\nv_exp_f32 v54,v54\nv_mfma_f32_32x32x16_bf16 v[0:15],v[124:127],v[100:103],v[0:15]\n" \
"v_exp_f32 v55,v55\nv_cvt_pk_bf16_f32 v98,v52,v53\nv_exp_f32 v56,v56\nv_exp_f32 v57,v57\nv_cvt_pk_bf16_f32 v99,v54,v55\nv_exp_f32 v58,v58\n" \
"v_exp_f32 v59,v59\nv_cvt_pk_bf16_f32 v100,v56,v57\nv_exp_f32 v60,v60\nv_mfma_f32_32x32x16_bf16 v[16:31],v[120:123],v[96:99],v[16:31]\ns_setprio 0\n" \
"v_exp_f32 v61,v61\nv_cvt_pk_bf16_f32 v101,v58,v59\nv_exp_f32 v62,v62\nv_exp_f32 v63,v63\ns_waitcnt lgkmcnt(0)\nv_exp_f32 v32,v32\n" \
"v_cvt_pk_bf16_f32 v102,v60,v61\nv_exp_f32 v33,v33\nv_exp_f32 v34,v34\nv_cvt_pk_bf16_f32 v103,v62,v63\n" \
"v_mfma_f32_32x32x16_bf16 v[48:63],v[104:107],v[132:135],v[80:95]\nv_exp_f32 v35,v35\nv_cvt_pk_bf16_f32 v96,v32,v33\nv_exp_f32 v36,v36\n" \
"ds_read_b128 v[116:119],v252 offset:3072\nv_exp_f32 v37,v37\nv_cvt_pk_bf16_f32 v97,v34,v35\nv_exp_f32 v38,v38\n" \
"v_mfma_f32_32x32x16_bf16 v[16:31],v[124:127],v[100:103],v[16:31]\ns_barrier\ns_setprio 1\nv_exp_f32 v39,v39\nv_cvt_pk_bf16_f32 v98,v36,v37\n" \
"v_exp_f32 v40,v40\nds_read_b128 v[120:123],v252 offset:4096\nv_exp_f32 v41,v41\nv_cvt_pk_bf16_f32 v99,v38,v39\nv_exp_f32 v42,v42\n" \
"ds_read_b128 v[124:127],v252 offset:5120\ns_and_b32 s50,s66,0xffff\ns_add_u32 m0,s50,65536\ns_add_u32 s49,s67,1048576\n" \
"buffer_load_dwordx4 v249,%[sq],s49 offen lds\nv_exp_f32 v43,v43\nv_cvt_pk_bf16_f32 v100,v40,v41\nv_exp_f32 v44,v44\n" \
"v_mfma_f32_32x32x16_bf16 v[0:15],v[108:111],v[96:99],v[0:15]\nv_exp_f32 v45,v45\nv_cvt_pk_bf16_f32 v101,v42,v43\nv_exp_f32 v46,v46\nv_exp_f32 v47,v47\n" \
"v_exp_f32 v48,v48\nv_cvt_pk_bf16_f32 v102,v44,v45\nv_exp_f32 v49,v49\nv_exp_f32 v50,v50\nv_cvt_pk_bf16_f32 v103,v46,v47\ns_waitcnt lgkmcnt(2)\n" \
"v_mfma_f32_32x32x16_bf16 v[32:47],v[116:119],v[128:131],v[64:79]\ns_add_u32 m0,m0,1024\ns_add_u32 s49,s49,32768\n" \
"buffer_load_dwordx4 v249,%[sq],s49 offen lds\nv_exp_f32 v51,v51\nv_cvt_pk_bf16_f32 v96,v48,v49\nv_exp_f32 v52,v52\nv_exp_f32 v53,v53\n" \
"v_cvt_pk_bf16_f32 v97,v50,v51\nv_exp_f32 v54,v54\nv_mfma_f32_32x32x16_bf16 v[0:15],v[112:115],v[100:103],v[0:15]\nv_exp_f32 v55,v55\n" \
"v_cvt_pk_bf16_f32 v98,v52,v53\nv_exp_f32 v56,v56\nv_exp_f32 v57,v57\nv_cvt_pk_bf16_f32 v99,v54,v55\nv_exp_f32 v58,v58\ns_add_u32 m0,m0,1024\n" \
"s_add_u32 s49,s49,32768\nbuffer_load_dwordx4 v249,%[sq],s49 offen lds\nv_exp_f32 v59,v59\nv_cvt_pk_bf16_f32 v100,v56,v57\nv_exp_f32 v60,v60\n" \
"v_mfma_f32_32x32x16_bf16 v[16:31],v[108:111],v[96:99],v[16:31]\nv_exp_f32 v61,v61\nv_cvt_pk_bf16_f32 v101,v58,v59\nv_exp_f32 v62,v62\n" \
"v_exp_f32 v63,v63\ns_waitcnt lgkmcnt(0)\nv_exp_f32 v32,v32\nv_cvt_pk_bf16_f32 v102,v60,v61\nv_exp_f32 v33,v33\nv_exp_f32 v34,v34\n" \
"v_cvt_pk_bf16_f32 v103,v62,v63\nv_mfma_f32_32x32x16_bf16 v[48:63],v[116:119],v[132:135],v[80:95]\ns_add_u32 m0,m0,1024\ns_add_u32 s49,s49,32768\n" \
"buffer_load_dwordx4 v249,%[sq],s49 offen lds\nv_exp_f32 v35,v35\nv_cvt_pk_bf16_f32 v96,v32,v33\nv_exp_f32 v36,v36\n" \
"ds_read_b128 v[104:107],v252 offset:6144\nv_exp_f32 v37,v37\nv_cvt_pk_bf16_f32 v97,v34,v35\nv_exp_f32 v38,v38\n" \
"v_mfma_f32_32x32x16_bf16 v[16:31],v[112:115],v[100:103],v[16:31]\nv_exp_f32 v39,v39\nv_cvt_pk_bf16_f32 v98,v36,v37\nv_exp_f32 v40,v40\n" \
"ds_read_b128 v[108:111],v252 offset:7168\nv_exp_f32 v41,v41\nv_cvt_pk_bf16_f32 v99,v38,v39\nv_exp_f32 v42,v42\n" \
"ds_read_b128 v[112:115],v252 offset:8192\ns_and_b32 s50,s66,0xffff\ns_add_u32 m0,s50,98304\ns_add_u32 s49,s67,2097152\n" \
"buffer_load_dwordx4 v249,%[sq],s49 offen lds\nv_exp_f32 v43,v43\nv_cvt_pk_bf16_f32 v100,v40,v41\nv_exp_f32 v44,v44\n" \
"v_mfma_f32_32x32x16_bf16 v[0:15],v[120:123],v[96:99],v[0:15]\nv_exp_f32 v45,v45\nv_cvt_pk_bf16_f32 v101,v42,v43\nv_exp_f32 v46,v46\nv_exp_f32 v47,v47\n" \
"v_exp_f32 v48,v48\nv_cvt_pk_bf16_f32 v102,v44,v45\nv_exp_f32 v49,v49\nv_exp_f32 v50,v50\nv_cvt_pk_bf16_f32 v103,v46,v47\ns_waitcnt lgkmcnt(2)\n" \
"v_mfma_f32_32x32x16_bf16 v[32:47],v[104:107],v[128:131],v[64:79]\ns_add_u32 m0,m0,1024\ns_add_u32 s49,s49,32768\n" \
"buffer_load_dwordx4 v249,%[sq],s49 offen lds\nv_exp_f32 v51,v51\nv_cvt_pk_bf16_f32 v96,v48,v49\nv_exp_f32 v52,v52\nv_exp_f32 v53,v53\n" \
"v_cvt_pk_bf16_f32 v97,v50,v51\nv_exp_f32 v54,v54\nv_mfma_f32_32x32x16_bf16 v[0:15],v[124:127],v[100:103],v[0:15]\nv_exp_f32 v55,v55\n" \
"v_cvt_pk_bf16_f32 v98,v52,v53\nv_exp_f32 v56,v56\nv_exp_f32 v57,v57\nv_cvt_pk_bf16_f32 v99,v54,v55\nv_exp_f32 v58,v58\ns_add_u32 m0,m0,1024\n" \
"s_add_u32 s49,s49,32768\nbuffer_load_dwordx4 v249,%[sq],s49 offen lds\nv_exp_f32 v59,v59\nv_cvt_pk_bf16_f32 v100,v56,v57\nv_exp_f32 v60,v60\n" \
"v_mfma_f32_32x32x16_bf16 v[16:31],v[120:123],v[96:99],v[16:31]\nv_exp_f32 v61,v61\nv_cvt_pk_bf16_f32 v101,v58,v59\nv_exp_f32 v62,v62\n" \
"v_exp_f32 v63,v63\ns_waitcnt lgkmcnt(0)\nv_exp_f32 v32,v32\nv_cvt_pk_bf16_f32 v102,v60,v61\nv_exp_f32 v33,v33\nv_exp_f32 v34,v34\n" \
"v_cvt_pk_bf16_f32 v103,v62,v63\nv_mfma_f32_32x32x16_bf16 v[48:63],v[104:107],v[132:135],v[80:95]\ns_setprio 0\ns_add_u32 m0,m0,1024\n" \
"s_add_u32 s49,s49,32768\nbuffer_load_dwordx4 v249,%[sq],s49 offen lds\nv_exp_f32 v35,v35\nv_cvt_pk_bf16_f32 v96,v32,v33\nv_exp_f32 v36,v36\n" \
"ds_read_b128 v[116:119],v252 offset:9216\nv_exp_f32 v37,v37\nv_cvt_pk_bf16_f32 v97,v34,v35\nv_exp_f32 v38,v38\n" \
"v_mfma_f32_32x32x16_bf16 v[16:31],v[124:127],v[100:103],v[16:31]\nv_exp_f32 v39,v39\nv_cvt_pk_bf16_f32 v98,v36,v37\nv_exp_f32 v40,v40\n" \
"ds_read_b128 v[120:123],v252 offset:10240\nv_exp_f32 v41,v41\nv_cvt_pk_bf16_f32 v99,v38,v39\nv_exp_f32 v42,v42\n" \
"ds_read_b128 v[124:127],v252 offset:11264\nv_exp_f32 v43,v43\nv_cvt_pk_bf16_f32 v100,v40,v41\nv_exp_f32 v44,v44\n" \
"v_mfma_f32_32x32x16_bf16 v[0:15],v[108:111],v[96:99],v[0:15]\nv_exp_f32 v45,v45\nv_cvt_pk_bf16_f32 v101,v42,v43\nv_exp_f32 v46,v46\nv_exp_f32 v47,v47\n" \
"v_exp_f32 v48,v48\nv_cvt_pk_bf16_f32 v102,v44,v45\nv_exp_f32 v49,v49\nv_exp_f32 v50,v50\nv_cvt_pk_bf16_f32 v103,v46,v47\ns_waitcnt lgkmcnt(2)\n" \
"v_mfma_f32_32x32x16_bf16 v[32:47],v[116:119],v[128:131],v[64:79]\ns_waitcnt vmcnt(0)\ns_barrier\ns_cmp_eq_u32 s77,0\ns_cbranch_scc1 20f\n" \
"v_mov_b32 v136,0\nv_mov_b32 v137,1\ns_mov_b64 exec,1\nglobal_atomic_add v136,v137,s[78:79]\ns_mov_b64 exec,-1\n20:\ns_setprio 1\nv_exp_f32 v51,v51\n" \
"v_cvt_pk_bf16_f32 v96,v48,v49\nv_exp_f32 v52,v52\nv_exp_f32 v53,v53\nv_cvt_pk_bf16_f32 v97,v50,v51\nv_exp_f32 v54,v54\n" \
"v_mfma_f32_32x32x16_bf16 v[0:15],v[112:115],v[100:103],v[0:15]\ns_add_u32 s58,s76,8192\nbuffer_load_dwordx4 v[136:139],v248,%[sws],s58 offen offset:0\n" \
"v_exp_f32 v55,v55\nv_cvt_pk_bf16_f32 v98,v52,v53\nv_exp_f32 v56,v56\nv_exp_f32 v57,v57\nv_cvt_pk_bf16_f32 v99,v54,v55\nv_exp_f32 v58,v58\n" \
"buffer_load_dwordx4 v[140:143],v248,%[sws],s58 offen offset:1024\nv_exp_f32 v59,v59\nv_cvt_pk_bf16_f32 v100,v56,v57\nv_exp_f32 v60,v60\n" \
"v_mfma_f32_32x32x16_bf16 v[16:31],v[108:111],v[96:99],v[16:31]\nbuffer_load_dwordx4 v[144:147],v248,%[sws],s58 offen offset:2048\nv_exp_f32 v61,v61\n" \
"v_cvt_pk_bf16_f32 v101,v58,v59\nv_exp_f32 v62,v62\nv_exp_f32 v63,v63\ns_waitcnt lgkmcnt(0)\nv_exp_f32 v32,v32\nv_cvt_pk_bf16_f32 v102,v60,v61\n" \
"v_exp_f32 v33,v33\nv_exp_f32 v34,v34\nv_cvt_pk_bf16_f32 v103,v62,v63\nv_mfma_f32_32x32x16_bf16 v[48:63],v[116:119],v[132:135],v[80:95]\n" \
"buffer_load_dwordx4 v[148:151],v248,%[sws],s58 offen offset:3072\nv_exp_f32 v35,v35\nv_cvt_pk_bf16_f32 v96,v32,v33\nv_exp_f32 v36,v36\n" \
"ds_read_b128 v[104:107],v252 offset:12288\nv_exp_f32 v37,v37\nv_cvt_pk_bf16_f32 v97,v34,v35\nv_exp_f32 v38,v38\n" \
"v_mfma_f32_32x32x16_bf16 v[16:31],v[112:115],v[100:103],v[16:31]\ns_add_u32 s58,s76,12288\n" \
"buffer_load_dwordx4 v[152:155],v248,%[sws],s58 offen offset:0\nv_exp_f32 v39,v39\nv_cvt_pk_bf16_f32 v98,v36,v37\nv_exp_f32 v40,v40\n" \
"ds_read_b128 v[108:111],v252 offset:13312\nv_exp_f32 v41,v41\nv_cvt_pk_bf16_f32 v99,v38,v39\nv_exp_f32 v42,v42\n" \
"ds_read_b128 v[112:115],v252 offset:14336\nbuffer_load_dwordx4 v[156:159],v248,%[sws],s58 offen offset:1024\nv_exp_f32 v43,v43\n" \
"v_cvt_pk_bf16_f32 v100,v40,v41\nv_exp_f32 v44,v44\nv_mfma_f32_32x32x16_bf16 v[0:15],v[120:123],v[96:99],v[0:15]\n" \
"buffer_load_dwordx4 v[160:163],v248,%[sws],s58 offen offset:2048\nv_exp_f32 v45,v45\nv_cvt_pk_bf16_f32 v101,v42,v43\nv_exp_f32 v46,v46\n" \
"v_exp_f32 v47,v47\nv_exp_f32 v48,v48\nv_cvt_pk_bf16_f32 v102,v44,v45\nv_exp_f32 v49,v49\nv_exp_f32 v50,v50\nv_cvt_pk_bf16_f32 v103,v46,v47\n" \
"s_waitcnt lgkmcnt(2)\nv_mfma_f32_32x32x16_bf16 v[32:47],v[104:107],v[128:131],v[64:79]\n" \
"buffer_load_dwordx4 v[164:167],v248,%[sws],s58 offen offset:3072\nv_exp_f32 v51,v51\nv_cvt_pk_bf16_f32 v96,v48,v49\nv_exp_f32 v52,v52\n" \
"v_exp_f32 v53,v53\nv_cvt_pk_bf16_f32 v97,v50,v51\nv_exp_f32 v54,v54\nv_mfma_f32_32x32x16_bf16 v[0:15],v[124:127],v[100:103],v[0:15]\n" \
"s_add_u32 s58,s76,16384\nbuffer_load_dwordx4 v[168:171],v248,%[sws],s58 offen offset:0\nv_exp_f32 v55,v55\nv_cvt_pk_bf16_f32 v98,v52,v53\n" \
"v_exp_f32 v56,v56\nv_exp_f32 v57,v57\nv_cvt_pk_bf16_f32 v99,v54,v55\nv_exp_f32 v58,v58\n" \
"buffer_load_dwordx4 v[172:175],v248,%[sws],s58 offen offset:1024\nv_exp_f32 v59,v59\nv_cvt_pk_bf16_f32 v100,v56,v57\nv_exp_f32 v60,v60\n" \
"v_mfma_f32_32x32x16_bf16 v[16:31],v[120:123],v[96:99],v[16:31]\nbuffer_load_dwordx4 v[176:179],v248,%[sws],s58 offen offset:2048\nv_exp_f32 v61,v61\n" \
"v_cvt_pk_bf16_f32 v101,v58,v59\nv_exp_f32 v62,v62\nv_exp_f32 v63,v63\ns_waitcnt lgkmcnt(0)\nv_exp_f32 v32,v32\nv_cvt_pk_bf16_f32 v102,v60,v61\n" \
"v_exp_f32 v33,v33\nv_exp_f32 v34,v34\nv_cvt_pk_bf16_f32 v103,v62,v63\nv_mfma_f32_32x32x16_bf16 v[48:63],v[104:107],v[132:135],v[80:95]\n" \
"buffer_load_dwordx4 v[180:183],v248,%[sws],s58 offen offset:3072\nv_exp_f32 v35,v35\nv_cvt_pk_bf16_f32 v96,v32,v33\nv_exp_f32 v36,v36\n" \
"ds_read_b128 v[116:119],v252 offset:15360\nv_exp_f32 v37,v37\nv_cvt_pk_bf16_f32 v97,v34,v35\nv_exp_f32 v38,v38\n" \
"v_mfma_f32_32x32x16_bf16 v[16:31],v[124:127],v[100:103],v[16:31]\ns_add_u32 s58,s76,20480\n" \
"buffer_load_dwordx4 v[184:187],v248,%[sws],s58 offen offset:0\nv_exp_f32 v39,v39\nv_cvt_pk_bf16_f32 v98,v36,v37\nv_exp_f32 v40,v40\n" \
"ds_read_b128 v[120:123],v252 offset:16384\nv_exp_f32 v41,v41\nv_cvt_pk_bf16_f32 v99,v38,v39\nv_exp_f32 v42,v42\n" \
"ds_read_b128 v[124:127],v252 offset:17408\nbuffer_load_dwordx4 v[188:191],v248,%[sws],s58 offen offset:1024\nv_exp_f32 v43,v43\n" \
"v_cvt_pk_bf16_f32 v100,v40,v41\nv_exp_f32 v44,v44\nv_mfma_f32_32x32x16_bf16 v[0:15],v[108:111],v[96:99],v[0:15]\nv_exp_f32 v45,v45\n" \
"v_cvt_pk_bf16_f32 v101,v42,v43\nv_exp_f32 v46,v46\nv_exp_f32 v47,v47\nv_exp_f32 v48,v48\nv_cvt_pk_bf16_f32 v102,v44,v45\nv_exp_f32 v49,v49\n" \
"v_exp_f32 v50,v50\nv_cvt_pk_bf16_f32 v103,v46,v47\ns_waitcnt lgkmcnt(2)\nv_mfma_f32_32x32x16_bf16 v[32:47],v[116:119],v[128:131],v[64:79]\n" \
"v_exp_f32 v51,v51\nv_cvt_pk_bf16_f32 v96,v48,v49\nv_exp_f32 v52,v52\nv_exp_f32 v53,v53\nv_cvt_pk_bf16_f32 v97,v50,v51\nv_exp_f32 v54,v54\n" \
"v_mfma_f32_32x32x16_bf16 v[0:15],v[112:115],v[100:103],v[0:15]\nv_exp_f32 v55,v55\nv_cvt_pk_bf16_f32 v98,v52,v53\nv_exp_f32 v56,v56\nv_exp_f32 v57,v57\n" \
"v_cvt_pk_bf16_f32 v99,v54,v55\nv_exp_f32 v58,v58\nv_exp_f32 v59,v59\nv_cvt_pk_bf16_f32 v100,v56,v57\nv_exp_f32 v60,v60\n" \
"v_mfma_f32_32x32x16_bf16 v[16:31],v[108:111],v[96:99],v[16:31]\nv_exp_f32 v61,v61\nv_cvt_pk_bf16_f32 v101,v58,v59\nv_exp_f32 v62,v62\n" \
"v_exp_f32 v63,v63\ns_waitcnt lgkmcnt(0)\nv_exp_f32 v32,v32\nv_cvt_pk_bf16_f32 v102,v60,v61\nv_exp_f32 v33,v33\nv_exp_f32 v34,v34\n" \
"v_cvt_pk_bf16_f32 v103,v62,v63\nv_mfma_f32_32x32x16_bf16 v[48:63],v[116:119],v[132:135],v[80:95]\nv_exp_f32 v35,v35\nv_cvt_pk_bf16_f32 v96,v32,v33\n" \
"v_exp_f32 v36,v36\nds_read_b128 v[104:107],v252 offset:18432\nv_exp_f32 v37,v37\nv_cvt_pk_bf16_f32 v97,v34,v35\nv_exp_f32 v38,v38\n" \
"v_mfma_f32_32x32x16_bf16 v[16:31],v[112:115],v[100:103],v[16:31]\nv_exp_f32 v39,v39\nv_cvt_pk_bf16_f32 v98,v36,v37\nv_exp_f32 v40,v40\n" \
"ds_read_b128 v[108:111],v252 offset:19456\nv_exp_f32 v41,v41\nv_cvt_pk_bf16_f32 v99,v38,v39\nv_exp_f32 v42,v42\n" \
"ds_read_b128 v[112:115],v252 offset:20480\nv_exp_f32 v43,v43\nv_cvt_pk_bf16_f32 v100,v40,v41\nv_exp_f32 v44,v44\n" \
"v_mfma_f32_32x32x16_bf16 v[0:15],v[120:123],v[96:99],v[0:15]\nv_exp_f32 v45,v45\nv_cvt_pk_bf16_f32 v101,v42,v43\nv_exp_f32 v46,v46\nv_exp_f32 v47,v47\n" \
"v_exp_f32 v48,v48\nv_cvt_pk_bf16_f32 v102,v44,v45\nv_exp_f32 v49,v49\nv_exp_f32 v50,v50\nv_cvt_pk_bf16_f32 v103,v46,v47\ns_waitcnt lgkmcnt(2)\n" \
"v_mfma_f32_32x32x16_bf16 v[32:47],v[104:107],v[128:131],v[64:79]\nv_exp_f32 v51,v51\nv_cvt_pk_bf16_f32 v96,v48,v49\nv_exp_f32 v52,v52\n" \
"v_exp_f32 v53,v53\nv_cvt_pk_bf16_f32 v97,v50,v51\nv_exp_f32 v54,v54\nv_mfma_f32_32x32x16_bf16 v[0:15],v[124:127],v[100:103],v[0:15]\nv_exp_f32 v55,v55\n" \
"v_cvt_pk_bf16_f32 v98,v52,v53\nv_exp_f32 v56,v56\nv_exp_f32 v57,v57\nv_cvt_pk_bf16_f32 v99,v54,v55\nv_exp_f32 v58,v58\nv_exp_f32 v59,v59\n" \
"v_cvt_pk_bf16_f32 v100,v56,v57\nv_exp_f32 v60,v60\nv_mfma_f32_32x32x16_bf16 v[16:31],v[120:123],v[96:99],v[16:31]\nv_exp_f32 v61,v61\n" \
"v_cvt_pk_bf16_f32 v101,v58,v59\nv_exp_f32 v62,v62\nv_exp_f32 v63,v63\ns_waitcnt lgkmcnt(0)\nv_exp_f32 v32,v32\nv_cvt_pk_bf16_f32 v102,v60,v61\n" \
"v_exp_f32 v33,v33\nv_exp_f32 v34,v34\nv_cvt_pk_bf16_f32 v103,v62,v63\nv_mfma_f32_32x32x16_bf16 v[48:63],v[104:107],v[132:135],v[80:95]\n" \
"v_exp_f32 v35,v35\nv_cvt_pk_bf16_f32 v96,v32,v33\nv_exp_f32 v36,v36\nds_read_b128 v[116:119],v252 offset:21504\nv_exp_f32 v37,v37\n" \
"v_cvt_pk_bf16_f32 v97,v34,v35\nv_exp_f32 v38,v38\nv_mfma_f32_32x32x16_bf16 v[16:31],v[124:127],v[100:103],v[16:31]\nv_exp_f32 v39,v39\n" \
"v_cvt_pk_bf16_f32 v98,v36,v37\nv_exp_f32 v40,v40\nds_read_b128 v[120:123],v252 offset:22528\nv_exp_f32 v41,v41\nv_cvt_pk_bf16_f32 v99,v38,v39\n" \
"v_exp_f32 v42,v42\nds_read_b128 v[124:127],v252 offset:23552\nv_exp_f32 v43,v43\nv_cvt_pk_bf16_f32 v100,v40,v41\nv_exp_f32 v44,v44\n" \
"v_mfma_f32_32x32x16_bf16 v[0:15],v[108:111],v[96:99],v[0:15]\nv_exp_f32 v45,v45\nv_cvt_pk_bf16_f32 v101,v42,v43\nv_exp_f32 v46,v46\nv_exp_f32 v47,v47\n" \
"v_exp_f32 v48,v48\nv_cvt_pk_bf16_f32 v102,v44,v45\nv_exp_f32 v49,v49\nv_exp_f32 v50,v50\nv_cvt_pk_bf16_f32 v103,v46,v47\ns_waitcnt lgkmcnt(2)\n" \
"v_mfma_f32_32x32x16_bf16 v[32:47],v[116:119],v[128:131],v[64:79]\nv_exp_f32 v51,v51\nv_cvt_pk_bf16_f32 v96,v48,v49\nv_exp_f32 v52,v52\n" \
"v_exp_f32 v53,v53\nv_cvt_pk_bf16_f32 v97,v50,v51\nv_exp_f32 v54,v54\nv_mfma_f32_32x32x16_bf16 v[0:15],v[112:115],v[100:103],v[0:15]\nv_exp_f32 v55,v55\n" \
"v_cvt_pk_bf16_f32 v98,v52,v53\nv_exp_f32 v56,v56\nv_exp_f32 v57,v57\nv_cvt_pk_bf16_f32 v99,v54,v55\nv_exp_f32 v58,v58\nv_exp_f32 v59,v59\n" \
"v_cvt_pk_bf16_f32 v100,v56,v57\nv_exp_f32 v60,v60\nv_mfma_f32_32x32x16_bf16 v[16:31],v[108:111],v[96:99],v[16:31]\ns_setprio 0\nv_exp_f32 v61,v61\n" \
"v_cvt_pk_bf16_f32 v101,v58,v59\nv_exp_f32 v62,v62\nv_exp_f32 v63,v63\ns_waitcnt lgkmcnt(0)\nv_exp_f32 v32,v32\nv_cvt_pk_bf16_f32 v102,v60,v61\n" \
"v_exp_f32 v33,v33\nv_exp_f32 v34,v34\nv_cvt_pk_bf16_f32 v103,v62,v63\nv_mfma_f32_32x32x16_bf16 v[48:63],v[116:119],v[132:135],v[80:95]\n" \
"v_exp_f32 v35,v35\nv_cvt_pk_bf16_f32 v96,v32,v33\nv_exp_f32 v36,v36\nv_exp_f32 v37,v37\nv_cvt_pk_bf16_f32 v97,v34,v35\nv_exp_f32 v38,v38\n" \
"v_mfma_f32_32x32x16_bf16 v[16:31],v[112:115],v[100:103],v[16:31]\nv_exp_f32 v39,v39\nv_cvt_pk_bf16_f32 v98,v36,v37\nv_exp_f32 v40,v40\n" \
"v_exp_f32 v41,v41\nv_cvt_pk_bf16_f32 v99,v38,v39\nv_exp_f32 v42,v42\nv_exp_f32 v43,v43\nv_cvt_pk_bf16_f32 v100,v40,v41\nv_exp_f32 v44,v44\n" \
"v_mfma_f32_32x32x16_bf16 v[0:15],v[120:123],v[96:99],v[0:15]\nv_exp_f32 v45,v45\nv_cvt_pk_bf16_f32 v101,v42,v43\nv_exp_f32 v46,v46\nv_exp_f32 v47,v47\n" \
"v_exp_f32 v48,v48\nv_cvt_pk_bf16_f32 v102,v44,v45\nv_exp_f32 v49,v49\nv_exp_f32 v50,v50\nv_cvt_pk_bf16_f32 v103,v46,v47\nv_exp_f32 v51,v51\n" \
"v_cvt_pk_bf16_f32 v96,v48,v49\nv_exp_f32 v52,v52\nv_exp_f32 v53,v53\nv_cvt_pk_bf16_f32 v97,v50,v51\nv_exp_f32 v54,v54\n" \
"v_mfma_f32_32x32x16_bf16 v[0:15],v[124:127],v[100:103],v[0:15]\nv_exp_f32 v55,v55\nv_cvt_pk_bf16_f32 v98,v52,v53\nv_exp_f32 v56,v56\nv_exp_f32 v57,v57\n" \
"v_cvt_pk_bf16_f32 v99,v54,v55\nv_exp_f32 v58,v58\nv_exp_f32 v59,v59\nv_cvt_pk_bf16_f32 v100,v56,v57\nv_exp_f32 v60,v60\n" \
"v_mfma_f32_32x32x16_bf16 v[16:31],v[120:123],v[96:99],v[16:31]\nv_exp_f32 v61,v61\nv_cvt_pk_bf16_f32 v101,v58,v59\nv_exp_f32 v62,v62\n" \
"v_exp_f32 v63,v63\ns_nop 1\nv_cvt_pk_bf16_f32 v102,v60,v61\nv_cvt_pk_bf16_f32 v103,v62,v63\ns_nop 1\n" \
"v_mfma_f32_32x32x16_bf16 v[16:31],v[124:127],v[100:103],v[16:31]\ns_waitcnt lgkmcnt(0)\ns_nop 7\ns_nop 7\nv_mov_b32 v104,v0\nv_mov_b32 v105,v1\n" \
"v_mov_b32 v106,v2\nv_mov_b32 v107,v3\nv_mov_b32 v108,v4\nv_mov_b32 v109,v5\nv_mov_b32 v110,v6\nv_mov_b32 v111,v7\nv_mov_b32 v112,v8\n" \
"v_mov_b32 v113,v16\nv_mov_b32 v114,v17\nv_mov_b32 v115,v18\nv_mov_b32 v116,v19\nv_mov_b32 v117,v20\nv_mov_b32 v118,v21\nv_mov_b32 v119,v22\n" \
"v_mov_b32 v120,v23\nv_mov_b32 v121,v24\nv_mov_b32 v122,v64\nv_mov_b32 v123,v80\nv_mov_b32 v124,v128\nv_mov_b32 v125,v129\nv_mov_b32 v126,v130\n" \
"v_mov_b32 v127,v131\nv_mov_b32 v128,v132\nv_mov_b32 v129,v133\nv_mov_b32 v130,v134\nv_mov_b32 v131,v135\nv_add_u32 v224,131072,v250\n" \
"v_add_u32 v225,65536,v250\nds_read_b64_tr_b16 v[64:65],v224 offset:0\nds_read_b64_tr_b16 v[66:67],v224 offset:1024\n" \
"ds_read_b64_tr_b16 v[68:69],v224 offset:256\nds_read_b64_tr_b16 v[70:71],v224 offset:1280\nds_read_b64_tr_b16 v[72:73],v224 offset:512\n" \
"ds_read_b64_tr_b16 v[74:75],v224 offset:1536\nds_read_b64_tr_b16 v[76:77],v224 offset:768\nds_read_b64_tr_b16 v[78:79],v224 offset:1792\n" \
"ds_read_b64_tr_b16 v[80:81],v224 offset:4096\nds_read_b64_tr_b16 v[82:83],v224 offset:5120\nds_read_b64_tr_b16 v[84:85],v224 offset:4352\n" \
"ds_read_b64_tr_b16 v[86:87],v224 offset:5376\ns_add_u32 s58,s76,20480\ns_waitcnt lgkmcnt(10)\nv_mfma_f32_32x32x16_bf16 v[0:15],v[64:67],v[192:195],0\n" \
"ds_read_b64_tr_b16 v[88:89],v224 offset:4608\nds_read_b64_tr_b16 v[90:91],v224 offset:5632\n" \
"buffer_load_dwordx4 v[96:99],v248,%[sws],s58 offen offset:2048\ns_waitcnt lgkmcnt(8)\nv_mfma_f32_32x32x16_bf16 v[16:31],v[68:71],v[192:195],0\n" \
"ds_read_b64_tr_b16 v[92:93],v224 offset:4864\nds_read_b64_tr_b16 v[94:95],v224 offset:5888\n" \
"buffer_load_dwordx4 v[100:103],v248,%[sws],s58 offen offset:3072\nv_mfma_f32_32x32x16_bf16 v[32:47],v[72:75],v[192:195],0\n" \
"ds_read_b64_tr_b16 v[64:65],v224 offset:8192\nds_read_b64_tr_b16 v[66:67],v224 offset:9216\ns_waitcnt lgkmcnt(8)\n" \
"v_mfma_f32_32x32x16_bf16 v[48:63],v[76:79],v[192:195],0\nds_read_b64_tr_b16 v[68:69],v224 offset:8448\nds_read_b64_tr_b16 v[70:71],v224 offset:9472\n" \
"s_add_u32 s58,s76,24576\nbuffer_load_dwordx4 v[192:195],v248,%[sws],s58 offen offset:0\nv_mfma_f32_32x32x16_bf16 v[0:15],v[80:83],v[196:199],v[0:15]\n" \
"ds_read_b64_tr_b16 v[72:73],v224 offset:8704\nds_read_b64_tr_b16 v[74:75],v224 offset:9728\ns_waitcnt lgkmcnt(8)\n" \
"v_mfma_f32_32x32x16_bf16 v[16:31],v[84:87],v[196:199],v[16:31]\nds_read_b64_tr_b16 v[76:77],v224 offset:8960\n" \
"ds_read_b64_tr_b16 v[78:79],v224 offset:9984\nv_mfma_f32_32x32x16_bf16 v[32:47],v[88:91],v[196:199],v[32:47]\n" \
"ds_read_b64_tr_b16 v[80:81],v224 offset:12288\nds_read_b64_tr_b16 v[82:83],v224 offset:13312\ns_waitcnt lgkmcnt(8)\n" \
"v_mfma_f32_32x32x16_bf16 v[48:63],v[92:95],v[196:199],v[48:63]\nds_read_b64_tr_b16 v[84:85],v224 offset:12544\n" \
"ds_read_b64_tr_b16 v[86:87],v224 offset:13568\nbuffer_load_dwordx4 v[196:199],v248,%[sws],s58 offen offset:1024\n" \
"v_mfma_f32_32x32x16_bf16 v[0:15],v[64:67],v[200:203],v[0:15]\nds_read_b64_tr_b16 v[88:89],v224 offset:12800\n" \
"ds_read_b64_tr_b16 v[90:91],v224 offset:13824\ns_waitcnt lgkmcnt(8)\nv_mfma_f32_32x32x16_bf16 v[16:31],v[68:71],v[200:203],v[16:31]\n" \
"ds_read_b64_tr_b16 v[92:93],v224 offset:13056\nds_read_b64_tr_b16 v[94:95],v224 offset:14080\n" \
"v_mfma_f32_32x32x16_bf16 v[32:47],v[72:75],v[200:203],v[32:47]\nds_read_b64_tr_b16 v[64:65],v224 offset:16384\n" \
"ds_read_b64_tr_b16 v[66:67],v224 offset:17408\ns_waitcnt lgkmcnt(8)\nv_mfma_f32_32x32x16_bf16 v[48:63],v[76:79],v[200:203],v[48:63]\n" \
"ds_read_b64_tr_b16 v[68:69],v224 offset:16640\nds_read_b64_tr_b16 v[70:71],v224 offset:17664\n" \
"buffer_load_dwordx4 v[200:203],v248,%[sws],s58 offen offset:2048\nv_mfma_f32_32x32x16_bf16 v[0:15],v[80:83],v[204:207],v[0:15]\n" \
"ds_read_b64_tr_b16 v[72:73],v224 offset:16896\nds_read_b64_tr_b16 v[74:75],v224 offset:17920\ns_waitcnt lgkmcnt(8)\n" \
"v_mfma_f32_32x32x16_bf16 v[16:31],v[84:87],v[204:207],v[16:31]\nds_read_b64_tr_b16 v[76:77],v224 offset:17152\n" \
"ds_read_b64_tr_b16 v[78:79],v224 offset:18176\nv_mfma_f32_32x32x16_bf16 v[32:47],v[88:91],v[204:207],v[32:47]\n" \
"ds_read_b64_tr_b16 v[80:81],v224 offset:20480\nds_read_b64_tr_b16 v[82:83],v224 offset:21504\ns_waitcnt lgkmcnt(8)\n" \
"v_mfma_f32_32x32x16_bf16 v[48:63],v[92:95],v[204:207],v[48:63]\nds_read_b64_tr_b16 v[84:85],v224 offset:20736\n" \
"ds_read_b64_tr_b16 v[86:87],v224 offset:21760\nbuffer_load_dwordx4 v[204:207],v248,%[sws],s58 offen offset:3072\n" \
"v_mfma_f32_32x32x16_bf16 v[0:15],v[64:67],v[208:211],v[0:15]\nds_read_b64_tr_b16 v[88:89],v224 offset:20992\n" \
"ds_read_b64_tr_b16 v[90:91],v224 offset:22016\ns_waitcnt lgkmcnt(8)\nv_mfma_f32_32x32x16_bf16 v[16:31],v[68:71],v[208:211],v[16:31]\n" \
"ds_read_b64_tr_b16 v[92:93],v224 offset:21248\nds_read_b64_tr_b16 v[94:95],v224 offset:22272\n" \
"v_mfma_f32_32x32x16_bf16 v[32:47],v[72:75],v[208:211],v[32:47]\nds_read_b64_tr_b16 v[64:65],v224 offset:24576\n" \
"ds_read_b64_tr_b16 v[66:67],v224 offset:25600\ns_waitcnt lgkmcnt(8)\nv_mfma_f32_32x32x16_bf16 v[48:63],v[76:79],v[208:211],v[48:63]\n" \
"ds_read_b64_tr_b16 v[68:69],v224 offset:24832\nds_read_b64_tr_b16 v[70:71],v224 offset:25856\ns_add_u32 s58,s76,28672\n" \
"buffer_load_dwordx4 v[208:211],v248,%[sws],s58 offen offset:0\nv_mfma_f32_32x32x16_bf16 v[0:15],v[80:83],v[212:215],v[0:15]\n" \
"ds_read_b64_tr_b16 v[72:73],v224 offset:25088\nds_read_b64_tr_b16 v[74:75],v224 offset:26112\ns_waitcnt lgkmcnt(8)\n" \
"v_mfma_f32_32x32x16_bf16 v[16:31],v[84:87],v[212:215],v[16:31]\nds_read_b64_tr_b16 v[76:77],v224 offset:25344\n" \
"ds_read_b64_tr_b16 v[78:79],v224 offset:26368\nv_mfma_f32_32x32x16_bf16 v[32:47],v[88:91],v[212:215],v[32:47]\n" \
"ds_read_b64_tr_b16 v[80:81],v224 offset:28672\nds_read_b64_tr_b16 v[82:83],v224 offset:29696\ns_waitcnt lgkmcnt(8)\n" \
"v_mfma_f32_32x32x16_bf16 v[48:63],v[92:95],v[212:215],v[48:63]\nds_read_b64_tr_b16 v[84:85],v224 offset:28928\n" \
"ds_read_b64_tr_b16 v[86:87],v224 offset:29952\nbuffer_load_dwordx4 v[212:215],v248,%[sws],s58 offen offset:1024\n" \
"v_mfma_f32_32x32x16_bf16 v[0:15],v[64:67],v[216:219],v[0:15]\nds_read_b64_tr_b16 v[88:89],v224 offset:29184\n" \
"ds_read_b64_tr_b16 v[90:91],v224 offset:30208\nv_mov_b32 v228,0\nglobal_load_dword v229,v228,s[78:79] sc1\ns_waitcnt lgkmcnt(8)\n" \
"v_mfma_f32_32x32x16_bf16 v[16:31],v[68:71],v[216:219],v[16:31]\nds_read_b64_tr_b16 v[92:93],v224 offset:29440\n" \
"ds_read_b64_tr_b16 v[94:95],v224 offset:30464\nv_mfma_f32_32x32x16_bf16 v[32:47],v[72:75],v[216:219],v[32:47]\n" \
"ds_read_b64_tr_b16 v[64:65],v225 offset:0\nds_read_b64_tr_b16 v[66:67],v225 offset:1024\ns_waitcnt lgkmcnt(8)\n" \
"v_mfma_f32_32x32x16_bf16 v[48:63],v[76:79],v[216:219],v[48:63]\nds_read_b64_tr_b16 v[68:69],v225 offset:256\n" \
"ds_read_b64_tr_b16 v[70:71],v225 offset:1280\nv_add_u32 v224,98304,v250\nbuffer_load_dwordx4 v[216:219],v248,%[sws],s58 offen offset:2048\n" \
"v_mfma_f32_32x32x16_bf16 v[0:15],v[80:83],v[220:223],v[0:15]\nds_read_b64_tr_b16 v[72:73],v225 offset:512\n" \
"ds_read_b64_tr_b16 v[74:75],v225 offset:1536\ns_waitcnt lgkmcnt(8)\nv_mfma_f32_32x32x16_bf16 v[16:31],v[84:87],v[220:223],v[16:31]\n" \
"ds_read_b64_tr_b16 v[76:77],v225 offset:768\nds_read_b64_tr_b16 v[78:79],v225 offset:1792\n" \
"v_mfma_f32_32x32x16_bf16 v[32:47],v[88:91],v[220:223],v[32:47]\nds_read_b64_tr_b16 v[80:81],v225 offset:4096\n" \
"ds_read_b64_tr_b16 v[82:83],v225 offset:5120\ns_waitcnt lgkmcnt(8)\nv_mfma_f32_32x32x16_bf16 v[48:63],v[92:95],v[220:223],v[48:63]\n" \
"ds_read_b64_tr_b16 v[84:85],v225 offset:4352\nds_read_b64_tr_b16 v[86:87],v225 offset:5376\n" \
"buffer_load_dwordx4 v[220:223],v248,%[sws],s58 offen offset:3072\ns_waitcnt vmcnt(17)\nv_mfma_f32_32x32x16_bf16 v[0:15],v[64:67],v[136:139],v[0:15]\n" \
"ds_read_b64_tr_b16 v[88:89],v225 offset:4608\nds_read_b64_tr_b16 v[90:91],v225 offset:5632\ns_waitcnt lgkmcnt(8)\n" \
"v_mfma_f32_32x32x16_bf16 v[16:31],v[68:71],v[136:139],v[16:31]\nds_read_b64_tr_b16 v[92:93],v225 offset:4864\n" \
"ds_read_b64_tr_b16 v[94:95],v225 offset:5888\ns_barrier\ns_and_b32 s50,s66,0xffff\ns_add_u32 m0,s50,131072\ns_add_u32 s49,s67,3145728\n" \
"buffer_load_dwordx4 v249,%[sq],s49 offen lds\nv_mfma_f32_32x32x16_bf16 v[32:47],v[72:75],v[136:139],v[32:47]\n" \
"ds_read_b64_tr_b16 v[64:65],v225 offset:8192\nds_read_b64_tr_b16 v[66:67],v225 offset:9216\ns_add_u32 m0,m0,1024\ns_add_u32 s49,s49,32768\n" \
"buffer_load_dwordx4 v249,%[sq],s49 offen lds\ns_waitcnt lgkmcnt(8)\nv_mfma_f32_32x32x16_bf16 v[48:63],v[76:79],v[136:139],v[48:63]\n" \
"ds_read_b64_tr_b16 v[68:69],v225 offset:8448\nds_read_b64_tr_b16 v[70:71],v225 offset:9472\ns_add_u32 m0,m0,1024\ns_add_u32 s49,s49,32768\n" \
"buffer_load_dwordx4 v249,%[sq],s49 offen lds\nv_mfma_f32_32x32x16_bf16 v[0:15],v[80:83],v[140:143],v[0:15]\n" \
"ds_read_b64_tr_b16 v[72:73],v225 offset:8704\nds_read_b64_tr_b16 v[74:75],v225 offset:9728\ns_add_u32 m0,m0,1024\ns_add_u32 s49,s49,32768\n" \
"buffer_load_dwordx4 v249,%[sq],s49 offen lds\ns_waitcnt lgkmcnt(8)\nv_mfma_f32_32x32x16_bf16 v[16:31],v[84:87],v[140:143],v[16:31]\n" \
"ds_read_b64_tr_b16 v[76:77],v225 offset:8960\nds_read_b64_tr_b16 v[78:79],v225 offset:9984\n" \
"v_mfma_f32_32x32x16_bf16 v[32:47],v[88:91],v[140:143],v[32:47]\nds_read_b64_tr_b16 v[80:81],v225 offset:12288\n" \
"ds_read_b64_tr_b16 v[82:83],v225 offset:13312\ns_waitcnt lgkmcnt(8)\nv_mfma_f32_32x32x16_bf16 v[48:63],v[92:95],v[140:143],v[48:63]\n" \
"ds_read_b64_tr_b16 v[84:85],v225 offset:12544\nds_read_b64_tr_b16 v[86:87],v225 offset:13568\ns_waitcnt vmcnt(0)\nv_readfirstlane_b32 s50,v229\n" \
"s_nop 0\ns_cmp_ge_u32 s50,4\ns_cbranch_scc1 22f\nv_mov_b32 v226,0\ns_mov_b32 s49,0x400000\n21:\nglobal_load_dword v227,v226,s[78:79] sc1\n" \
"s_waitcnt vmcnt(0)\nv_readfirstlane_b32 s50,v227\ns_sub_u32 s49,s49,1\ns_cbranch_scc1 22f\ns_cmp_ge_u32 s50,4\ns_cbranch_scc1 22f\ns_sleep 8\n" \
"s_branch 21b\n22:\nv_mfma_f32_32x32x16_bf16 v[0:15],v[64:67],v[144:147],v[0:15]\nds_read_b64_tr_b16 v[88:89],v225 offset:12800\n" \
"ds_read_b64_tr_b16 v[90:91],v225 offset:13824\ns_sub_u32 s48,s72,0\ns_and_b32 s46,s48,3\ns_sub_u32 s48,s48,s69\ns_lshl_b32 s44,s48,15\n" \
"s_add_u32 s45,s44,4096\ns_lshl_b32 s46,s46,15\ns_add_u32 s46,s46,s74\ns_mov_b32 m0,s46\ns_nop 0\n" \
"buffer_load_dwordx4 v254,%[srd],s44 offen offset:0 sc1 lds\ns_waitcnt lgkmcnt(8)\nv_mfma_f32_32x32x16_bf16 v[16:31],v[68:71],v[144:147],v[16:31]\n" \
"ds_read_b64_tr_b16 v[92:93],v225 offset:13056\nds_read_b64_tr_b16 v[94:95],v225 offset:14080\n" \
"buffer_load_dwordx4 v254,%[srd],s44 offen offset:1024 sc1 lds\nv_mfma_f32_32x32x16_bf16 v[32:47],v[72:75],v[144:147],v[32:47]\n" \
"ds_read_b64_tr_b16 v[64:65],v225 offset:16384\nds_read_b64_tr_b16 v[66:67],v225 offset:17408\n" \
"buffer_load_dwordx4 v254,%[srd],s44 offen offset:2048 sc1 lds\ns_waitcnt lgkmcnt(8)\nv_mfma_f32_32x32x16_bf16 v[48:63],v[76:79],v[144:147],v[48:63]\n" \
"ds_read_b64_tr_b16 v[68:69],v225 offset:16640\nds_read_b64_tr_b16 v[70:71],v225 offset:17664\n" \
"buffer_load_dwordx4 v254,%[srd],s44 offen offset:3072 sc1 lds\nv_mfma_f32_32x32x16_bf16 v[0:15],v[80:83],v[148:151],v[0:15]\n" \
"ds_read_b64_tr_b16 v[72:73],v225 offset:16896\nds_read_b64_tr_b16 v[74:75],v225 offset:17920\ns_add_u32 m0,s46,4096\ns_nop 0\n" \
"buffer_load_dwordx4 v254,%[srd],s45 offen offset:0 sc1 lds\ns_waitcnt lgkmcnt(8)\nv_mfma_f32_32x32x16_bf16 v[16:31],v[84:87],v[148:151],v[16:31]\n" \
"ds_read_b64_tr_b16 v[76:77],v225 offset:17152\nds_read_b64_tr_b16 v[78:79],v225 offset:18176\n" \
"buffer_load_dwordx4 v254,%[srd],s45 offen offset:1024 sc1 lds\nv_mfma_f32_32x32x16_bf16 v[32:47],v[88:91],v[148:151],v[32:47]\n" \
"ds_read_b64_tr_b16 v[80:81],v225 offset:20480\nds_read_b64_tr_b16 v[82:83],v225 offset:21504\n" \
"buffer_load_dwordx4 v254,%[srd],s45 offen offset:2048 sc1 lds\ns_waitcnt lgkmcnt(8)\nv_mfma_f32_32x32x16_bf16 v[48:63],v[92:95],v[148:151],v[48:63]\n" \
"ds_read_b64_tr_b16 v[84:85],v225 offset:20736\nds_read_b64_tr_b16 v[86:87],v225 offset:21760\n" \
"buffer_load_dwordx4 v254,%[srd],s45 offen offset:3072 sc1 lds\nv_mfma_f32_32x32x16_bf16 v[0:15],v[64:67],v[152:155],v[0:15]\n" \
"ds_read_b64_tr_b16 v[88:89],v225 offset:20992\nds_read_b64_tr_b16 v[90:91],v225 offset:22016\ns_sub_u32 s48,s72,1\ns_and_b32 s46,s48,3\n" \
"s_sub_u32 s48,s48,s69\ns_lshl_b32 s44,s48,15\ns_add_u32 s45,s44,4096\ns_lshl_b32 s46,s46,15\ns_add_u32 s46,s46,s74\ns_mov_b32 m0,s46\ns_nop 0\n" \
"buffer_load_dwordx4 v254,%[srd],s44 offen offset:0 sc1 lds\ns_waitcnt lgkmcnt(8)\nv_mfma_f32_32x32x16_bf16 v[16:31],v[68:71],v[152:155],v[16:31]\n" \
"ds_read_b64_tr_b16 v[92:93],v225 offset:21248\nds_read_b64_tr_b16 v[94:95],v225 offset:22272\n" \
"buffer_load_dwordx4 v254,%[srd],s44 offen offset:1024 sc1 lds\nv_mfma_f32_32x32x16_bf16 v[32:47],v[72:75],v[152:155],v[32:47]\n" \
"ds_read_b64_tr_b16 v[64:65],v225 offset:24576\nds_read_b64_tr_b16 v[66:67],v225 offset:25600\n" \
"buffer_load_dwordx4 v254,%[srd],s44 offen offset:2048 sc1 lds\ns_waitcnt lgkmcnt(8)\nv_mfma_f32_32x32x16_bf16 v[48:63],v[76:79],v[152:155],v[48:63]\n" \
"ds_read_b64_tr_b16 v[68:69],v225 offset:24832\nds_read_b64_tr_b16 v[70:71],v225 offset:25856\n" \
"buffer_load_dwordx4 v254,%[srd],s44 offen offset:3072 sc1 lds\nv_mfma_f32_32x32x16_bf16 v[0:15],v[80:83],v[156:159],v[0:15]\n" \
"ds_read_b64_tr_b16 v[72:73],v225 offset:25088\nds_read_b64_tr_b16 v[74:75],v225 offset:26112\ns_add_u32 m0,s46,4096\ns_nop 0\n" \
"buffer_load_dwordx4 v254,%[srd],s45 offen offset:0 sc1 lds\ns_waitcnt lgkmcnt(8)\nv_mfma_f32_32x32x16_bf16 v[16:31],v[84:87],v[156:159],v[16:31]\n" \
"ds_read_b64_tr_b16 v[76:77],v225 offset:25344\nds_read_b64_tr_b16 v[78:79],v225 offset:26368\n" \
"buffer_load_dwordx4 v254,%[srd],s45 offen offset:1024 sc1 lds\nv_mfma_f32_32x32x16_bf16 v[32:47],v[88:91],v[156:159],v[32:47]\n" \
"ds_read_b64_tr_b16 v[80:81],v225 offset:28672\nds_read_b64_tr_b16 v[82:83],v225 offset:29696\n" \
"buffer_load_dwordx4 v254,%[srd],s45 offen offset:2048 sc1 lds\ns_waitcnt lgkmcnt(8)\nv_mfma_f32_32x32x16_bf16 v[48:63],v[92:95],v[156:159],v[48:63]\n" \
"ds_read_b64_tr_b16 v[84:85],v225 offset:28928\nds_read_b64_tr_b16 v[86:87],v225 offset:29952\n" \
"buffer_load_dwordx4 v254,%[srd],s45 offen offset:3072 sc1 lds\nv_mfma_f32_32x32x16_bf16 v[0:15],v[64:67],v[160:163],v[0:15]\n" \
"ds_read_b64_tr_b16 v[88:89],v225 offset:29184\nds_read_b64_tr_b16 v[90:91],v225 offset:30208\ns_waitcnt lgkmcnt(8)\n" \
"v_mfma_f32_32x32x16_bf16 v[16:31],v[68:71],v[160:163],v[16:31]\nds_read_b64_tr_b16 v[92:93],v225 offset:29440\n" \
"ds_read_b64_tr_b16 v[94:95],v225 offset:30464\nv_mfma_f32_32x32x16_bf16 v[32:47],v[72:75],v[160:163],v[32:47]\n" \
"ds_read_b64_tr_b16 v[64:65],v224 offset:0\nds_read_b64_tr_b16 v[66:67],v224 offset:1024\ns_waitcnt lgkmcnt(8)\n" \
"v_mfma_f32_32x32x16_bf16 v[48:63],v[76:79],v[160:163],v[48:63]\nds_read_b64_tr_b16 v[68:69],v224 offset:256\n" \
"ds_read_b64_tr_b16 v[70:71],v224 offset:1280\nv_add_u32 v225,131072,v250\nv_mfma_f32_32x32x16_bf16 v[0:15],v[80:83],v[164:167],v[0:15]\n" \
"ds_read_b64_tr_b16 v[72:73],v224 offset:512\nds_read_b64_tr_b16 v[74:75],v224 offset:1536\ns_waitcnt lgkmcnt(8)\n" \
"v_mfma_f32_32x32x16_bf16 v[16:31],v[84:87],v[164:167],v[16:31]\nds_read_b64_tr_b16 v[76:77],v224 offset:768\n" \
"ds_read_b64_tr_b16 v[78:79],v224 offset:1792\nv_mfma_f32_32x32x16_bf16 v[32:47],v[88:91],v[164:167],v[32:47]\n" \
"ds_read_b64_tr_b16 v[80:81],v224 offset:4096\nds_read_b64_tr_b16 v[82:83],v224 offset:5120\ns_waitcnt lgkmcnt(8)\n" \
"v_mfma_f32_32x32x16_bf16 v[48:63],v[92:95],v[164:167],v[48:63]\nds_read_b64_tr_b16 v[84:85],v224 offset:4352\n" \
"ds_read_b64_tr_b16 v[86:87],v224 offset:5376\nv_mfma_f32_32x32x16_bf16 v[0:15],v[64:67],v[168:171],v[0:15]\n" \
"ds_read_b64_tr_b16 v[88:89],v224 offset:4608\nds_read_b64_tr_b16 v[90:91],v224 offset:5632\ns_waitcnt lgkmcnt(8)\n" \
"v_mfma_f32_32x32x16_bf16 v[16:31],v[68:71],v[168:171],v[16:31]\nds_read_b64_tr_b16 v[92:93],v224 offset:4864\n" \
"ds_read_b64_tr_b16 v[94:95],v224 offset:5888\nv_mfma_f32_32x32x16_bf16 v[32:47],v[72:75],v[168:171],v[32:47]\n" \
"ds_read_b64_tr_b16 v[64:65],v224 offset:8192\nds_read_b64_tr_b16 v[66:67],v224 offset:9216\ns_waitcnt lgkmcnt(8)\n" \
"v_mfma_f32_32x32x16_bf16 v[48:63],v[76:79],v[168:171],v[48:63]\nds_read_b64_tr_b16 v[68:69],v224 offset:8448\n" \
"ds_read_b64_tr_b16 v[70:71],v224 offset:9472\nv_mfma_f32_32x32x16_bf16 v[0:15],v[80:83],v[172:175],v[0:15]\n" \
"ds_read_b64_tr_b16 v[72:73],v224 offset:8704\nds_read_b64_tr_b16 v[74:75],v224 offset:9728\ns_waitcnt lgkmcnt(8)\n" \
"v_mfma_f32_32x32x16_bf16 v[16:31],v[84:87],v[172:175],v[16:31]\nds_read_b64_tr_b16 v[76:77],v224 offset:8960\n" \
"ds_read_b64_tr_b16 v[78:79],v224 offset:9984\nv_mfma_f32_32x32x16_bf16 v[32:47],v[88:91],v[172:175],v[32:47]\n" \
"ds_read_b64_tr_b16 v[80:81],v224 offset:12288\nds_read_b64_tr_b16 v[82:83],v224 offset:13312\ns_waitcnt lgkmcnt(8)\n" \
"v_mfma_f32_32x32x16_bf16 v[48:63],v[92:95],v[172:175],v[48:63]\nds_read_b64_tr_b16 v[84:85],v224 offset:12544\n" \
"ds_read_b64_tr_b16 v[86:87],v224 offset:13568\nv_mfma_f32_32x32x16_bf16 v[0:15],v[64:67],v[176:179],v[0:15]\n" \
"ds_read_b64_tr_b16 v[88:89],v224 offset:12800\nds_read_b64_tr_b16 v[90:91],v224 offset:13824\ns_waitcnt lgkmcnt(8)\n" \
"v_mfma_f32_32x32x16_bf16 v[16:31],v[68:71],v[176:179],v[16:31]\nds_read_b64_tr_b16 v[92:93],v224 offset:13056\n" \
"ds_read_b64_tr_b16 v[94:95],v224 offset:14080\nv_mfma_f32_32x32x16_bf16 v[32:47],v[72:75],v[176:179],v[32:47]\n" \
"ds_read_b64_tr_b16 v[64:65],v224 offset:16384\nds_read_b64_tr_b16 v[66:67],v224 offset:17408\ns_waitcnt lgkmcnt(8)\n" \
"v_mfma_f32_32x32x16_bf16 v[48:63],v[76:79],v[176:179],v[48:63]\nds_read_b64_tr_b16 v[68:69],v224 offset:16640\n" \
"ds_read_b64_tr_b16 v[70:71],v224 offset:17664\nv_mfma_f32_32x32x16_bf16 v[0:15],v[80:83],v[180:183],v[0:15]\n" \
"ds_read_b64_tr_b16 v[72:73],v224 offset:16896\nds_read_b64_tr_b16 v[74:75],v224 offset:17920\ns_waitcnt lgkmcnt(8)\n" \
"v_mfma_f32_32x32x16_bf16 v[16:31],v[84:87],v[180:183],v[16:31]\nds_read_b64_tr_b16 v[76:77],v224 offset:17152\n" \
"ds_read_b64_tr_b16 v[78:79],v224 offset:18176\nv_mfma_f32_32x32x16_bf16 v[32:47],v[88:91],v[180:183],v[32:47]\n" \
"ds_read_b64_tr_b16 v[80:81],v224 offset:20480\nds_read_b64_tr_b16 v[82:83],v224 offset:21504\ns_waitcnt lgkmcnt(8)\n" \
"v_mfma_f32_32x32x16_bf16 v[48:63],v[92:95],v[180:183],v[48:63]\nds_read_b64_tr_b16 v[84:85],v224 offset:20736\n" \
"ds_read_b64_tr_b16 v[86:87],v224 offset:21760\nv_mfma_f32_32x32x16_bf16 v[0:15],v[64:67],v[184:187],v[0:15]\n" \
"ds_read_b64_tr_b16 v[88:89],v224 offset:20992\nds_read_b64_tr_b16 v[90:91],v224 offset:22016\ns_waitcnt lgkmcnt(8)\n" \
"v_mfma_f32_32x32x16_bf16 v[16:31],v[68:71],v[184:187],v[16:31]\nds_read_b64_tr_b16 v[92:93],v224 offset:21248\n" \
"ds_read_b64_tr_b16 v[94:95],v224 offset:22272\nv_mfma_f32_32x32x16_bf16 v[32:47],v[72:75],v[184:187],v[32:47]\n" \
"ds_read_b64_tr_b16 v[64:65],v224 offset:24576\nds_read_b64_tr_b16 v[66:67],v224 offset:25600\ns_waitcnt lgkmcnt(8)\n" \
"v_mfma_f32_32x32x16_bf16 v[48:63],v[76:79],v[184:187],v[48:63]\nds_read_b64_tr_b16 v[68:69],v224 offset:24832\n" \
"ds_read_b64_tr_b16 v[70:71],v224 offset:25856\nv_mfma_f32_32x32x16_bf16 v[0:15],v[80:83],v[188:191],v[0:15]\n" \
"ds_read_b64_tr_b16 v[72:73],v224 offset:25088\nds_read_b64_tr_b16 v[74:75],v224 offset:26112\ns_waitcnt lgkmcnt(8)\n" \
"v_mfma_f32_32x32x16_bf16 v[16:31],v[84:87],v[188:191],v[16:31]\nds_read_b64_tr_b16 v[76:77],v224 offset:25344\n" \
"ds_read_b64_tr_b16 v[78:79],v224 offset:26368\nv_mfma_f32_32x32x16_bf16 v[32:47],v[88:91],v[188:191],v[32:47]\n" \
"ds_read_b64_tr_b16 v[80:81],v224 offset:28672\nds_read_b64_tr_b16 v[82:83],v224 offset:29696\ns_waitcnt lgkmcnt(8)\n" \
"v_mfma_f32_32x32x16_bf16 v[48:63],v[92:95],v[188:191],v[48:63]\nds_read_b64_tr_b16 v[84:85],v224 offset:28928\n" \
"ds_read_b64_tr_b16 v[86:87],v224 offset:29952\nv_mfma_f32_32x32x16_bf16 v[0:15],v[64:67],v[96:99],v[0:15]\n" \
"ds_read_b64_tr_b16 v[88:89],v224 offset:29184\nds_read_b64_tr_b16 v[90:91],v224 offset:30208\ns_barrier\ns_waitcnt lgkmcnt(8)\n" \
"v_mfma_f32_32x32x16_bf16 v[16:31],v[68:71],v[96:99],v[16:31]\nds_read_b64_tr_b16 v[92:93],v224 offset:29440\n" \
"ds_read_b64_tr_b16 v[94:95],v224 offset:30464\nv_mfma_f32_32x32x16_bf16 v[32:47],v[72:75],v[96:99],v[32:47]\nds_read_b64_tr_b16 v[64:65],v225 offset:0\n" \
"ds_read_b64_tr_b16 v[66:67],v225 offset:1024\ns_waitcnt lgkmcnt(8)\nv_mfma_f32_32x32x16_bf16 v[48:63],v[76:79],v[96:99],v[48:63]\n" \
"ds_read_b64_tr_b16 v[68:69],v225 offset:256\nds_read_b64_tr_b16 v[70:71],v225 offset:1280\n" \
"v_mfma_f32_32x32x16_bf16 v[0:15],v[80:83],v[100:103],v[0:15]\nds_read_b64_tr_b16 v[72:73],v225 offset:512\n" \
"ds_read_b64_tr_b16 v[74:75],v225 offset:1536\ns_waitcnt lgkmcnt(8)\nv_mfma_f32_32x32x16_bf16 v[16:31],v[84:87],v[100:103],v[16:31]\n" \
"ds_read_b64_tr_b16 v[76:77],v225 offset:768\nds_read_b64_tr_b16 v[78:79],v225 offset:1792\n" \
"v_mfma_f32_32x32x16_bf16 v[32:47],v[88:91],v[100:103],v[32:47]\nds_read_b64_tr_b16 v[80:81],v225 offset:4096\n" \
"ds_read_b64_tr_b16 v[82:83],v225 offset:5120\ns_waitcnt lgkmcnt(8)\nv_mfma_f32_32x32x16_bf16 v[48:63],v[92:95],v[100:103],v[48:63]\n" \
"ds_read_b64_tr_b16 v[84:85],v225 offset:4352\nds_read_b64_tr_b16 v[86:87],v225 offset:5376\n" \
"v_mfma_f32_32x32x16_bf16 v[0:15],v[64:67],v[192:195],v[0:15]\nds_read_b64_tr_b16 v[88:89],v225 offset:4608\n" \
"ds_read_b64_tr_b16 v[90:91],v225 offset:5632\ns_waitcnt lgkmcnt(8)\nv_mfma_f32_32x32x16_bf16 v[16:31],v[68:71],v[192:195],v[16:31]\n" \
"ds_read_b64_tr_b16 v[92:93],v225 offset:4864\nds_read_b64_tr_b16 v[94:95],v225 offset:5888\n" \
"v_mfma_f32_32x32x16_bf16 v[32:47],v[72:75],v[192:195],v[32:47]\nds_read_b64_tr_b16 v[64:65],v225 offset:8192\n" \
"ds_read_b64_tr_b16 v[66:67],v225 offset:9216\ns_waitcnt lgkmcnt(8)\nv_mfma_f32_32x32x16_bf16 v[48:63],v[76:79],v[192:195],v[48:63]\n" \
"ds_read_b64_tr_b16 v[68:69],v225 offset:8448\nds_read_b64_tr_b16 v[70:71],v225 offset:9472\n" \
"v_mfma_f32_32x32x16_bf16 v[0:15],v[80:83],v[196:199],v[0:15]\nds_read_b64_tr_b16 v[72:73],v225 offset:8704\n" \
"ds_read_b64_tr_b16 v[74:75],v225 offset:9728\ns_waitcnt lgkmcnt(8)\nv_mfma_f32_32x32x16_bf16 v[16:31],v[84:87],v[196:199],v[16:31]\n" \
"ds_read_b64_tr_b16 v[76:77],v225 offset:8960\nds_read_b64_tr_b16 v[78:79],v225 offset:9984\n" \
"v_mfma_f32_32x32x16_bf16 v[32:47],v[88:91],v[196:199],v[32:47]\nds_read_b64_tr_b16 v[80:81],v225 offset:12288\n" \
"ds_read_b64_tr_b16 v[82:83],v225 offset:13312\ns_waitcnt lgkmcnt(8)\nv_mfma_f32_32x32x16_bf16 v[48:63],v[92:95],v[196:199],v[48:63]\n" \
"ds_read_b64_tr_b16 v[84:85],v225 offset:12544\nds_read_b64_tr_b16 v[86:87],v225 offset:13568\n" \
"v_mfma_f32_32x32x16_bf16 v[0:15],v[64:67],v[200:203],v[0:15]\nds_read_b64_tr_b16 v[88:89],v225 offset:12800\n" \
"ds_read_b64_tr_b16 v[90:91],v225 offset:13824\ns_waitcnt lgkmcnt(8)\nv_mfma_f32_32x32x16_bf16 v[16:31],v[68:71],v[200:203],v[16:31]\n" \
"ds_read_b64_tr_b16 v[92:93],v225 offset:13056\nds_read_b64_tr_b16 v[94:95],v225 offset:14080\n" \
"v_mfma_f32_32x32x16_bf16 v[32:47],v[72:75],v[200:203],v[32:47]\nds_read_b64_tr_b16 v[64:65],v225 offset:16384\n" \
"ds_read_b64_tr_b16 v[66:67],v225 offset:17408\ns_waitcnt lgkmcnt(8)\nv_mfma_f32_32x32x16_bf16 v[48:63],v[76:79],v[200:203],v[48:63]\n" \
"ds_read_b64_tr_b16 v[68:69],v225 offset:16640\nds_read_b64_tr_b16 v[70:71],v225 offset:17664\n" \
"v_mfma_f32_32x32x16_bf16 v[0:15],v[80:83],v[204:207],v[0:15]\nds_read_b64_tr_b16 v[72:73],v225 offset:16896\n" \
"ds_read_b64_tr_b16 v[74:75],v225 offset:17920\nglobal_load_dword v229,v228,s[78:79] offset:64 sc1\ns_waitcnt lgkmcnt(8)\n" \
"v_mfma_f32_32x32x16_bf16 v[16:31],v[84:87],v[204:207],v[16:31]\nds_read_b64_tr_b16 v[76:77],v225 offset:17152\n" \
"ds_read_b64_tr_b16 v[78:79],v225 offset:18176\nv_mfma_f32_32x32x16_bf16 v[32:47],v[88:91],v[204:207],v[32:47]\n" \
"ds_read_b64_tr_b16 v[80:81],v225 offset:20480\nds_read_b64_tr_b16 v[82:83],v225 offset:21504\ns_waitcnt lgkmcnt(8)\n" \
"v_mfma_f32_32x32x16_bf16 v[48:63],v[92:95],v[204:207],v[48:63]\nds_read_b64_tr_b16 v[84:85],v225 offset:20736\n" \
"ds_read_b64_tr_b16 v[86:87],v225 offset:21760\nv_mfma_f32_32x32x16_bf16 v[0:15],v[64:67],v[208:211],v[0:15]\n" \
"ds_read_b64_tr_b16 v[88:89],v225 offset:20992\nds_read_b64_tr_b16 v[90:91],v225 offset:22016\ns_waitcnt lgkmcnt(8)\n" \
"v_mfma_f32_32x32x16_bf16 v[16:31],v[68:71],v[208:211],v[16:31]\nds_read_b64_tr_b16 v[92:93],v225 offset:21248\n" \
"ds_read_b64_tr_b16 v[94:95],v225 offset:22272\nv_mfma_f32_32x32x16_bf16 v[32:47],v[72:75],v[208:211],v[32:47]\n" \
"ds_read_b64_tr_b16 v[64:65],v225 offset:24576\nds_read_b64_tr_b16 v[66:67],v225 offset:25600\ns_waitcnt lgkmcnt(8)\n" \
"v_mfma_f32_32x32x16_bf16 v[48:63],v[76:79],v[208:211],v[48:63]\nds_read_b64_tr_b16 v[68:69],v225 offset:24832\n" \
"ds_read_b64_tr_b16 v[70:71],v225 offset:25856\nv_mfma_f32_32x32x16_bf16 v[0:15],v[80:83],v[212:215],v[0:15]\n" \
"ds_read_b64_tr_b16 v[72:73],v225 offset:25088\nds_read_b64_tr_b16 v[74:75],v225 offset:26112\ns_waitcnt lgkmcnt(8)\n" \
"v_mfma_f32_32x32x16_bf16 v[16:31],v[84:87],v[212:215],v[16:31]\nds_read_b64_tr_b16 v[76:77],v225 offset:25344\n" \
"ds_read_b64_tr_b16 v[78:79],v225 offset:26368\nv_mfma_f32_32x32x16_bf16 v[32:47],v[88:91],v[212:215],v[32:47]\n" \
"ds_read_b64_tr_b16 v[80:81],v225 offset:28672\nds_read_b64_tr_b16 v[82:83],v225 offset:29696\ns_waitcnt lgkmcnt(8)\n" \
"v_mfma_f32_32x32x16_bf16 v[48:63],v[92:95],v[212:215],v[48:63]\nds_read_b64_tr_b16 v[84:85],v225 offset:28928\n" \
"ds_read_b64_tr_b16 v[86:87],v225 offset:29952\nv_mfma_f32_32x32x16_bf16 v[0:15],v[64:67],v[216:219],v[0:15]\n" \
"ds_read_b64_tr_b16 v[88:89],v225 offset:29184\nds_read_b64_tr_b16 v[90:91],v225 offset:30208\ns_waitcnt lgkmcnt(8)\n" \
"v_mfma_f32_32x32x16_bf16 v[16:31],v[68:71],v[216:219],v[16:31]\nds_read_b64_tr_b16 v[92:93],v225 offset:29440\n" \
"ds_read_b64_tr_b16 v[94:95],v225 offset:30464\nv_mfma_f32_32x32x16_bf16 v[32:47],v[72:75],v[216:219],v[32:47]\ns_waitcnt lgkmcnt(6)\n" \
"v_mfma_f32_32x32x16_bf16 v[48:63],v[76:79],v[216:219],v[48:63]\nv_mfma_f32_32x32x16_bf16 v[0:15],v[80:83],v[220:223],v[0:15]\ns_waitcnt lgkmcnt(2)\n" \
"v_mfma_f32_32x32x16_bf16 v[16:31],v[84:87],v[220:223],v[16:31]\nv_mfma_f32_32x32x16_bf16 v[32:47],v[88:91],v[220:223],v[32:47]\ns_waitcnt lgkmcnt(0)\n" \
"v_mfma_f32_32x32x16_bf16 v[48:63],v[92:95],v[220:223],v[48:63]\ns_nop 7\ns_nop 7\ns_mov_b32 s49,0x3e0293ee\nv_mul_f32 v0,s49,v0\nv_mul_f32 v1,s49,v1\n" \
"v_mul_f32 v2,s49,v2\nv_mul_f32 v3,s49,v3\nv_mul_f32 v4,s49,v4\nv_mul_f32 v5,s49,v5\nv_mul_f32 v6,s49,v6\nv_mul_f32 v7,s49,v7\nv_mul_f32 v8,s49,v8\n" \
"v_mul_f32 v9,s49,v9\nv_mul_f32 v10,s49,v10\nv_mul_f32 v11,s49,v11\nv_mul_f32 v12,s49,v12\nv_mul_f32 v13,s49,v13\nv_mul_f32 v14,s49,v14\n" \
"v_mul_f32 v15,s49,v15\nv_mul_f32 v16,s49,v16\nv_mul_f32 v17,s49,v17\nv_mul_f32 v18,s49,v18\nv_mul_f32 v19,s49,v19\nv_mul_f32 v20,s49,v20\n" \
"v_mul_f32 v21,s49,v21\nv_mul_f32 v22,s49,v22\nv_mul_f32 v23,s49,v23\nv_mul_f32 v24,s49,v24\nv_mul_f32 v25,s49,v25\nv_mul_f32 v26,s49,v26\n" \
"v_mul_f32 v27,s49,v27\nv_mul_f32 v28,s49,v28\nv_mul_f32 v29,s49,v29\nv_mul_f32 v30,s49,v30\nv_mul_f32 v31,s49,v31\nv_mul_f32 v32,s49,v32\n" \
"v_mul_f32 v33,s49,v33\nv_mul_f32 v34,s49,v34\nv_mul_f32 v35,s49,v35\nv_mul_f32 v36,s49,v36\nv_mul_f32 v37,s49,v37\nv_mul_f32 v38,s49,v38\n" \
"v_mul_f32 v39,s49,v39\nv_mul_f32 v40,s49,v40\nv_mul_f32 v41,s49,v41\nv_mul_f32 v42,s49,v42\nv_mul_f32 v43,s49,v43\nv_mul_f32 v44,s49,v44\n" \
"v_mul_f32 v45,s49,v45\nv_mul_f32 v46,s49,v46\nv_mul_f32 v47,s49,v47\nv_mul_f32 v48,s49,v48\nv_mul_f32 v49,s49,v49\nv_mul_f32 v50,s49,v50\n" \
"v_mul_f32 v51,s49,v51\nv_mul_f32 v52,s49,v52\nv_mul_f32 v53,s49,v53\nv_mul_f32 v54,s49,v54\nv_mul_f32 v55,s49,v55\nv_mul_f32 v56,s49,v56\n" \
"v_mul_f32 v57,s49,v57\nv_mul_f32 v58,s49,v58\nv_mul_f32 v59,s49,v59\nv_mul_f32 v60,s49,v60\nv_mul_f32 v61,s49,v61\nv_mul_f32 v62,s49,v62\n" \
"v_mul_f32 v63,s49,v63\nv_cvt_pk_bf16_f32 v64,v0,v1\nv_cvt_pk_bf16_f32 v65,v2,v3\nv_cvt_pk_bf16_f32 v66,v4,v5\nv_cvt_pk_bf16_f32 v67,v6,v7\n" \
"v_cvt_pk_bf16_f32 v68,v8,v9\nv_cvt_pk_bf16_f32 v69,v10,v11\nv_cvt_pk_bf16_f32 v70,v12,v13\nv_cvt_pk_bf16_f32 v71,v14,v15\n" \
"v_cvt_pk_bf16_f32 v72,v16,v17\nv_cvt_pk_bf16_f32 v73,v18,v19\nv_cvt_pk_bf16_f32 v74,v20,v21\nv_cvt_pk_bf16_f32 v75,v22,v23\n" \
"v_cvt_pk_bf16_f32 v76,v24,v25\nv_cvt_pk_bf16_f32 v77,v26,v27\nv_cvt_pk_bf16_f32 v78,v28,v29\nv_cvt_pk_bf16_f32 v79,v30,v31\n" \
"v_cvt_pk_bf16_f32 v80,v32,v33\nv_cvt_pk_bf16_f32 v81,v34,v35\nv_cvt_pk_bf16_f32 v82,v36,v37\nv_cvt_pk_bf16_f32 v83,v38,v39\n" \
"v_cvt_pk_bf16_f32 v84,v40,v41\nv_cvt_pk_bf16_f32 v85,v42,v43\nv_cvt_pk_bf16_f32 v86,v44,v45\nv_cvt_pk_bf16_f32 v87,v46,v47\n" \
"v_cvt_pk_bf16_f32 v88,v48,v49\nv_cvt_pk_bf16_f32 v89,v50,v51\nv_cvt_pk_bf16_f32 v90,v52,v53\nv_cvt_pk_bf16_f32 v91,v54,v55\n" \
"v_cvt_pk_bf16_f32 v92,v56,v57\nv_cvt_pk_bf16_f32 v93,v58,v59\nv_cvt_pk_bf16_f32 v94,v60,v61\nv_cvt_pk_bf16_f32 v95,v62,v63\nv_mov_b32 v0,0\n" \
"v_mov_b32 v1,0\nv_mov_b32 v2,0\nv_mov_b32 v3,0\nv_mov_b32 v4,0\nv_mov_b32 v5,0\nv_mov_b32 v6,0\nv_mov_b32 v7,0\nv_mov_b32 v8,0\nv_mov_b32 v9,0\n" \
"v_mov_b32 v10,0\nv_mov_b32 v11,0\nv_mov_b32 v12,0\nv_mov_b32 v13,0\nv_mov_b32 v14,0\nv_mov_b32 v15,0\nv_mov_b32 v16,0\nv_mov_b32 v17,0\n" \
"v_mov_b32 v18,0\nv_mov_b32 v19,0\nv_mov_b32 v20,0\nv_mov_b32 v21,0\nv_mov_b32 v22,0\nv_mov_b32 v23,0\nv_mov_b32 v24,0\nv_mov_b32 v25,0\n" \
"v_mov_b32 v26,0\nv_mov_b32 v27,0\nv_mov_b32 v28,0\nv_mov_b32 v29,0\nv_mov_b32 v30,0\nv_mov_b32 v31,0\nv_mov_b32 v32,0\nv_mov_b32 v33,0\n" \
"v_mov_b32 v34,0\nv_mov_b32 v35,0\nv_mov_b32 v36,0\nv_mov_b32 v37,0\nv_mov_b32 v38,0\nv_mov_b32 v39,0\nv_mov_b32 v40,0\nv_mov_b32 v41,0\n" \
"v_mov_b32 v42,0\nv_mov_b32 v43,0\nv_mov_b32 v44,0\nv_mov_b32 v45,0\nv_mov_b32 v46,0\nv_mov_b32 v47,0\nv_mov_b32 v48,0\nv_mov_b32 v49,0\n" \
"v_mov_b32 v50,0\nv_mov_b32 v51,0\nv_mov_b32 v52,0\nv_mov_b32 v53,0\nv_mov_b32 v54,0\nv_mov_b32 v55,0\nv_mov_b32 v56,0\nv_mov_b32 v57,0\n" \
"v_mov_b32 v58,0\nv_mov_b32 v59,0\nv_mov_b32 v60,0\nv_mov_b32 v61,0\nv_mov_b32 v62,0\nv_mov_b32 v63,0\ns_waitcnt vmcnt(0)\nv_readfirstlane_b32 s51,v229\n" \
"v_mov_b32 v230,v124\nv_mov_b32 v231,v125\nv_mov_b32 v249,v126\nv_mov_b32 v250,v127\nv_mov_b32 v251,v128\nv_mov_b32 v252,v129\nv_mov_b32 v255,v130\n" \
"v_mov_b32 v222,v131\nv_mov_b32 v128,v232\nv_mov_b32 v129,v233\nv_mov_b32 v130,v234\nv_mov_b32 v131,v235\nv_mov_b32 v132,v236\nv_mov_b32 v133,v237\n" \
"v_mov_b32 v134,v238\nv_mov_b32 v135,v239\nv_mov_b32 v136,v240\nv_mov_b32 v137,v241\nv_mov_b32 v138,v242\nv_mov_b32 v139,v243\nv_mov_b32 v140,v244\n" \
"v_mov_b32 v141,v245\nv_mov_b32 v142,v246\nv_mov_b32 v143,v247\nv_mov_b32 v216,0\nv_mov_b32 v232,v104\nv_mov_b32 v233,v105\nv_mov_b32 v234,v106\n" \
"v_mov_b32 v235,v107\nv_mov_b32 v236,v108\nv_mov_b32 v237,v109\nv_mov_b32 v238,v110\nv_mov_b32 v239,v111\nv_mov_b32 v240,v112\nv_mov_b32 v241,v113\n" \
"v_mov_b32 v242,v114\nv_mov_b32 v243,v115\nv_mov_b32 v244,v116\nv_mov_b32 v245,v117\nv_mov_b32 v246,v118\nv_mov_b32 v247,v119\nv_mov_b32 v226,v120\n" \
"v_mov_b32 v227,v121\nv_mov_b32 v228,v122\nv_mov_b32 v229,v123\ns_waitcnt vmcnt(8)\ns_barrier\ns_cmp_ge_u32 s51,4\ns_cbranch_scc1 602f\n" \
"v_mov_b32 v220,0\ns_mov_b32 s49,0x400000\n601:\nglobal_load_dword v221,v220,s[78:79] offset:64 sc1\ns_waitcnt vmcnt(0)\nv_readfirstlane_b32 s50,v221\n" \
"s_sub_u32 s49,s49,1\ns_cbranch_scc1 602f\ns_cmp_ge_u32 s50,4\ns_cbranch_scc1 602f\ns_sleep 8\ns_branch 601b\n602:\ns_sub_u32 s48,s72,2\n" \
"s_and_b32 s46,s48,3\ns_sub_u32 s48,s48,s69\ns_lshl_b32 s44,s48,15\ns_add_u32 s45,s44,4096\ns_lshl_b32 s46,s46,15\ns_add_u32 s46,s46,s74\n" \
"s_mov_b32 m0,s46\ns_nop 0\nbuffer_load_dwordx4 v254,%[srd],s44 offen offset:0 sc1 lds\nbuffer_load_dwordx4 v254,%[srd],s44 offen offset:1024 sc1 lds\n" \
"buffer_load_dwordx4 v254,%[srd],s44 offen offset:2048 sc1 lds\nbuffer_load_dwordx4 v254,%[srd],s44 offen offset:3072 sc1 lds\ns_add_u32 m0,s46,4096\n" \
"s_nop 0\nbuffer_load_dwordx4 v254,%[srd],s45 offen offset:0 sc1 lds\nbuffer_load_dwordx4 v254,%[srd],s45 offen offset:1024 sc1 lds\n" \
"buffer_load_dwordx4 v254,%[srd],s45 offen offset:2048 sc1 lds\nbuffer_load_dwordx4 v254,%[srd],s45 offen offset:3072 sc1 lds\ns_mov_b32 s40,s72\n" \
"s_lshr_b32 s41,s73,1\ns_add_u32 s41,s41,s69\ns_mov_b32 s47,s74\n10:\ns_cmp_le_i32 s40,s41\ns_cbranch_scc1 11f\ns_sub_u32 s43,s40,1\n" \
"s_and_b32 s43,s43,3\ns_lshl_b32 s43,s43,15\ns_sub_u32 s48,s40,s70\ns_lshl_b32 s44,s48,15\ns_cmp_lg_u32 s48,15\ns_cbranch_scc1 702f\nv_mov_b32 v220,0\n" \
"s_mov_b32 s49,0x400000\n701:\nglobal_load_dword v221,v220,s[78:79] offset:64 sc1\ns_waitcnt vmcnt(0)\nv_readfirstlane_b32 s50,v221\n" \
"s_sub_u32 s49,s49,1\ns_cbranch_scc1 702f\ns_cmp_ge_u32 s50,4\ns_cbranch_scc1 702f\ns_sleep 8\ns_branch 701b\n702:\ns_sub_u32 s49,s81,s44\n" \
"s_cmp_lt_i32 s48,0\ns_cselect_b32 s44,s49,s44\ns_add_u32 s45,s44,4096\ns_xor_b32 s46,s43,0x10000\ns_add_u32 s46,s46,s47\ns_waitcnt vmcnt(8)\ns_barrier\n" \
"s_mov_b32 m0,s46\ns_nop 0\nbuffer_load_dwordx4 v254,%[srd],s44 offen offset:0 sc1 lds\nbuffer_load_dwordx4 v254,%[srd],s44 offen offset:1024 sc1 lds\n" \
"buffer_load_dwordx4 v254,%[srd],s44 offen offset:2048 sc1 lds\nbuffer_load_dwordx4 v254,%[srd],s44 offen offset:3072 sc1 lds\ns_add_u32 m0,s46,4096\n" \
"s_nop 0\nbuffer_load_dwordx4 v254,%[srd],s45 offen offset:0 sc1 lds\nbuffer_load_dwordx4 v254,%[srd],s45 offen offset:1024 sc1 lds\n" \
"buffer_load_dwordx4 v254,%[srd],s45 offen offset:2048 sc1 lds\nbuffer_load_dwordx4 v254,%[srd],s45 offen offset:3072 sc1 lds\ns_sub_u32 s40,s40,1\n" \
"s_branch 10b\n11:\ns_and_b32 s43,s40,3\ns_lshl_b32 s43,s43,15\nv_add_u32 v217,s43,v253\nv_mov_b32 v218,v217\ns_bitcmp1_b32 s73,0\ns_cbranch_scc1 12f\n" \
"s_sub_u32 s43,s40,1\ns_and_b32 s43,s43,3\ns_lshl_b32 s43,s43,15\ns_sub_u32 s48,s40,s70\ns_lshl_b32 s44,s48,15\ns_cmp_lg_u32 s48,15\n" \
"s_cbranch_scc1 712f\nv_mov_b32 v220,0\ns_mov_b32 s49,0x400000\n711:\nglobal_load_dword v221,v220,s[78:79] offset:64 sc1\ns_waitcnt vmcnt(0)\n" \
"v_readfirstlane_b32 s50,v221\ns_sub_u32 s49,s49,1\ns_cbranch_scc1 712f\ns_cmp_ge_u32 s50,4\ns_cbranch_scc1 712f\ns_sleep 8\ns_branch 711b\n712:\n" \
"s_sub_u32 s49,s81,s44\ns_cmp_lt_i32 s48,0\ns_cselect_b32 s44,s49,s44\ns_add_u32 s45,s44,4096\nds_read_b128 v[152:155],v217 offset:0\n" \
"s_xor_b32 s46,s43,0x10000\nds_read_b128 v[156:159],v217 offset:1024\ns_add_u32 s46,s46,s47\nds_read_b128 v[160:163],v217 offset:2048\n" \
"v_add_u32 v219,s43,v253\nds_read_b128 v[164:167],v217 offset:3072\nds_read_b128 v[168:171],v217 offset:4096\nds_read_b128 v[172:175],v217 offset:5120\n" \
"ds_read_b128 v[176:179],v217 offset:6144\nds_read_b128 v[180:183],v217 offset:7168\ns_waitcnt lgkmcnt(7)\n" \
"v_mfma_f32_32x32x16_bf16 v[96:111],v[152:155],v[64:67],v[128:143]\ns_waitcnt lgkmcnt(5)\n" \
"v_mfma_f32_32x32x16_bf16 v[96:111],v[156:159],v[68:71],v[96:111]\nv_mfma_f32_32x32x16_bf16 v[96:111],v[160:163],v[72:75],v[96:111]\ns_waitcnt vmcnt(8)\n" \
"s_barrier\ns_mov_b32 m0,s46\ns_waitcnt lgkmcnt(3)\nv_mfma_f32_32x32x16_bf16 v[96:111],v[164:167],v[76:79],v[96:111]\n" \
"buffer_load_dwordx4 v254,%[srd],s44 offen offset:0 sc1 lds\nv_mfma_f32_32x32x16_bf16 v[96:111],v[168:171],v[80:83],v[96:111]\n" \
"buffer_load_dwordx4 v254,%[srd],s44 offen offset:1024 sc1 lds\ns_waitcnt lgkmcnt(1)\nv_mfma_f32_32x32x16_bf16 v[96:111],v[172:175],v[84:87],v[96:111]\n" \
"buffer_load_dwordx4 v254,%[srd],s44 offen offset:2048 sc1 lds\nv_mfma_f32_32x32x16_bf16 v[96:111],v[176:179],v[88:91],v[96:111]\n" \
"buffer_load_dwordx4 v254,%[srd],s44 offen offset:3072 sc1 lds\ns_add_u32 m0,s46,4096\ns_waitcnt lgkmcnt(0)\n" \
"v_mfma_f32_32x32x16_bf16 v[96:111],v[180:183],v[92:95],v[96:111]\nbuffer_load_dwordx4 v254,%[srd],s45 offen offset:0 sc1 lds\n" \
"ds_read_b128 v[152:155],v219 offset:16384\nbuffer_load_dwordx4 v254,%[srd],s45 offen offset:1024 sc1 lds\nds_read_b128 v[156:159],v219 offset:17408\n" \
"s_nop 7\ns_nop 7\nv_max3_f32 v223,v96,v97,v98\nv_max3_f32 v224,v99,v100,v101\nv_max3_f32 v223,v223,v102,v103\nv_max3_f32 v224,v224,v104,v105\n" \
"v_max3_f32 v223,v223,v106,v107\nv_max3_f32 v224,v224,v108,v109\nv_max3_f32 v223,v223,v110,v111\nv_max3_f32 v223,v223,v224,v224\nv_mov_b32 v224,v223\n" \
"s_nop 1\nv_permlane32_swap_b32 v223,v224\ns_nop 1\nv_max_f32 v223,v223,v224\nv_sub_f32 v96,v96,v223\nv_sub_f32 v97,v97,v223\nv_sub_f32 v98,v98,v223\n" \
"v_sub_f32 v99,v99,v223\nv_sub_f32 v100,v100,v223\nv_sub_f32 v101,v101,v223\nv_sub_f32 v102,v102,v223\nv_sub_f32 v103,v103,v223\n" \
"v_sub_f32 v104,v104,v223\nv_sub_f32 v105,v105,v223\nv_sub_f32 v106,v106,v223\nv_sub_f32 v107,v107,v223\nv_sub_f32 v108,v108,v223\n" \
"v_sub_f32 v109,v109,v223\nv_sub_f32 v110,v110,v223\nv_sub_f32 v111,v111,v223\nv_sub_f32 v128,0,v223\nv_sub_f32 v129,0,v223\nv_sub_f32 v130,0,v223\n" \
"v_sub_f32 v131,0,v223\nv_sub_f32 v132,0,v223\nv_sub_f32 v133,0,v223\nv_sub_f32 v134,0,v223\nv_sub_f32 v135,0,v223\nv_sub_f32 v136,0,v223\n" \
"v_sub_f32 v137,0,v223\nv_sub_f32 v138,0,v223\nv_sub_f32 v139,0,v223\nv_sub_f32 v140,0,v223\nv_sub_f32 v141,0,v223\nv_sub_f32 v142,0,v223\n" \
"v_sub_f32 v143,0,v223\ns_nop 1\nv_exp_f32 v96,v96\nbuffer_load_dwordx4 v254,%[srd],s45 offen offset:2048 sc1 lds\n" \
"ds_read_b128 v[160:163],v219 offset:18432\nv_exp_f32 v97,v97\nv_add_f32 v216,v216,v96\nbuffer_load_dwordx4 v254,%[srd],s45 offen offset:3072 sc1 lds\n" \
"ds_read_b128 v[164:167],v219 offset:19456\nv_exp_f32 v98,v98\nv_add_f32 v216,v216,v97\nv_cvt_pk_bf16_f32 v144,v96,v97\n" \
"ds_read_b128 v[168:171],v219 offset:20480\nv_exp_f32 v99,v99\nv_add_f32 v216,v216,v98\nv_mov_b32 v218,v217\nds_read_b128 v[172:175],v219 offset:21504\n" \
"v_exp_f32 v100,v100\nv_add_f32 v216,v216,v99\nv_cvt_pk_bf16_f32 v145,v98,v99\ns_sub_u32 s40,s40,1\nds_read_b128 v[176:179],v219 offset:22528\n" \
"v_exp_f32 v101,v101\nv_add_f32 v216,v216,v100\ns_cmp_ge_i32 s40,s69\nds_read_b128 v[180:183],v219 offset:23552\nv_exp_f32 v102,v102\n" \
"v_add_f32 v216,v216,v101\nv_cvt_pk_bf16_f32 v146,v100,v101\nv_mov_b32 v217,v219\ns_branch 13f\n12:\nds_read_b128 v[152:155],v217 offset:16384\n" \
"ds_read_b128 v[156:159],v217 offset:17408\nds_read_b128 v[160:163],v217 offset:18432\nds_read_b128 v[164:167],v217 offset:19456\n" \
"ds_read_b128 v[168:171],v217 offset:20480\nds_read_b128 v[172:175],v217 offset:21504\nds_read_b128 v[176:179],v217 offset:22528\n" \
"ds_read_b128 v[180:183],v217 offset:23552\ns_waitcnt lgkmcnt(7)\nv_mfma_f32_32x32x16_bf16 v[112:127],v[152:155],v[64:67],v[128:143]\n" \
"s_waitcnt lgkmcnt(5)\nv_mfma_f32_32x32x16_bf16 v[112:127],v[156:159],v[68:71],v[112:127]\ns_sub_u32 s43,s40,1\n" \
"v_mfma_f32_32x32x16_bf16 v[112:127],v[160:163],v[72:75],v[112:127]\ns_and_b32 s43,s43,3\ns_waitcnt lgkmcnt(3)\n" \
"v_mfma_f32_32x32x16_bf16 v[112:127],v[164:167],v[76:79],v[112:127]\ns_lshl_b32 s43,s43,15\n" \
"v_mfma_f32_32x32x16_bf16 v[112:127],v[168:171],v[80:83],v[112:127]\ns_sub_u32 s48,s40,s70\ns_waitcnt lgkmcnt(1)\n" \
"v_mfma_f32_32x32x16_bf16 v[112:127],v[172:175],v[84:87],v[112:127]\ns_lshl_b32 s44,s48,15\n" \
"v_mfma_f32_32x32x16_bf16 v[112:127],v[176:179],v[88:91],v[112:127]\ns_cmp_lg_u32 s48,15\ns_cbranch_scc1 722f\nv_mov_b32 v220,0\ns_mov_b32 s49,0x400000\n" \
"721:\nglobal_load_dword v221,v220,s[78:79] offset:64 sc1\ns_waitcnt vmcnt(0)\nv_readfirstlane_b32 s50,v221\ns_sub_u32 s49,s49,1\ns_cbranch_scc1 722f\n" \
"s_cmp_ge_u32 s50,4\ns_cbranch_scc1 722f\ns_sleep 8\ns_branch 721b\n722:\ns_sub_u32 s49,s81,s44\ns_cmp_lt_i32 s48,0\ns_cselect_b32 s44,s49,s44\n" \
"s_waitcnt lgkmcnt(0)\nv_mfma_f32_32x32x16_bf16 v[112:127],v[180:183],v[92:95],v[112:127]\ns_add_u32 s45,s44,4096\n" \
"ds_read_b128 v[152:155],v217 offset:0\ns_xor_b32 s46,s43,0x10000\nds_read_b128 v[156:159],v217 offset:1024\ns_nop 7\ns_nop 7\n" \
"v_max3_f32 v223,v112,v113,v114\nv_max3_f32 v224,v115,v116,v117\nv_max3_f32 v223,v223,v118,v119\nv_max3_f32 v224,v224,v120,v121\n" \
"v_max3_f32 v223,v223,v122,v123\nv_max3_f32 v224,v224,v124,v125\nv_max3_f32 v223,v223,v126,v127\nv_max3_f32 v223,v223,v224,v224\nv_mov_b32 v224,v223\n" \
"s_nop 1\nv_permlane32_swap_b32 v223,v224\ns_nop 1\nv_max_f32 v223,v223,v224\nv_sub_f32 v112,v112,v223\nv_sub_f32 v113,v113,v223\n" \
"v_sub_f32 v114,v114,v223\nv_sub_f32 v115,v115,v223\nv_sub_f32 v116,v116,v223\nv_sub_f32 v117,v117,v223\nv_sub_f32 v118,v118,v223\n" \
"v_sub_f32 v119,v119,v223\nv_sub_f32 v120,v120,v223\nv_sub_f32 v121,v121,v223\nv_sub_f32 v122,v122,v223\nv_sub_f32 v123,v123,v223\n" \
"v_sub_f32 v124,v124,v223\nv_sub_f32 v125,v125,v223\nv_sub_f32 v126,v126,v223\nv_sub_f32 v127,v127,v223\nv_sub_f32 v128,0,v223\nv_sub_f32 v129,0,v223\n" \
"v_sub_f32 v130,0,v223\nv_sub_f32 v131,0,v223\nv_sub_f32 v132,0,v223\nv_sub_f32 v133,0,v223\nv_sub_f32 v134,0,v223\nv_sub_f32 v135,0,v223\n" \
"v_sub_f32 v136,0,v223\nv_sub_f32 v137,0,v223\nv_sub_f32 v138,0,v223\nv_sub_f32 v139,0,v223\nv_sub_f32 v140,0,v223\nv_sub_f32 v141,0,v223\n" \
"v_sub_f32 v142,0,v223\nv_sub_f32 v143,0,v223\ns_nop 1\nv_exp_f32 v112,v112\ns_add_u32 s46,s46,s47\nds_read_b128 v[160:163],v217 offset:2048\n" \
"v_exp_f32 v113,v113\nv_add_f32 v216,v216,v112\nv_add_u32 v219,s43,v253\nds_read_b128 v[164:167],v217 offset:3072\nv_exp_f32 v114,v114\n" \
"v_add_f32 v216,v216,v113\nv_cvt_pk_bf16_f32 v144,v112,v113\nds_read_b128 v[168:171],v217 offset:4096\nv_exp_f32 v115,v115\nv_add_f32 v216,v216,v114\n" \
"ds_read_b128 v[172:175],v217 offset:5120\nv_exp_f32 v116,v116\nv_add_f32 v216,v216,v115\nv_cvt_pk_bf16_f32 v145,v114,v115\n" \
"ds_read_b128 v[176:179],v217 offset:6144\nv_exp_f32 v117,v117\nv_add_f32 v216,v216,v116\nds_read_b128 v[180:183],v217 offset:7168\nv_exp_f32 v118,v118\n" \
"v_add_f32 v216,v216,v117\nv_cvt_pk_bf16_f32 v146,v116,v117\ns_waitcnt lgkmcnt(7)\nv_mfma_f32_32x32x16_bf16 v[96:111],v[152:155],v[64:67],v[128:143]\n" \
"ds_read_b128 v[184:187],v217 offset:24576\nv_exp_f32 v119,v119\nv_add_f32 v216,v216,v118\ns_waitcnt lgkmcnt(6)\n" \
"v_mfma_f32_32x32x16_bf16 v[96:111],v[156:159],v[68:71],v[96:111]\nds_read_b128 v[188:191],v217 offset:25600\nv_exp_f32 v120,v120\n" \
"v_add_f32 v216,v216,v119\nv_cvt_pk_bf16_f32 v147,v118,v119\nv_mfma_f32_32x32x16_bf16 v[96:111],v[160:163],v[72:75],v[96:111]\n" \
"ds_read_b128 v[192:195],v217 offset:26624\nv_exp_f32 v121,v121\nv_add_f32 v216,v216,v120\ns_waitcnt vmcnt(8)\ns_barrier\ns_mov_b32 m0,s46\n" \
"s_waitcnt lgkmcnt(6)\nv_mfma_f32_32x32x16_bf16 v[96:111],v[164:167],v[76:79],v[96:111]\nds_read_b128 v[196:199],v217 offset:27648\nv_exp_f32 v122,v122\n" \
"v_add_f32 v216,v216,v121\nv_cvt_pk_bf16_f32 v148,v120,v121\nbuffer_load_dwordx4 v254,%[srd],s44 offen offset:0 sc1 lds\n" \
"v_mfma_f32_32x32x16_bf16 v[96:111],v[168:171],v[80:83],v[96:111]\nds_read_b128 v[200:203],v217 offset:28672\nv_exp_f32 v123,v123\n" \
"v_add_f32 v216,v216,v122\nbuffer_load_dwordx4 v254,%[srd],s44 offen offset:1024 sc1 lds\ns_waitcnt lgkmcnt(6)\n" \
"v_mfma_f32_32x32x16_bf16 v[96:111],v[172:175],v[84:87],v[96:111]\nds_read_b128 v[204:207],v217 offset:29696\nv_exp_f32 v124,v124\n" \
"v_add_f32 v216,v216,v123\nv_cvt_pk_bf16_f32 v149,v122,v123\nbuffer_load_dwordx4 v254,%[srd],s44 offen offset:2048 sc1 lds\n" \
"v_mfma_f32_32x32x16_bf16 v[96:111],v[176:179],v[88:91],v[96:111]\nds_read_b128 v[208:211],v217 offset:30720\nv_exp_f32 v125,v125\n" \
"v_add_f32 v216,v216,v124\nbuffer_load_dwordx4 v254,%[srd],s44 offen offset:3072 sc1 lds\ns_add_u32 m0,s46,4096\ns_waitcnt lgkmcnt(6)\n" \
"v_mfma_f32_32x32x16_bf16 v[96:111],v[180:183],v[92:95],v[96:111]\nds_read_b128 v[212:215],v217 offset:31744\nv_exp_f32 v126,v126\n" \
"v_add_f32 v216,v216,v125\nv_cvt_pk_bf16_f32 v150,v124,v125\nbuffer_load_dwordx4 v254,%[srd],s45 offen offset:0 sc1 lds\n" \
"v_mfma_f32_32x32x16_bf16 v[0:15],v[184:187],v[144:147],v[0:15]\nds_read_b128 v[152:155],v219 offset:16384\nv_exp_f32 v127,v127\n" \
"v_add_f32 v216,v216,v126\nbuffer_load_dwordx4 v254,%[srd],s45 offen offset:1024 sc1 lds\ns_waitcnt lgkmcnt(6)\n" \
"v_mfma_f32_32x32x16_bf16 v[16:31],v[188:191],v[144:147],v[16:31]\nds_read_b128 v[156:159],v219 offset:17408\nv_exp_f32 v96,v96\n" \
"v_add_f32 v216,v216,v127\nv_cvt_pk_bf16_f32 v151,v126,v127\nbuffer_load_dwordx4 v254,%[srd],s45 offen offset:2048 sc1 lds\n" \
"v_mfma_f32_32x32x16_bf16 v[32:47],v[192:195],v[144:147],v[32:47]\nds_read_b128 v[160:163],v219 offset:18432\nv_exp_f32 v97,v97\n" \
"v_add_f32 v216,v216,v96\nbuffer_load_dwordx4 v254,%[srd],s45 offen offset:3072 sc1 lds\ns_waitcnt lgkmcnt(6)\n" \
"v_mfma_f32_32x32x16_bf16 v[48:63],v[196:199],v[144:147],v[48:63]\nds_read_b128 v[164:167],v219 offset:19456\nv_exp_f32 v98,v98\n" \
"v_add_f32 v216,v216,v97\nv_cvt_pk_bf16_f32 v144,v96,v97\nv_mfma_f32_32x32x16_bf16 v[0:15],v[200:203],v[148:151],v[0:15]\n" \
"ds_read_b128 v[168:171],v219 offset:20480\nv_exp_f32 v99,v99\nv_add_f32 v216,v216,v98\nv_mov_b32 v218,v217\ns_waitcnt lgkmcnt(6)\n" \
"v_mfma_f32_32x32x16_bf16 v[16:31],v[204:207],v[148:151],v[16:31]\nds_read_b128 v[172:175],v219 offset:21504\nv_exp_f32 v100,v100\n" \
"v_add_f32 v216,v216,v99\nv_cvt_pk_bf16_f32 v145,v98,v99\ns_sub_u32 s40,s40,1\nv_mfma_f32_32x32x16_bf16 v[32:47],v[208:211],v[148:151],v[32:47]\n" \
"ds_read_b128 v[176:179],v219 offset:22528\nv_exp_f32 v101,v101\nv_add_f32 v216,v216,v100\ns_cmp_ge_i32 s40,s69\ns_waitcnt lgkmcnt(7)\n" \
"v_mfma_f32_32x32x16_bf16 v[48:63],v[212:215],v[148:151],v[48:63]\nds_read_b128 v[180:183],v219 offset:23552\nv_exp_f32 v102,v102\n" \
"v_add_f32 v216,v216,v101\nv_cvt_pk_bf16_f32 v146,v100,v101\nv_mov_b32 v217,v219\n13:\ns_cbranch_scc0 15f\n14:\ns_waitcnt lgkmcnt(7)\n" \
"v_mfma_f32_32x32x16_bf16 v[112:127],v[152:155],v[64:67],v[128:143]\nds_read_b128 v[184:187],v218 offset:8192\nv_exp_f32 v103,v103\n" \
"v_add_f32 v216,v216,v102\ns_waitcnt lgkmcnt(6)\nv_mfma_f32_32x32x16_bf16 v[112:127],v[156:159],v[68:71],v[112:127]\n" \
"ds_read_b128 v[188:191],v218 offset:9216\nv_exp_f32 v104,v104\nv_add_f32 v216,v216,v103\nv_cvt_pk_bf16_f32 v147,v102,v103\ns_sub_u32 s43,s40,1\n" \
"v_mfma_f32_32x32x16_bf16 v[112:127],v[160:163],v[72:75],v[112:127]\nds_read_b128 v[192:195],v218 offset:10240\nv_exp_f32 v105,v105\n" \
"v_add_f32 v216,v216,v104\ns_and_b32 s43,s43,3\ns_waitcnt lgkmcnt(6)\nv_mfma_f32_32x32x16_bf16 v[112:127],v[164:167],v[76:79],v[112:127]\n" \
"ds_read_b128 v[196:199],v218 offset:11264\nv_exp_f32 v106,v106\nv_add_f32 v216,v216,v105\nv_cvt_pk_bf16_f32 v148,v104,v105\ns_lshl_b32 s43,s43,15\n" \
"v_mfma_f32_32x32x16_bf16 v[112:127],v[168:171],v[80:83],v[112:127]\nds_read_b128 v[200:203],v218 offset:12288\nv_exp_f32 v107,v107\n" \
"v_add_f32 v216,v216,v106\ns_sub_u32 s48,s40,s70\ns_waitcnt lgkmcnt(6)\nv_mfma_f32_32x32x16_bf16 v[112:127],v[172:175],v[84:87],v[112:127]\n" \
"ds_read_b128 v[204:207],v218 offset:13312\nv_exp_f32 v108,v108\nv_add_f32 v216,v216,v107\nv_cvt_pk_bf16_f32 v149,v106,v107\ns_lshl_b32 s44,s48,15\n" \
"v_mfma_f32_32x32x16_bf16 v[112:127],v[176:179],v[88:91],v[112:127]\nds_read_b128 v[208:211],v218 offset:14336\nv_exp_f32 v109,v109\n" \
"v_add_f32 v216,v216,v108\ns_cmp_lg_u32 s48,15\ns_cbranch_scc1 732f\nv_mov_b32 v220,0\ns_mov_b32 s49,0x400000\n731:\n" \
"global_load_dword v221,v220,s[78:79] offset:64 sc1\ns_waitcnt vmcnt(0)\nv_readfirstlane_b32 s50,v221\ns_sub_u32 s49,s49,1\ns_cbranch_scc1 732f\n" \
"s_cmp_ge_u32 s50,4\ns_cbranch_scc1 732f\ns_sleep 8\ns_branch 731b\n732:\ns_sub_u32 s49,s81,s44\ns_cmp_lt_i32 s48,0\ns_cselect_b32 s44,s49,s44\n" \
"s_waitcnt lgkmcnt(6)\nv_mfma_f32_32x32x16_bf16 v[112:127],v[180:183],v[92:95],v[112:127]\nds_read_b128 v[212:215],v218 offset:15360\n" \
"v_exp_f32 v110,v110\nv_add_f32 v216,v216,v109\nv_cvt_pk_bf16_f32 v150,v108,v109\ns_add_u32 s45,s44,4096\n" \
"v_mfma_f32_32x32x16_bf16 v[0:15],v[184:187],v[144:147],v[0:15]\nds_read_b128 v[152:155],v217 offset:0\nv_exp_f32 v111,v111\nv_add_f32 v216,v216,v110\n" \
"s_xor_b32 s46,s43,0x10000\ns_waitcnt lgkmcnt(6)\nv_mfma_f32_32x32x16_bf16 v[16:31],v[188:191],v[144:147],v[16:31]\n" \
"ds_read_b128 v[156:159],v217 offset:1024\nv_exp_f32 v112,v112\nv_add_f32 v216,v216,v111\nv_cvt_pk_bf16_f32 v151,v110,v111\ns_add_u32 s46,s46,s47\n" \
"v_mfma_f32_32x32x16_bf16 v[32:47],v[192:195],v[144:147],v[32:47]\nds_read_b128 v[160:163],v217 offset:2048\nv_exp_f32 v113,v113\n" \
"v_add_f32 v216,v216,v112\nv_add_u32 v219,s43,v253\ns_waitcnt lgkmcnt(6)\nv_mfma_f32_32x32x16_bf16 v[48:63],v[196:199],v[144:147],v[48:63]\n" \
"ds_read_b128 v[164:167],v217 offset:3072\nv_exp_f32 v114,v114\nv_add_f32 v216,v216,v113\nv_cvt_pk_bf16_f32 v144,v112,v113\n" \
"v_mfma_f32_32x32x16_bf16 v[0:15],v[200:203],v[148:151],v[0:15]\nds_read_b128 v[168:171],v217 offset:4096\nv_exp_f32 v115,v115\n" \
"v_add_f32 v216,v216,v114\ns_waitcnt lgkmcnt(6)\nv_mfma_f32_32x32x16_bf16 v[16:31],v[204:207],v[148:151],v[16:31]\n" \
"ds_read_b128 v[172:175],v217 offset:5120\nv_exp_f32 v116,v116\nv_add_f32 v216,v216,v115\nv_cvt_pk_bf16_f32 v145,v114,v115\n" \
"v_mfma_f32_32x32x16_bf16 v[32:47],v[208:211],v[148:151],v[32:47]\nds_read_b128 v[176:179],v217 offset:6144\nv_exp_f32 v117,v117\n" \
"v_add_f32 v216,v216,v116\ns_waitcnt lgkmcnt(6)\nv_mfma_f32_32x32x16_bf16 v[48:63],v[212:215],v[148:151],v[48:63]\n" \
"ds_read_b128 v[180:183],v217 offset:7168\nv_exp_f32 v118,v118\nv_add_f32 v216,v216,v117\nv_cvt_pk_bf16_f32 v146,v116,v117\n" \
"v_mfma_f32_32x32x16_bf16 v[96:111],v[152:155],v[64:67],v[128:143]\nds_read_b128 v[184:187],v217 offset:24576\nv_exp_f32 v119,v119\n" \
"v_add_f32 v216,v216,v118\ns_waitcnt lgkmcnt(6)\nv_mfma_f32_32x32x16_bf16 v[96:111],v[156:159],v[68:71],v[96:111]\n" \
"ds_read_b128 v[188:191],v217 offset:25600\nv_exp_f32 v120,v120\nv_add_f32 v216,v216,v119\nv_cvt_pk_bf16_f32 v147,v118,v119\n" \
"v_mfma_f32_32x32x16_bf16 v[96:111],v[160:163],v[72:75],v[96:111]\nds_read_b128 v[192:195],v217 offset:26624\nv_exp_f32 v121,v121\n" \
"v_add_f32 v216,v216,v120\ns_waitcnt vmcnt(8)\ns_barrier\ns_mov_b32 m0,s46\ns_waitcnt lgkmcnt(6)\n" \
"v_mfma_f32_32x32x16_bf16 v[96:111],v[164:167],v[76:79],v[96:111]\nds_read_b128 v[196:199],v217 offset:27648\nv_exp_f32 v122,v122\n" \
"v_add_f32 v216,v216,v121\nv_cvt_pk_bf16_f32 v148,v120,v121\nbuffer_load_dwordx4 v254,%[srd],s44 offen offset:0 sc1 lds\n" \
"v_mfma_f32_32x32x16_bf16 v[96:111],v[168:171],v[80:83],v[96:111]\nds_read_b128 v[200:203],v217 offset:28672\nv_exp_f32 v123,v123\n" \
"v_add_f32 v216,v216,v122\nbuffer_load_dwordx4 v254,%[srd],s44 offen offset:1024 sc1 lds\ns_waitcnt lgkmcnt(6)\n" \
"v_mfma_f32_32x32x16_bf16 v[96:111],v[172:175],v[84:87],v[96:111]\nds_read_b128 v[204:207],v217 offset:29696\nv_exp_f32 v124,v124\n" \
"v_add_f32 v216,v216,v123\nv_cvt_pk_bf16_f32 v149,v122,v123\nbuffer_load_dwordx4 v254,%[srd],s44 offen offset:2048 sc1 lds\n" \
"v_mfma_f32_32x32x16_bf16 v[96:111],v[176:179],v[88:91],v[96:111]\nds_read_b128 v[208:211],v217 offset:30720\nv_exp_f32 v125,v125\n" \
"v_add_f32 v216,v216,v124\nbuffer_load_dwordx4 v254,%[srd],s44 offen offset:3072 sc1 lds\ns_add_u32 m0,s46,4096\ns_waitcnt lgkmcnt(6)\n" \
"v_mfma_f32_32x32x16_bf16 v[96:111],v[180:183],v[92:95],v[96:111]\nds_read_b128 v[212:215],v217 offset:31744\nv_exp_f32 v126,v126\n" \
"v_add_f32 v216,v216,v125\nv_cvt_pk_bf16_f32 v150,v124,v125\nbuffer_load_dwordx4 v254,%[srd],s45 offen offset:0 sc1 lds\n" \
"v_mfma_f32_32x32x16_bf16 v[0:15],v[184:187],v[144:147],v[0:15]\nds_read_b128 v[152:155],v219 offset:16384\nv_exp_f32 v127,v127\n" \
"v_add_f32 v216,v216,v126\nbuffer_load_dwordx4 v254,%[srd],s45 offen offset:1024 sc1 lds\ns_waitcnt lgkmcnt(6)\n" \
"v_mfma_f32_32x32x16_bf16 v[16:31],v[188:191],v[144:147],v[16:31]\nds_read_b128 v[156:159],v219 offset:17408\nv_exp_f32 v96,v96\n" \
"v_add_f32 v216,v216,v127\nv_cvt_pk_bf16_f32 v151,v126,v127\nbuffer_load_dwordx4 v254,%[srd],s45 offen offset:2048 sc1 lds\n" \
"v_mfma_f32_32x32x16_bf16 v[32:47],v[192:195],v[144:147],v[32:47]\nds_read_b128 v[160:163],v219 offset:18432\nv_exp_f32 v97,v97\n" \
"v_add_f32 v216,v216,v96\nbuffer_load_dwordx4 v254,%[srd],s45 offen offset:3072 sc1 lds\ns_waitcnt lgkmcnt(6)\n" \
"v_mfma_f32_32x32x16_bf16 v[48:63],v[196:199],v[144:147],v[48:63]\nds_read_b128 v[164:167],v219 offset:19456\nv_exp_f32 v98,v98\n" \
"v_add_f32 v216,v216,v97\nv_cvt_pk_bf16_f32 v144,v96,v97\nv_mfma_f32_32x32x16_bf16 v[0:15],v[200:203],v[148:151],v[0:15]\n" \
"ds_read_b128 v[168:171],v219 offset:20480\nv_exp_f32 v99,v99\nv_add_f32 v216,v216,v98\nv_mov_b32 v218,v217\ns_waitcnt lgkmcnt(6)\n" \
"v_mfma_f32_32x32x16_bf16 v[16:31],v[204:207],v[148:151],v[16:31]\nds_read_b128 v[172:175],v219 offset:21504\nv_exp_f32 v100,v100\n" \
"v_add_f32 v216,v216,v99\nv_cvt_pk_bf16_f32 v145,v98,v99\ns_sub_u32 s40,s40,1\nv_mfma_f32_32x32x16_bf16 v[32:47],v[208:211],v[148:151],v[32:47]\n" \
"ds_read_b128 v[176:179],v219 offset:22528\nv_exp_f32 v101,v101\nv_add_f32 v216,v216,v100\ns_cmp_ge_i32 s40,s69\ns_waitcnt lgkmcnt(7)\n" \
"v_mfma_f32_32x32x16_bf16 v[48:63],v[212:215],v[148:151],v[48:63]\nds_read_b128 v[180:183],v219 offset:23552\nv_exp_f32 v102,v102\n" \
"v_add_f32 v216,v216,v101\nv_cvt_pk_bf16_f32 v146,v100,v101\nv_mov_b32 v217,v219\ns_cbranch_scc1 14b\n15:\nv_lshrrev_b32 v114,2,v248\n" \
"v_and_b32 v114,0x7c,v114\ns_lshl_b32 s49,s73,7\ns_add_u32 s49,s49,73662464\nbuffer_load_dword v112,v114,%[sws],s49 offen\n" \
"buffer_load_dword v113,v114,%[sws],s82 offen\nds_read_b128 v[184:187],v218 offset:8192\nv_exp_f32 v103,v103\nv_add_f32 v216,v216,v102\n" \
"ds_read_b128 v[188:191],v218 offset:9216\nv_exp_f32 v104,v104\nv_add_f32 v216,v216,v103\nv_cvt_pk_bf16_f32 v147,v102,v103\n" \
"ds_read_b128 v[192:195],v218 offset:10240\nv_exp_f32 v105,v105\nv_add_f32 v216,v216,v104\nds_read_b128 v[196:199],v218 offset:11264\n" \
"v_exp_f32 v106,v106\nv_add_f32 v216,v216,v105\nv_cvt_pk_bf16_f32 v148,v104,v105\nds_read_b128 v[200:203],v218 offset:12288\nv_exp_f32 v107,v107\n" \
"v_add_f32 v216,v216,v106\nds_read_b128 v[204:207],v218 offset:13312\nv_exp_f32 v108,v108\nv_add_f32 v216,v216,v107\nv_cvt_pk_bf16_f32 v149,v106,v107\n" \
"ds_read_b128 v[208:211],v218 offset:14336\nv_exp_f32 v109,v109\nv_add_f32 v216,v216,v108\nds_read_b128 v[212:215],v218 offset:15360\n" \
"v_exp_f32 v110,v110\nv_add_f32 v216,v216,v109\nv_cvt_pk_bf16_f32 v150,v108,v109\ns_waitcnt lgkmcnt(7)\n" \
"v_mfma_f32_32x32x16_bf16 v[0:15],v[184:187],v[144:147],v[0:15]\nv_exp_f32 v111,v111\nv_add_f32 v216,v216,v110\ns_waitcnt lgkmcnt(5)\n" \
"v_mfma_f32_32x32x16_bf16 v[16:31],v[188:191],v[144:147],v[16:31]\nv_add_f32 v216,v216,v111\nv_cvt_pk_bf16_f32 v151,v110,v111\n" \
"v_mfma_f32_32x32x16_bf16 v[32:47],v[192:195],v[144:147],v[32:47]\ns_waitcnt lgkmcnt(3)\n" \
"v_mfma_f32_32x32x16_bf16 v[48:63],v[196:199],v[144:147],v[48:63]\nv_mfma_f32_32x32x16_bf16 v[0:15],v[200:203],v[148:151],v[0:15]\ns_waitcnt lgkmcnt(1)\n" \
"v_mfma_f32_32x32x16_bf16 v[16:31],v[204:207],v[148:151],v[16:31]\nv_mfma_f32_32x32x16_bf16 v[32:47],v[208:211],v[148:151],v[32:47]\n" \
"s_waitcnt lgkmcnt(0)\nv_mfma_f32_32x32x16_bf16 v[48:63],v[212:215],v[148:151],v[48:63]\ns_waitcnt vmcnt(0) lgkmcnt(0)\ns_nop 7\ns_nop 7\n"

__global__ __launch_bounds__(512, 2) void k12_main(const u16* __restrict__ ckv, const u16* __restrict__ w_q, const u16* __restrict__ w_uk,
                                                   const u16* __restrict__ w_uv, const u16* __restrict__ w_o, const u16* __restrict__ wo_a,
                                                   char* __restrict__ ws, u16* __restrict__ out) {
  extern __shared__ __attribute__((aligned(16))) char smem[];
  const unsigned lds0 = (unsigned)(uintptr_t)smem;
  int wid0 = rfl(threadIdx.x >> 6), bid0 = blockIdx.x;
  f32x16 o0, o1, o2, o3, pk;
  f32x4 pk2;
  u32x8 qa;
  float l, gate, ga;
  {
    const int wid = wid0;
    const int xcd = bid0 & 7, idx = bid0 >> 3, h = xcd * 4 + (idx & 3), m = idx >> 2, pi = m;
    const int wr = wid & 1, wc = wid >> 1;
    const int rb = wid < 4 ? (15 - pi) * 4 + wid : pi * 4 + (7 - wid);
    const int ttop = 31 - 2 * pi;
    const char* KV = ws + WS_KV + (size_t)h * 64 * 16384;
    const unsigned z = (ttop + (m < 4 ? 1 : 3)) & 3;
    auto mk_aa = [&]() {
      const int ln = lane_now(), c16 = ln & 15, g = ln >> 4, q = (ln >> 2) & 3, pp = ln & 3;
      const unsigned pi16 = (c16 & 7) + 16 * (c16 >> 3);
      u32x8 aa;
      aa[0] = lds0 + 65536 + wr * 16384 + pi16 * 128 + ((g ^ ((c16 >> 1) & 7)) * 16); aa[1] = aa[0] ^ 64;
      aa[2] = lds0 + 65536 + 32768 + g * 4096 + wc * 512 + q * 64 + ((2 * (pp >> 1) + (g & 1)) * 16) + 8 * (pp & 1); aa[3] = aa[2] ^ 16;
      aa[4] = (ln >> 3) * 1024 + (((ln & 7) ^ (ln >> 4)) * 16); aa[5] = aa[4] ^ 64;
      aa[6] = q * 8192 + g * 64 + ((pp ^ (wid & 1)) * 16); aa[7] = 0;
      return aa;
    };
    auto mk_mask = [&]() {
      const int ln = lane_now(), r = ln & 31, hi = ln >> 5;
      f32x16 mask;
#pragma unroll
      for (int e = 0; e < 16; ++e) mask[e] = ((e & 3) + 8 * (e >> 2) + 4 * hi > r) ? -INFINITY : 0.f;
      return mask;
    };
    auto mk_mv = [&]() {
      const int ln = lane_now(), c16 = ln & 15, g = ln >> 4, q = (ln >> 2) & 3, pp = ln & 3, hi = ln >> 5;
      const unsigned pi16 = (c16 & 7) + 16 * (c16 >> 3);
      u32x8 mv;
      mv[0] = ln * 16;
      mv[1] = q * 8192 + g * 64 + pp * 16;
      mv[2] = lds0 + hi * 2048 + q * 64 + (g & 1) * 32 + pp * 8;
      mv[3] = (g & 1) * 512 + pi16 * 16 + (wid < 4 ? ((wc & 1) * 4 + (g >> 1)) * 1024 : 8192 + (g >> 1) * 4096 + (wc & 1) * 2048);
      mv[4] = 0;
      mv[5] = lds0 + ln * 16;
      mv[6] = ln * 16 + (wid & 3) * 8192;
      mv[7] = 0;
      return mv;
    };
    const unsigned long long fp = (unsigned long long)(ws + WS_FLAG + h * 256);
    const u32x4 sc0 = {rfl((m * 256 + wid * 32) * 1024), rfl(wid * 65536), rfl(lds0 + 65536 + wid * 4096), rfl(wid * 4 * 32768)};
    const int apw = bid0 * 4 + (wid & 3);
    const u32x4 sc1 = {rfl((unsigned)WS_KA + (32 + 16 * (wid >> 2)) * 3072 + (wid & 3) * 6144), rfl(z), rfl(z + 3), rfl((unsigned)WS_QA + ((apw >> 5) * 64 + 2 * (apw & 31)) * 1024)};
    const unsigned sam0 = rfl(lds0 + wid * 6144);
    const u32x4 sc2 = {rfl(ttop + z), rfl(rb), rfl(lds0 + (wid & 3) * 8192), rfl((unsigned)WS_KV + (unsigned)(h * 64 + m * 8 + wr * 4) * 16384u)};
    const u32x4 sc3 = {rfl((unsigned)WS_XF + rb * 32768), rfl(wid == 4 ? 1u : 0u), rfl((unsigned)fp), rfl((unsigned)(fp >> 32))};
    const __amdgpu_buffer_rsrc_t sx = __builtin_amdgcn_make_buffer_rsrc((void*)ckv, (short)0, S * DM * 2, 0x00020000);
    const __amdgpu_buffer_rsrc_t sk = __builtin_amdgcn_make_buffer_rsrc((void*)((const char*)w_uk + h * 256), (short)0, 4194304 - h * 256, 0x00020000);
    const __amdgpu_buffer_rsrc_t sv = __builtin_amdgcn_make_buffer_rsrc((void*)((const char*)w_uv + h * 256), (short)0, 4194304 - h * 256, 0x00020000);
    const __amdgpu_buffer_rsrc_t sq = __builtin_amdgcn_make_buffer_rsrc((void*)((const char*)w_q + h * 256), (short)0, 4194304 - h * 256, 0x00020000);
    const __amdgpu_buffer_rsrc_t sws = __builtin_amdgcn_make_buffer_rsrc((void*)ws, (short)0, 72 << 20, 0x00020000);
    const __amdgpu_buffer_rsrc_t srd = __builtin_amdgcn_make_buffer_rsrc((void*)KV, (short)0, (72 - h) << 20, 0x00020000);
    const unsigned skac = rfl((unsigned)(WS_KA - WS_KV) - h * 1048576u - 32768u);
    unsigned q7;
    const char* wwo = (const char*)w_o + (bid0 * 4 + (wid & 3)) * 4096;
    const char* wwa = (const char*)wo_a + ((bid0 * 4 + (wid & 3)) & 127) * 4096;
    const unsigned sga = rfl((unsigned)WS_G + (2 * (apw & 31) + (wid >> 2)) * 128);
#define K12_OPERANDS                                                                                                                  \
    : "={v[0:15]}"(o0), "={v[16:31]}"(o1), "={v[32:47]}"(o2), "={v[48:63]}"(o3), "={v216}"(l), "+{v[224:231]}"(aa), "+{v[248:255]}"(mv), "+{v[232:247]}"(pk), "={v222}"(q7), "={v112}"(gate), "={v113}"(ga) \
    : "{s[64:67]}"(sc0), "{s[68:71]}"(sc1), "{s[72:75]}"(sc2), "{s[76:79]}"(sc3), "{s80}"(sam0), "{s81}"(skac), "{s82}"(sga), "{s[84:85]}"(wwo), "{s[86:87]}"(wwa), [sx] "s"(sx), [sk] "s"(sk), [sv] "s"(sv), [sq] "s"(sq), [sws] "s"(sws), [srd] "s"(srd)      \
    : "v64", "v65", "v66", "v67", "v68", "v69", "v70", "v71", "v72", "v73", "v74", "v75", "v76", "v77", "v78", "v79", "v80", "v81", "v82", "v83", "v84", "v85", "v86", "v87", "v88", "v89", "v90", "v91", "v92", "v93", "v94", "v95", "v96", "v97", "v98", "v99", "v100", "v101", "v102", "v103", "v104", "v105", "v106", "v107", "v108", "v109", "v110", "v111", "v114", "v115", "v116", "v117", "v118", "v119", "v120", "v121", "v122", "v123", "v124", "v125", "v126", "v127", "v128", "v129", "v130", "v131", "v132", "v133", "v134", "v135", "v136", "v137", "v138", "v139", "v140", "v141", "v142", "v143", "v144", "v145", "v146", "v147", "v148", "v149", "v150", "v151", "v152", "v153", "v154", "v155", "v156", "v157", "v158", "v159", "v160", "v161", "v162", "v163", "v164", "v165", "v166", "v167", "v168", "v169", "v170", "v171", "v172", "v173", "v174", "v175", "v176", "v177", "v178", "v179", "v180", "v181", "v182", "v183", "v184", "v185", "v186", "v187", "v188", "v189", "v190", "v191", "v192", "v193", "v194", "v195", "v196", "v197", "v198", "v199", "v200", "v201", "v202", "v203", "v204", "v205", "v206", "v207", "v208", "v209", "v210", "v211", "v212", "v213", "v214", "v215", "v217", "v218", "v219", "v220", "v221", "v223", "s40", "s41", "s42", "s43", "s44", "s45", "s46", "s47", "s48", "s49", "s50", "s51", "s52", "s53", "s54", "s55", "s56", "s57", "s58", "s59", "s60", "vcc", "scc", "memory"
#define K12_RUN(PROG) do { u32x8 aa = mk_aa(); pk = mk_mask(); u32x8 mv = mk_mv();                                                   \
      asm volatile(PROG K12_OPERANDS);                                                                                                  \
      pk2 = (f32x4){__uint_as_float(aa[2]), __uint_as_float(aa[3]), __uint_as_float(aa[4]), __uint_as_float(aa[5])};                    \
      qa = (u32x8){aa[6], aa[7], mv[1], mv[2], mv[3], mv[4], mv[7], q7}; } while (0)
    if (m < 4) {
      if (wid < 4) K12_RUN(K12A_LONG); else K12_RUN(K12A_LOADER);
    } else {
      if (wid < 4) K12_RUN(K12B_LONG); else K12_RUN(K12B_LOADER);
    }
  }
  asm volatile("" : "+s"(wid0), "+s"(bid0));
  const int wid = wid0, bid = bid0;
  const int xcd = bid & 7, idx = bid >> 3, h = xcd * 4 + (idx & 3), pi = idx >> 2;
  const int rb = wid < 4 ? (15 - pi) * 4 + wid : pi * 4 + (7 - wid);
  const int ttop = 31 - 2 * pi;
  const char* KV = ws + WS_KV + (size_t)h * 64 * 16384;
  const int lane = lane_now();
  const int r = lane & 31, hi = lane >> 5;
  l = swap_add(l);
  const f32x16 nf = __builtin_elementwise_fma(o0, (f32x16)(0.f), o1 * 0.f) + __builtin_elementwise_fma(o2, (f32x16)(0.f), o3 * 0.f);
  const f32x4 n4 = (nf.lo.lo + nf.lo.hi) + (nf.hi.lo + nf.hi.hi);
  const float n1_ = (n4[0] + n4[1]) + (n4[2] + n4[3]);
  const bool bad = !(l < L_LIMIT) || n1_ != n1_;
  u32x16 ca, cb;
  auto pack_ctx = [&]() {
    const float sc = rbf(1.f - gate) / l;
    const u32x4 c0 = pack8(o0, 0, sc), c1 = pack8(o0, 8, sc), c2 = pack8(o1, 0, sc), c3 = pack8(o1, 8, sc);
    const u32x4 c4 = pack8(o2, 0, sc), c5 = pack8(o2, 8, sc), c6 = pack8(o3, 0, sc), c7 = pack8(o3, 8, sc);
    ca = (u32x16){c0[0], c0[1], c0[2], c0[3], c1[0], c1[1], c1[2], c1[3], c2[0], c2[1], c2[2], c2[3], c3[0], c3[1], c3[2], c3[3]};
    cb = (u32x16){c4[0], c4[1], c4[2], c4[3], c5[0], c5[1], c5[2], c5[3], c6[0], c6[1], c6[2], c6[3], c7[0], c7[1], c7[2], c7[3]};
  };
  pack_ctx();
  if (__any(bad)) {
    __builtin_amdgcn_fence(__ATOMIC_ACQUIRE, "agent");
    bf16x8 q[8];
    const f32x16 z = {};
    for (int db = 0; db < 4; ++db) {
      f32x16 a = z;
      for (int s = 0; s < 32; ++s) {
        bf16x8 wf;
#pragma unroll
        for (int j = 0; j < 8; ++j) wf[j] = (short)w_q[(size_t)(16 * s + 8 * hi + j) * 4096 + h * 128 + db * 32 + r];
        a = MFMA32(wf, *(const bf16x8*)(ws + WS_XF + (size_t)(rb * 32 + s) * 1024 + lane * 16), a);
      }
      const bf16x8 p0 = __builtin_bit_cast(bf16x8, pack8(a, 0, QB_SCALE)), p1 = __builtin_bit_cast(bf16x8, pack8(a, 8, QB_SCALE));
      if (db == 0) { q[0] = p0; q[1] = p1; } else if (db == 1) { q[2] = p0; q[3] = p1; } else if (db == 2) { q[4] = p0; q[5] = p1; } else { q[6] = p0; q[7] = p1; }
    }
    float mx = -INFINITY;
    for (int u = rb; u >= 0; --u) {
      f32x16 s = z;
#pragma unroll
      for (int ks = 0; ks < 8; ++ks) s = MFMA32(*(const bf16x8*)(KV + (size_t)u * 16384 + ks * 1024 + lane * 16), q[ks], s);
      if (u == rb) mask_diag(s, r, hi);
      mx = fmaxf(mx, rowmax16(s));
    }
    o0 = z; o1 = z; o2 = z; o3 = z; l = 0.f;
    for (int u = rb; u >= 0; --u) {
      const char* up = KV + (size_t)u * 16384 + lane * 16;
      f32x16 s = z;
#pragma unroll
      for (int ks = 0; ks < 8; ++ks) s = MFMA32(*(const bf16x8*)(up + ks * 1024), q[ks], s);
      if (u == rb) mask_diag(s, r, hi);
#pragma unroll
      for (int e = 0; e < 16; ++e) { s[e] = __builtin_amdgcn_exp2f(s[e] - mx); l += s[e]; }
      const bf16x8 p0 = __builtin_bit_cast(bf16x8, pack8(s, 0, 1.f)), p1 = __builtin_bit_cast(bf16x8, pack8(s, 8, 1.f));
      o0 = MFMA32(*(const bf16x8*)(up + 8192), p0, o0); o1 = MFMA32(*(const bf16x8*)(up + 9216), p0, o1);
      o2 = MFMA32(*(const bf16x8*)(up + 10240), p0, o2); o3 = MFMA32(*(const bf16x8*)(up + 11264), p0, o3);
      o0 = MFMA32(*(const bf16x8*)(up + 12288), p1, o0); o1 = MFMA32(*(const bf16x8*)(up + 13312), p1, o1);
      o2 = MFMA32(*(const bf16x8*)(up + 14336), p1, o2); o3 = MFMA32(*(const bf16x8*)(up + 15360), p1, o3);
    }
    l = swap_add(l);
    pack_ctx();
  }
  const int grp = wid >> 2, apw = bid * 4 + (wid & 3), ah = apw >> 5, qp = apw & 31;
  const int z = (ttop + (pi < 4 ? 1 : 3)) & 3;
  const int sp = xcd >> 1, mt = (xcd & 1) * 8 + (idx & 7), nt = idx >> 3;
  const __amdgpu_buffer_rsrc_t sws = __builtin_amdgcn_make_buffer_rsrc((void*)ws, (short)0, 72 << 20, 0x00020000);
  const __amdgpu_buffer_rsrc_t sw = __builtin_amdgcn_make_buffer_rsrc((void*)w_o, (short)0, 4194304, 0x00020000);
  const __amdgpu_buffer_rsrc_t swa = __builtin_amdgcn_make_buffer_rsrc((void*)wo_a, (short)0, 524288, 0x00020000);
  const unsigned long long f2 = (unsigned long long)(ws + WS_FLAG + 8192), f3 = (unsigned long long)(ws + WS_FLAG + 12288);
  const unsigned long long f2a = f2 + ((15 - pi) * 4 + h / 9) * 64, f2b = f2 + (pi * 4 + h / 9) * 64, f2p = f2 + (mt * 4 + sp) * 64;
  const unsigned is4 = (unsigned)(wid == 4), is3 = (unsigned)(sp == 3);
#define T0_ (u32x4){rfl((unsigned)WS_C + (rb * 288 + h * 8) * 1024), rfl(z * 32768), rfl((unsigned)WS_C + ((mt * 4 + (wid >> 1)) * 288 + sp * 72 + 2 * (wid & 1)) * 1024), \
                    rfl(sp * 18 * 65536 + nt * 256 + wid * 8192)}
#define T1_ (u32x4){rfl(is4), rfl(9u - 4u * is3), rfl(lds0 + wid * 2048), rfl(grp)}
#define T2_ (u32x4){rfl((unsigned)f2a), rfl((unsigned)(f2a >> 32)), rfl((unsigned)f2b), rfl((unsigned)(f2b >> 32))}
#define T3_ (u32x4){rfl((unsigned)f2p), rfl((unsigned)(f2p >> 32)), rfl((unsigned)f3), rfl((unsigned)(f3 >> 32))}
  u32x8 tv;
  tv[0] = lane * 16; tv[1] = lds0 + lane * 16;
  tv[2] = ((lane >> 2) & 3) * 1024 + (lane >> 4) * 64 + (lane & 3) * 16;
  tv[3] = lds0 + 16384 + (wid >> 1) * 256 + hi * 1024 + ((lane >> 2) & 3) * 64 + ((lane >> 4) & 1) * 32 + (lane & 3) * 8;
  tv[4] = lds0 + (wid & 1) * 8192 + lane * 16;
  tv[5] = 0; tv[6] = 0; tv[7] = 0;
  f32x16 a0 = {}, a1 = {};
  {
#pragma unroll
    for (int e = 0; e < 9; ++e) a0[e] = pk[e];
#pragma unroll
    for (int e = 0; e < 7; ++e) a1[e] = pk[9 + e];
    a1[7] = pk2[0]; a1[8] = pk2[1];
    f32x16 n0, n1;
#pragma unroll
    for (int e = 0; e < 16; ++e) { n0[e] = pk2[2]; n1[e] = pk2[3]; }
    asm volatile(
"s_mov_b32 s43,s65\ns_mov_b32 s44,0x20000\ns_add_u32 s45,s65,0x8000\ns_xor_b32 s46,s65,0x10000\ns_add_u32 s40,s65,0x18000\ns_and_b32 s40,s40,0x1ffff\n"
"v_add_u32 v168,s40,v177\nv_add_u32 v169,s46,v177\nv_add_u32 v170,s45,v177\nv_add_u32 v171,s44,v177\ns_barrier\ns_cmp_lg_u32 s71,0\ns_cbranch_scc1 40f\n"
"v_mov_b32 v100,0\nv_mov_b32 v101,0\nv_mov_b32 v102,0\nv_mov_b32 v103,0\nv_mov_b32 v60,0\nv_mov_b32 v61,0\nv_mov_b32 v62,0\nv_mov_b32 v63,0\n"
"v_mov_b32 v124,0\nv_mov_b32 v125,0\nv_mov_b32 v126,0\nv_mov_b32 v127,0\nds_read_b128 v[104:107],v168 offset:0\n"
"ds_read_b128 v[108:111],v168 offset:1024\nds_read_b128 v[112:115],v168 offset:2048\ns_waitcnt lgkmcnt(0)\n"
"v_mfma_f32_32x32x16_bf16 v[32:47],v[104:107],v[128:131],v[64:79]\ns_nop 7\ns_nop 7\nv_exp_f32 v32,v32\nv_cvt_pk_bf16_f32 v102,v60,v61\n"
"v_exp_f32 v33,v33\nv_exp_f32 v34,v34\nv_cvt_pk_bf16_f32 v103,v62,v63\nv_mfma_f32_32x32x16_bf16 v[48:63],v[104:107],v[132:135],v[80:95]\n"
"v_exp_f32 v35,v35\nv_cvt_pk_bf16_f32 v96,v32,v33\nv_exp_f32 v36,v36\nds_read_b128 v[116:119],v168 offset:3072\nv_exp_f32 v37,v37\n"
"v_cvt_pk_bf16_f32 v97,v34,v35\nv_exp_f32 v38,v38\nv_mfma_f32_32x32x16_bf16 v[16:31],v[124:127],v[100:103],v[16:31]\n"
"buffer_store_dwordx4 v[136:139],v176,%[sws],s64 offen offset:0 sc1\nv_exp_f32 v39,v39\nv_cvt_pk_bf16_f32 v98,v36,v37\nv_exp_f32 v40,v40\n"
"ds_read_b128 v[120:123],v168 offset:4096\nv_exp_f32 v41,v41\nv_cvt_pk_bf16_f32 v99,v38,v39\nv_exp_f32 v42,v42\n"
"ds_read_b128 v[124:127],v168 offset:5120\nv_exp_f32 v43,v43\nv_cvt_pk_bf16_f32 v100,v40,v41\nv_exp_f32 v44,v44\n"
"v_mfma_f32_32x32x16_bf16 v[0:15],v[108:111],v[96:99],v[0:15]\nv_exp_f32 v45,v45\nv_cvt_pk_bf16_f32 v101,v42,v43\nv_exp_f32 v46,v46\nv_exp_f32 v47,v47\n"
"v_exp_f32 v48,v48\nv_cvt_pk_bf16_f32 v102,v44,v45\nv_exp_f32 v49,v49\nv_exp_f32 v50,v50\nv_cvt_pk_bf16_f32 v103,v46,v47\ns_waitcnt lgkmcnt(2)\n"
"v_mfma_f32_32x32x16_bf16 v[32:47],v[116:119],v[128:131],v[64:79]\nv_exp_f32 v51,v51\nv_cvt_pk_bf16_f32 v96,v48,v49\nv_exp_f32 v52,v52\n"
"v_exp_f32 v53,v53\nv_cvt_pk_bf16_f32 v97,v50,v51\nv_exp_f32 v54,v54\nv_mfma_f32_32x32x16_bf16 v[0:15],v[112:115],v[100:103],v[0:15]\nv_exp_f32 v55,v55\n"
"v_cvt_pk_bf16_f32 v98,v52,v53\nv_exp_f32 v56,v56\nv_exp_f32 v57,v57\nv_cvt_pk_bf16_f32 v99,v54,v55\nv_exp_f32 v58,v58\nv_exp_f32 v59,v59\n"
"v_cvt_pk_bf16_f32 v100,v56,v57\nv_exp_f32 v60,v60\nv_mfma_f32_32x32x16_bf16 v[16:31],v[108:111],v[96:99],v[16:31]\n"
"buffer_store_dwordx4 v[140:143],v176,%[sws],s64 offen offset:1024 sc1\nv_exp_f32 v61,v61\nv_cvt_pk_bf16_f32 v101,v58,v59\nv_exp_f32 v62,v62\n"
"v_exp_f32 v63,v63\ns_waitcnt lgkmcnt(0)\nv_exp_f32 v32,v32\nv_cvt_pk_bf16_f32 v102,v60,v61\nv_exp_f32 v33,v33\nv_exp_f32 v34,v34\n"
"v_cvt_pk_bf16_f32 v103,v62,v63\nv_mfma_f32_32x32x16_bf16 v[48:63],v[116:119],v[132:135],v[80:95]\nv_exp_f32 v35,v35\nv_cvt_pk_bf16_f32 v96,v32,v33\n"
"v_exp_f32 v36,v36\nds_read_b128 v[104:107],v168 offset:6144\nv_exp_f32 v37,v37\nv_cvt_pk_bf16_f32 v97,v34,v35\nv_exp_f32 v38,v38\n"
"v_mfma_f32_32x32x16_bf16 v[16:31],v[112:115],v[100:103],v[16:31]\nv_exp_f32 v39,v39\nv_cvt_pk_bf16_f32 v98,v36,v37\nv_exp_f32 v40,v40\n"
"ds_read_b128 v[108:111],v168 offset:7168\nv_exp_f32 v41,v41\nv_cvt_pk_bf16_f32 v99,v38,v39\nv_exp_f32 v42,v42\n"
"ds_read_b128 v[112:115],v168 offset:8192\nv_exp_f32 v43,v43\nv_cvt_pk_bf16_f32 v100,v40,v41\nv_exp_f32 v44,v44\n"
"v_mfma_f32_32x32x16_bf16 v[0:15],v[120:123],v[96:99],v[0:15]\nv_exp_f32 v45,v45\nv_cvt_pk_bf16_f32 v101,v42,v43\nv_exp_f32 v46,v46\nv_exp_f32 v47,v47\n"
"v_exp_f32 v48,v48\nv_cvt_pk_bf16_f32 v102,v44,v45\nv_exp_f32 v49,v49\nv_exp_f32 v50,v50\nv_cvt_pk_bf16_f32 v103,v46,v47\ns_waitcnt lgkmcnt(2)\n"
"v_mfma_f32_32x32x16_bf16 v[32:47],v[104:107],v[128:131],v[64:79]\nv_exp_f32 v51,v51\nv_cvt_pk_bf16_f32 v96,v48,v49\nv_exp_f32 v52,v52\n"
"v_exp_f32 v53,v53\nv_cvt_pk_bf16_f32 v97,v50,v51\nv_exp_f32 v54,v54\nv_mfma_f32_32x32x16_bf16 v[0:15],v[124:127],v[100:103],v[0:15]\n"
"buffer_store_dwordx4 v[144:147],v176,%[sws],s64 offen offset:2048 sc1\nv_exp_f32 v55,v55\nv_cvt_pk_bf16_f32 v98,v52,v53\nv_exp_f32 v56,v56\n"
"v_exp_f32 v57,v57\nv_cvt_pk_bf16_f32 v99,v54,v55\nv_exp_f32 v58,v58\nv_exp_f32 v59,v59\nv_cvt_pk_bf16_f32 v100,v56,v57\nv_exp_f32 v60,v60\n"
"v_mfma_f32_32x32x16_bf16 v[16:31],v[120:123],v[96:99],v[16:31]\nv_exp_f32 v61,v61\nv_cvt_pk_bf16_f32 v101,v58,v59\nv_exp_f32 v62,v62\n"
"v_exp_f32 v63,v63\ns_waitcnt lgkmcnt(0)\nv_exp_f32 v32,v32\nv_cvt_pk_bf16_f32 v102,v60,v61\nv_exp_f32 v33,v33\nv_exp_f32 v34,v34\n"
"v_cvt_pk_bf16_f32 v103,v62,v63\nv_mfma_f32_32x32x16_bf16 v[48:63],v[104:107],v[132:135],v[80:95]\nv_exp_f32 v35,v35\nv_cvt_pk_bf16_f32 v96,v32,v33\n"
"v_exp_f32 v36,v36\nds_read_b128 v[116:119],v168 offset:9216\nv_exp_f32 v37,v37\nv_cvt_pk_bf16_f32 v97,v34,v35\nv_exp_f32 v38,v38\n"
"v_mfma_f32_32x32x16_bf16 v[16:31],v[124:127],v[100:103],v[16:31]\nv_exp_f32 v39,v39\nv_cvt_pk_bf16_f32 v98,v36,v37\nv_exp_f32 v40,v40\n"
"ds_read_b128 v[120:123],v168 offset:10240\nv_exp_f32 v41,v41\nv_cvt_pk_bf16_f32 v99,v38,v39\nv_exp_f32 v42,v42\n"
"ds_read_b128 v[124:127],v168 offset:11264\nv_exp_f32 v43,v43\nv_cvt_pk_bf16_f32 v100,v40,v41\nv_exp_f32 v44,v44\n"
"v_mfma_f32_32x32x16_bf16 v[0:15],v[108:111],v[96:99],v[0:15]\nbuffer_store_dwordx4 v[148:151],v176,%[sws],s64 offen offset:3072 sc1\nv_exp_f32 v45,v45\n"
"v_cvt_pk_bf16_f32 v101,v42,v43\nv_exp_f32 v46,v46\nv_exp_f32 v47,v47\nv_exp_f32 v48,v48\nv_cvt_pk_bf16_f32 v102,v44,v45\nv_exp_f32 v49,v49\n"
"v_exp_f32 v50,v50\nv_cvt_pk_bf16_f32 v103,v46,v47\ns_waitcnt lgkmcnt(2)\nv_mfma_f32_32x32x16_bf16 v[32:47],v[116:119],v[128:131],v[64:79]\n"
"v_exp_f32 v51,v51\nv_cvt_pk_bf16_f32 v96,v48,v49\nv_exp_f32 v52,v52\nv_exp_f32 v53,v53\nv_cvt_pk_bf16_f32 v97,v50,v51\nv_exp_f32 v54,v54\n"
"v_mfma_f32_32x32x16_bf16 v[0:15],v[112:115],v[100:103],v[0:15]\nv_exp_f32 v55,v55\nv_cvt_pk_bf16_f32 v98,v52,v53\nv_exp_f32 v56,v56\nv_exp_f32 v57,v57\n"
"v_cvt_pk_bf16_f32 v99,v54,v55\nv_exp_f32 v58,v58\nv_exp_f32 v59,v59\nv_cvt_pk_bf16_f32 v100,v56,v57\nv_exp_f32 v60,v60\n"
"v_mfma_f32_32x32x16_bf16 v[16:31],v[108:111],v[96:99],v[16:31]\nv_exp_f32 v61,v61\nv_cvt_pk_bf16_f32 v101,v58,v59\nv_exp_f32 v62,v62\n"
"v_exp_f32 v63,v63\ns_waitcnt lgkmcnt(0)\nv_exp_f32 v32,v32\nv_cvt_pk_bf16_f32 v102,v60,v61\nv_exp_f32 v33,v33\nv_exp_f32 v34,v34\n"
"v_cvt_pk_bf16_f32 v103,v62,v63\nv_mfma_f32_32x32x16_bf16 v[48:63],v[116:119],v[132:135],v[80:95]\nv_exp_f32 v35,v35\nv_cvt_pk_bf16_f32 v96,v32,v33\n"
"v_exp_f32 v36,v36\nds_read_b128 v[104:107],v168 offset:12288\nv_exp_f32 v37,v37\nv_cvt_pk_bf16_f32 v97,v34,v35\nv_exp_f32 v38,v38\n"
"v_mfma_f32_32x32x16_bf16 v[16:31],v[112:115],v[100:103],v[16:31]\ns_add_u32 s42,s64,4096\n"
"buffer_store_dwordx4 v[152:155],v176,%[sws],s42 offen offset:0 sc1\nv_exp_f32 v39,v39\nv_cvt_pk_bf16_f32 v98,v36,v37\nv_exp_f32 v40,v40\n"
"ds_read_b128 v[108:111],v168 offset:13312\nv_exp_f32 v41,v41\nv_cvt_pk_bf16_f32 v99,v38,v39\nv_exp_f32 v42,v42\n"
"ds_read_b128 v[112:115],v168 offset:14336\nv_exp_f32 v43,v43\nv_cvt_pk_bf16_f32 v100,v40,v41\nv_exp_f32 v44,v44\n"
"v_mfma_f32_32x32x16_bf16 v[0:15],v[120:123],v[96:99],v[0:15]\nv_exp_f32 v45,v45\nv_cvt_pk_bf16_f32 v101,v42,v43\nv_exp_f32 v46,v46\nv_exp_f32 v47,v47\n"
"v_exp_f32 v48,v48\nv_cvt_pk_bf16_f32 v102,v44,v45\nv_exp_f32 v49,v49\nv_exp_f32 v50,v50\nv_cvt_pk_bf16_f32 v103,v46,v47\ns_waitcnt lgkmcnt(2)\n"
"v_mfma_f32_32x32x16_bf16 v[32:47],v[104:107],v[128:131],v[64:79]\nv_exp_f32 v51,v51\nv_cvt_pk_bf16_f32 v96,v48,v49\nv_exp_f32 v52,v52\n"
"v_exp_f32 v53,v53\nv_cvt_pk_bf16_f32 v97,v50,v51\nv_exp_f32 v54,v54\nv_mfma_f32_32x32x16_bf16 v[0:15],v[124:127],v[100:103],v[0:15]\nv_exp_f32 v55,v55\n"
"v_cvt_pk_bf16_f32 v98,v52,v53\nv_exp_f32 v56,v56\nv_exp_f32 v57,v57\nv_cvt_pk_bf16_f32 v99,v54,v55\nv_exp_f32 v58,v58\nv_exp_f32 v59,v59\n"
"v_cvt_pk_bf16_f32 v100,v56,v57\nv_exp_f32 v60,v60\nv_mfma_f32_32x32x16_bf16 v[16:31],v[120:123],v[96:99],v[16:31]\n"
"buffer_store_dwordx4 v[156:159],v176,%[sws],s42 offen offset:1024 sc1\nv_exp_f32 v61,v61\nv_cvt_pk_bf16_f32 v101,v58,v59\nv_exp_f32 v62,v62\n"
"v_exp_f32 v63,v63\ns_waitcnt lgkmcnt(0)\nv_exp_f32 v32,v32\nv_cvt_pk_bf16_f32 v102,v60,v61\nv_exp_f32 v33,v33\nv_exp_f32 v34,v34\n"
"v_cvt_pk_bf16_f32 v103,v62,v63\nv_mfma_f32_32x32x16_bf16 v[48:63],v[104:107],v[132:135],v[80:95]\nv_exp_f32 v35,v35\nv_cvt_pk_bf16_f32 v96,v32,v33\n"
"v_exp_f32 v36,v36\nds_read_b128 v[116:119],v168 offset:15360\nv_exp_f32 v37,v37\nv_cvt_pk_bf16_f32 v97,v34,v35\nv_exp_f32 v38,v38\n"
"v_mfma_f32_32x32x16_bf16 v[16:31],v[124:127],v[100:103],v[16:31]\nv_exp_f32 v39,v39\nv_cvt_pk_bf16_f32 v98,v36,v37\nv_exp_f32 v40,v40\n"
"ds_read_b128 v[120:123],v168 offset:16384\nv_exp_f32 v41,v41\nv_cvt_pk_bf16_f32 v99,v38,v39\nv_exp_f32 v42,v42\n"
"ds_read_b128 v[124:127],v168 offset:17408\nv_exp_f32 v43,v43\nv_cvt_pk_bf16_f32 v100,v40,v41\nv_exp_f32 v44,v44\n"
"v_mfma_f32_32x32x16_bf16 v[0:15],v[108:111],v[96:99],v[0:15]\nv_exp_f32 v45,v45\nv_cvt_pk_bf16_f32 v101,v42,v43\nv_exp_f32 v46,v46\nv_exp_f32 v47,v47\n"
"v_exp_f32 v48,v48\nv_cvt_pk_bf16_f32 v102,v44,v45\nv_exp_f32 v49,v49\nv_exp_f32 v50,v50\nv_cvt_pk_bf16_f32 v103,v46,v47\ns_waitcnt lgkmcnt(2)\n"
"v_mfma_f32_32x32x16_bf16 v[32:47],v[116:119],v[128:131],v[64:79]\nv_exp_f32 v51,v51\nv_cvt_pk_bf16_f32 v96,v48,v49\nv_exp_f32 v52,v52\n"
"v_exp_f32 v53,v53\nv_cvt_pk_bf16_f32 v97,v50,v51\nv_exp_f32 v54,v54\nv_mfma_f32_32x32x16_bf16 v[0:15],v[112:115],v[100:103],v[0:15]\n"
"buffer_store_dwordx4 v[160:163],v176,%[sws],s42 offen offset:2048 sc1\nv_exp_f32 v55,v55\nv_cvt_pk_bf16_f32 v98,v52,v53\nv_exp_f32 v56,v56\n"
"v_exp_f32 v57,v57\nv_cvt_pk_bf16_f32 v99,v54,v55\nv_exp_f32 v58,v58\nv_exp_f32 v59,v59\nv_cvt_pk_bf16_f32 v100,v56,v57\nv_exp_f32 v60,v60\n"
"v_mfma_f32_32x32x16_bf16 v[16:31],v[108:111],v[96:99],v[16:31]\nv_exp_f32 v61,v61\nv_cvt_pk_bf16_f32 v101,v58,v59\nv_exp_f32 v62,v62\n"
"v_exp_f32 v63,v63\ns_waitcnt lgkmcnt(0)\nv_exp_f32 v32,v32\nv_cvt_pk_bf16_f32 v102,v60,v61\nv_exp_f32 v33,v33\nv_exp_f32 v34,v34\n"
"v_cvt_pk_bf16_f32 v103,v62,v63\nv_mfma_f32_32x32x16_bf16 v[48:63],v[116:119],v[132:135],v[80:95]\nv_exp_f32 v35,v35\nv_cvt_pk_bf16_f32 v96,v32,v33\n"
"v_exp_f32 v36,v36\nds_read_b128 v[104:107],v168 offset:18432\nv_exp_f32 v37,v37\nv_cvt_pk_bf16_f32 v97,v34,v35\nv_exp_f32 v38,v38\n"
"v_mfma_f32_32x32x16_bf16 v[16:31],v[112:115],v[100:103],v[16:31]\nv_exp_f32 v39,v39\nv_cvt_pk_bf16_f32 v98,v36,v37\nv_exp_f32 v40,v40\n"
"ds_read_b128 v[108:111],v168 offset:19456\nv_exp_f32 v41,v41\nv_cvt_pk_bf16_f32 v99,v38,v39\nv_exp_f32 v42,v42\n"
"ds_read_b128 v[112:115],v168 offset:20480\nv_exp_f32 v43,v43\nv_cvt_pk_bf16_f32 v100,v40,v41\nv_exp_f32 v44,v44\n"
"v_mfma_f32_32x32x16_bf16 v[0:15],v[120:123],v[96:99],v[0:15]\nbuffer_store_dwordx4 v[164:167],v176,%[sws],s42 offen offset:3072 sc1\nv_exp_f32 v45,v45\n"
"v_cvt_pk_bf16_f32 v101,v42,v43\nv_exp_f32 v46,v46\nv_exp_f32 v47,v47\nv_exp_f32 v48,v48\nv_cvt_pk_bf16_f32 v102,v44,v45\nv_exp_f32 v49,v49\n"
"v_exp_f32 v50,v50\nv_cvt_pk_bf16_f32 v103,v46,v47\ns_waitcnt lgkmcnt(2)\nv_mfma_f32_32x32x16_bf16 v[32:47],v[104:107],v[128:131],v[64:79]\n"
"v_exp_f32 v51,v51\nv_cvt_pk_bf16_f32 v96,v48,v49\nv_exp_f32 v52,v52\nv_exp_f32 v53,v53\nv_cvt_pk_bf16_f32 v97,v50,v51\nv_exp_f32 v54,v54\n"
"v_mfma_f32_32x32x16_bf16 v[0:15],v[124:127],v[100:103],v[0:15]\nv_exp_f32 v55,v55\nv_cvt_pk_bf16_f32 v98,v52,v53\nv_exp_f32 v56,v56\nv_exp_f32 v57,v57\n"
"v_cvt_pk_bf16_f32 v99,v54,v55\nv_exp_f32 v58,v58\nv_exp_f32 v59,v59\nv_cvt_pk_bf16_f32 v100,v56,v57\nv_exp_f32 v60,v60\n"
"v_mfma_f32_32x32x16_bf16 v[16:31],v[120:123],v[96:99],v[16:31]\nv_exp_f32 v61,v61\nv_cvt_pk_bf16_f32 v101,v58,v59\nv_exp_f32 v62,v62\n"
"v_exp_f32 v63,v63\ns_waitcnt lgkmcnt(0)\nv_exp_f32 v32,v32\nv_cvt_pk_bf16_f32 v102,v60,v61\nv_exp_f32 v33,v33\nv_exp_f32 v34,v34\n"
"v_cvt_pk_bf16_f32 v103,v62,v63\nv_mfma_f32_32x32x16_bf16 v[48:63],v[104:107],v[132:135],v[80:95]\nv_exp_f32 v35,v35\nv_cvt_pk_bf16_f32 v96,v32,v33\n"
"v_exp_f32 v36,v36\nds_read_b128 v[116:119],v168 offset:21504\nv_exp_f32 v37,v37\nv_cvt_pk_bf16_f32 v97,v34,v35\nv_exp_f32 v38,v38\n"
"v_mfma_f32_32x32x16_bf16 v[16:31],v[124:127],v[100:103],v[16:31]\nv_exp_f32 v39,v39\nv_cvt_pk_bf16_f32 v98,v36,v37\nv_exp_f32 v40,v40\n"
"ds_read_b128 v[120:123],v168 offset:22528\nv_exp_f32 v41,v41\nv_cvt_pk_bf16_f32 v99,v38,v39\nv_exp_f32 v42,v42\n"
"ds_read_b128 v[124:127],v168 offset:23552\nv_exp_f32 v43,v43\nv_cvt_pk_bf16_f32 v100,v40,v41\nv_exp_f32 v44,v44\n"
"v_mfma_f32_32x32x16_bf16 v[0:15],v[108:111],v[96:99],v[0:15]\nv_exp_f32 v45,v45\nv_cvt_pk_bf16_f32 v101,v42,v43\nv_exp_f32 v46,v46\nv_exp_f32 v47,v47\n"
"v_exp_f32 v48,v48\nv_cvt_pk_bf16_f32 v102,v44,v45\nv_exp_f32 v49,v49\nv_exp_f32 v50,v50\nv_cvt_pk_bf16_f32 v103,v46,v47\ns_waitcnt lgkmcnt(2)\n"
"v_mfma_f32_32x32x16_bf16 v[32:47],v[116:119],v[128:131],v[64:79]\nv_exp_f32 v51,v51\nv_cvt_pk_bf16_f32 v96,v48,v49\nv_exp_f32 v52,v52\n"
"v_exp_f32 v53,v53\nv_cvt_pk_bf16_f32 v97,v50,v51\nv_exp_f32 v54,v54\nv_mfma_f32_32x32x16_bf16 v[0:15],v[112:115],v[100:103],v[0:15]\nv_exp_f32 v55,v55\n"
"v_cvt_pk_bf16_f32 v98,v52,v53\nv_exp_f32 v56,v56\nv_exp_f32 v57,v57\nv_cvt_pk_bf16_f32 v99,v54,v55\nv_exp_f32 v58,v58\nv_exp_f32 v59,v59\n"
"v_cvt_pk_bf16_f32 v100,v56,v57\nv_exp_f32 v60,v60\nv_mfma_f32_32x32x16_bf16 v[16:31],v[108:111],v[96:99],v[16:31]\nv_exp_f32 v61,v61\n"
"v_cvt_pk_bf16_f32 v101,v58,v59\nv_exp_f32 v62,v62\nv_exp_f32 v63,v63\ns_waitcnt lgkmcnt(0)\nv_exp_f32 v32,v32\nv_cvt_pk_bf16_f32 v102,v60,v61\n"
"v_exp_f32 v33,v33\nv_exp_f32 v34,v34\nv_cvt_pk_bf16_f32 v103,v62,v63\nv_mfma_f32_32x32x16_bf16 v[48:63],v[116:119],v[132:135],v[80:95]\n"
"v_exp_f32 v35,v35\nv_cvt_pk_bf16_f32 v96,v32,v33\nv_exp_f32 v36,v36\nds_read_b128 v[104:107],v168 offset:24576\nv_exp_f32 v37,v37\n"
"v_cvt_pk_bf16_f32 v97,v34,v35\nv_exp_f32 v38,v38\nv_mfma_f32_32x32x16_bf16 v[16:31],v[112:115],v[100:103],v[16:31]\nv_exp_f32 v39,v39\n"
"v_cvt_pk_bf16_f32 v98,v36,v37\nv_exp_f32 v40,v40\nds_read_b128 v[108:111],v168 offset:25600\nv_exp_f32 v41,v41\nv_cvt_pk_bf16_f32 v99,v38,v39\n"
"v_exp_f32 v42,v42\nds_read_b128 v[112:115],v168 offset:26624\nv_exp_f32 v43,v43\nv_cvt_pk_bf16_f32 v100,v40,v41\nv_exp_f32 v44,v44\n"
"v_mfma_f32_32x32x16_bf16 v[0:15],v[120:123],v[96:99],v[0:15]\nv_exp_f32 v45,v45\nv_cvt_pk_bf16_f32 v101,v42,v43\nv_exp_f32 v46,v46\nv_exp_f32 v47,v47\n"
"v_exp_f32 v48,v48\nv_cvt_pk_bf16_f32 v102,v44,v45\nv_exp_f32 v49,v49\nv_exp_f32 v50,v50\nv_cvt_pk_bf16_f32 v103,v46,v47\ns_waitcnt lgkmcnt(2)\n"
"v_mfma_f32_32x32x16_bf16 v[32:47],v[104:107],v[128:131],v[64:79]\ns_waitcnt vmcnt(0)\ns_barrier\nv_exp_f32 v51,v51\nv_cvt_pk_bf16_f32 v96,v48,v49\n"
"v_exp_f32 v52,v52\nv_exp_f32 v53,v53\nv_cvt_pk_bf16_f32 v97,v50,v51\nv_exp_f32 v54,v54\nv_mfma_f32_32x32x16_bf16 v[0:15],v[124:127],v[100:103],v[0:15]\n"
"v_exp_f32 v55,v55\nv_cvt_pk_bf16_f32 v98,v52,v53\nv_exp_f32 v56,v56\nv_exp_f32 v57,v57\nv_cvt_pk_bf16_f32 v99,v54,v55\nv_exp_f32 v58,v58\n"
"v_exp_f32 v59,v59\nv_cvt_pk_bf16_f32 v100,v56,v57\nv_exp_f32 v60,v60\nv_mfma_f32_32x32x16_bf16 v[16:31],v[120:123],v[96:99],v[16:31]\n"
"v_exp_f32 v61,v61\nv_cvt_pk_bf16_f32 v101,v58,v59\nv_exp_f32 v62,v62\nv_exp_f32 v63,v63\ns_waitcnt lgkmcnt(0)\nv_exp_f32 v32,v32\n"
"v_cvt_pk_bf16_f32 v102,v60,v61\nv_exp_f32 v33,v33\nv_exp_f32 v34,v34\nv_cvt_pk_bf16_f32 v103,v62,v63\n"
"v_mfma_f32_32x32x16_bf16 v[48:63],v[104:107],v[132:135],v[80:95]\nv_exp_f32 v35,v35\nv_cvt_pk_bf16_f32 v96,v32,v33\nv_exp_f32 v36,v36\n"
"ds_read_b128 v[116:119],v168 offset:27648\nv_exp_f32 v37,v37\nv_cvt_pk_bf16_f32 v97,v34,v35\nv_exp_f32 v38,v38\n"
"v_mfma_f32_32x32x16_bf16 v[16:31],v[124:127],v[100:103],v[16:31]\nv_exp_f32 v39,v39\nv_cvt_pk_bf16_f32 v98,v36,v37\nv_exp_f32 v40,v40\n"
"ds_read_b128 v[120:123],v168 offset:28672\nv_exp_f32 v41,v41\nv_cvt_pk_bf16_f32 v99,v38,v39\nv_exp_f32 v42,v42\n"
"ds_read_b128 v[124:127],v168 offset:29696\nv_exp_f32 v43,v43\nv_cvt_pk_bf16_f32 v100,v40,v41\nv_exp_f32 v44,v44\n"
"v_mfma_f32_32x32x16_bf16 v[0:15],v[108:111],v[96:99],v[0:15]\nv_exp_f32 v45,v45\nv_cvt_pk_bf16_f32 v101,v42,v43\nv_exp_f32 v46,v46\nv_exp_f32 v47,v47\n"
"v_exp_f32 v48,v48\nv_cvt_pk_bf16_f32 v102,v44,v45\nv_exp_f32 v49,v49\nv_exp_f32 v50,v50\nv_cvt_pk_bf16_f32 v103,v46,v47\ns_waitcnt lgkmcnt(2)\n"
"v_mfma_f32_32x32x16_bf16 v[32:47],v[116:119],v[128:131],v[64:79]\nv_exp_f32 v51,v51\nv_cvt_pk_bf16_f32 v96,v48,v49\nv_exp_f32 v52,v52\n"
"v_exp_f32 v53,v53\nv_cvt_pk_bf16_f32 v97,v50,v51\nv_exp_f32 v54,v54\nv_mfma_f32_32x32x16_bf16 v[0:15],v[112:115],v[100:103],v[0:15]\n"
"s_add_u32 s40,s70,s43\ns_add_u32 m0,s40,16384\ns_add_u32 s41,s67,0\nbuffer_load_dwordx4 v178,%[sw],s41 offen offset:0 lds\nv_exp_f32 v55,v55\n"
"v_cvt_pk_bf16_f32 v98,v52,v53\nv_exp_f32 v56,v56\nv_exp_f32 v57,v57\nv_cvt_pk_bf16_f32 v99,v54,v55\nv_exp_f32 v58,v58\ns_add_u32 s41,s67,3072\n"
"buffer_load_dwordx4 v178,%[sw],s41 offen offset:1024 lds\nv_exp_f32 v59,v59\nv_cvt_pk_bf16_f32 v100,v56,v57\nv_exp_f32 v60,v60\n"
"v_mfma_f32_32x32x16_bf16 v[16:31],v[108:111],v[96:99],v[16:31]\ns_add_u32 s40,s70,s44\ns_add_u32 m0,s40,16384\ns_add_u32 s41,s67,65536\n"
"buffer_load_dwordx4 v178,%[sw],s41 offen offset:0 lds\nv_exp_f32 v61,v61\nv_cvt_pk_bf16_f32 v101,v58,v59\nv_exp_f32 v62,v62\nv_exp_f32 v63,v63\n"
"s_waitcnt lgkmcnt(0)\nv_exp_f32 v32,v32\nv_cvt_pk_bf16_f32 v102,v60,v61\nv_exp_f32 v33,v33\nv_exp_f32 v34,v34\nv_cvt_pk_bf16_f32 v103,v62,v63\n"
"v_mfma_f32_32x32x16_bf16 v[48:63],v[116:119],v[132:135],v[80:95]\ns_add_u32 s41,s67,68608\nbuffer_load_dwordx4 v178,%[sw],s41 offen offset:1024 lds\n"
"v_exp_f32 v35,v35\nv_cvt_pk_bf16_f32 v96,v32,v33\nv_exp_f32 v36,v36\nds_read_b128 v[104:107],v168 offset:30720\nv_exp_f32 v37,v37\n"
"v_cvt_pk_bf16_f32 v97,v34,v35\nv_exp_f32 v38,v38\nv_mfma_f32_32x32x16_bf16 v[16:31],v[112:115],v[100:103],v[16:31]\nv_exp_f32 v39,v39\n"
"v_cvt_pk_bf16_f32 v98,v36,v37\nv_exp_f32 v40,v40\nds_read_b128 v[108:111],v168 offset:31744\nv_exp_f32 v41,v41\nv_cvt_pk_bf16_f32 v99,v38,v39\n"
"v_exp_f32 v42,v42\nds_read_b128 v[112:115],v169 offset:0\nv_exp_f32 v43,v43\nv_cvt_pk_bf16_f32 v100,v40,v41\nv_exp_f32 v44,v44\n"
"v_mfma_f32_32x32x16_bf16 v[0:15],v[120:123],v[96:99],v[0:15]\nv_exp_f32 v45,v45\nv_cvt_pk_bf16_f32 v101,v42,v43\nv_exp_f32 v46,v46\nv_exp_f32 v47,v47\n"
"v_exp_f32 v48,v48\nv_cvt_pk_bf16_f32 v102,v44,v45\nv_exp_f32 v49,v49\nv_exp_f32 v50,v50\nv_cvt_pk_bf16_f32 v103,v46,v47\ns_waitcnt lgkmcnt(2)\n"
"v_mfma_f32_32x32x16_bf16 v[32:47],v[104:107],v[128:131],v[64:79]\nv_exp_f32 v51,v51\nv_cvt_pk_bf16_f32 v96,v48,v49\nv_exp_f32 v52,v52\n"
"v_exp_f32 v53,v53\nv_cvt_pk_bf16_f32 v97,v50,v51\nv_exp_f32 v54,v54\nv_mfma_f32_32x32x16_bf16 v[0:15],v[124:127],v[100:103],v[0:15]\nv_exp_f32 v55,v55\n"
"v_cvt_pk_bf16_f32 v98,v52,v53\nv_exp_f32 v56,v56\nv_exp_f32 v57,v57\nv_cvt_pk_bf16_f32 v99,v54,v55\nv_exp_f32 v58,v58\nv_exp_f32 v59,v59\n"
"v_cvt_pk_bf16_f32 v100,v56,v57\nv_exp_f32 v60,v60\nv_mfma_f32_32x32x16_bf16 v[16:31],v[120:123],v[96:99],v[16:31]\nv_exp_f32 v61,v61\n"
"v_cvt_pk_bf16_f32 v101,v58,v59\nv_exp_f32 v62,v62\nv_exp_f32 v63,v63\ns_waitcnt lgkmcnt(0)\nv_exp_f32 v32,v32\nv_cvt_pk_bf16_f32 v102,v60,v61\n"
"v_exp_f32 v33,v33\nv_exp_f32 v34,v34\nv_cvt_pk_bf16_f32 v103,v62,v63\nv_mfma_f32_32x32x16_bf16 v[48:63],v[104:107],v[132:135],v[80:95]\n"
"v_exp_f32 v35,v35\nv_cvt_pk_bf16_f32 v96,v32,v33\nv_exp_f32 v36,v36\nds_read_b128 v[116:119],v169 offset:1024\nv_exp_f32 v37,v37\n"
"v_cvt_pk_bf16_f32 v97,v34,v35\nv_exp_f32 v38,v38\nv_mfma_f32_32x32x16_bf16 v[16:31],v[124:127],v[100:103],v[16:31]\nv_exp_f32 v39,v39\n"
"v_cvt_pk_bf16_f32 v98,v36,v37\nv_exp_f32 v40,v40\nds_read_b128 v[120:123],v169 offset:2048\nv_exp_f32 v41,v41\nv_cvt_pk_bf16_f32 v99,v38,v39\n"
"v_exp_f32 v42,v42\nds_read_b128 v[124:127],v169 offset:3072\nv_exp_f32 v43,v43\nv_cvt_pk_bf16_f32 v100,v40,v41\nv_exp_f32 v44,v44\n"
"v_mfma_f32_32x32x16_bf16 v[0:15],v[108:111],v[96:99],v[0:15]\nv_exp_f32 v45,v45\nv_cvt_pk_bf16_f32 v101,v42,v43\nv_exp_f32 v46,v46\nv_exp_f32 v47,v47\n"
"v_exp_f32 v48,v48\nv_cvt_pk_bf16_f32 v102,v44,v45\nv_exp_f32 v49,v49\nv_exp_f32 v50,v50\nv_cvt_pk_bf16_f32 v103,v46,v47\ns_waitcnt lgkmcnt(2)\n"
"v_mfma_f32_32x32x16_bf16 v[32:47],v[116:119],v[128:131],v[64:79]\nv_exp_f32 v51,v51\nv_cvt_pk_bf16_f32 v96,v48,v49\nv_exp_f32 v52,v52\n"
"v_exp_f32 v53,v53\nv_cvt_pk_bf16_f32 v97,v50,v51\nv_exp_f32 v54,v54\nv_mfma_f32_32x32x16_bf16 v[0:15],v[112:115],v[100:103],v[0:15]\nv_exp_f32 v55,v55\n"
"v_cvt_pk_bf16_f32 v98,v52,v53\nv_exp_f32 v56,v56\nv_exp_f32 v57,v57\nv_cvt_pk_bf16_f32 v99,v54,v55\nv_exp_f32 v58,v58\nv_exp_f32 v59,v59\n"
"v_cvt_pk_bf16_f32 v100,v56,v57\nv_exp_f32 v60,v60\nv_mfma_f32_32x32x16_bf16 v[16:31],v[108:111],v[96:99],v[16:31]\nv_exp_f32 v61,v61\n"
"v_cvt_pk_bf16_f32 v101,v58,v59\nv_exp_f32 v62,v62\nv_exp_f32 v63,v63\ns_waitcnt lgkmcnt(0)\nv_exp_f32 v32,v32\nv_cvt_pk_bf16_f32 v102,v60,v61\n"
"v_exp_f32 v33,v33\nv_exp_f32 v34,v34\nv_cvt_pk_bf16_f32 v103,v62,v63\nv_mfma_f32_32x32x16_bf16 v[48:63],v[116:119],v[132:135],v[80:95]\n"
"v_exp_f32 v35,v35\nv_cvt_pk_bf16_f32 v96,v32,v33\nv_exp_f32 v36,v36\nds_read_b128 v[104:107],v169 offset:4096\nv_exp_f32 v37,v37\n"
"v_cvt_pk_bf16_f32 v97,v34,v35\nv_exp_f32 v38,v38\nv_mfma_f32_32x32x16_bf16 v[16:31],v[112:115],v[100:103],v[16:31]\nv_exp_f32 v39,v39\n"
"v_cvt_pk_bf16_f32 v98,v36,v37\nv_exp_f32 v40,v40\nds_read_b128 v[108:111],v169 offset:5120\nv_exp_f32 v41,v41\nv_cvt_pk_bf16_f32 v99,v38,v39\n"
"v_exp_f32 v42,v42\nds_read_b128 v[112:115],v169 offset:6144\nv_exp_f32 v43,v43\nv_cvt_pk_bf16_f32 v100,v40,v41\nv_exp_f32 v44,v44\n"
"v_mfma_f32_32x32x16_bf16 v[0:15],v[120:123],v[96:99],v[0:15]\nv_exp_f32 v45,v45\nv_cvt_pk_bf16_f32 v101,v42,v43\nv_exp_f32 v46,v46\nv_exp_f32 v47,v47\n"
"v_exp_f32 v48,v48\nv_cvt_pk_bf16_f32 v102,v44,v45\nv_exp_f32 v49,v49\nv_exp_f32 v50,v50\nv_cvt_pk_bf16_f32 v103,v46,v47\ns_waitcnt lgkmcnt(2)\n"
"v_mfma_f32_32x32x16_bf16 v[32:47],v[104:107],v[128:131],v[64:79]\nv_exp_f32 v51,v51\nv_cvt_pk_bf16_f32 v96,v48,v49\nv_exp_f32 v52,v52\n"
"v_exp_f32 v53,v53\nv_cvt_pk_bf16_f32 v97,v50,v51\nv_exp_f32 v54,v54\nv_mfma_f32_32x32x16_bf16 v[0:15],v[124:127],v[100:103],v[0:15]\nv_exp_f32 v55,v55\n"
"v_cvt_pk_bf16_f32 v98,v52,v53\nv_exp_f32 v56,v56\nv_exp_f32 v57,v57\nv_cvt_pk_bf16_f32 v99,v54,v55\nv_exp_f32 v58,v58\nv_exp_f32 v59,v59\n"
"v_cvt_pk_bf16_f32 v100,v56,v57\nv_exp_f32 v60,v60\nv_mfma_f32_32x32x16_bf16 v[16:31],v[120:123],v[96:99],v[16:31]\nv_exp_f32 v61,v61\n"
"v_cvt_pk_bf16_f32 v101,v58,v59\nv_exp_f32 v62,v62\nv_exp_f32 v63,v63\ns_waitcnt lgkmcnt(0)\nv_exp_f32 v32,v32\nv_cvt_pk_bf16_f32 v102,v60,v61\n"
"v_exp_f32 v33,v33\nv_exp_f32 v34,v34\nv_cvt_pk_bf16_f32 v103,v62,v63\nv_mfma_f32_32x32x16_bf16 v[48:63],v[104:107],v[132:135],v[80:95]\n"
"v_exp_f32 v35,v35\nv_cvt_pk_bf16_f32 v96,v32,v33\nv_exp_f32 v36,v36\nds_read_b128 v[116:119],v169 offset:7168\nv_exp_f32 v37,v37\n"
"v_cvt_pk_bf16_f32 v97,v34,v35\nv_exp_f32 v38,v38\nv_mfma_f32_32x32x16_bf16 v[16:31],v[124:127],v[100:103],v[16:31]\nv_exp_f32 v39,v39\n"
"v_cvt_pk_bf16_f32 v98,v36,v37\nv_exp_f32 v40,v40\nds_read_b128 v[120:123],v169 offset:8192\nv_exp_f32 v41,v41\nv_cvt_pk_bf16_f32 v99,v38,v39\n"
"v_exp_f32 v42,v42\nds_read_b128 v[124:127],v169 offset:9216\nv_exp_f32 v43,v43\nv_cvt_pk_bf16_f32 v100,v40,v41\nv_exp_f32 v44,v44\n"
"v_mfma_f32_32x32x16_bf16 v[0:15],v[108:111],v[96:99],v[0:15]\nv_exp_f32 v45,v45\nv_cvt_pk_bf16_f32 v101,v42,v43\nv_exp_f32 v46,v46\nv_exp_f32 v47,v47\n"
"v_exp_f32 v48,v48\nv_cvt_pk_bf16_f32 v102,v44,v45\nv_exp_f32 v49,v49\nv_exp_f32 v50,v50\nv_cvt_pk_bf16_f32 v103,v46,v47\ns_waitcnt lgkmcnt(2)\n"
"v_mfma_f32_32x32x16_bf16 v[32:47],v[116:119],v[128:131],v[64:79]\nv_exp_f32 v51,v51\nv_cvt_pk_bf16_f32 v96,v48,v49\nv_exp_f32 v52,v52\n"
"v_exp_f32 v53,v53\nv_cvt_pk_bf16_f32 v97,v50,v51\nv_exp_f32 v54,v54\nv_mfma_f32_32x32x16_bf16 v[0:15],v[112:115],v[100:103],v[0:15]\nv_exp_f32 v55,v55\n"
"v_cvt_pk_bf16_f32 v98,v52,v53\nv_exp_f32 v56,v56\nv_exp_f32 v57,v57\nv_cvt_pk_bf16_f32 v99,v54,v55\nv_exp_f32 v58,v58\nv_exp_f32 v59,v59\n"
"v_cvt_pk_bf16_f32 v100,v56,v57\nv_exp_f32 v60,v60\nv_mfma_f32_32x32x16_bf16 v[16:31],v[108:111],v[96:99],v[16:31]\nv_exp_f32 v61,v61\n"
"v_cvt_pk_bf16_f32 v101,v58,v59\nv_exp_f32 v62,v62\nv_exp_f32 v63,v63\ns_waitcnt lgkmcnt(0)\nv_exp_f32 v32,v32\nv_cvt_pk_bf16_f32 v102,v60,v61\n"
"v_exp_f32 v33,v33\nv_exp_f32 v34,v34\nv_cvt_pk_bf16_f32 v103,v62,v63\nv_mfma_f32_32x32x16_bf16 v[48:63],v[116:119],v[132:135],v[80:95]\n"
"v_exp_f32 v35,v35\nv_cvt_pk_bf16_f32 v96,v32,v33\nv_exp_f32 v36,v36\nds_read_b128 v[104:107],v169 offset:10240\nv_exp_f32 v37,v37\n"
"v_cvt_pk_bf16_f32 v97,v34,v35\nv_exp_f32 v38,v38\nv_mfma_f32_32x32x16_bf16 v[16:31],v[112:115],v[100:103],v[16:31]\nv_exp_f32 v39,v39\n"
"v_cvt_pk_bf16_f32 v98,v36,v37\nv_exp_f32 v40,v40\nds_read_b128 v[108:111],v169 offset:11264\nv_exp_f32 v41,v41\nv_cvt_pk_bf16_f32 v99,v38,v39\n"
"v_exp_f32 v42,v42\nds_read_b128 v[112:115],v169 offset:12288\nv_exp_f32 v43,v43\nv_cvt_pk_bf16_f32 v100,v40,v41\nv_exp_f32 v44,v44\n"
"v_mfma_f32_32x32x16_bf16 v[0:15],v[120:123],v[96:99],v[0:15]\nv_exp_f32 v45,v45\nv_cvt_pk_bf16_f32 v101,v42,v43\nv_exp_f32 v46,v46\nv_exp_f32 v47,v47\n"
"v_exp_f32 v48,v48\nv_cvt_pk_bf16_f32 v102,v44,v45\nv_exp_f32 v49,v49\nv_exp_f32 v50,v50\nv_cvt_pk_bf16_f32 v103,v46,v47\ns_waitcnt lgkmcnt(2)\n"
"v_mfma_f32_32x32x16_bf16 v[32:47],v[104:107],v[128:131],v[64:79]\nv_exp_f32 v51,v51\nv_cvt_pk_bf16_f32 v96,v48,v49\nv_exp_f32 v52,v52\n"
"v_exp_f32 v53,v53\nv_cvt_pk_bf16_f32 v97,v50,v51\nv_exp_f32 v54,v54\nv_mfma_f32_32x32x16_bf16 v[0:15],v[124:127],v[100:103],v[0:15]\nv_exp_f32 v55,v55\n"
"v_cvt_pk_bf16_f32 v98,v52,v53\nv_exp_f32 v56,v56\nv_exp_f32 v57,v57\nv_cvt_pk_bf16_f32 v99,v54,v55\nv_exp_f32 v58,v58\nv_exp_f32 v59,v59\n"
"v_cvt_pk_bf16_f32 v100,v56,v57\nv_exp_f32 v60,v60\nv_mfma_f32_32x32x16_bf16 v[16:31],v[120:123],v[96:99],v[16:31]\nv_exp_f32 v61,v61\n"
"v_cvt_pk_bf16_f32 v101,v58,v59\nv_exp_f32 v62,v62\nv_exp_f32 v63,v63\ns_waitcnt lgkmcnt(0)\nv_exp_f32 v32,v32\nv_cvt_pk_bf16_f32 v102,v60,v61\n"
"v_exp_f32 v33,v33\nv_exp_f32 v34,v34\nv_cvt_pk_bf16_f32 v103,v62,v63\nv_mfma_f32_32x32x16_bf16 v[48:63],v[104:107],v[132:135],v[80:95]\n"
"v_exp_f32 v35,v35\nv_cvt_pk_bf16_f32 v96,v32,v33\nv_exp_f32 v36,v36\nds_read_b128 v[116:119],v169 offset:13312\nv_exp_f32 v37,v37\n"
"v_cvt_pk_bf16_f32 v97,v34,v35\nv_exp_f32 v38,v38\nv_mfma_f32_32x32x16_bf16 v[16:31],v[124:127],v[100:103],v[16:31]\nv_mov_b32 v174,0\n"
"global_load_dword v175,v174,s[76:77] sc1\nv_exp_f32 v39,v39\nv_cvt_pk_bf16_f32 v98,v36,v37\nv_exp_f32 v40,v40\n"
"ds_read_b128 v[120:123],v169 offset:14336\nv_exp_f32 v41,v41\nv_cvt_pk_bf16_f32 v99,v38,v39\nv_exp_f32 v42,v42\n"
"ds_read_b128 v[124:127],v169 offset:15360\nv_exp_f32 v43,v43\nv_cvt_pk_bf16_f32 v100,v40,v41\nv_exp_f32 v44,v44\n"
"v_mfma_f32_32x32x16_bf16 v[0:15],v[108:111],v[96:99],v[0:15]\nv_exp_f32 v45,v45\nv_cvt_pk_bf16_f32 v101,v42,v43\nv_exp_f32 v46,v46\nv_exp_f32 v47,v47\n"
"v_exp_f32 v48,v48\nv_cvt_pk_bf16_f32 v102,v44,v45\nv_exp_f32 v49,v49\nv_exp_f32 v50,v50\nv_cvt_pk_bf16_f32 v103,v46,v47\ns_waitcnt lgkmcnt(2)\n"
"v_mfma_f32_32x32x16_bf16 v[32:47],v[116:119],v[128:131],v[64:79]\nv_exp_f32 v51,v51\nv_cvt_pk_bf16_f32 v96,v48,v49\nv_exp_f32 v52,v52\n"
"v_exp_f32 v53,v53\nv_cvt_pk_bf16_f32 v97,v50,v51\nv_exp_f32 v54,v54\nv_mfma_f32_32x32x16_bf16 v[0:15],v[112:115],v[100:103],v[0:15]\nv_exp_f32 v55,v55\n"
"v_cvt_pk_bf16_f32 v98,v52,v53\nv_exp_f32 v56,v56\nv_exp_f32 v57,v57\nv_cvt_pk_bf16_f32 v99,v54,v55\nv_exp_f32 v58,v58\nv_exp_f32 v59,v59\n"
"v_cvt_pk_bf16_f32 v100,v56,v57\nv_exp_f32 v60,v60\nv_mfma_f32_32x32x16_bf16 v[16:31],v[108:111],v[96:99],v[16:31]\nv_exp_f32 v61,v61\n"
"v_cvt_pk_bf16_f32 v101,v58,v59\nv_exp_f32 v62,v62\nv_exp_f32 v63,v63\ns_waitcnt lgkmcnt(0)\nv_exp_f32 v32,v32\nv_cvt_pk_bf16_f32 v102,v60,v61\n"
"v_exp_f32 v33,v33\nv_exp_f32 v34,v34\nv_cvt_pk_bf16_f32 v103,v62,v63\nv_mfma_f32_32x32x16_bf16 v[48:63],v[116:119],v[132:135],v[80:95]\n"
"s_waitcnt vmcnt(0)\nv_readfirstlane_b32 s41,v175\ns_nop 0\ns_cmp_ge_u32 s41,s69\ns_cbranch_scc1 312f\nv_mov_b32 v172,0\ns_mov_b32 s42,0x400000\n311:\n"
"global_load_dword v173,v172,s[76:77] sc1\ns_waitcnt vmcnt(0)\nv_readfirstlane_b32 s41,v173\ns_sub_u32 s42,s42,1\ns_cbranch_scc1 312f\n"
"s_cmp_ge_u32 s41,s69\ns_cbranch_scc1 312f\ns_sleep 8\ns_branch 311b\n312:\ns_add_u32 m0,s70,s43\ns_add_u32 s41,s66,0\n"
"buffer_load_dwordx4 v176,%[sws],s41 offen offset:0 sc1 lds\nv_exp_f32 v35,v35\nv_cvt_pk_bf16_f32 v96,v32,v33\nv_exp_f32 v36,v36\nv_exp_f32 v37,v37\n"
"v_cvt_pk_bf16_f32 v97,v34,v35\nv_exp_f32 v38,v38\nv_mfma_f32_32x32x16_bf16 v[16:31],v[112:115],v[100:103],v[16:31]\n"
"buffer_load_dwordx4 v176,%[sws],s41 offen offset:1024 sc1 lds\nv_exp_f32 v39,v39\nv_cvt_pk_bf16_f32 v98,v36,v37\nv_exp_f32 v40,v40\nv_exp_f32 v41,v41\n"
"v_cvt_pk_bf16_f32 v99,v38,v39\nv_exp_f32 v42,v42\ns_add_u32 m0,s70,s44\ns_add_u32 s41,s66,4096\n"
"buffer_load_dwordx4 v176,%[sws],s41 offen offset:0 sc1 lds\nv_exp_f32 v43,v43\nv_cvt_pk_bf16_f32 v100,v40,v41\nv_exp_f32 v44,v44\n"
"v_mfma_f32_32x32x16_bf16 v[0:15],v[120:123],v[96:99],v[0:15]\nbuffer_load_dwordx4 v176,%[sws],s41 offen offset:1024 sc1 lds\nv_exp_f32 v45,v45\n"
"v_cvt_pk_bf16_f32 v101,v42,v43\nv_exp_f32 v46,v46\nv_exp_f32 v47,v47\nv_exp_f32 v48,v48\nv_cvt_pk_bf16_f32 v102,v44,v45\nv_exp_f32 v49,v49\n"
"v_exp_f32 v50,v50\nv_cvt_pk_bf16_f32 v103,v46,v47\nv_exp_f32 v51,v51\nv_cvt_pk_bf16_f32 v96,v48,v49\nv_exp_f32 v52,v52\nv_exp_f32 v53,v53\n"
"v_cvt_pk_bf16_f32 v97,v50,v51\nv_exp_f32 v54,v54\nv_mfma_f32_32x32x16_bf16 v[0:15],v[124:127],v[100:103],v[0:15]\nv_exp_f32 v55,v55\n"
"v_cvt_pk_bf16_f32 v98,v52,v53\nv_exp_f32 v56,v56\nv_exp_f32 v57,v57\nv_cvt_pk_bf16_f32 v99,v54,v55\nv_exp_f32 v58,v58\nv_exp_f32 v59,v59\n"
"v_cvt_pk_bf16_f32 v100,v56,v57\nv_exp_f32 v60,v60\nv_mfma_f32_32x32x16_bf16 v[16:31],v[120:123],v[96:99],v[16:31]\nv_exp_f32 v61,v61\n"
"v_cvt_pk_bf16_f32 v101,v58,v59\nv_exp_f32 v62,v62\nv_exp_f32 v63,v63\ns_nop 1\nv_cvt_pk_bf16_f32 v102,v60,v61\nv_cvt_pk_bf16_f32 v103,v62,v63\ns_nop 1\n"
"v_mfma_f32_32x32x16_bf16 v[16:31],v[124:127],v[100:103],v[16:31]\ns_waitcnt lgkmcnt(0)\ns_nop 7\ns_nop 7\ns_branch 41f\n40:\nv_mov_b32 v100,0\n"
"v_mov_b32 v101,0\nv_mov_b32 v102,0\nv_mov_b32 v103,0\nv_mov_b32 v60,0\nv_mov_b32 v61,0\nv_mov_b32 v62,0\nv_mov_b32 v63,0\nv_mov_b32 v124,0\n"
"v_mov_b32 v125,0\nv_mov_b32 v126,0\nv_mov_b32 v127,0\nds_read_b128 v[104:107],v169 offset:16384\nds_read_b128 v[108:111],v169 offset:17408\n"
"ds_read_b128 v[112:115],v169 offset:18432\ns_waitcnt lgkmcnt(0)\nv_mfma_f32_32x32x16_bf16 v[32:47],v[104:107],v[128:131],v[64:79]\ns_nop 7\ns_nop 7\n"
"v_exp_f32 v32,v32\nv_cvt_pk_bf16_f32 v102,v60,v61\nv_exp_f32 v33,v33\nv_exp_f32 v34,v34\nv_cvt_pk_bf16_f32 v103,v62,v63\n"
"v_mfma_f32_32x32x16_bf16 v[48:63],v[104:107],v[132:135],v[80:95]\ns_setprio 1\nv_exp_f32 v35,v35\nv_cvt_pk_bf16_f32 v96,v32,v33\nv_exp_f32 v36,v36\n"
"ds_read_b128 v[116:119],v169 offset:19456\nv_exp_f32 v37,v37\nv_cvt_pk_bf16_f32 v97,v34,v35\nv_exp_f32 v38,v38\n"
"v_mfma_f32_32x32x16_bf16 v[16:31],v[124:127],v[100:103],v[16:31]\nbuffer_store_dwordx4 v[136:139],v176,%[sws],s64 offen offset:0 sc1\n"
"v_exp_f32 v39,v39\nv_cvt_pk_bf16_f32 v98,v36,v37\nv_exp_f32 v40,v40\nds_read_b128 v[120:123],v169 offset:20480\nv_exp_f32 v41,v41\n"
"v_cvt_pk_bf16_f32 v99,v38,v39\nv_exp_f32 v42,v42\nds_read_b128 v[124:127],v169 offset:21504\nv_exp_f32 v43,v43\nv_cvt_pk_bf16_f32 v100,v40,v41\n"
"v_exp_f32 v44,v44\nv_mfma_f32_32x32x16_bf16 v[0:15],v[108:111],v[96:99],v[0:15]\nv_exp_f32 v45,v45\nv_cvt_pk_bf16_f32 v101,v42,v43\nv_exp_f32 v46,v46\n"
"v_exp_f32 v47,v47\nv_exp_f32 v48,v48\nv_cvt_pk_bf16_f32 v102,v44,v45\nv_exp_f32 v49,v49\nv_exp_f32 v50,v50\nv_cvt_pk_bf16_f32 v103,v46,v47\n"
"s_waitcnt lgkmcnt(2)\nv_mfma_f32_32x32x16_bf16 v[32:47],v[116:119],v[128:131],v[64:79]\nv_exp_f32 v51,v51\nv_cvt_pk_bf16_f32 v96,v48,v49\n"
"v_exp_f32 v52,v52\nv_exp_f32 v53,v53\nv_cvt_pk_bf16_f32 v97,v50,v51\nv_exp_f32 v54,v54\nv_mfma_f32_32x32x16_bf16 v[0:15],v[112:115],v[100:103],v[0:15]\n"
"v_exp_f32 v55,v55\nv_cvt_pk_bf16_f32 v98,v52,v53\nv_exp_f32 v56,v56\nv_exp_f32 v57,v57\nv_cvt_pk_bf16_f32 v99,v54,v55\nv_exp_f32 v58,v58\n"
"v_exp_f32 v59,v59\nv_cvt_pk_bf16_f32 v100,v56,v57\nv_exp_f32 v60,v60\nv_mfma_f32_32x32x16_bf16 v[16:31],v[108:111],v[96:99],v[16:31]\n"
"buffer_store_dwordx4 v[140:143],v176,%[sws],s64 offen offset:1024 sc1\nv_exp_f32 v61,v61\nv_cvt_pk_bf16_f32 v101,v58,v59\nv_exp_f32 v62,v62\n"
"v_exp_f32 v63,v63\ns_waitcnt lgkmcnt(0)\nv_exp_f32 v32,v32\nv_cvt_pk_bf16_f32 v102,v60,v61\nv_exp_f32 v33,v33\nv_exp_f32 v34,v34\n"
"v_cvt_pk_bf16_f32 v103,v62,v63\nv_mfma_f32_32x32x16_bf16 v[48:63],v[116:119],v[132:135],v[80:95]\nv_exp_f32 v35,v35\nv_cvt_pk_bf16_f32 v96,v32,v33\n"
"v_exp_f32 v36,v36\nds_read_b128 v[104:107],v169 offset:22528\nv_exp_f32 v37,v37\nv_cvt_pk_bf16_f32 v97,v34,v35\nv_exp_f32 v38,v38\n"
"v_mfma_f32_32x32x16_bf16 v[16:31],v[112:115],v[100:103],v[16:31]\nv_exp_f32 v39,v39\nv_cvt_pk_bf16_f32 v98,v36,v37\nv_exp_f32 v40,v40\n"
"ds_read_b128 v[108:111],v169 offset:23552\nv_exp_f32 v41,v41\nv_cvt_pk_bf16_f32 v99,v38,v39\nv_exp_f32 v42,v42\n"
"ds_read_b128 v[112:115],v169 offset:24576\nv_exp_f32 v43,v43\nv_cvt_pk_bf16_f32 v100,v40,v41\nv_exp_f32 v44,v44\n"
"v_mfma_f32_32x32x16_bf16 v[0:15],v[120:123],v[96:99],v[0:15]\nv_exp_f32 v45,v45\nv_cvt_pk_bf16_f32 v101,v42,v43\nv_exp_f32 v46,v46\nv_exp_f32 v47,v47\n"
"v_exp_f32 v48,v48\nv_cvt_pk_bf16_f32 v102,v44,v45\nv_exp_f32 v49,v49\nv_exp_f32 v50,v50\nv_cvt_pk_bf16_f32 v103,v46,v47\ns_waitcnt lgkmcnt(2)\n"
"v_mfma_f32_32x32x16_bf16 v[32:47],v[104:107],v[128:131],v[64:79]\nv_exp_f32 v51,v51\nv_cvt_pk_bf16_f32 v96,v48,v49\nv_exp_f32 v52,v52\n"
"v_exp_f32 v53,v53\nv_cvt_pk_bf16_f32 v97,v50,v51\nv_exp_f32 v54,v54\nv_mfma_f32_32x32x16_bf16 v[0:15],v[124:127],v[100:103],v[0:15]\n"
"buffer_store_dwordx4 v[144:147],v176,%[sws],s64 offen offset:2048 sc1\nv_exp_f32 v55,v55\nv_cvt_pk_bf16_f32 v98,v52,v53\nv_exp_f32 v56,v56\n"
"v_exp_f32 v57,v57\nv_cvt_pk_bf16_f32 v99,v54,v55\nv_exp_f32 v58,v58\nv_exp_f32 v59,v59\nv_cvt_pk_bf16_f32 v100,v56,v57\nv_exp_f32 v60,v60\n"
"v_mfma_f32_32x32x16_bf16 v[16:31],v[120:123],v[96:99],v[16:31]\nv_exp_f32 v61,v61\nv_cvt_pk_bf16_f32 v101,v58,v59\nv_exp_f32 v62,v62\n"
"v_exp_f32 v63,v63\ns_waitcnt lgkmcnt(0)\nv_exp_f32 v32,v32\nv_cvt_pk_bf16_f32 v102,v60,v61\nv_exp_f32 v33,v33\nv_exp_f32 v34,v34\n"
"v_cvt_pk_bf16_f32 v103,v62,v63\nv_mfma_f32_32x32x16_bf16 v[48:63],v[104:107],v[132:135],v[80:95]\nv_exp_f32 v35,v35\nv_cvt_pk_bf16_f32 v96,v32,v33\n"
"v_exp_f32 v36,v36\nds_read_b128 v[116:119],v169 offset:25600\nv_exp_f32 v37,v37\nv_cvt_pk_bf16_f32 v97,v34,v35\nv_exp_f32 v38,v38\n"
"v_mfma_f32_32x32x16_bf16 v[16:31],v[124:127],v[100:103],v[16:31]\nv_exp_f32 v39,v39\nv_cvt_pk_bf16_f32 v98,v36,v37\nv_exp_f32 v40,v40\n"
"ds_read_b128 v[120:123],v169 offset:26624\nv_exp_f32 v41,v41\nv_cvt_pk_bf16_f32 v99,v38,v39\nv_exp_f32 v42,v42\n"
"ds_read_b128 v[124:127],v169 offset:27648\nv_exp_f32 v43,v43\nv_cvt_pk_bf16_f32 v100,v40,v41\nv_exp_f32 v44,v44\n"
"v_mfma_f32_32x32x16_bf16 v[0:15],v[108:111],v[96:99],v[0:15]\nbuffer_store_dwordx4 v[148:151],v176,%[sws],s64 offen offset:3072 sc1\nv_exp_f32 v45,v45\n"
"v_cvt_pk_bf16_f32 v101,v42,v43\nv_exp_f32 v46,v46\nv_exp_f32 v47,v47\nv_exp_f32 v48,v48\nv_cvt_pk_bf16_f32 v102,v44,v45\nv_exp_f32 v49,v49\n"
"v_exp_f32 v50,v50\nv_cvt_pk_bf16_f32 v103,v46,v47\ns_waitcnt lgkmcnt(2)\nv_mfma_f32_32x32x16_bf16 v[32:47],v[116:119],v[128:131],v[64:79]\n"
"v_exp_f32 v51,v51\nv_cvt_pk_bf16_f32 v96,v48,v49\nv_exp_f32 v52,v52\nv_exp_f32 v53,v53\nv_cvt_pk_bf16_f32 v97,v50,v51\nv_exp_f32 v54,v54\n"
"v_mfma_f32_32x32x16_bf16 v[0:15],v[112:115],v[100:103],v[0:15]\nv_exp_f32 v55,v55\nv_cvt_pk_bf16_f32 v98,v52,v53\nv_exp_f32 v56,v56\nv_exp_f32 v57,v57\n"
"v_cvt_pk_bf16_f32 v99,v54,v55\nv_exp_f32 v58,v58\nv_exp_f32 v59,v59\nv_cvt_pk_bf16_f32 v100,v56,v57\nv_exp_f32 v60,v60\n"
"v_mfma_f32_32x32x16_bf16 v[16:31],v[108:111],v[96:99],v[16:31]\nv_exp_f32 v61,v61\nv_cvt_pk_bf16_f32 v101,v58,v59\nv_exp_f32 v62,v62\n"
"v_exp_f32 v63,v63\ns_waitcnt lgkmcnt(0)\nv_exp_f32 v32,v32\nv_cvt_pk_bf16_f32 v102,v60,v61\nv_exp_f32 v33,v33\nv_exp_f32 v34,v34\n"
"v_cvt_pk_bf16_f32 v103,v62,v63\nv_mfma_f32_32x32x16_bf16 v[48:63],v[116:119],v[132:135],v[80:95]\nv_exp_f32 v35,v35\nv_cvt_pk_bf16_f32 v96,v32,v33\n"
"v_exp_f32 v36,v36\nds_read_b128 v[104:107],v169 offset:28672\nv_exp_f32 v37,v37\nv_cvt_pk_bf16_f32 v97,v34,v35\nv_exp_f32 v38,v38\n"
"v_mfma_f32_32x32x16_bf16 v[16:31],v[112:115],v[100:103],v[16:31]\ns_add_u32 s42,s64,4096\n"
"buffer_store_dwordx4 v[152:155],v176,%[sws],s42 offen offset:0 sc1\nv_exp_f32 v39,v39\nv_cvt_pk_bf16_f32 v98,v36,v37\nv_exp_f32 v40,v40\n"
"ds_read_b128 v[108:111],v169 offset:29696\nv_exp_f32 v41,v41\nv_cvt_pk_bf16_f32 v99,v38,v39\nv_exp_f32 v42,v42\n"
"ds_read_b128 v[112:115],v169 offset:30720\nv_exp_f32 v43,v43\nv_cvt_pk_bf16_f32 v100,v40,v41\nv_exp_f32 v44,v44\n"
"v_mfma_f32_32x32x16_bf16 v[0:15],v[120:123],v[96:99],v[0:15]\nv_exp_f32 v45,v45\nv_cvt_pk_bf16_f32 v101,v42,v43\nv_exp_f32 v46,v46\nv_exp_f32 v47,v47\n"
"v_exp_f32 v48,v48\nv_cvt_pk_bf16_f32 v102,v44,v45\nv_exp_f32 v49,v49\nv_exp_f32 v50,v50\nv_cvt_pk_bf16_f32 v103,v46,v47\ns_waitcnt lgkmcnt(2)\n"
"v_mfma_f32_32x32x16_bf16 v[32:47],v[104:107],v[128:131],v[64:79]\nv_exp_f32 v51,v51\nv_cvt_pk_bf16_f32 v96,v48,v49\nv_exp_f32 v52,v52\n"
"v_exp_f32 v53,v53\nv_cvt_pk_bf16_f32 v97,v50,v51\nv_exp_f32 v54,v54\nv_mfma_f32_32x32x16_bf16 v[0:15],v[124:127],v[100:103],v[0:15]\nv_exp_f32 v55,v55\n"
"v_cvt_pk_bf16_f32 v98,v52,v53\nv_exp_f32 v56,v56\nv_exp_f32 v57,v57\nv_cvt_pk_bf16_f32 v99,v54,v55\nv_exp_f32 v58,v58\nv_exp_f32 v59,v59\n"
"v_cvt_pk_bf16_f32 v100,v56,v57\nv_exp_f32 v60,v60\nv_mfma_f32_32x32x16_bf16 v[16:31],v[120:123],v[96:99],v[16:31]\n"
"buffer_store_dwordx4 v[156:159],v176,%[sws],s42 offen offset:1024 sc1\nv_exp_f32 v61,v61\nv_cvt_pk_bf16_f32 v101,v58,v59\nv_exp_f32 v62,v62\n"
"v_exp_f32 v63,v63\ns_waitcnt lgkmcnt(0)\nv_exp_f32 v32,v32\nv_cvt_pk_bf16_f32 v102,v60,v61\nv_exp_f32 v33,v33\nv_exp_f32 v34,v34\n"
"v_cvt_pk_bf16_f32 v103,v62,v63\nv_mfma_f32_32x32x16_bf16 v[48:63],v[104:107],v[132:135],v[80:95]\nv_exp_f32 v35,v35\nv_cvt_pk_bf16_f32 v96,v32,v33\n"
"v_exp_f32 v36,v36\nds_read_b128 v[116:119],v169 offset:31744\nv_exp_f32 v37,v37\nv_cvt_pk_bf16_f32 v97,v34,v35\nv_exp_f32 v38,v38\n"
"v_mfma_f32_32x32x16_bf16 v[16:31],v[124:127],v[100:103],v[16:31]\nv_exp_f32 v39,v39\nv_cvt_pk_bf16_f32 v98,v36,v37\nv_exp_f32 v40,v40\n"
"ds_read_b128 v[120:123],v170 offset:0\nv_exp_f32 v41,v41\nv_cvt_pk_bf16_f32 v99,v38,v39\nv_exp_f32 v42,v42\nds_read_b128 v[124:127],v170 offset:1024\n"
"v_exp_f32 v43,v43\nv_cvt_pk_bf16_f32 v100,v40,v41\nv_exp_f32 v44,v44\nv_mfma_f32_32x32x16_bf16 v[0:15],v[108:111],v[96:99],v[0:15]\nv_exp_f32 v45,v45\n"
"v_cvt_pk_bf16_f32 v101,v42,v43\nv_exp_f32 v46,v46\nv_exp_f32 v47,v47\nv_exp_f32 v48,v48\nv_cvt_pk_bf16_f32 v102,v44,v45\nv_exp_f32 v49,v49\n"
"v_exp_f32 v50,v50\nv_cvt_pk_bf16_f32 v103,v46,v47\ns_waitcnt lgkmcnt(2)\nv_mfma_f32_32x32x16_bf16 v[32:47],v[116:119],v[128:131],v[64:79]\n"
"v_exp_f32 v51,v51\nv_cvt_pk_bf16_f32 v96,v48,v49\nv_exp_f32 v52,v52\nv_exp_f32 v53,v53\nv_cvt_pk_bf16_f32 v97,v50,v51\nv_exp_f32 v54,v54\n"
"v_mfma_f32_32x32x16_bf16 v[0:15],v[112:115],v[100:103],v[0:15]\nbuffer_store_dwordx4 v[160:163],v176,%[sws],s42 offen offset:2048 sc1\n"
"v_exp_f32 v55,v55\nv_cvt_pk_bf16_f32 v98,v52,v53\nv_exp_f32 v56,v56\nv_exp_f32 v57,v57\nv_cvt_pk_bf16_f32 v99,v54,v55\nv_exp_f32 v58,v58\n"
"v_exp_f32 v59,v59\nv_cvt_pk_bf16_f32 v100,v56,v57\nv_exp_f32 v60,v60\nv_mfma_f32_32x32x16_bf16 v[16:31],v[108:111],v[96:99],v[16:31]\n"
"v_exp_f32 v61,v61\nv_cvt_pk_bf16_f32 v101,v58,v59\nv_exp_f32 v62,v62\nv_exp_f32 v63,v63\ns_waitcnt lgkmcnt(0)\nv_exp_f32 v32,v32\n"
"v_cvt_pk_bf16_f32 v102,v60,v61\nv_exp_f32 v33,v33\nv_exp_f32 v34,v34\nv_cvt_pk_bf16_f32 v103,v62,v63\n"
"v_mfma_f32_32x32x16_bf16 v[48:63],v[116:119],v[132:135],v[80:95]\nv_exp_f32 v35,v35\nv_cvt_pk_bf16_f32 v96,v32,v33\nv_exp_f32 v36,v36\n"
"ds_read_b128 v[104:107],v170 offset:2048\nv_exp_f32 v37,v37\nv_cvt_pk_bf16_f32 v97,v34,v35\nv_exp_f32 v38,v38\n"
"v_mfma_f32_32x32x16_bf16 v[16:31],v[112:115],v[100:103],v[16:31]\nv_exp_f32 v39,v39\nv_cvt_pk_bf16_f32 v98,v36,v37\nv_exp_f32 v40,v40\n"
"ds_read_b128 v[108:111],v170 offset:3072\nv_exp_f32 v41,v41\nv_cvt_pk_bf16_f32 v99,v38,v39\nv_exp_f32 v42,v42\n"
"ds_read_b128 v[112:115],v170 offset:4096\nv_exp_f32 v43,v43\nv_cvt_pk_bf16_f32 v100,v40,v41\nv_exp_f32 v44,v44\n"
"v_mfma_f32_32x32x16_bf16 v[0:15],v[120:123],v[96:99],v[0:15]\nbuffer_store_dwordx4 v[164:167],v176,%[sws],s42 offen offset:3072 sc1\nv_exp_f32 v45,v45\n"
"v_cvt_pk_bf16_f32 v101,v42,v43\nv_exp_f32 v46,v46\nv_exp_f32 v47,v47\nv_exp_f32 v48,v48\nv_cvt_pk_bf16_f32 v102,v44,v45\nv_exp_f32 v49,v49\n"
"v_exp_f32 v50,v50\nv_cvt_pk_bf16_f32 v103,v46,v47\ns_waitcnt lgkmcnt(2)\nv_mfma_f32_32x32x16_bf16 v[32:47],v[104:107],v[128:131],v[64:79]\n"
"v_exp_f32 v51,v51\nv_cvt_pk_bf16_f32 v96,v48,v49\nv_exp_f32 v52,v52\nv_exp_f32 v53,v53\nv_cvt_pk_bf16_f32 v97,v50,v51\nv_exp_f32 v54,v54\n"
"v_mfma_f32_32x32x16_bf16 v[0:15],v[124:127],v[100:103],v[0:15]\nv_exp_f32 v55,v55\nv_cvt_pk_bf16_f32 v98,v52,v53\nv_exp_f32 v56,v56\nv_exp_f32 v57,v57\n"
"v_cvt_pk_bf16_f32 v99,v54,v55\nv_exp_f32 v58,v58\nv_exp_f32 v59,v59\nv_cvt_pk_bf16_f32 v100,v56,v57\nv_exp_f32 v60,v60\n"
"v_mfma_f32_32x32x16_bf16 v[16:31],v[120:123],v[96:99],v[16:31]\nv_exp_f32 v61,v61\nv_cvt_pk_bf16_f32 v101,v58,v59\nv_exp_f32 v62,v62\n"
"v_exp_f32 v63,v63\ns_waitcnt lgkmcnt(0)\nv_exp_f32 v32,v32\nv_cvt_pk_bf16_f32 v102,v60,v61\nv_exp_f32 v33,v33\nv_exp_f32 v34,v34\n"
"v_cvt_pk_bf16_f32 v103,v62,v63\nv_mfma_f32_32x32x16_bf16 v[48:63],v[104:107],v[132:135],v[80:95]\ns_setprio 0\nv_exp_f32 v35,v35\n"
"v_cvt_pk_bf16_f32 v96,v32,v33\nv_exp_f32 v36,v36\nds_read_b128 v[116:119],v170 offset:5120\nv_exp_f32 v37,v37\nv_cvt_pk_bf16_f32 v97,v34,v35\n"
"v_exp_f32 v38,v38\nv_mfma_f32_32x32x16_bf16 v[16:31],v[124:127],v[100:103],v[16:31]\nv_exp_f32 v39,v39\nv_cvt_pk_bf16_f32 v98,v36,v37\n"
"v_exp_f32 v40,v40\nds_read_b128 v[120:123],v170 offset:6144\nv_exp_f32 v41,v41\nv_cvt_pk_bf16_f32 v99,v38,v39\nv_exp_f32 v42,v42\n"
"ds_read_b128 v[124:127],v170 offset:7168\nv_exp_f32 v43,v43\nv_cvt_pk_bf16_f32 v100,v40,v41\nv_exp_f32 v44,v44\n"
"v_mfma_f32_32x32x16_bf16 v[0:15],v[108:111],v[96:99],v[0:15]\nv_exp_f32 v45,v45\nv_cvt_pk_bf16_f32 v101,v42,v43\nv_exp_f32 v46,v46\nv_exp_f32 v47,v47\n"
"v_exp_f32 v48,v48\nv_cvt_pk_bf16_f32 v102,v44,v45\nv_exp_f32 v49,v49\nv_exp_f32 v50,v50\nv_cvt_pk_bf16_f32 v103,v46,v47\ns_waitcnt lgkmcnt(2)\n"
"v_mfma_f32_32x32x16_bf16 v[32:47],v[116:119],v[128:131],v[64:79]\nv_exp_f32 v51,v51\nv_cvt_pk_bf16_f32 v96,v48,v49\nv_exp_f32 v52,v52\n"
"v_exp_f32 v53,v53\nv_cvt_pk_bf16_f32 v97,v50,v51\nv_exp_f32 v54,v54\nv_mfma_f32_32x32x16_bf16 v[0:15],v[112:115],v[100:103],v[0:15]\nv_exp_f32 v55,v55\n"
"v_cvt_pk_bf16_f32 v98,v52,v53\nv_exp_f32 v56,v56\nv_exp_f32 v57,v57\nv_cvt_pk_bf16_f32 v99,v54,v55\nv_exp_f32 v58,v58\nv_exp_f32 v59,v59\n"
"v_cvt_pk_bf16_f32 v100,v56,v57\nv_exp_f32 v60,v60\nv_mfma_f32_32x32x16_bf16 v[16:31],v[108:111],v[96:99],v[16:31]\nv_exp_f32 v61,v61\n"
"v_cvt_pk_bf16_f32 v101,v58,v59\nv_exp_f32 v62,v62\nv_exp_f32 v63,v63\ns_waitcnt lgkmcnt(0)\nv_exp_f32 v32,v32\nv_cvt_pk_bf16_f32 v102,v60,v61\n"
"v_exp_f32 v33,v33\nv_exp_f32 v34,v34\nv_cvt_pk_bf16_f32 v103,v62,v63\nv_mfma_f32_32x32x16_bf16 v[48:63],v[116:119],v[132:135],v[80:95]\n"
"v_exp_f32 v35,v35\nv_cvt_pk_bf16_f32 v96,v32,v33\nv_exp_f32 v36,v36\nds_read_b128 v[104:107],v170 offset:8192\nv_exp_f32 v37,v37\n"
"v_cvt_pk_bf16_f32 v97,v34,v35\nv_exp_f32 v38,v38\nv_mfma_f32_32x32x16_bf16 v[16:31],v[112:115],v[100:103],v[16:31]\nv_exp_f32 v39,v39\n"
"v_cvt_pk_bf16_f32 v98,v36,v37\nv_exp_f32 v40,v40\nds_read_b128 v[108:111],v170 offset:9216\nv_exp_f32 v41,v41\nv_cvt_pk_bf16_f32 v99,v38,v39\n"
"v_exp_f32 v42,v42\nds_read_b128 v[112:115],v170 offset:10240\nv_exp_f32 v43,v43\nv_cvt_pk_bf16_f32 v100,v40,v41\nv_exp_f32 v44,v44\n"
"v_mfma_f32_32x32x16_bf16 v[0:15],v[120:123],v[96:99],v[0:15]\nv_exp_f32 v45,v45\nv_cvt_pk_bf16_f32 v101,v42,v43\nv_exp_f32 v46,v46\nv_exp_f32 v47,v47\n"
"v_exp_f32 v48,v48\nv_cvt_pk_bf16_f32 v102,v44,v45\nv_exp_f32 v49,v49\nv_exp_f32 v50,v50\nv_cvt_pk_bf16_f32 v103,v46,v47\ns_waitcnt lgkmcnt(2)\n"
"v_mfma_f32_32x32x16_bf16 v[32:47],v[104:107],v[128:131],v[64:79]\ns_waitcnt vmcnt(0)\ns_barrier\ns_cmp_eq_u32 s68,0\ns_cbranch_scc1 30f\n"
"s_mov_b64 exec,1\nv_mov_b32 v172,0\nv_mov_b32 v173,1\nglobal_atomic_add v172,v173,s[72:73]\nglobal_atomic_add v172,v173,s[74:75]\ns_mov_b64 exec,-1\n"
"30:\ns_setprio 1\nv_exp_f32 v51,v51\nv_cvt_pk_bf16_f32 v96,v48,v49\nv_exp_f32 v52,v52\nv_exp_f32 v53,v53\nv_cvt_pk_bf16_f32 v97,v50,v51\n"
"v_exp_f32 v54,v54\nv_mfma_f32_32x32x16_bf16 v[0:15],v[124:127],v[100:103],v[0:15]\nv_exp_f32 v55,v55\nv_cvt_pk_bf16_f32 v98,v52,v53\nv_exp_f32 v56,v56\n"
"v_exp_f32 v57,v57\nv_cvt_pk_bf16_f32 v99,v54,v55\nv_exp_f32 v58,v58\nv_exp_f32 v59,v59\nv_cvt_pk_bf16_f32 v100,v56,v57\nv_exp_f32 v60,v60\n"
"v_mfma_f32_32x32x16_bf16 v[16:31],v[120:123],v[96:99],v[16:31]\nv_exp_f32 v61,v61\nv_cvt_pk_bf16_f32 v101,v58,v59\nv_exp_f32 v62,v62\n"
"v_exp_f32 v63,v63\ns_waitcnt lgkmcnt(0)\nv_exp_f32 v32,v32\nv_cvt_pk_bf16_f32 v102,v60,v61\nv_exp_f32 v33,v33\nv_exp_f32 v34,v34\n"
"v_cvt_pk_bf16_f32 v103,v62,v63\nv_mfma_f32_32x32x16_bf16 v[48:63],v[104:107],v[132:135],v[80:95]\nv_exp_f32 v35,v35\nv_cvt_pk_bf16_f32 v96,v32,v33\n"
"v_exp_f32 v36,v36\nds_read_b128 v[116:119],v170 offset:11264\nv_exp_f32 v37,v37\nv_cvt_pk_bf16_f32 v97,v34,v35\nv_exp_f32 v38,v38\n"
"v_mfma_f32_32x32x16_bf16 v[16:31],v[124:127],v[100:103],v[16:31]\nv_exp_f32 v39,v39\nv_cvt_pk_bf16_f32 v98,v36,v37\nv_exp_f32 v40,v40\n"
"ds_read_b128 v[120:123],v170 offset:12288\nv_exp_f32 v41,v41\nv_cvt_pk_bf16_f32 v99,v38,v39\nv_exp_f32 v42,v42\n"
"ds_read_b128 v[124:127],v170 offset:13312\nv_exp_f32 v43,v43\nv_cvt_pk_bf16_f32 v100,v40,v41\nv_exp_f32 v44,v44\n"
"v_mfma_f32_32x32x16_bf16 v[0:15],v[108:111],v[96:99],v[0:15]\nv_exp_f32 v45,v45\nv_cvt_pk_bf16_f32 v101,v42,v43\nv_exp_f32 v46,v46\nv_exp_f32 v47,v47\n"
"v_exp_f32 v48,v48\nv_cvt_pk_bf16_f32 v102,v44,v45\nv_exp_f32 v49,v49\nv_exp_f32 v50,v50\nv_cvt_pk_bf16_f32 v103,v46,v47\ns_waitcnt lgkmcnt(2)\n"
"v_mfma_f32_32x32x16_bf16 v[32:47],v[116:119],v[128:131],v[64:79]\nv_exp_f32 v51,v51\nv_cvt_pk_bf16_f32 v96,v48,v49\nv_exp_f32 v52,v52\n"
"v_exp_f32 v53,v53\nv_cvt_pk_bf16_f32 v97,v50,v51\nv_exp_f32 v54,v54\nv_mfma_f32_32x32x16_bf16 v[0:15],v[112:115],v[100:103],v[0:15]\n"
"s_add_u32 s40,s70,s43\ns_add_u32 m0,s40,16384\ns_add_u32 s41,s67,0\nbuffer_load_dwordx4 v178,%[sw],s41 offen offset:0 lds\nv_exp_f32 v55,v55\n"
"v_cvt_pk_bf16_f32 v98,v52,v53\nv_exp_f32 v56,v56\nv_exp_f32 v57,v57\nv_cvt_pk_bf16_f32 v99,v54,v55\nv_exp_f32 v58,v58\ns_add_u32 s41,s67,3072\n"
"buffer_load_dwordx4 v178,%[sw],s41 offen offset:1024 lds\nv_exp_f32 v59,v59\nv_cvt_pk_bf16_f32 v100,v56,v57\nv_exp_f32 v60,v60\n"
"v_mfma_f32_32x32x16_bf16 v[16:31],v[108:111],v[96:99],v[16:31]\ns_add_u32 s40,s70,s44\ns_add_u32 m0,s40,16384\ns_add_u32 s41,s67,65536\n"
"buffer_load_dwordx4 v178,%[sw],s41 offen offset:0 lds\nv_exp_f32 v61,v61\nv_cvt_pk_bf16_f32 v101,v58,v59\nv_exp_f32 v62,v62\nv_exp_f32 v63,v63\n"
"s_waitcnt lgkmcnt(0)\nv_exp_f32 v32,v32\nv_cvt_pk_bf16_f32 v102,v60,v61\nv_exp_f32 v33,v33\nv_exp_f32 v34,v34\nv_cvt_pk_bf16_f32 v103,v62,v63\n"
"v_mfma_f32_32x32x16_bf16 v[48:63],v[116:119],v[132:135],v[80:95]\ns_add_u32 s41,s67,68608\nbuffer_load_dwordx4 v178,%[sw],s41 offen offset:1024 lds\n"
"v_exp_f32 v35,v35\nv_cvt_pk_bf16_f32 v96,v32,v33\nv_exp_f32 v36,v36\nds_read_b128 v[104:107],v170 offset:14336\nv_exp_f32 v37,v37\n"
"v_cvt_pk_bf16_f32 v97,v34,v35\nv_exp_f32 v38,v38\nv_mfma_f32_32x32x16_bf16 v[16:31],v[112:115],v[100:103],v[16:31]\nv_exp_f32 v39,v39\n"
"v_cvt_pk_bf16_f32 v98,v36,v37\nv_exp_f32 v40,v40\nds_read_b128 v[108:111],v170 offset:15360\nv_exp_f32 v41,v41\nv_cvt_pk_bf16_f32 v99,v38,v39\n"
"v_exp_f32 v42,v42\nds_read_b128 v[112:115],v170 offset:16384\nv_exp_f32 v43,v43\nv_cvt_pk_bf16_f32 v100,v40,v41\nv_exp_f32 v44,v44\n"
"v_mfma_f32_32x32x16_bf16 v[0:15],v[120:123],v[96:99],v[0:15]\nv_exp_f32 v45,v45\nv_cvt_pk_bf16_f32 v101,v42,v43\nv_exp_f32 v46,v46\nv_exp_f32 v47,v47\n"
"v_exp_f32 v48,v48\nv_cvt_pk_bf16_f32 v102,v44,v45\nv_exp_f32 v49,v49\nv_exp_f32 v50,v50\nv_cvt_pk_bf16_f32 v103,v46,v47\ns_waitcnt lgkmcnt(2)\n"
"v_mfma_f32_32x32x16_bf16 v[32:47],v[104:107],v[128:131],v[64:79]\nv_exp_f32 v51,v51\nv_cvt_pk_bf16_f32 v96,v48,v49\nv_exp_f32 v52,v52\n"
"v_exp_f32 v53,v53\nv_cvt_pk_bf16_f32 v97,v50,v51\nv_exp_f32 v54,v54\nv_mfma_f32_32x32x16_bf16 v[0:15],v[124:127],v[100:103],v[0:15]\nv_exp_f32 v55,v55\n"
"v_cvt_pk_bf16_f32 v98,v52,v53\nv_exp_f32 v56,v56\nv_exp_f32 v57,v57\nv_cvt_pk_bf16_f32 v99,v54,v55\nv_exp_f32 v58,v58\nv_exp_f32 v59,v59\n"
"v_cvt_pk_bf16_f32 v100,v56,v57\nv_exp_f32 v60,v60\nv_mfma_f32_32x32x16_bf16 v[16:31],v[120:123],v[96:99],v[16:31]\nv_exp_f32 v61,v61\n"
"v_cvt_pk_bf16_f32 v101,v58,v59\nv_exp_f32 v62,v62\nv_exp_f32 v63,v63\ns_waitcnt lgkmcnt(0)\nv_exp_f32 v32,v32\nv_cvt_pk_bf16_f32 v102,v60,v61\n"
"v_exp_f32 v33,v33\nv_exp_f32 v34,v34\nv_cvt_pk_bf16_f32 v103,v62,v63\nv_mfma_f32_32x32x16_bf16 v[48:63],v[104:107],v[132:135],v[80:95]\n"
"v_exp_f32 v35,v35\nv_cvt_pk_bf16_f32 v96,v32,v33\nv_exp_f32 v36,v36\nds_read_b128 v[116:119],v170 offset:17408\nv_exp_f32 v37,v37\n"
"v_cvt_pk_bf16_f32 v97,v34,v35\nv_exp_f32 v38,v38\nv_mfma_f32_32x32x16_bf16 v[16:31],v[124:127],v[100:103],v[16:31]\nv_exp_f32 v39,v39\n"
"v_cvt_pk_bf16_f32 v98,v36,v37\nv_exp_f32 v40,v40\nds_read_b128 v[120:123],v170 offset:18432\nv_exp_f32 v41,v41\nv_cvt_pk_bf16_f32 v99,v38,v39\n"
"v_exp_f32 v42,v42\nds_read_b128 v[124:127],v170 offset:19456\nv_exp_f32 v43,v43\nv_cvt_pk_bf16_f32 v100,v40,v41\nv_exp_f32 v44,v44\n"
"v_mfma_f32_32x32x16_bf16 v[0:15],v[108:111],v[96:99],v[0:15]\nv_exp_f32 v45,v45\nv_cvt_pk_bf16_f32 v101,v42,v43\nv_exp_f32 v46,v46\nv_exp_f32 v47,v47\n"
"v_exp_f32 v48,v48\nv_cvt_pk_bf16_f32 v102,v44,v45\nv_exp_f32 v49,v49\nv_exp_f32 v50,v50\nv_cvt_pk_bf16_f32 v103,v46,v47\ns_waitcnt lgkmcnt(2)\n"
"v_mfma_f32_32x32x16_bf16 v[32:47],v[116:119],v[128:131],v[64:79]\nv_exp_f32 v51,v51\nv_cvt_pk_bf16_f32 v96,v48,v49\nv_exp_f32 v52,v52\n"
"v_exp_f32 v53,v53\nv_cvt_pk_bf16_f32 v97,v50,v51\nv_exp_f32 v54,v54\nv_mfma_f32_32x32x16_bf16 v[0:15],v[112:115],v[100:103],v[0:15]\nv_exp_f32 v55,v55\n"
"v_cvt_pk_bf16_f32 v98,v52,v53\nv_exp_f32 v56,v56\nv_exp_f32 v57,v57\nv_cvt_pk_bf16_f32 v99,v54,v55\nv_exp_f32 v58,v58\nv_exp_f32 v59,v59\n"
"v_cvt_pk_bf16_f32 v100,v56,v57\nv_exp_f32 v60,v60\nv_mfma_f32_32x32x16_bf16 v[16:31],v[108:111],v[96:99],v[16:31]\nv_exp_f32 v61,v61\n"
"v_cvt_pk_bf16_f32 v101,v58,v59\nv_exp_f32 v62,v62\nv_exp_f32 v63,v63\ns_waitcnt lgkmcnt(0)\nv_exp_f32 v32,v32\nv_cvt_pk_bf16_f32 v102,v60,v61\n"
"v_exp_f32 v33,v33\nv_exp_f32 v34,v34\nv_cvt_pk_bf16_f32 v103,v62,v63\nv_mfma_f32_32x32x16_bf16 v[48:63],v[116:119],v[132:135],v[80:95]\n"
"v_exp_f32 v35,v35\nv_cvt_pk_bf16_f32 v96,v32,v33\nv_exp_f32 v36,v36\nds_read_b128 v[104:107],v170 offset:20480\nv_exp_f32 v37,v37\n"
"v_cvt_pk_bf16_f32 v97,v34,v35\nv_exp_f32 v38,v38\nv_mfma_f32_32x32x16_bf16 v[16:31],v[112:115],v[100:103],v[16:31]\nv_exp_f32 v39,v39\n"
"v_cvt_pk_bf16_f32 v98,v36,v37\nv_exp_f32 v40,v40\nds_read_b128 v[108:111],v170 offset:21504\nv_exp_f32 v41,v41\nv_cvt_pk_bf16_f32 v99,v38,v39\n"
"v_exp_f32 v42,v42\nds_read_b128 v[112:115],v170 offset:22528\nv_exp_f32 v43,v43\nv_cvt_pk_bf16_f32 v100,v40,v41\nv_exp_f32 v44,v44\n"
"v_mfma_f32_32x32x16_bf16 v[0:15],v[120:123],v[96:99],v[0:15]\nv_exp_f32 v45,v45\nv_cvt_pk_bf16_f32 v101,v42,v43\nv_exp_f32 v46,v46\nv_exp_f32 v47,v47\n"
"v_exp_f32 v48,v48\nv_cvt_pk_bf16_f32 v102,v44,v45\nv_exp_f32 v49,v49\nv_exp_f32 v50,v50\nv_cvt_pk_bf16_f32 v103,v46,v47\ns_waitcnt lgkmcnt(2)\n"
"v_mfma_f32_32x32x16_bf16 v[32:47],v[104:107],v[128:131],v[64:79]\nv_exp_f32 v51,v51\nv_cvt_pk_bf16_f32 v96,v48,v49\nv_exp_f32 v52,v52\n"
"v_exp_f32 v53,v53\nv_cvt_pk_bf16_f32 v97,v50,v51\nv_exp_f32 v54,v54\nv_mfma_f32_32x32x16_bf16 v[0:15],v[124:127],v[100:103],v[0:15]\nv_exp_f32 v55,v55\n"
"v_cvt_pk_bf16_f32 v98,v52,v53\nv_exp_f32 v56,v56\nv_exp_f32 v57,v57\nv_cvt_pk_bf16_f32 v99,v54,v55\nv_exp_f32 v58,v58\nv_exp_f32 v59,v59\n"
"v_cvt_pk_bf16_f32 v100,v56,v57\nv_exp_f32 v60,v60\nv_mfma_f32_32x32x16_bf16 v[16:31],v[120:123],v[96:99],v[16:31]\nv_exp_f32 v61,v61\n"
"v_cvt_pk_bf16_f32 v101,v58,v59\nv_exp_f32 v62,v62\nv_exp_f32 v63,v63\ns_waitcnt lgkmcnt(0)\nv_exp_f32 v32,v32\nv_cvt_pk_bf16_f32 v102,v60,v61\n"
"v_exp_f32 v33,v33\nv_exp_f32 v34,v34\nv_cvt_pk_bf16_f32 v103,v62,v63\nv_mfma_f32_32x32x16_bf16 v[48:63],v[104:107],v[132:135],v[80:95]\n"
"v_exp_f32 v35,v35\nv_cvt_pk_bf16_f32 v96,v32,v33\nv_exp_f32 v36,v36\nds_read_b128 v[116:119],v170 offset:23552\nv_exp_f32 v37,v37\n"
"v_cvt_pk_bf16_f32 v97,v34,v35\nv_exp_f32 v38,v38\nv_mfma_f32_32x32x16_bf16 v[16:31],v[124:127],v[100:103],v[16:31]\nv_exp_f32 v39,v39\n"
"v_cvt_pk_bf16_f32 v98,v36,v37\nv_exp_f32 v40,v40\nds_read_b128 v[120:123],v170 offset:24576\nv_exp_f32 v41,v41\nv_cvt_pk_bf16_f32 v99,v38,v39\n"
"v_exp_f32 v42,v42\nds_read_b128 v[124:127],v170 offset:25600\nv_exp_f32 v43,v43\nv_cvt_pk_bf16_f32 v100,v40,v41\nv_exp_f32 v44,v44\n"
"v_mfma_f32_32x32x16_bf16 v[0:15],v[108:111],v[96:99],v[0:15]\nv_exp_f32 v45,v45\nv_cvt_pk_bf16_f32 v101,v42,v43\nv_exp_f32 v46,v46\nv_exp_f32 v47,v47\n"
"v_exp_f32 v48,v48\nv_cvt_pk_bf16_f32 v102,v44,v45\nv_exp_f32 v49,v49\nv_exp_f32 v50,v50\nv_cvt_pk_bf16_f32 v103,v46,v47\ns_waitcnt lgkmcnt(2)\n"
"v_mfma_f32_32x32x16_bf16 v[32:47],v[116:119],v[128:131],v[64:79]\nv_exp_f32 v51,v51\nv_cvt_pk_bf16_f32 v96,v48,v49\nv_exp_f32 v52,v52\n"
"v_exp_f32 v53,v53\nv_cvt_pk_bf16_f32 v97,v50,v51\nv_exp_f32 v54,v54\nv_mfma_f32_32x32x16_bf16 v[0:15],v[112:115],v[100:103],v[0:15]\nv_exp_f32 v55,v55\n"
"v_cvt_pk_bf16_f32 v98,v52,v53\nv_exp_f32 v56,v56\nv_exp_f32 v57,v57\nv_cvt_pk_bf16_f32 v99,v54,v55\nv_exp_f32 v58,v58\nv_exp_f32 v59,v59\n"
"v_cvt_pk_bf16_f32 v100,v56,v57\nv_exp_f32 v60,v60\nv_mfma_f32_32x32x16_bf16 v[16:31],v[108:111],v[96:99],v[16:31]\nv_exp_f32 v61,v61\n"
"v_cvt_pk_bf16_f32 v101,v58,v59\nv_exp_f32 v62,v62\nv_exp_f32 v63,v63\ns_waitcnt lgkmcnt(0)\nv_exp_f32 v32,v32\nv_cvt_pk_bf16_f32 v102,v60,v61\n"
"v_exp_f32 v33,v33\nv_exp_f32 v34,v34\nv_cvt_pk_bf16_f32 v103,v62,v63\nv_mfma_f32_32x32x16_bf16 v[48:63],v[116:119],v[132:135],v[80:95]\n"
"v_exp_f32 v35,v35\nv_cvt_pk_bf16_f32 v96,v32,v33\nv_exp_f32 v36,v36\nds_read_b128 v[104:107],v170 offset:26624\nv_exp_f32 v37,v37\n"
"v_cvt_pk_bf16_f32 v97,v34,v35\nv_exp_f32 v38,v38\nv_mfma_f32_32x32x16_bf16 v[16:31],v[112:115],v[100:103],v[16:31]\nv_exp_f32 v39,v39\n"
"v_cvt_pk_bf16_f32 v98,v36,v37\nv_exp_f32 v40,v40\nds_read_b128 v[108:111],v170 offset:27648\nv_exp_f32 v41,v41\nv_cvt_pk_bf16_f32 v99,v38,v39\n"
"v_exp_f32 v42,v42\nds_read_b128 v[112:115],v170 offset:28672\nv_exp_f32 v43,v43\nv_cvt_pk_bf16_f32 v100,v40,v41\nv_exp_f32 v44,v44\n"
"v_mfma_f32_32x32x16_bf16 v[0:15],v[120:123],v[96:99],v[0:15]\nv_exp_f32 v45,v45\nv_cvt_pk_bf16_f32 v101,v42,v43\nv_exp_f32 v46,v46\nv_exp_f32 v47,v47\n"
"v_exp_f32 v48,v48\nv_cvt_pk_bf16_f32 v102,v44,v45\nv_exp_f32 v49,v49\nv_exp_f32 v50,v50\nv_cvt_pk_bf16_f32 v103,v46,v47\ns_waitcnt lgkmcnt(2)\n"
"v_mfma_f32_32x32x16_bf16 v[32:47],v[104:107],v[128:131],v[64:79]\nv_exp_f32 v51,v51\nv_cvt_pk_bf16_f32 v96,v48,v49\nv_exp_f32 v52,v52\n"
"v_exp_f32 v53,v53\nv_cvt_pk_bf16_f32 v97,v50,v51\nv_exp_f32 v54,v54\nv_mfma_f32_32x32x16_bf16 v[0:15],v[124:127],v[100:103],v[0:15]\nv_exp_f32 v55,v55\n"
"v_cvt_pk_bf16_f32 v98,v52,v53\nv_exp_f32 v56,v56\nv_exp_f32 v57,v57\nv_cvt_pk_bf16_f32 v99,v54,v55\nv_exp_f32 v58,v58\nv_exp_f32 v59,v59\n"
"v_cvt_pk_bf16_f32 v100,v56,v57\nv_exp_f32 v60,v60\nv_mfma_f32_32x32x16_bf16 v[16:31],v[120:123],v[96:99],v[16:31]\nv_exp_f32 v61,v61\n"
"v_cvt_pk_bf16_f32 v101,v58,v59\nv_exp_f32 v62,v62\nv_exp_f32 v63,v63\ns_waitcnt lgkmcnt(0)\nv_exp_f32 v32,v32\nv_cvt_pk_bf16_f32 v102,v60,v61\n"
"v_exp_f32 v33,v33\nv_exp_f32 v34,v34\nv_cvt_pk_bf16_f32 v103,v62,v63\nv_mfma_f32_32x32x16_bf16 v[48:63],v[104:107],v[132:135],v[80:95]\n"
"v_exp_f32 v35,v35\nv_cvt_pk_bf16_f32 v96,v32,v33\nv_exp_f32 v36,v36\nds_read_b128 v[116:119],v170 offset:29696\nv_exp_f32 v37,v37\n"
"v_cvt_pk_bf16_f32 v97,v34,v35\nv_exp_f32 v38,v38\nv_mfma_f32_32x32x16_bf16 v[16:31],v[124:127],v[100:103],v[16:31]\nv_mov_b32 v174,0\n"
"global_load_dword v175,v174,s[76:77] sc1\nv_exp_f32 v39,v39\nv_cvt_pk_bf16_f32 v98,v36,v37\nv_exp_f32 v40,v40\n"
"ds_read_b128 v[120:123],v170 offset:30720\nv_exp_f32 v41,v41\nv_cvt_pk_bf16_f32 v99,v38,v39\nv_exp_f32 v42,v42\n"
"ds_read_b128 v[124:127],v170 offset:31744\ns_setprio 0\nv_exp_f32 v43,v43\nv_cvt_pk_bf16_f32 v100,v40,v41\nv_exp_f32 v44,v44\n"
"v_mfma_f32_32x32x16_bf16 v[0:15],v[108:111],v[96:99],v[0:15]\nv_exp_f32 v45,v45\nv_cvt_pk_bf16_f32 v101,v42,v43\nv_exp_f32 v46,v46\nv_exp_f32 v47,v47\n"
"v_exp_f32 v48,v48\nv_cvt_pk_bf16_f32 v102,v44,v45\nv_exp_f32 v49,v49\nv_exp_f32 v50,v50\nv_cvt_pk_bf16_f32 v103,v46,v47\ns_waitcnt lgkmcnt(2)\n"
"v_mfma_f32_32x32x16_bf16 v[32:47],v[116:119],v[128:131],v[64:79]\nv_exp_f32 v51,v51\nv_cvt_pk_bf16_f32 v96,v48,v49\nv_exp_f32 v52,v52\n"
"v_exp_f32 v53,v53\nv_cvt_pk_bf16_f32 v97,v50,v51\nv_exp_f32 v54,v54\nv_mfma_f32_32x32x16_bf16 v[0:15],v[112:115],v[100:103],v[0:15]\nv_exp_f32 v55,v55\n"
"v_cvt_pk_bf16_f32 v98,v52,v53\nv_exp_f32 v56,v56\nv_exp_f32 v57,v57\nv_cvt_pk_bf16_f32 v99,v54,v55\nv_exp_f32 v58,v58\nv_exp_f32 v59,v59\n"
"v_cvt_pk_bf16_f32 v100,v56,v57\nv_exp_f32 v60,v60\nv_mfma_f32_32x32x16_bf16 v[16:31],v[108:111],v[96:99],v[16:31]\nv_exp_f32 v61,v61\n"
"v_cvt_pk_bf16_f32 v101,v58,v59\nv_exp_f32 v62,v62\nv_exp_f32 v63,v63\ns_waitcnt lgkmcnt(0)\nv_exp_f32 v32,v32\nv_cvt_pk_bf16_f32 v102,v60,v61\n"
"v_exp_f32 v33,v33\nv_exp_f32 v34,v34\nv_cvt_pk_bf16_f32 v103,v62,v63\nv_mfma_f32_32x32x16_bf16 v[48:63],v[116:119],v[132:135],v[80:95]\n"
"s_waitcnt vmcnt(0)\nv_readfirstlane_b32 s41,v175\ns_nop 0\ns_cmp_ge_u32 s41,s69\ns_cbranch_scc1 322f\nv_mov_b32 v172,0\ns_mov_b32 s42,0x400000\n321:\n"
"global_load_dword v173,v172,s[76:77] sc1\ns_waitcnt vmcnt(0)\nv_readfirstlane_b32 s41,v173\ns_sub_u32 s42,s42,1\ns_cbranch_scc1 322f\n"
"s_cmp_ge_u32 s41,s69\ns_cbranch_scc1 322f\ns_sleep 8\ns_branch 321b\n322:\ns_add_u32 m0,s70,s43\ns_add_u32 s41,s66,0\n"
"buffer_load_dwordx4 v176,%[sws],s41 offen offset:0 sc1 lds\nv_exp_f32 v35,v35\nv_cvt_pk_bf16_f32 v96,v32,v33\nv_exp_f32 v36,v36\nv_exp_f32 v37,v37\n"
"v_cvt_pk_bf16_f32 v97,v34,v35\nv_exp_f32 v38,v38\nv_mfma_f32_32x32x16_bf16 v[16:31],v[112:115],v[100:103],v[16:31]\n"
"buffer_load_dwordx4 v176,%[sws],s41 offen offset:1024 sc1 lds\nv_exp_f32 v39,v39\nv_cvt_pk_bf16_f32 v98,v36,v37\nv_exp_f32 v40,v40\nv_exp_f32 v41,v41\n"
"v_cvt_pk_bf16_f32 v99,v38,v39\nv_exp_f32 v42,v42\ns_add_u32 m0,s70,s44\ns_add_u32 s41,s66,4096\n"
"buffer_load_dwordx4 v176,%[sws],s41 offen offset:0 sc1 lds\nv_exp_f32 v43,v43\nv_cvt_pk_bf16_f32 v100,v40,v41\nv_exp_f32 v44,v44\n"
"v_mfma_f32_32x32x16_bf16 v[0:15],v[120:123],v[96:99],v[0:15]\nbuffer_load_dwordx4 v176,%[sws],s41 offen offset:1024 sc1 lds\nv_exp_f32 v45,v45\n"
"v_cvt_pk_bf16_f32 v101,v42,v43\nv_exp_f32 v46,v46\nv_exp_f32 v47,v47\nv_exp_f32 v48,v48\nv_cvt_pk_bf16_f32 v102,v44,v45\nv_exp_f32 v49,v49\n"
"v_exp_f32 v50,v50\nv_cvt_pk_bf16_f32 v103,v46,v47\nv_exp_f32 v51,v51\nv_cvt_pk_bf16_f32 v96,v48,v49\nv_exp_f32 v52,v52\nv_exp_f32 v53,v53\n"
"v_cvt_pk_bf16_f32 v97,v50,v51\nv_exp_f32 v54,v54\nv_mfma_f32_32x32x16_bf16 v[0:15],v[124:127],v[100:103],v[0:15]\nv_exp_f32 v55,v55\n"
"v_cvt_pk_bf16_f32 v98,v52,v53\nv_exp_f32 v56,v56\nv_exp_f32 v57,v57\nv_cvt_pk_bf16_f32 v99,v54,v55\nv_exp_f32 v58,v58\nv_exp_f32 v59,v59\n"
"v_cvt_pk_bf16_f32 v100,v56,v57\nv_exp_f32 v60,v60\nv_mfma_f32_32x32x16_bf16 v[16:31],v[120:123],v[96:99],v[16:31]\nv_exp_f32 v61,v61\n"
"v_cvt_pk_bf16_f32 v101,v58,v59\nv_exp_f32 v62,v62\nv_exp_f32 v63,v63\ns_nop 1\nv_cvt_pk_bf16_f32 v102,v60,v61\nv_cvt_pk_bf16_f32 v103,v62,v63\ns_nop 1\n"
"v_mfma_f32_32x32x16_bf16 v[16:31],v[124:127],v[100:103],v[16:31]\ns_waitcnt lgkmcnt(0)\ns_nop 7\ns_nop 7\n41:\n"
    : "+{v[0:15]}"(a0), "+{v[16:31]}"(a1)
    : "{v[64:79]}"(n0), "{v[80:95]}"(n1), "{v[128:135]}"(qa), "{v[136:151]}"(ca), "{v[152:167]}"(cb), "{v[176:183]}"(tv),
      "{s[64:67]}"(T0_), "{s[68:71]}"(T1_), "{s[72:75]}"(T2_), "{s[76:79]}"(T3_), [sws] "s"(sws), [sw] "s"(sw)
    : "v32", "v33", "v34", "v35", "v36", "v37", "v38", "v39", "v40", "v41", "v42", "v43", "v44", "v45", "v46", "v47", "v48", "v49", "v50", "v51", "v52", "v53", "v54", "v55", "v56", "v57", "v58", "v59", "v60", "v61", "v62", "v63", "v96", "v97", "v98", "v99", "v100", "v101", "v102", "v103", "v104", "v105", "v106", "v107", "v108", "v109", "v110", "v111", "v112", "v113", "v114", "v115", "v116", "v117", "v118", "v119", "v120", "v121", "v122", "v123", "v124", "v125", "v126", "v127", "v168", "v169", "v170", "v171", "v172", "v173", "v174", "v175", "s40", "s41", "s42", "s43", "s44", "s45", "s46", "s47", "vcc", "scc", "memory");
  }
  {
    LAS float* mg = (LAS float*)((LAS char*)smem + ((z + 3) & 3) * 32768) + lane;
    asm volatile("s_barrier" ::: "memory");
    const f32x16 give = grp ? a0 : a1;
    f32x16 mine = grp ? a1 : a0;
#pragma unroll
    for (int e = 0; e < 9; ++e) mg[(wid * 9 + e) * 64] = give[e];
    asm volatile("s_waitcnt lgkmcnt(0)\n\ts_barrier" ::: "memory");
#pragma unroll
    for (int e = 0; e < 9; ++e) mine[e] += mg[((wid ^ 4) * 9 + e) * 64];
    float am = 0.f;
#pragma unroll
    for (int e = 0; e < 8; ++e) am = __builtin_fmaf(mine[e], 0.f, am);
    if (__any(!(mine[8] < L_LIMIT) || am != am)) {
      const char* KA = ws + WS_KA + lane * 16;
      const bf16x8 q = *(const bf16x8*)(ws + WS_QA + (size_t)(ah * 64 + 2 * qp + grp) * 1024 + lane * 16);
      const f32x16 zz = {};
      float mx = -INFINITY;
      for (int kb = 0; kb < 64; ++kb) mx = fmaxf(mx, rowmax16(MFMA32(*(const bf16x8*)(KA + kb * 3072), q, zz)));
      mine = zz;
      for (int kb = 0; kb < 64; ++kb) {
        f32x16 s = MFMA32(*(const bf16x8*)(KA + kb * 3072), q, zz);
#pragma unroll
        for (int e = 0; e < 16; ++e) s[e] = __builtin_amdgcn_exp2f(s[e] - mx);
        mine = MFMA32(*(const bf16x8*)(KA + kb * 3072 + 1024), __builtin_bit_cast(bf16x8, pack8(s, 0, 1.f)), mine);
        mine = MFMA32(*(const bf16x8*)(KA + kb * 3072 + 2048), __builtin_bit_cast(bf16x8, pack8(s, 8, 1.f)), mine);
      }
    }
    __builtin_amdgcn_raw_buffer_store_b128(pack8(mine, 0, ga / mine[8]), sws, lane * 16 + (unsigned)WS_C + ((2 * qp + grp) * 288 + 256 + ah) * 1024, 0, 16);
    asm volatile("s_nop 1" ::: "memory");
  }
  {
    f32x16 m0, m1;
    asm volatile(
"s_mov_b32 s43,s65\ns_mov_b32 s44,0x20000\ns_add_u32 s45,s65,0x8000\ns_xor_b32 s46,s65,0x10000\ns_mov_b64 s[48:49],s[56:57]\n"
"s_mov_b64 s[50:51],s[58:59]\ns_mov_b32 s47,s67\nv_add_u32 v160,s43,v179\nv_add_u32 v162,s43,v180\ns_waitcnt vmcnt(0)\ns_barrier\ns_cmp_eq_u32 s68,0\n"
"s_cbranch_scc1 54f\ns_mov_b64 exec,1\nv_mov_b32 v172,s81\nv_mov_b32 v173,1\nglobal_atomic_add v172,v173,s[78:79]\n"
"global_atomic_add v172,v173,s[78:79] offset:64\ns_mov_b64 exec,-1\n54:\ns_add_u32 m0,s70,s45\ns_add_u32 s41,s66,8192\n"
"buffer_load_dwordx4 v176,%[sws],s41 offen offset:0 sc1 lds\nbuffer_load_dwordx4 v176,%[sws],s41 offen offset:1024 sc1 lds\ns_add_u32 s40,s70,s45\n"
"s_add_u32 m0,s40,16384\ns_add_u32 s41,s47,131072\nbuffer_load_dwordx4 v178,s[48:51],s41 offen offset:0 lds\ns_add_u32 s41,s47,134144\n"
"buffer_load_dwordx4 v178,s[48:51],s41 offen offset:1024 lds\ns_add_u32 m0,s70,s46\ns_add_u32 s41,s66,12288\n"
"buffer_load_dwordx4 v176,%[sws],s41 offen offset:0 sc1 lds\nbuffer_load_dwordx4 v176,%[sws],s41 offen offset:1024 sc1 lds\ns_add_u32 s40,s70,s46\n"
"s_add_u32 m0,s40,16384\ns_add_u32 s41,s47,196608\nbuffer_load_dwordx4 v178,s[48:51],s41 offen offset:0 lds\ns_add_u32 s41,s47,199680\n"
"buffer_load_dwordx4 v178,s[48:51],s41 offen offset:1024 lds\nds_read_b64_tr_b16 v[64:65],v160 offset:0\nds_read_b64_tr_b16 v[66:67],v160 offset:2048\n"
"ds_read_b128 v[68:71],v162 offset:0\nds_read_b128 v[72:75],v162 offset:4096\nds_read_b64_tr_b16 v[76:77],v160 offset:4096\n"
"ds_read_b64_tr_b16 v[78:79],v160 offset:6144\nds_read_b128 v[80:83],v162 offset:1024\nds_read_b128 v[84:87],v162 offset:5120\n"
"ds_read_b64_tr_b16 v[88:89],v160 offset:8192\nds_read_b64_tr_b16 v[90:91],v160 offset:10240\nds_read_b128 v[92:95],v162 offset:2048\n"
"ds_read_b128 v[96:99],v162 offset:6144\nds_read_b64_tr_b16 v[100:101],v160 offset:12288\nds_read_b64_tr_b16 v[102:103],v160 offset:14336\n"
"ds_read_b128 v[104:107],v162 offset:3072\nds_read_b128 v[108:111],v162 offset:7168\ns_waitcnt lgkmcnt(13)\n"
"v_mfma_f32_32x32x16_bf16 v[0:15],v[64:67],v[68:71],0\ns_waitcnt lgkmcnt(12)\nv_mfma_f32_32x32x16_bf16 v[16:31],v[64:67],v[72:75],0\n"
"s_waitcnt lgkmcnt(9)\nv_mfma_f32_32x32x16_bf16 v[0:15],v[76:79],v[80:83],v[0:15]\ns_waitcnt lgkmcnt(8)\n"
"v_mfma_f32_32x32x16_bf16 v[16:31],v[76:79],v[84:87],v[16:31]\nv_add_u32 v161,s44,v179\nv_add_u32 v163,s44,v180\ns_waitcnt lgkmcnt(0)\ns_barrier\n"
"v_mfma_f32_32x32x16_bf16 v[0:15],v[88:91],v[92:95],v[0:15]\nds_read_b64_tr_b16 v[112:113],v161 offset:0\n"
"ds_read_b64_tr_b16 v[114:115],v161 offset:2048\nds_read_b128 v[116:119],v163 offset:0\nds_read_b128 v[120:123],v163 offset:4096\ns_add_u32 m0,s70,s43\n"
"s_add_u32 s41,s66,16384\nbuffer_load_dwordx4 v176,%[sws],s41 offen offset:0 sc1 lds\nv_mfma_f32_32x32x16_bf16 v[16:31],v[88:91],v[96:99],v[16:31]\n"
"ds_read_b64_tr_b16 v[124:125],v161 offset:4096\nds_read_b64_tr_b16 v[126:127],v161 offset:6144\nds_read_b128 v[128:131],v163 offset:1024\n"
"ds_read_b128 v[132:135],v163 offset:5120\nbuffer_load_dwordx4 v176,%[sws],s41 offen offset:1024 sc1 lds\n"
"v_mfma_f32_32x32x16_bf16 v[0:15],v[100:103],v[104:107],v[0:15]\nds_read_b64_tr_b16 v[136:137],v161 offset:8192\n"
"ds_read_b64_tr_b16 v[138:139],v161 offset:10240\nds_read_b128 v[140:143],v163 offset:2048\nds_read_b128 v[144:147],v163 offset:6144\n"
"s_add_u32 s40,s70,s43\ns_add_u32 m0,s40,16384\ns_add_u32 s41,s47,262144\nbuffer_load_dwordx4 v178,s[48:51],s41 offen offset:0 lds\n"
"v_mfma_f32_32x32x16_bf16 v[16:31],v[100:103],v[108:111],v[16:31]\nds_read_b64_tr_b16 v[148:149],v161 offset:12288\n"
"ds_read_b64_tr_b16 v[150:151],v161 offset:14336\nds_read_b128 v[152:155],v163 offset:3072\nds_read_b128 v[156:159],v163 offset:7168\n"
"s_add_u32 s41,s47,265216\nbuffer_load_dwordx4 v178,s[48:51],s41 offen offset:1024 lds\ns_waitcnt lgkmcnt(13)\n"
"v_mfma_f32_32x32x16_bf16 v[0:15],v[112:115],v[116:119],v[0:15]\ns_waitcnt lgkmcnt(12)\n"
"v_mfma_f32_32x32x16_bf16 v[16:31],v[112:115],v[120:123],v[16:31]\ns_waitcnt lgkmcnt(9)\nv_mfma_f32_32x32x16_bf16 v[0:15],v[124:127],v[128:131],v[0:15]\n"
"s_waitcnt lgkmcnt(8)\nv_mfma_f32_32x32x16_bf16 v[16:31],v[124:127],v[132:135],v[16:31]\nv_add_u32 v160,s45,v179\nv_add_u32 v162,s45,v180\n"
"s_waitcnt vmcnt(8) lgkmcnt(0)\ns_barrier\nv_mfma_f32_32x32x16_bf16 v[0:15],v[136:139],v[140:143],v[0:15]\nds_read_b64_tr_b16 v[64:65],v160 offset:0\n"
"ds_read_b64_tr_b16 v[66:67],v160 offset:2048\nds_read_b128 v[68:71],v162 offset:0\nds_read_b128 v[72:75],v162 offset:4096\ns_add_u32 m0,s70,s44\n"
"s_add_u32 s41,s66,20480\nbuffer_load_dwordx4 v176,%[sws],s41 offen offset:0 sc1 lds\nv_mfma_f32_32x32x16_bf16 v[16:31],v[136:139],v[144:147],v[16:31]\n"
"ds_read_b64_tr_b16 v[76:77],v160 offset:4096\nds_read_b64_tr_b16 v[78:79],v160 offset:6144\nds_read_b128 v[80:83],v162 offset:1024\n"
"ds_read_b128 v[84:87],v162 offset:5120\nbuffer_load_dwordx4 v176,%[sws],s41 offen offset:1024 sc1 lds\n"
"v_mfma_f32_32x32x16_bf16 v[0:15],v[148:151],v[152:155],v[0:15]\nds_read_b64_tr_b16 v[88:89],v160 offset:8192\n"
"ds_read_b64_tr_b16 v[90:91],v160 offset:10240\nds_read_b128 v[92:95],v162 offset:2048\nds_read_b128 v[96:99],v162 offset:6144\ns_add_u32 s40,s70,s44\n"
"s_add_u32 m0,s40,16384\ns_add_u32 s41,s47,327680\nbuffer_load_dwordx4 v178,s[48:51],s41 offen offset:0 lds\n"
"v_mfma_f32_32x32x16_bf16 v[16:31],v[148:151],v[156:159],v[16:31]\nds_read_b64_tr_b16 v[100:101],v160 offset:12288\n"
"ds_read_b64_tr_b16 v[102:103],v160 offset:14336\nds_read_b128 v[104:107],v162 offset:3072\nds_read_b128 v[108:111],v162 offset:7168\n"
"s_add_u32 s41,s47,330752\nbuffer_load_dwordx4 v178,s[48:51],s41 offen offset:1024 lds\ns_waitcnt lgkmcnt(13)\n"
"v_mfma_f32_32x32x16_bf16 v[0:15],v[64:67],v[68:71],v[0:15]\ns_waitcnt lgkmcnt(12)\nv_mfma_f32_32x32x16_bf16 v[16:31],v[64:67],v[72:75],v[16:31]\n"
"s_waitcnt lgkmcnt(9)\nv_mfma_f32_32x32x16_bf16 v[0:15],v[76:79],v[80:83],v[0:15]\ns_waitcnt lgkmcnt(8)\n"
"v_mfma_f32_32x32x16_bf16 v[16:31],v[76:79],v[84:87],v[16:31]\nv_add_u32 v161,s46,v179\nv_add_u32 v163,s46,v180\ns_waitcnt vmcnt(8) lgkmcnt(0)\n"
"s_barrier\nv_mfma_f32_32x32x16_bf16 v[0:15],v[88:91],v[92:95],v[0:15]\nds_read_b64_tr_b16 v[112:113],v161 offset:0\n"
"ds_read_b64_tr_b16 v[114:115],v161 offset:2048\nds_read_b128 v[116:119],v163 offset:0\nds_read_b128 v[120:123],v163 offset:4096\ns_add_u32 m0,s70,s45\n"
"s_add_u32 s41,s66,24576\nbuffer_load_dwordx4 v176,%[sws],s41 offen offset:0 sc1 lds\nv_mfma_f32_32x32x16_bf16 v[16:31],v[88:91],v[96:99],v[16:31]\n"
"ds_read_b64_tr_b16 v[124:125],v161 offset:4096\nds_read_b64_tr_b16 v[126:127],v161 offset:6144\nds_read_b128 v[128:131],v163 offset:1024\n"
"ds_read_b128 v[132:135],v163 offset:5120\nbuffer_load_dwordx4 v176,%[sws],s41 offen offset:1024 sc1 lds\n"
"v_mfma_f32_32x32x16_bf16 v[0:15],v[100:103],v[104:107],v[0:15]\nds_read_b64_tr_b16 v[136:137],v161 offset:8192\n"
"ds_read_b64_tr_b16 v[138:139],v161 offset:10240\nds_read_b128 v[140:143],v163 offset:2048\nds_read_b128 v[144:147],v163 offset:6144\n"
"s_add_u32 s40,s70,s45\ns_add_u32 m0,s40,16384\ns_add_u32 s41,s47,393216\nbuffer_load_dwordx4 v178,s[48:51],s41 offen offset:0 lds\n"
"v_mfma_f32_32x32x16_bf16 v[16:31],v[100:103],v[108:111],v[16:31]\nds_read_b64_tr_b16 v[148:149],v161 offset:12288\n"
"ds_read_b64_tr_b16 v[150:151],v161 offset:14336\nds_read_b128 v[152:155],v163 offset:3072\nds_read_b128 v[156:159],v163 offset:7168\n"
"s_add_u32 s41,s47,396288\nbuffer_load_dwordx4 v178,s[48:51],s41 offen offset:1024 lds\ns_waitcnt lgkmcnt(13)\n"
"v_mfma_f32_32x32x16_bf16 v[0:15],v[112:115],v[116:119],v[0:15]\ns_waitcnt lgkmcnt(12)\n"
"v_mfma_f32_32x32x16_bf16 v[16:31],v[112:115],v[120:123],v[16:31]\ns_waitcnt lgkmcnt(9)\nv_mfma_f32_32x32x16_bf16 v[0:15],v[124:127],v[128:131],v[0:15]\n"
"s_waitcnt lgkmcnt(8)\nv_mfma_f32_32x32x16_bf16 v[16:31],v[124:127],v[132:135],v[16:31]\nv_add_u32 v160,s43,v179\nv_add_u32 v162,s43,v180\n"
"s_waitcnt vmcnt(8) lgkmcnt(0)\ns_barrier\nv_mfma_f32_32x32x16_bf16 v[0:15],v[136:139],v[140:143],v[0:15]\nds_read_b64_tr_b16 v[64:65],v160 offset:0\n"
"ds_read_b64_tr_b16 v[66:67],v160 offset:2048\nds_read_b128 v[68:71],v162 offset:0\nds_read_b128 v[72:75],v162 offset:4096\ns_add_u32 m0,s70,s46\n"
"s_add_u32 s41,s66,28672\nbuffer_load_dwordx4 v176,%[sws],s41 offen offset:0 sc1 lds\nv_mfma_f32_32x32x16_bf16 v[16:31],v[136:139],v[144:147],v[16:31]\n"
"ds_read_b64_tr_b16 v[76:77],v160 offset:4096\nds_read_b64_tr_b16 v[78:79],v160 offset:6144\nds_read_b128 v[80:83],v162 offset:1024\n"
"ds_read_b128 v[84:87],v162 offset:5120\nbuffer_load_dwordx4 v176,%[sws],s41 offen offset:1024 sc1 lds\n"
"v_mfma_f32_32x32x16_bf16 v[0:15],v[148:151],v[152:155],v[0:15]\nds_read_b64_tr_b16 v[88:89],v160 offset:8192\n"
"ds_read_b64_tr_b16 v[90:91],v160 offset:10240\nds_read_b128 v[92:95],v162 offset:2048\nds_read_b128 v[96:99],v162 offset:6144\ns_add_u32 s40,s70,s46\n"
"s_add_u32 m0,s40,16384\ns_add_u32 s41,s47,458752\nbuffer_load_dwordx4 v178,s[48:51],s41 offen offset:0 lds\n"
"v_mfma_f32_32x32x16_bf16 v[16:31],v[148:151],v[156:159],v[16:31]\nds_read_b64_tr_b16 v[100:101],v160 offset:12288\n"
"ds_read_b64_tr_b16 v[102:103],v160 offset:14336\nds_read_b128 v[104:107],v162 offset:3072\nds_read_b128 v[108:111],v162 offset:7168\n"
"s_add_u32 s41,s47,461824\nbuffer_load_dwordx4 v178,s[48:51],s41 offen offset:1024 lds\ns_waitcnt lgkmcnt(13)\n"
"v_mfma_f32_32x32x16_bf16 v[0:15],v[64:67],v[68:71],v[0:15]\ns_waitcnt lgkmcnt(12)\nv_mfma_f32_32x32x16_bf16 v[16:31],v[64:67],v[72:75],v[16:31]\n"
"s_waitcnt lgkmcnt(9)\nv_mfma_f32_32x32x16_bf16 v[0:15],v[76:79],v[80:83],v[0:15]\ns_waitcnt lgkmcnt(8)\n"
"v_mfma_f32_32x32x16_bf16 v[16:31],v[76:79],v[84:87],v[16:31]\nv_add_u32 v161,s44,v179\nv_add_u32 v163,s44,v180\ns_waitcnt vmcnt(8) lgkmcnt(0)\n"
"s_barrier\nv_mfma_f32_32x32x16_bf16 v[0:15],v[88:91],v[92:95],v[0:15]\nds_read_b64_tr_b16 v[112:113],v161 offset:0\n"
"ds_read_b64_tr_b16 v[114:115],v161 offset:2048\nds_read_b128 v[116:119],v163 offset:0\nds_read_b128 v[120:123],v163 offset:4096\ns_add_u32 m0,s70,s43\n"
"s_add_u32 s41,s66,32768\nbuffer_load_dwordx4 v176,%[sws],s41 offen offset:0 sc1 lds\nv_mfma_f32_32x32x16_bf16 v[16:31],v[88:91],v[96:99],v[16:31]\n"
"ds_read_b64_tr_b16 v[124:125],v161 offset:4096\nds_read_b64_tr_b16 v[126:127],v161 offset:6144\nds_read_b128 v[128:131],v163 offset:1024\n"
"ds_read_b128 v[132:135],v163 offset:5120\nbuffer_load_dwordx4 v176,%[sws],s41 offen offset:1024 sc1 lds\n"
"v_mfma_f32_32x32x16_bf16 v[0:15],v[100:103],v[104:107],v[0:15]\nds_read_b64_tr_b16 v[136:137],v161 offset:8192\n"
"ds_read_b64_tr_b16 v[138:139],v161 offset:10240\nds_read_b128 v[140:143],v163 offset:2048\nds_read_b128 v[144:147],v163 offset:6144\n"
"s_add_u32 s40,s70,s43\ns_add_u32 m0,s40,16384\ns_add_u32 s41,s47,524288\nbuffer_load_dwordx4 v178,s[48:51],s41 offen offset:0 lds\n"
"v_mfma_f32_32x32x16_bf16 v[16:31],v[100:103],v[108:111],v[16:31]\nds_read_b64_tr_b16 v[148:149],v161 offset:12288\n"
"ds_read_b64_tr_b16 v[150:151],v161 offset:14336\nds_read_b128 v[152:155],v163 offset:3072\nds_read_b128 v[156:159],v163 offset:7168\n"
"s_add_u32 s41,s47,527360\nbuffer_load_dwordx4 v178,s[48:51],s41 offen offset:1024 lds\ns_waitcnt lgkmcnt(13)\n"
"v_mfma_f32_32x32x16_bf16 v[0:15],v[112:115],v[116:119],v[0:15]\ns_waitcnt lgkmcnt(12)\n"
"v_mfma_f32_32x32x16_bf16 v[16:31],v[112:115],v[120:123],v[16:31]\ns_waitcnt lgkmcnt(9)\nv_mfma_f32_32x32x16_bf16 v[0:15],v[124:127],v[128:131],v[0:15]\n"
"s_waitcnt lgkmcnt(8)\nv_mfma_f32_32x32x16_bf16 v[16:31],v[124:127],v[132:135],v[16:31]\nv_add_u32 v160,s45,v179\nv_add_u32 v162,s45,v180\n"
"s_and_b32 s40,s68,s80\ns_cmp_eq_u32 s40,0\ns_cbranch_scc1 53f\nv_mov_b32 v172,s82\ns_mov_b32 s42,0x400000\n511:\n"
"global_load_dword v173,v172,s[78:79] sc1\ns_waitcnt vmcnt(0)\nv_readfirstlane_b32 s41,v173\ns_sub_u32 s42,s42,1\ns_cbranch_scc1 512f\n"
"s_cmp_ge_u32 s41,32\ns_cbranch_scc1 512f\ns_sleep 8\ns_branch 511b\n512:\n53:\ns_waitcnt vmcnt(8) lgkmcnt(0)\ns_barrier\n"
"v_mfma_f32_32x32x16_bf16 v[0:15],v[136:139],v[140:143],v[0:15]\nds_read_b64_tr_b16 v[64:65],v160 offset:0\n"
"ds_read_b64_tr_b16 v[66:67],v160 offset:2048\nds_read_b128 v[68:71],v162 offset:0\nds_read_b128 v[72:75],v162 offset:4096\ns_add_u32 m0,s70,s44\n"
"s_add_u32 s41,s66,36864\nbuffer_load_dwordx4 v176,%[sws],s41 offen offset:0 sc1 lds\nv_mfma_f32_32x32x16_bf16 v[16:31],v[136:139],v[144:147],v[16:31]\n"
"ds_read_b64_tr_b16 v[76:77],v160 offset:4096\nds_read_b64_tr_b16 v[78:79],v160 offset:6144\nds_read_b128 v[80:83],v162 offset:1024\n"
"ds_read_b128 v[84:87],v162 offset:5120\nbuffer_load_dwordx4 v176,%[sws],s41 offen offset:1024 sc1 lds\n"
"v_mfma_f32_32x32x16_bf16 v[0:15],v[148:151],v[152:155],v[0:15]\nds_read_b64_tr_b16 v[88:89],v160 offset:8192\n"
"ds_read_b64_tr_b16 v[90:91],v160 offset:10240\nds_read_b128 v[92:95],v162 offset:2048\nds_read_b128 v[96:99],v162 offset:6144\ns_add_u32 s40,s70,s44\n"
"s_add_u32 m0,s40,16384\ns_add_u32 s41,s47,589824\nbuffer_load_dwordx4 v178,s[48:51],s41 offen offset:0 lds\n"
"v_mfma_f32_32x32x16_bf16 v[16:31],v[148:151],v[156:159],v[16:31]\nds_read_b64_tr_b16 v[100:101],v160 offset:12288\n"
"ds_read_b64_tr_b16 v[102:103],v160 offset:14336\nds_read_b128 v[104:107],v162 offset:3072\nds_read_b128 v[108:111],v162 offset:7168\n"
"s_add_u32 s41,s47,592896\nbuffer_load_dwordx4 v178,s[48:51],s41 offen offset:1024 lds\ns_waitcnt lgkmcnt(13)\n"
"v_mfma_f32_32x32x16_bf16 v[0:15],v[64:67],v[68:71],v[0:15]\ns_waitcnt lgkmcnt(12)\nv_mfma_f32_32x32x16_bf16 v[16:31],v[64:67],v[72:75],v[16:31]\n"
"s_waitcnt lgkmcnt(9)\nv_mfma_f32_32x32x16_bf16 v[0:15],v[76:79],v[80:83],v[0:15]\ns_waitcnt lgkmcnt(8)\n"
"v_mfma_f32_32x32x16_bf16 v[16:31],v[76:79],v[84:87],v[16:31]\nv_add_u32 v161,s46,v179\nv_add_u32 v163,s46,v180\ns_waitcnt vmcnt(8) lgkmcnt(0)\n"
"s_barrier\ns_cmp_lg_u32 s80,0\ns_cselect_b64 s[48:49],s[60:61],s[56:57]\ns_cselect_b64 s[50:51],s[62:63],s[58:59]\ns_sub_u32 s40,s67,4194304\n"
"s_cmp_lg_u32 s80,0\ns_cselect_b32 s47,s40,s67\nv_mfma_f32_32x32x16_bf16 v[0:15],v[88:91],v[92:95],v[0:15]\nds_read_b64_tr_b16 v[112:113],v161 offset:0\n"
"ds_read_b64_tr_b16 v[114:115],v161 offset:2048\nds_read_b128 v[116:119],v163 offset:0\nds_read_b128 v[120:123],v163 offset:4096\ns_add_u32 m0,s70,s45\n"
"s_add_u32 s41,s66,40960\nbuffer_load_dwordx4 v176,%[sws],s41 offen offset:0 sc1 lds\nv_mfma_f32_32x32x16_bf16 v[16:31],v[88:91],v[96:99],v[16:31]\n"
"ds_read_b64_tr_b16 v[124:125],v161 offset:4096\nds_read_b64_tr_b16 v[126:127],v161 offset:6144\nds_read_b128 v[128:131],v163 offset:1024\n"
"ds_read_b128 v[132:135],v163 offset:5120\nbuffer_load_dwordx4 v176,%[sws],s41 offen offset:1024 sc1 lds\n"
"v_mfma_f32_32x32x16_bf16 v[0:15],v[100:103],v[104:107],v[0:15]\nds_read_b64_tr_b16 v[136:137],v161 offset:8192\n"
"ds_read_b64_tr_b16 v[138:139],v161 offset:10240\nds_read_b128 v[140:143],v163 offset:2048\nds_read_b128 v[144:147],v163 offset:6144\n"
"s_add_u32 s40,s70,s45\ns_add_u32 m0,s40,16384\ns_add_u32 s41,s47,655360\nbuffer_load_dwordx4 v178,s[48:51],s41 offen offset:0 lds\n"
"v_mfma_f32_32x32x16_bf16 v[16:31],v[100:103],v[108:111],v[16:31]\nds_read_b64_tr_b16 v[148:149],v161 offset:12288\n"
"ds_read_b64_tr_b16 v[150:151],v161 offset:14336\nds_read_b128 v[152:155],v163 offset:3072\nds_read_b128 v[156:159],v163 offset:7168\n"
"s_add_u32 s41,s47,658432\nbuffer_load_dwordx4 v178,s[48:51],s41 offen offset:1024 lds\ns_waitcnt lgkmcnt(13)\n"
"v_mfma_f32_32x32x16_bf16 v[0:15],v[112:115],v[116:119],v[0:15]\ns_waitcnt lgkmcnt(12)\n"
"v_mfma_f32_32x32x16_bf16 v[16:31],v[112:115],v[120:123],v[16:31]\ns_waitcnt lgkmcnt(9)\nv_mfma_f32_32x32x16_bf16 v[0:15],v[124:127],v[128:131],v[0:15]\n"
"s_waitcnt lgkmcnt(8)\nv_mfma_f32_32x32x16_bf16 v[16:31],v[124:127],v[132:135],v[16:31]\nv_add_u32 v160,s43,v179\nv_add_u32 v162,s43,v180\n"
"s_waitcnt vmcnt(8) lgkmcnt(0)\ns_barrier\nv_mfma_f32_32x32x16_bf16 v[0:15],v[136:139],v[140:143],v[0:15]\nds_read_b64_tr_b16 v[64:65],v160 offset:0\n"
"ds_read_b64_tr_b16 v[66:67],v160 offset:2048\nds_read_b128 v[68:71],v162 offset:0\nds_read_b128 v[72:75],v162 offset:4096\ns_add_u32 m0,s70,s46\n"
"s_add_u32 s41,s66,45056\nbuffer_load_dwordx4 v176,%[sws],s41 offen offset:0 sc1 lds\nv_mfma_f32_32x32x16_bf16 v[16:31],v[136:139],v[144:147],v[16:31]\n"
"ds_read_b64_tr_b16 v[76:77],v160 offset:4096\nds_read_b64_tr_b16 v[78:79],v160 offset:6144\nds_read_b128 v[80:83],v162 offset:1024\n"
"ds_read_b128 v[84:87],v162 offset:5120\nbuffer_load_dwordx4 v176,%[sws],s41 offen offset:1024 sc1 lds\n"
"v_mfma_f32_32x32x16_bf16 v[0:15],v[148:151],v[152:155],v[0:15]\nds_read_b64_tr_b16 v[88:89],v160 offset:8192\n"
"ds_read_b64_tr_b16 v[90:91],v160 offset:10240\nds_read_b128 v[92:95],v162 offset:2048\nds_read_b128 v[96:99],v162 offset:6144\ns_add_u32 s40,s70,s46\n"
"s_add_u32 m0,s40,16384\ns_add_u32 s41,s47,720896\nbuffer_load_dwordx4 v178,s[48:51],s41 offen offset:0 lds\n"
"v_mfma_f32_32x32x16_bf16 v[16:31],v[148:151],v[156:159],v[16:31]\nds_read_b64_tr_b16 v[100:101],v160 offset:12288\n"
"ds_read_b64_tr_b16 v[102:103],v160 offset:14336\nds_read_b128 v[104:107],v162 offset:3072\nds_read_b128 v[108:111],v162 offset:7168\n"
"s_add_u32 s41,s47,723968\nbuffer_load_dwordx4 v178,s[48:51],s41 offen offset:1024 lds\ns_waitcnt lgkmcnt(13)\n"
"v_mfma_f32_32x32x16_bf16 v[0:15],v[64:67],v[68:71],v[0:15]\ns_waitcnt lgkmcnt(12)\nv_mfma_f32_32x32x16_bf16 v[16:31],v[64:67],v[72:75],v[16:31]\n"
"s_waitcnt lgkmcnt(9)\nv_mfma_f32_32x32x16_bf16 v[0:15],v[76:79],v[80:83],v[0:15]\ns_waitcnt lgkmcnt(8)\n"
"v_mfma_f32_32x32x16_bf16 v[16:31],v[76:79],v[84:87],v[16:31]\nv_add_u32 v161,s44,v179\nv_add_u32 v163,s44,v180\ns_waitcnt vmcnt(8) lgkmcnt(0)\n"
"s_barrier\nv_mfma_f32_32x32x16_bf16 v[0:15],v[88:91],v[92:95],v[0:15]\nds_read_b64_tr_b16 v[112:113],v161 offset:0\n"
"ds_read_b64_tr_b16 v[114:115],v161 offset:2048\nds_read_b128 v[116:119],v163 offset:0\nds_read_b128 v[120:123],v163 offset:4096\ns_add_u32 m0,s70,s43\n"
"s_add_u32 s41,s66,49152\nbuffer_load_dwordx4 v176,%[sws],s41 offen offset:0 sc1 lds\nv_mfma_f32_32x32x16_bf16 v[16:31],v[88:91],v[96:99],v[16:31]\n"
"ds_read_b64_tr_b16 v[124:125],v161 offset:4096\nds_read_b64_tr_b16 v[126:127],v161 offset:6144\nds_read_b128 v[128:131],v163 offset:1024\n"
"ds_read_b128 v[132:135],v163 offset:5120\nbuffer_load_dwordx4 v176,%[sws],s41 offen offset:1024 sc1 lds\n"
"v_mfma_f32_32x32x16_bf16 v[0:15],v[100:103],v[104:107],v[0:15]\nds_read_b64_tr_b16 v[136:137],v161 offset:8192\n"
"ds_read_b64_tr_b16 v[138:139],v161 offset:10240\nds_read_b128 v[140:143],v163 offset:2048\nds_read_b128 v[144:147],v163 offset:6144\n"
"s_add_u32 s40,s70,s43\ns_add_u32 m0,s40,16384\ns_add_u32 s41,s47,786432\nbuffer_load_dwordx4 v178,s[48:51],s41 offen offset:0 lds\n"
"v_mfma_f32_32x32x16_bf16 v[16:31],v[100:103],v[108:111],v[16:31]\nds_read_b64_tr_b16 v[148:149],v161 offset:12288\n"
"ds_read_b64_tr_b16 v[150:151],v161 offset:14336\nds_read_b128 v[152:155],v163 offset:3072\nds_read_b128 v[156:159],v163 offset:7168\n"
"s_add_u32 s41,s47,789504\nbuffer_load_dwordx4 v178,s[48:51],s41 offen offset:1024 lds\ns_waitcnt lgkmcnt(13)\n"
"v_mfma_f32_32x32x16_bf16 v[0:15],v[112:115],v[116:119],v[0:15]\ns_waitcnt lgkmcnt(12)\n"
"v_mfma_f32_32x32x16_bf16 v[16:31],v[112:115],v[120:123],v[16:31]\ns_waitcnt lgkmcnt(9)\nv_mfma_f32_32x32x16_bf16 v[0:15],v[124:127],v[128:131],v[0:15]\n"
"s_waitcnt lgkmcnt(8)\nv_mfma_f32_32x32x16_bf16 v[16:31],v[124:127],v[132:135],v[16:31]\nv_add_u32 v160,s45,v179\nv_add_u32 v162,s45,v180\n"
"s_waitcnt vmcnt(8) lgkmcnt(0)\ns_barrier\nv_mfma_f32_32x32x16_bf16 v[0:15],v[136:139],v[140:143],v[0:15]\nds_read_b64_tr_b16 v[64:65],v160 offset:0\n"
"ds_read_b64_tr_b16 v[66:67],v160 offset:2048\nds_read_b128 v[68:71],v162 offset:0\nds_read_b128 v[72:75],v162 offset:4096\ns_add_u32 m0,s70,s44\n"
"s_add_u32 s41,s66,53248\nbuffer_load_dwordx4 v176,%[sws],s41 offen offset:0 sc1 lds\nv_mfma_f32_32x32x16_bf16 v[16:31],v[136:139],v[144:147],v[16:31]\n"
"ds_read_b64_tr_b16 v[76:77],v160 offset:4096\nds_read_b64_tr_b16 v[78:79],v160 offset:6144\nds_read_b128 v[80:83],v162 offset:1024\n"
"ds_read_b128 v[84:87],v162 offset:5120\nbuffer_load_dwordx4 v176,%[sws],s41 offen offset:1024 sc1 lds\n"
"v_mfma_f32_32x32x16_bf16 v[0:15],v[148:151],v[152:155],v[0:15]\nds_read_b64_tr_b16 v[88:89],v160 offset:8192\n"
"ds_read_b64_tr_b16 v[90:91],v160 offset:10240\nds_read_b128 v[92:95],v162 offset:2048\nds_read_b128 v[96:99],v162 offset:6144\ns_add_u32 s40,s70,s44\n"
"s_add_u32 m0,s40,16384\ns_add_u32 s41,s47,851968\nbuffer_load_dwordx4 v178,s[48:51],s41 offen offset:0 lds\n"
"v_mfma_f32_32x32x16_bf16 v[16:31],v[148:151],v[156:159],v[16:31]\nds_read_b64_tr_b16 v[100:101],v160 offset:12288\n"
"ds_read_b64_tr_b16 v[102:103],v160 offset:14336\nds_read_b128 v[104:107],v162 offset:3072\nds_read_b128 v[108:111],v162 offset:7168\n"
"s_add_u32 s41,s47,855040\nbuffer_load_dwordx4 v178,s[48:51],s41 offen offset:1024 lds\ns_waitcnt lgkmcnt(13)\n"
"v_mfma_f32_32x32x16_bf16 v[0:15],v[64:67],v[68:71],v[0:15]\ns_waitcnt lgkmcnt(12)\nv_mfma_f32_32x32x16_bf16 v[16:31],v[64:67],v[72:75],v[16:31]\n"
"s_waitcnt lgkmcnt(9)\nv_mfma_f32_32x32x16_bf16 v[0:15],v[76:79],v[80:83],v[0:15]\ns_waitcnt lgkmcnt(8)\n"
"v_mfma_f32_32x32x16_bf16 v[16:31],v[76:79],v[84:87],v[16:31]\nv_add_u32 v161,s46,v179\nv_add_u32 v163,s46,v180\ns_waitcnt vmcnt(8) lgkmcnt(0)\n"
"s_barrier\nv_mfma_f32_32x32x16_bf16 v[0:15],v[88:91],v[92:95],v[0:15]\nds_read_b64_tr_b16 v[112:113],v161 offset:0\n"
"ds_read_b64_tr_b16 v[114:115],v161 offset:2048\nds_read_b128 v[116:119],v163 offset:0\nds_read_b128 v[120:123],v163 offset:4096\ns_add_u32 m0,s70,s45\n"
"s_add_u32 s41,s66,57344\nbuffer_load_dwordx4 v176,%[sws],s41 offen offset:0 sc1 lds\nv_mfma_f32_32x32x16_bf16 v[16:31],v[88:91],v[96:99],v[16:31]\n"
"ds_read_b64_tr_b16 v[124:125],v161 offset:4096\nds_read_b64_tr_b16 v[126:127],v161 offset:6144\nds_read_b128 v[128:131],v163 offset:1024\n"
"ds_read_b128 v[132:135],v163 offset:5120\nbuffer_load_dwordx4 v176,%[sws],s41 offen offset:1024 sc1 lds\n"
"v_mfma_f32_32x32x16_bf16 v[0:15],v[100:103],v[104:107],v[0:15]\nds_read_b64_tr_b16 v[136:137],v161 offset:8192\n"
"ds_read_b64_tr_b16 v[138:139],v161 offset:10240\nds_read_b128 v[140:143],v163 offset:2048\nds_read_b128 v[144:147],v163 offset:6144\n"
"s_add_u32 s40,s70,s45\ns_add_u32 m0,s40,16384\ns_add_u32 s41,s47,917504\nbuffer_load_dwordx4 v178,s[48:51],s41 offen offset:0 lds\n"
"v_mfma_f32_32x32x16_bf16 v[16:31],v[100:103],v[108:111],v[16:31]\nds_read_b64_tr_b16 v[148:149],v161 offset:12288\n"
"ds_read_b64_tr_b16 v[150:151],v161 offset:14336\nds_read_b128 v[152:155],v163 offset:3072\nds_read_b128 v[156:159],v163 offset:7168\n"
"s_add_u32 s41,s47,920576\nbuffer_load_dwordx4 v178,s[48:51],s41 offen offset:1024 lds\ns_waitcnt lgkmcnt(13)\n"
"v_mfma_f32_32x32x16_bf16 v[0:15],v[112:115],v[116:119],v[0:15]\ns_waitcnt lgkmcnt(12)\n"
"v_mfma_f32_32x32x16_bf16 v[16:31],v[112:115],v[120:123],v[16:31]\ns_waitcnt lgkmcnt(9)\nv_mfma_f32_32x32x16_bf16 v[0:15],v[124:127],v[128:131],v[0:15]\n"
"s_waitcnt lgkmcnt(8)\nv_mfma_f32_32x32x16_bf16 v[16:31],v[124:127],v[132:135],v[16:31]\nv_add_u32 v160,s43,v179\nv_add_u32 v162,s43,v180\n"
"s_waitcnt vmcnt(8) lgkmcnt(0)\ns_barrier\nv_mfma_f32_32x32x16_bf16 v[0:15],v[136:139],v[140:143],v[0:15]\nds_read_b64_tr_b16 v[64:65],v160 offset:0\n"
"ds_read_b64_tr_b16 v[66:67],v160 offset:2048\nds_read_b128 v[68:71],v162 offset:0\nds_read_b128 v[72:75],v162 offset:4096\ns_add_u32 m0,s70,s46\n"
"s_add_u32 s41,s66,61440\nbuffer_load_dwordx4 v176,%[sws],s41 offen offset:0 sc1 lds\nv_mfma_f32_32x32x16_bf16 v[16:31],v[136:139],v[144:147],v[16:31]\n"
"ds_read_b64_tr_b16 v[76:77],v160 offset:4096\nds_read_b64_tr_b16 v[78:79],v160 offset:6144\nds_read_b128 v[80:83],v162 offset:1024\n"
"ds_read_b128 v[84:87],v162 offset:5120\nbuffer_load_dwordx4 v176,%[sws],s41 offen offset:1024 sc1 lds\n"
"v_mfma_f32_32x32x16_bf16 v[0:15],v[148:151],v[152:155],v[0:15]\nds_read_b64_tr_b16 v[88:89],v160 offset:8192\n"
"ds_read_b64_tr_b16 v[90:91],v160 offset:10240\nds_read_b128 v[92:95],v162 offset:2048\nds_read_b128 v[96:99],v162 offset:6144\ns_add_u32 s40,s70,s46\n"
"s_add_u32 m0,s40,16384\ns_add_u32 s41,s47,983040\nbuffer_load_dwordx4 v178,s[48:51],s41 offen offset:0 lds\n"
"v_mfma_f32_32x32x16_bf16 v[16:31],v[148:151],v[156:159],v[16:31]\nds_read_b64_tr_b16 v[100:101],v160 offset:12288\n"
"ds_read_b64_tr_b16 v[102:103],v160 offset:14336\nds_read_b128 v[104:107],v162 offset:3072\nds_read_b128 v[108:111],v162 offset:7168\n"
"s_add_u32 s41,s47,986112\nbuffer_load_dwordx4 v178,s[48:51],s41 offen offset:1024 lds\ns_waitcnt lgkmcnt(13)\n"
"v_mfma_f32_32x32x16_bf16 v[0:15],v[64:67],v[68:71],v[0:15]\ns_waitcnt lgkmcnt(12)\nv_mfma_f32_32x32x16_bf16 v[16:31],v[64:67],v[72:75],v[16:31]\n"
"s_waitcnt lgkmcnt(9)\nv_mfma_f32_32x32x16_bf16 v[0:15],v[76:79],v[80:83],v[0:15]\ns_waitcnt lgkmcnt(8)\n"
"v_mfma_f32_32x32x16_bf16 v[16:31],v[76:79],v[84:87],v[16:31]\nv_add_u32 v161,s44,v179\nv_add_u32 v163,s44,v180\ns_waitcnt vmcnt(8) lgkmcnt(0)\n"
"s_barrier\nv_mfma_f32_32x32x16_bf16 v[0:15],v[88:91],v[92:95],v[0:15]\nds_read_b64_tr_b16 v[112:113],v161 offset:0\n"
"ds_read_b64_tr_b16 v[114:115],v161 offset:2048\nds_read_b128 v[116:119],v163 offset:0\nds_read_b128 v[120:123],v163 offset:4096\ns_add_u32 m0,s70,s43\n"
"s_add_u32 s41,s66,65536\nbuffer_load_dwordx4 v176,%[sws],s41 offen offset:0 sc1 lds\nv_mfma_f32_32x32x16_bf16 v[16:31],v[88:91],v[96:99],v[16:31]\n"
"ds_read_b64_tr_b16 v[124:125],v161 offset:4096\nds_read_b64_tr_b16 v[126:127],v161 offset:6144\nds_read_b128 v[128:131],v163 offset:1024\n"
"ds_read_b128 v[132:135],v163 offset:5120\nbuffer_load_dwordx4 v176,%[sws],s41 offen offset:1024 sc1 lds\n"
"v_mfma_f32_32x32x16_bf16 v[0:15],v[100:103],v[104:107],v[0:15]\nds_read_b64_tr_b16 v[136:137],v161 offset:8192\n"
"ds_read_b64_tr_b16 v[138:139],v161 offset:10240\nds_read_b128 v[140:143],v163 offset:2048\nds_read_b128 v[144:147],v163 offset:6144\n"
"s_add_u32 s40,s70,s43\ns_add_u32 m0,s40,16384\ns_add_u32 s41,s47,1048576\nbuffer_load_dwordx4 v178,s[48:51],s41 offen offset:0 lds\n"
"v_mfma_f32_32x32x16_bf16 v[16:31],v[100:103],v[108:111],v[16:31]\nds_read_b64_tr_b16 v[148:149],v161 offset:12288\n"
"ds_read_b64_tr_b16 v[150:151],v161 offset:14336\nds_read_b128 v[152:155],v163 offset:3072\nds_read_b128 v[156:159],v163 offset:7168\n"
"s_add_u32 s41,s47,1051648\nbuffer_load_dwordx4 v178,s[48:51],s41 offen offset:1024 lds\ns_waitcnt lgkmcnt(13)\n"
"v_mfma_f32_32x32x16_bf16 v[0:15],v[112:115],v[116:119],v[0:15]\ns_waitcnt lgkmcnt(12)\n"
"v_mfma_f32_32x32x16_bf16 v[16:31],v[112:115],v[120:123],v[16:31]\ns_waitcnt lgkmcnt(9)\nv_mfma_f32_32x32x16_bf16 v[0:15],v[124:127],v[128:131],v[0:15]\n"
"s_waitcnt lgkmcnt(8)\nv_mfma_f32_32x32x16_bf16 v[16:31],v[124:127],v[132:135],v[16:31]\nv_add_u32 v160,s45,v179\nv_add_u32 v162,s45,v180\n"
"s_waitcnt vmcnt(8) lgkmcnt(0)\ns_barrier\nv_mfma_f32_32x32x16_bf16 v[0:15],v[136:139],v[140:143],v[0:15]\nds_read_b64_tr_b16 v[64:65],v160 offset:0\n"
"ds_read_b64_tr_b16 v[66:67],v160 offset:2048\nds_read_b128 v[68:71],v162 offset:0\nds_read_b128 v[72:75],v162 offset:4096\ns_add_u32 m0,s70,s44\n"
"s_add_u32 s41,s66,69632\nbuffer_load_dwordx4 v176,%[sws],s41 offen offset:0 sc1 lds\nv_mfma_f32_32x32x16_bf16 v[16:31],v[136:139],v[144:147],v[16:31]\n"
"ds_read_b64_tr_b16 v[76:77],v160 offset:4096\nds_read_b64_tr_b16 v[78:79],v160 offset:6144\nds_read_b128 v[80:83],v162 offset:1024\n"
"ds_read_b128 v[84:87],v162 offset:5120\nbuffer_load_dwordx4 v176,%[sws],s41 offen offset:1024 sc1 lds\n"
"v_mfma_f32_32x32x16_bf16 v[0:15],v[148:151],v[152:155],v[0:15]\nds_read_b64_tr_b16 v[88:89],v160 offset:8192\n"
"ds_read_b64_tr_b16 v[90:91],v160 offset:10240\nds_read_b128 v[92:95],v162 offset:2048\nds_read_b128 v[96:99],v162 offset:6144\ns_add_u32 s40,s70,s44\n"
"s_add_u32 m0,s40,16384\ns_add_u32 s41,s47,1114112\nbuffer_load_dwordx4 v178,s[48:51],s41 offen offset:0 lds\n"
"v_mfma_f32_32x32x16_bf16 v[16:31],v[148:151],v[156:159],v[16:31]\nds_read_b64_tr_b16 v[100:101],v160 offset:12288\n"
"ds_read_b64_tr_b16 v[102:103],v160 offset:14336\nds_read_b128 v[104:107],v162 offset:3072\nds_read_b128 v[108:111],v162 offset:7168\n"
"s_add_u32 s41,s47,1117184\nbuffer_load_dwordx4 v178,s[48:51],s41 offen offset:1024 lds\ns_waitcnt lgkmcnt(13)\n"
"v_mfma_f32_32x32x16_bf16 v[0:15],v[64:67],v[68:71],v[0:15]\ns_waitcnt lgkmcnt(12)\nv_mfma_f32_32x32x16_bf16 v[16:31],v[64:67],v[72:75],v[16:31]\n"
"s_waitcnt lgkmcnt(9)\nv_mfma_f32_32x32x16_bf16 v[0:15],v[76:79],v[80:83],v[0:15]\ns_waitcnt lgkmcnt(8)\n"
"v_mfma_f32_32x32x16_bf16 v[16:31],v[76:79],v[84:87],v[16:31]\nv_add_u32 v161,s46,v179\nv_add_u32 v163,s46,v180\ns_waitcnt vmcnt(8) lgkmcnt(0)\n"
"s_barrier\nv_mfma_f32_32x32x16_bf16 v[0:15],v[88:91],v[92:95],v[0:15]\nds_read_b64_tr_b16 v[112:113],v161 offset:0\n"
"ds_read_b64_tr_b16 v[114:115],v161 offset:2048\nds_read_b128 v[116:119],v163 offset:0\nds_read_b128 v[120:123],v163 offset:4096\n"
"v_mfma_f32_32x32x16_bf16 v[16:31],v[88:91],v[96:99],v[16:31]\nds_read_b64_tr_b16 v[124:125],v161 offset:4096\n"
"ds_read_b64_tr_b16 v[126:127],v161 offset:6144\nds_read_b128 v[128:131],v163 offset:1024\nds_read_b128 v[132:135],v163 offset:5120\n"
"v_mfma_f32_32x32x16_bf16 v[0:15],v[100:103],v[104:107],v[0:15]\nds_read_b64_tr_b16 v[136:137],v161 offset:8192\n"
"ds_read_b64_tr_b16 v[138:139],v161 offset:10240\nds_read_b128 v[140:143],v163 offset:2048\nds_read_b128 v[144:147],v163 offset:6144\n"
"v_mfma_f32_32x32x16_bf16 v[16:31],v[100:103],v[108:111],v[16:31]\nds_read_b64_tr_b16 v[148:149],v161 offset:12288\n"
"ds_read_b64_tr_b16 v[150:151],v161 offset:14336\nds_read_b128 v[152:155],v163 offset:3072\nds_read_b128 v[156:159],v163 offset:7168\n"
"s_waitcnt lgkmcnt(13)\nv_mfma_f32_32x32x16_bf16 v[0:15],v[112:115],v[116:119],v[0:15]\ns_waitcnt lgkmcnt(12)\n"
"v_mfma_f32_32x32x16_bf16 v[16:31],v[112:115],v[120:123],v[16:31]\ns_waitcnt lgkmcnt(9)\nv_mfma_f32_32x32x16_bf16 v[0:15],v[124:127],v[128:131],v[0:15]\n"
"s_waitcnt lgkmcnt(8)\nv_mfma_f32_32x32x16_bf16 v[16:31],v[124:127],v[132:135],v[16:31]\nv_add_u32 v160,s43,v179\nv_add_u32 v162,s43,v180\n"
"s_waitcnt vmcnt(4) lgkmcnt(0)\ns_barrier\nv_mfma_f32_32x32x16_bf16 v[0:15],v[136:139],v[140:143],v[0:15]\nds_read_b64_tr_b16 v[64:65],v160 offset:0\n"
"ds_read_b64_tr_b16 v[66:67],v160 offset:2048\nds_read_b128 v[68:71],v162 offset:0\nds_read_b128 v[72:75],v162 offset:4096\n"
"v_mfma_f32_32x32x16_bf16 v[16:31],v[136:139],v[144:147],v[16:31]\nds_read_b64_tr_b16 v[76:77],v160 offset:4096\n"
"ds_read_b64_tr_b16 v[78:79],v160 offset:6144\nds_read_b128 v[80:83],v162 offset:1024\nds_read_b128 v[84:87],v162 offset:5120\n"
"v_mfma_f32_32x32x16_bf16 v[0:15],v[148:151],v[152:155],v[0:15]\nds_read_b64_tr_b16 v[88:89],v160 offset:8192\n"
"ds_read_b64_tr_b16 v[90:91],v160 offset:10240\nds_read_b128 v[92:95],v162 offset:2048\nds_read_b128 v[96:99],v162 offset:6144\n"
"v_mfma_f32_32x32x16_bf16 v[16:31],v[148:151],v[156:159],v[16:31]\nds_read_b64_tr_b16 v[100:101],v160 offset:12288\n"
"ds_read_b64_tr_b16 v[102:103],v160 offset:14336\nds_read_b128 v[104:107],v162 offset:3072\nds_read_b128 v[108:111],v162 offset:7168\n"
"s_waitcnt lgkmcnt(13)\nv_mfma_f32_32x32x16_bf16 v[0:15],v[64:67],v[68:71],v[0:15]\ns_waitcnt lgkmcnt(12)\n"
"v_mfma_f32_32x32x16_bf16 v[16:31],v[64:67],v[72:75],v[16:31]\ns_waitcnt lgkmcnt(9)\nv_mfma_f32_32x32x16_bf16 v[0:15],v[76:79],v[80:83],v[0:15]\n"
"s_waitcnt lgkmcnt(8)\nv_mfma_f32_32x32x16_bf16 v[16:31],v[76:79],v[84:87],v[16:31]\nv_add_u32 v161,s44,v179\nv_add_u32 v163,s44,v180\n"
"s_waitcnt vmcnt(0) lgkmcnt(0)\ns_barrier\nv_mfma_f32_32x32x16_bf16 v[0:15],v[88:91],v[92:95],v[0:15]\nds_read_b64_tr_b16 v[112:113],v161 offset:0\n"
"ds_read_b64_tr_b16 v[114:115],v161 offset:2048\nds_read_b128 v[116:119],v163 offset:0\nds_read_b128 v[120:123],v163 offset:4096\n"
"v_mfma_f32_32x32x16_bf16 v[16:31],v[88:91],v[96:99],v[16:31]\nds_read_b64_tr_b16 v[124:125],v161 offset:4096\n"
"ds_read_b64_tr_b16 v[126:127],v161 offset:6144\nds_read_b128 v[128:131],v163 offset:1024\nds_read_b128 v[132:135],v163 offset:5120\n"
"v_mfma_f32_32x32x16_bf16 v[0:15],v[100:103],v[104:107],v[0:15]\nds_read_b64_tr_b16 v[136:137],v161 offset:8192\n"
"ds_read_b64_tr_b16 v[138:139],v161 offset:10240\nds_read_b128 v[140:143],v163 offset:2048\nds_read_b128 v[144:147],v163 offset:6144\n"
"v_mfma_f32_32x32x16_bf16 v[16:31],v[100:103],v[108:111],v[16:31]\nds_read_b64_tr_b16 v[148:149],v161 offset:12288\n"
"ds_read_b64_tr_b16 v[150:151],v161 offset:14336\nds_read_b128 v[152:155],v163 offset:3072\nds_read_b128 v[156:159],v163 offset:7168\n"
"s_waitcnt lgkmcnt(13)\nv_mfma_f32_32x32x16_bf16 v[0:15],v[112:115],v[116:119],v[0:15]\ns_waitcnt lgkmcnt(12)\n"
"v_mfma_f32_32x32x16_bf16 v[16:31],v[112:115],v[120:123],v[16:31]\ns_waitcnt lgkmcnt(9)\nv_mfma_f32_32x32x16_bf16 v[0:15],v[124:127],v[128:131],v[0:15]\n"
"s_waitcnt lgkmcnt(8)\nv_mfma_f32_32x32x16_bf16 v[16:31],v[124:127],v[132:135],v[16:31]\ns_waitcnt lgkmcnt(5)\n"
"v_mfma_f32_32x32x16_bf16 v[0:15],v[136:139],v[140:143],v[0:15]\ns_waitcnt lgkmcnt(4)\nv_mfma_f32_32x32x16_bf16 v[16:31],v[136:139],v[144:147],v[16:31]\n"
"s_waitcnt lgkmcnt(1)\nv_mfma_f32_32x32x16_bf16 v[0:15],v[148:151],v[152:155],v[0:15]\ns_waitcnt lgkmcnt(0)\n"
"v_mfma_f32_32x32x16_bf16 v[16:31],v[148:151],v[156:159],v[16:31]\ns_nop 7\ns_nop 7\n"
    : "={v[0:15]}"(m0), "={v[16:31]}"(m1)
    : "{v[176:183]}"(tv), "{s[64:67]}"(T0_), "{s[68:71]}"(T1_), "{s[76:79]}"(T3_), "{s80}"(rfl(is3)), "{s81}"(rfl((bid & 7) * 128)), "{s82}"(rfl(mt * 64)), [sws] "s"(sws), "{s[56:59]}"(sw), "{s[60:63]}"(swa)
    : "v64", "v65", "v66", "v67", "v68", "v69", "v70", "v71", "v72", "v73", "v74", "v75", "v76", "v77", "v78", "v79", "v80", "v81", "v82", "v83", "v84", "v85", "v86", "v87", "v88", "v89", "v90", "v91", "v92", "v93", "v94", "v95", "v96", "v97", "v98", "v99", "v100", "v101", "v102", "v103", "v104", "v105", "v106", "v107", "v108", "v109", "v110", "v111", "v112", "v113", "v114", "v115", "v116", "v117", "v118", "v119", "v120", "v121", "v122", "v123", "v124", "v125", "v126", "v127", "v128", "v129", "v130", "v131", "v132", "v133", "v134", "v135", "v136", "v137", "v138", "v139", "v140", "v141", "v142", "v143", "v144", "v145", "v146", "v147", "v148", "v149", "v150", "v151", "v152", "v153", "v154", "v155", "v156", "v157", "v158", "v159", "v160", "v161", "v162", "v163", "v172", "v173", "s40", "s41", "s42", "s43", "s44", "s45", "s46", "s47", "s48", "s49", "s50", "s51", "vcc", "scc", "memory");
    const int tb = mt * 4 + (wid & 1) * 2, nb = nt * 4 + (wid >> 1);
    const __amdgpu_buffer_rsrc_t sp_ = __builtin_amdgcn_make_buffer_rsrc((void*)(ws + WS_P), (short)0, 8 << 20, 0x00020000);
    const unsigned po = ((tb * 16 + nb * 2) * 2048 + lane * 16);
    const u32x4 u0 = pack8(m0, 0, 1.f), u1 = pack8(m0, 8, 1.f), u2 = pack8(m1, 0, 1.f), u3 = pack8(m1, 8, 1.f);
    if (sp != 0) __builtin_amdgcn_raw_buffer_store_b128(u0, sp_, po + sp * 2097152, 0, 16);
    if (sp != 1) __builtin_amdgcn_raw_buffer_store_b128(u1, sp_, po + sp * 2097152 + 1024, 0, 16);
    if (sp != 2) __builtin_amdgcn_raw_buffer_store_b128(u2, sp_, po + sp * 2097152 + 2048, 0, 16);
    if (sp != 3) __builtin_amdgcn_raw_buffer_store_b128(u3, sp_, po + sp * 2097152 + 3072, 0, 16);
    asm volatile("s_nop 1\n\ts_waitcnt vmcnt(0)\n\ts_barrier" ::: "memory");
    unsigned* f4 = (unsigned*)(ws + WS_FLAG + 13312) + (mt * 4 + nt) * 16;
    if (wid == 0) {
      if (lane == 0) __hip_atomic_fetch_add(f4, 1u, __ATOMIC_RELAXED, __HIP_MEMORY_SCOPE_AGENT);
      for (int spin = 0; spin < (1 << 22); ++spin) {
        if (rfl(__hip_atomic_load(f4, __ATOMIC_RELAXED, __HIP_MEMORY_SCOPE_AGENT)) >= 4u) break;
        __builtin_amdgcn_s_sleep(FSLEEP);
      }
    }
    asm volatile("s_waitcnt vmcnt(0)\n\ts_barrier" ::: "memory");
    const f32x16 mm = sp < 2 ? m0 : m1;
    float f[8];
#pragma unroll
    for (int j = 0; j < 8; ++j) f[j] = (sp & 1) ? mm[8 + j] : mm[j];
    u32x4 pv[3];
#pragma unroll
    for (int k = 0; k < 3; ++k) pv[k] = __builtin_amdgcn_raw_buffer_load_b128(sp_, po + ((sp + 1 + k) & 3) * 2097152 + sp * 1024, 0, 16);
#pragma unroll
    for (int k = 0; k < 3; ++k)
#pragma unroll
      for (int d = 0; d < 4; ++d) { f[2 * d] += __uint_as_float(pv[k][d] << 16); f[2 * d + 1] += __uint_as_float(pv[k][d] & 0xffff0000u); }
    u16* op = out + (size_t)((tb + (sp >> 1)) * 32 + r) * DM + nb * 32 + (sp & 1) * 16 + 4 * hi;
    *(u32x2*)(op) = (u32x2){cvtpk(f[0], f[1]), cvtpk(f[2], f[3])};
    *(u32x2*)(op + 8) = (u32x2){cvtpk(f[4], f[5]), cvtpk(f[6], f[7])};
  }
}

extern "C" void kernel_launch(void* const* d_in, const int* in_sizes, int n_in, void* d_out, int out_size, void* d_ws, size_t ws_size,
                              hipStream_t stream) {
  const u16* x = (const u16*)d_in[0];
  const u16* wq = (const u16*)d_in[1];
  const u16* wk = (const u16*)d_in[2];
  const u16* wv = (const u16*)d_in[3];
  const u16* wo = (const u16*)d_in[4];
  const u16* ckv = (const u16*)d_in[6];
  const u16* w_q = (const u16*)d_in[7];
  const u16* w_uk = (const u16*)d_in[8];
  const u16* w_uv = (const u16*)d_in[9];
  const u16* w_o = (const u16*)d_in[10];
  const u16* gw = (const u16*)d_in[11];
  char* ws = (char*)d_ws;
  (void)hipFuncSetAttribute((const void*)k0_prep, hipFuncAttributeMaxDynamicSharedMemorySize, 163840);
  (void)hipFuncSetAttribute((const void*)k12_main, hipFuncAttributeMaxDynamicSharedMemorySize, 163840);
  k0_prep<<<256, 768, 163840, stream>>>(x, wq, wk, wv, gw, ckv, w_q, w_uk, w_uv, ws);
  k12_main<<<256, 512, 163840, stream>>>(ckv, w_q, w_uk, w_uv, w_o, wo, ws, (u16*)d_out);
}
```
